```python
import torch

def _softmax(x, axis=-1):
    return torch.softmax(x, dim=axis)

def _gelu(x):
    return torch.nn.functional.gelu(x, approximate='tanh')

def _silu(x):
    return torch.nn.functional.silu(x)

def _sigmoid(x):
    return torch.sigmoid(x)

def _layernorm(x, axis=-1, eps=1e-05):
    m = x.mean(dim=axis, keepdim=True)
    v = x.var(dim=axis, keepdim=True, unbiased=False)
    return (x - m) / torch.sqrt(v + eps)

def _scatter_add(tgt, idx, src, axis=0):
    return tgt.clone().index_add_(axis, idx, src)

def _segment_sum(src, seg, n):
    out = torch.zeros((n,) + src.shape[1:], dtype=src.dtype)
    return out.index_add_(0, seg, src)

def _pad(x, pw):
    flat = tuple((v for pair in reversed(pw) for v in pair))
    return torch.nn.functional.pad(x, flat)
kernel_name = 'adaLN_cond__ssm_conv_s4d__mixer_gmlp__prompt'

def setup_inputs():

    _w = lambda a: (lambda t: t.to(torch.bfloat16) if t.is_floating_point() else t)(torch.as_tensor(a))

    def _A_setup():
        Bsz, L, D, N = (1, 2048, 512, 128)
        return {'u': _w((lambda *s: torch.randn(*s, dtype=torch.bfloat16) * 0.05)(Bsz, L, D)), 'log_A_real': _w((lambda *s: torch.randn(*s, dtype=torch.bfloat16) * 3.2)(D, N) * 0.5), 'A_imag': _w((lambda *s: torch.randn(*s, dtype=torch.bfloat16) * 0.2)(D, N) * 3.14), 'C_real': _w((lambda *s: torch.randn(*s, dtype=torch.bfloat16) * 0.8)(D, N) / N ** 0.5), 'C_imag': _w((lambda *s: torch.randn(*s, dtype=torch.bfloat16) * 0.8)(D, N) / N ** 0.5), 'log_dt': _w((lambda *s: torch.randn(*s, dtype=torch.bfloat16) * 0.2)(D) * 0.5 - 4.0), 'D_skip': _w((lambda *s: torch.randn(*s, dtype=torch.bfloat16) * 0.2)(D))}
    _A_in = _A_setup()

    def _B_setup():
        B, S, D, F = (1, 2048, 512, 1376)
        Fh = F // 2
        return {'x': _w((lambda *s: torch.randn(*s, dtype=torch.bfloat16) * 0.05)(B, S, D)), 'w_in': _w((lambda *s: torch.randn(*s, dtype=torch.bfloat16) * 0.4)(D, 2 * Fh)), 'w_sp': _w(0.01 * (lambda *s: torch.randn(*s, dtype=torch.bfloat16) * 0.4)(S, S)), 'b_sp': _w(1.0 + 0.01 * (lambda *s: torch.randn(*s, dtype=torch.bfloat16) * 0.05)(S)), 'w_out': _w((lambda *s: torch.randn(*s, dtype=torch.bfloat16) * 0.05)(Fh, D))}
    _B_in = _B_setup()
    return {'A_u': _A_in['u'], 'A_log_A_real': _A_in['log_A_real'], 'A_A_imag': _A_in['A_imag'], 'A_C_real': _A_in['C_real'], 'A_C_imag': _A_in['C_imag'], 'A_log_dt': _A_in['log_dt'], 'A_D_skip': _A_in['D_skip'], 'B_x': _B_in['x'], 'B_w_in': _B_in['w_in'], 'B_w_sp': _B_in['w_sp'], 'B_b_sp': _B_in['b_sp'], 'B_w_out': _B_in['w_out'], 'c': _w((lambda *s: torch.randn(*s, dtype=torch.bfloat16) * 0.2)(1, 512) * 8.0), 'w_mod': _w((lambda *s: torch.randn(*s, dtype=torch.bfloat16) * 0.2)(512, 6 * 512) * 512 ** (-0.5) * 8.0)}

def reference(A_u, A_log_A_real, A_A_imag, A_C_real, A_C_imag, A_log_dt, A_D_skip, B_x, B_w_in, B_w_sp, B_b_sp, B_w_out, c, w_mod):

    def _A_ref(u, log_A_real, A_imag, C_real, C_imag, log_dt, D_skip):
        L = u.shape[1]
        dt = torch.exp(log_dt)
        A = -torch.exp(log_A_real) + 1j * A_imag
        C = (C_real + 1j * C_imag) * dt[:, None]
        pos = (lambda x, dt: x.to(dt))(torch.arange(L), log_dt.dtype)
        P = torch.exp((A * dt[:, None])[:, :, None] * pos[None, None, :])
        K = 2.0 * torch.einsum('dn,dnl->dl', C, P).real
        L2 = 2 * L
        u_t = (lambda x, *d: x.permute(*d))(u, 0, 2, 1)
        Kf = (lambda x, *a, **k: torch.fft.rfft(x.float(), *a, **k))(K, L2)
        Uf = (lambda x, *a, **k: torch.fft.rfft(x.float(), *a, **k))(u_t, L2)
        y = (lambda x, *a, **k: torch.fft.irfft(x, *a, **k).to(torch.bfloat16))(Uf * Kf[None, :, :], L2)[:, :, :L]
        y = (lambda x, *d: x.permute(*d))(y, 0, 2, 1)
        return y + u * D_skip[None, None, :]

    def _B_ref(x, w_in, w_sp, b_sp, w_out):
        B, S, D = x.shape
        F = w_in.shape[-1]
        Fh = F // 2
        z = _gelu(torch.einsum('bsd,df->bsf', x, w_in))
        z1 = z[..., :Fh].clone()
        z2 = _layernorm(z[..., Fh:], axis=-1)
        g = torch.einsum('ts,bsf->btf', w_sp, z2) + b_sp[None, :, None]
        return torch.einsum('bsf,fd->bsd', z1 * g, w_out)
    x = A_u
    mod = (_silu(c) @ w_mod).reshape(c.shape[0], 6, 1, -1)
    sa, ca, ga, sb, cb, gb = (mod[:, 0].clone(), mod[:, 1].clone(), mod[:, 2].clone(), mod[:, 3].clone(), mod[:, 4].clone(), mod[:, 5].clone())
    h = _layernorm(x, axis=-1) * (1 + ca) + sa
    x = x + ga * _A_ref(h, A_log_A_real, A_A_imag, A_C_real, A_C_imag, A_log_dt, A_D_skip)
    h = _layernorm(x, axis=-1) * (1 + cb) + sb
    x = x + gb * _B_ref(h, B_w_in, B_w_sp, B_b_sp, B_w_out)
    return x
```

```cpp
#include <hip/hip_runtime.h>
#include <hip/hip_bf16.h>
#include <stdint.h>

#define GAS __attribute__((address_space(1)))
#define LAS __attribute__((address_space(3)))
typedef unsigned v4u __attribute__((ext_vector_type(4)));
typedef unsigned v2u __attribute__((ext_vector_type(2)));
typedef float f32x4 __attribute__((ext_vector_type(4)));
typedef float f32x2 __attribute__((ext_vector_type(2)));
typedef short bf16x8 __attribute__((ext_vector_type(8)));
typedef short s16x4 __attribute__((ext_vector_type(4)));
typedef _Float16 f16x8 __attribute__((ext_vector_type(8)));
typedef _Float16 f16x2 __attribute__((ext_vector_type(2)));
typedef __bf16 bf16x2_t __attribute__((ext_vector_type(2)));
typedef unsigned short u16;

namespace {

constexpr int L = 2048, D = 512, NM = 128, FH = 688, FF = 1376;
constexpr int NU = 44;
constexpr int KROW = 2112;
constexpr float EPS = 1e-5f;

constexpr size_t WS_MODP = 0;
constexpr size_t WS_ST1 = WS_MODP + 4 * 3072 * 4;
constexpr size_t WS_CSB = WS_ST1 + 2048 * 8;
constexpr size_t WS_ST2 = WS_CSB + 4 * 2 * 1408 * 4;
constexpr size_t WS_UT = (size_t)1 << 20;
constexpr size_t WS_KREV = WS_UT + 512 * 2048 * 2;
constexpr size_t WS_X1T = WS_KREV + (size_t)512 * KROW * 2;
constexpr size_t WS_WP = WS_X1T + 512 * 2048 * 2;
constexpr size_t WS_WOT = WS_WP + 2 * 44 * 16 * 1024;
constexpr size_t WS_Z2 = WS_WOT + 32 * 22 * 1024;
constexpr size_t WS_Z1 = WS_Z2 + 44 * 64 * 1024;
constexpr size_t WS_V = WS_Z1 + 128 * 22 * 1024;
constexpr size_t WS_X1F = WS_V + 128 * 22 * 1024;
constexpr size_t WS_END = WS_X1F + 128 * 16 * 1024;

struct Params {
  const u16 *u, *lar, *aim, *cre, *cim, *ldt, *dsk, *win, *wsp, *bsp, *wout, *c, *wmod;
  u16* out;
  unsigned char* ws;
};

__device__ __forceinline__ float bf2f(unsigned b) { return __builtin_bit_cast(float, b << 16); }
__device__ __forceinline__ float bflo(unsigned w) { return __builtin_bit_cast(float, w << 16); }
__device__ __forceinline__ float bfhi(unsigned w) { return __builtin_bit_cast(float, w & 0xFFFF0000u); }
__device__ __forceinline__ unsigned pk2(float lo, float hi) {
  f32x2 v = {lo, hi};
  return __builtin_bit_cast(unsigned, __builtin_convertvector(v, bf16x2_t));
}
__device__ __forceinline__ float rbf(float x) { return bflo(pk2(x, 0.f)); }
__device__ __forceinline__ f32x4 mfma(v4u a, v4u b, f32x4 c) {
  return __builtin_amdgcn_mfma_f32_16x16x32_bf16(__builtin_bit_cast(bf16x8, a), __builtin_bit_cast(bf16x8, b), c, 0, 0, 0);
}
__device__ __forceinline__ f32x4 mfma_h(v4u a, v4u b, f32x4 c) {
  return __builtin_amdgcn_mfma_f32_16x16x32_f16(__builtin_bit_cast(f16x8, a), __builtin_bit_cast(f16x8, b), c, 0, 0, 0);
}
__device__ __forceinline__ int perm(int u, int c) { return 32 * (u >> 1) + 8 * (c >> 2) + 4 * (u & 1) + (c & 3); }
__device__ __forceinline__ float wave_sum(float v) {
#pragma unroll
  for (int o = 32; o > 0; o >>= 1) v += __shfl_xor(v, o);
  return v;
}
__device__ __forceinline__ float wave_max(float v) {
#pragma unroll
  for (int o = 32; o > 0; o >>= 1) v = fmaxf(v, __shfl_xor(v, o));
  return v;
}
__device__ __forceinline__ float gelu_tanh(float x) {
  const float k0 = -2.0f * 0.7978845608028654f * 1.4426950408889634f;
  const float k1 = k0 * 0.044715f;
  const float e = __builtin_amdgcn_exp2f(x * (k0 + k1 * x * x));
  return x * __builtin_amdgcn_rcpf(1.0f + e);
}
__device__ __forceinline__ float modval(const unsigned char* ws, int j) {
  const float* mp = (const float*)(ws + WS_MODP);
  return rbf((mp[j] + mp[3072 + j]) + (mp[2 * 3072 + j] + mp[3 * 3072 + j]));
}
struct cpx { float re, im; };
__device__ __forceinline__ cpx cmul(cpx a, cpx b) { return cpx{a.re * b.re - a.im * b.im, a.re * b.im + a.im * b.re}; }
__device__ __forceinline__ cpx cexp_p(float are, float aim, float p) {
  const float mag = __builtin_amdgcn_exp2f(are * p * 1.4426950408889634f);
  const float th = aim * p;
  const float hi = th * 0.15915494309189535f;
  const float lo = __builtin_fmaf(th, 0.15915494309189535f, -hi) + th * 6.4206383e-9f;
  const float fr = (hi - __builtin_rintf(hi)) + lo;
  return cpx{mag * __builtin_amdgcn_cosf(fr), mag * __builtin_amdgcn_sinf(fr)};
}

constexpr int K0_LN = 128, K0_MOD = 192, K0_KG = 512, K0_WO = 88;
constexpr int K0_GRID = K0_LN + K0_MOD + K0_KG + K0_WO;
constexpr int K0_LDS = 64 * 528 + 64;

__device__ __forceinline__ void role_ln(const Params& p, unsigned char* smem, int b) {
  const int tid = threadIdx.x, lane = tid & 63, w = tid >> 6;
  const int rg = (b & 7) * 16 + (b >> 3);
  const int s0 = rg * 16;
  u16* tile = (u16*)smem;
  v4u x[4];
#pragma unroll
  for (int i = 0; i < 4; ++i) x[i] = *(const v4u*)(p.u + (size_t)(s0 + w * 4 + i) * D + lane * 8);
#pragma unroll
  for (int i = 0; i < 4; ++i) {
    float f[8];
#pragma unroll
    for (int j = 0; j < 4; ++j) { f[2 * j] = bflo(x[i][j]); f[2 * j + 1] = bfhi(x[i][j]); }
    float s = 0.f;
#pragma unroll
    for (int j = 0; j < 8; ++j) s += f[j];
    const float m = wave_sum(s) * (1.0f / D);
    float q = 0.f;
#pragma unroll
    for (int j = 0; j < 8; ++j) { const float dlt = f[j] - m; q += dlt * dlt; }
    const float var = wave_sum(q) * (1.0f / D);
    if (lane == 0) ((f32x2*)(p.ws + WS_ST1))[s0 + w * 4 + i] = f32x2{m, 1.0f / sqrtf(var + EPS)};
    *(v4u*)(tile + (w * 4 + i) * 520 + lane * 8) = x[i];
  }
  __syncthreads();
  u16* ut = (u16*)(p.ws + WS_UT);
#pragma unroll
  for (int it = 0; it < 4; ++it) {
    const int item = it * 256 + tid, d = item >> 1, hf = item & 1;
    v4u o;
#pragma unroll
    for (int j = 0; j < 4; ++j)
      o[j] = (unsigned)tile[(hf * 8 + 2 * j) * 520 + d] | ((unsigned)tile[(hf * 8 + 2 * j + 1) * 520 + d] << 16);
    *(v4u*)(ut + (size_t)d * L + s0 + hf * 8) = o;
  }
}

__device__ __forceinline__ void role_mod(const Params& p, unsigned char* smem, int b) {
  const int tid = threadIdx.x;
  const int cg = b % 48, kg = b / 48;
  const int r = tid >> 3, ch = tid & 7;
  v4u x[4];
  float sc[4];
#pragma unroll
  for (int i = 0; i < 4; ++i) {
    const int k = kg * 128 + i * 32 + r;
    x[i] = *(const v4u*)(p.wmod + (size_t)k * 3072 + cg * 64 + ch * 8);
    const float cv = bf2f(p.c[k]);
    sc[i] = rbf(cv / (1.0f + __expf(-cv)));
  }
  float a[8];
#pragma unroll
  for (int j = 0; j < 8; ++j) a[j] = 0.f;
#pragma unroll
  for (int i = 0; i < 4; ++i)
#pragma unroll
    for (int j = 0; j < 4; ++j) { a[2 * j] += sc[i] * bflo(x[i][j]); a[2 * j + 1] += sc[i] * bfhi(x[i][j]); }
  float* red = (float*)smem;
  *(f32x4*)(red + r * 68 + ch * 8) = f32x4{a[0], a[1], a[2], a[3]};
  *(f32x4*)(red + r * 68 + ch * 8 + 4) = f32x4{a[4], a[5], a[6], a[7]};
  __syncthreads();
  if (tid < 64) {
    float s = 0.f;
#pragma unroll
    for (int i = 0; i < 32; ++i) s += red[i * 68 + tid];
    ((float*)(p.ws + WS_MODP))[kg * 3072 + cg * 64 + tid] = s;
  }
}

__device__ __forceinline__ unsigned pkh(float a, float b) {
  f16x2 v = {(_Float16)a, (_Float16)b};
  return __builtin_bit_cast(unsigned, v);
}

__device__ __forceinline__ void role_kgen(const Params& p, unsigned char* smem, int d) {
  const int tid = threadIdx.x, lane = tid & 63, w = __builtin_amdgcn_readfirstlane(tid >> 6);
  const int hf = w >> 1, n = tid & 127;
  unsigned char* tab = smem;
  float* kpos = (float*)smem;
  float* wmax = (float*)(smem + 64 * 528);
  const float dt = rbf(expf(bf2f(p.ldt[d])));
  const float are = -rbf(expf(bf2f(p.lar[d * NM + n]))) * dt;
  const float aim = bf2f(p.aim[d * NM + n]) * dt;
  const float cre = bf2f(p.cre[d * NM + n]) * dt, cim = bf2f(p.cim[d * NM + n]) * dt;
  const float mx = wave_max(fmaxf(fabsf(cre), fabsf(cim)));
  if (lane == 0) wmax[w] = mx;
  const cpx e128 = cexp_p(are, aim, 128.f);
  cpx t1[8];
  if (hf == 0) {
    const cpx e8 = cexp_p(are, aim, 8.f);
    cpx f = {1.f, 0.f};
#pragma unroll
    for (int j = 0; j < 16; ++j) {
      *(unsigned*)(tab + (48 + j) * 528 + n * 4) = pkh(f.re, f.im);
      f = cmul(f, e8);
    }
    t1[0] = cexp_p(are, aim, 4.f);
    t1[1] = cexp_p(are, aim, 512.f);
#pragma unroll
    for (int rr = 41; rr < 48; ++rr) *(unsigned*)(tab + rr * 528 + n * 4) = 0u;
  } else {
    const cpx e1 = cexp_p(are, aim, 1.f);
    t1[0] = cpx{1.f, 0.f};
#pragma unroll
    for (int v = 1; v < 8; ++v) t1[v] = cmul(t1[v - 1], e1);
  }
  __syncthreads();
  const float cm = fmaxf(wmax[0], wmax[1]);
  int ex = 0;
  (void)frexpf(cm, &ex);
  ex = cm > 0.f ? ex + 1 : 0;
  ex = ex < -100 ? -100 : ex;
  const float scale = ldexpf(1.f, -ex), unscale = ldexpf(2.f, ex);
  cpx cu = {cre * scale, cim * scale};
  if (hf == 0) {
    cu = cmul(cu, t1[1]);
#pragma unroll
    for (int uu = 4; uu <= 16; ++uu) {
      if (uu < 8) {
        const cpx z = cmul(cu, t1[0]);
        *(unsigned*)(tab + (24 + (uu - 4) * 2) * 528 + n * 4) = pkh(cu.re, -cu.im);
        *(unsigned*)(tab + (24 + (uu - 4) * 2 + 1) * 528 + n * 4) = pkh(z.re, -z.im);
      } else {
        *(unsigned*)(tab + (32 + uu - 8) * 528 + n * 4) = pkh(cu.re, -cu.im);
      }
      cu = cmul(cu, e128);
    }
  } else {
#pragma unroll
    for (int uu = 0; uu < 4; ++uu) {
      if (uu < 2) {
#pragma unroll
        for (int v = 0; v < 8; ++v) { const cpx z = cmul(cu, t1[v]); *(unsigned*)(tab + (uu * 8 + v) * 528 + n * 4) = pkh(z.re, -z.im); }
      } else {
#pragma unroll
        for (int v = 0; v < 4; ++v) { const cpx z = cmul(cu, t1[2 * v]); *(unsigned*)(tab + (16 + (uu - 2) * 4 + v) * 528 + n * 4) = pkh(z.re, -z.im); }
      }
      cu = cmul(cu, e128);
    }
  }
  __syncthreads();
  const int c = lane & 15, q = lane >> 4;
  f32x4 acc = {0.f, 0.f, 0.f, 0.f};
  if (w < 3) {
#pragma unroll
    for (int ks = 0; ks < 8; ++ks) {
      const v4u a = *(const v4u*)(tab + (w * 16 + c) * 528 + ks * 64 + q * 16);
      const v4u bb = *(const v4u*)(tab + (48 + c) * 528 + ks * 64 + q * 16);
      acc = mfma_h(a, bb, acc);
    }
  }
  __syncthreads();
  if (w < 3) {
#pragma unroll
    for (int r = 0; r < 4; ++r) {
      const int i = w * 16 + q * 4 + r;
      int co;
      if (i < 16) co = 128 * (i >> 3) + (i & 7);
      else if (i < 24) co = 256 + 128 * ((i - 16) >> 2) + 2 * ((i - 16) & 3);
      else if (i < 32) co = 512 + 128 * ((i - 24) >> 1) + 4 * ((i - 24) & 1);
      else co = 1024 + 128 * (i - 32);
      if (i <= 40) kpos[co + 8 * c] = acc[r] * unscale;
    }
  }
  __syncthreads();
  u16* krev = (u16*)(p.ws + WS_KREV) + (size_t)d * KROW;
  {
    float v[8];
#pragma unroll
    for (int j = 0; j < 8; ++j) {
      const int lag = 2047 - (tid * 8 + j);
      const int pos = (int)rbf((float)lag);
      v[j] = kpos[pos];
    }
    *(v4u*)(krev + tid * 8) = v4u{pk2(v[0], v[1]), pk2(v[2], v[3]), pk2(v[4], v[5]), pk2(v[6], v[7])};
    if (tid < KROW / 8 - 256) *(v4u*)(krev + 2048 + tid * 8) = v4u{0u, 0u, 0u, 0u};
  }
}

__device__ __forceinline__ void role_wout(const Params& p, unsigned char* smem, int b) {
  const int tid = threadIdx.x, lane = tid & 63, w = tid >> 6;
  const int ks = b >> 2, dq = b & 3;
  u16* tile = (u16*)smem;
#pragma unroll
  for (int it = 0; it < 2; ++it) {
    const int item = it * 256 + tid, r = item >> 4, chn = item & 15, f = ks * 32 + r;
    v4u x = {0u, 0u, 0u, 0u};
    if (f < FH) x = *(const v4u*)(p.wout + (size_t)f * D + dq * 128 + chn * 8);
    *(v4u*)(tile + r * 136 + chn * 8) = x;
  }
  __syncthreads();
  const int c = lane & 15, q = lane >> 4;
#pragma unroll
  for (int i = 0; i < 2; ++i) {
    const int ul = w * 2 + i, col = perm(ul, c);
    v4u o;
#pragma unroll
    for (int j = 0; j < 4; ++j)
      o[j] = (unsigned)tile[(8 * q + 2 * j) * 136 + col] | ((unsigned)tile[(8 * q + 2 * j + 1) * 136 + col] << 16);
    *(v4u*)(p.ws + WS_WOT + ((size_t)(dq * 8 + ul) * 22 + ks) * 1024 + lane * 16) = o;
  }
}

__global__ void __launch_bounds__(256) k0(Params p) {
  extern __shared__ __attribute__((aligned(16))) unsigned char smem[];
  int b = blockIdx.x;
  if (b < K0_LN) { role_ln(p, smem, b); return; }
  b -= K0_LN;
  if (b < K0_MOD) { role_mod(p, smem, b); return; }
  b -= K0_MOD;
  if (b < K0_KG) { role_kgen(p, smem, b); return; }
  b -= K0_KG;
  role_wout(p, smem, b);
}

constexpr int CV_RK = 4160, CV_HB = 4640;
constexpr int CV_CH = 4 * CV_RK + 2 * CV_HB + 4096;
constexpr int K1_LDS = 2 * CV_CH + 4 * 4096;
constexpr int K1_CONV = 256, K1_WP = 176, K1_GRID = K1_CONV + K1_WP;

__device__ __forceinline__ void role_conv(const Params& p, unsigned char* smem, int b) {
  const int tid = threadIdx.x, lane = tid & 63, w = __builtin_amdgcn_readfirstlane(tid >> 6);
  const int d0 = 2 * b;
  {
    const int ch = tid >> 7, n = tid & 127;
    const unsigned* kr = (const unsigned*)((const u16*)(p.ws + WS_KREV) + (size_t)(d0 + ch) * KROW);
    unsigned char* rk = smem + ch * CV_CH;
#pragma unroll
    for (int it = 0; it < 2; ++it) {
      const int m8 = it * 128 + n;
      const v4u a = *(const v4u*)(kr + m8 * 4), bq = *(const v4u*)(kr + m8 * 4 + 4);
      *(v4u*)(rk + 3 * CV_RK + m8 * 16) = a;
      *(v4u*)(rk + 2 * CV_RK + m8 * 16) = v4u{a[1], a[2], a[3], bq[0]};
      *(v4u*)(rk + 1 * CV_RK + m8 * 16) = v4u{a[2], a[3], bq[0], bq[1]};
      *(v4u*)(rk + 0 * CV_RK + m8 * 16) = v4u{a[3], bq[0], bq[1], bq[2]};
    }
    if (n < 16) *(v4u*)(rk + (n >> 2) * CV_RK + 4096 + (n & 3) * 16) = v4u{0u, 0u, 0u, 0u};
    if (n < 64) *(v4u*)(rk + 4 * CV_RK + (n >> 5) * CV_HB + (n & 31) * 16) = v4u{0u, 0u, 0u, 0u};
  }
  {
    const int s0 = tid * 8;
    const f32x4* st = (const f32x4*)(p.ws + WS_ST1);
    f32x4 sv[4];
#pragma unroll
    for (int j = 0; j < 4; ++j) sv[j] = st[tid * 4 + j];
    const f32x2 sp = ((const f32x2*)(p.ws + WS_ST1))[s0 > 0 ? s0 - 1 : 0];
#pragma unroll
    for (int ch = 0; ch < 2; ++ch) {
      const int d = d0 + ch;
      const u16* ut = (const u16*)(p.ws + WS_UT) + (size_t)d * L;
      const v4u x = *(const v4u*)(ut + s0);
      const float xp = bf2f(ut[s0 > 0 ? s0 - 1 : 0]);
      const float sa = modval(p.ws, d), g = rbf(1.0f + modval(p.ws, 512 + d));
      float h[9];
      h[0] = s0 > 0 ? (xp - sp[0]) * sp[1] * g + sa : 0.f;
#pragma unroll
      for (int j = 0; j < 4; ++j) {
        h[1 + 2 * j] = (bflo(x[j]) - sv[j][0]) * sv[j][1] * g + sa;
        h[2 + 2 * j] = (bfhi(x[j]) - sv[j][2]) * sv[j][3] * g + sa;
      }
      unsigned char* hb = smem + ch * CV_CH + 4 * CV_RK;
      *(v4u*)(hb + 512 + tid * 16) = v4u{pk2(h[1], h[2]), pk2(h[3], h[4]), pk2(h[5], h[6]), pk2(h[7], h[8])};
      *(v4u*)(hb + CV_HB + 512 + tid * 16) = v4u{pk2(h[0], h[1]), pk2(h[2], h[3]), pk2(h[4], h[5]), pk2(h[6], h[7])};
      *(v4u*)(hb + 2 * CV_HB + tid * 16) = x;
    }
  }
  __syncthreads();
  const int ch = w >> 1, eh = w & 1;
  const int i = lane & 15, q = lane >> 4;
  const unsigned char* abase = smem + ch * CV_CH + (i & 3) * CV_RK + 2 * (2040 - 8 * (i >> 2) + 8 * q);
  const unsigned char* bbase = smem + ch * CV_CH + 4 * CV_RK + (i & 1) * CV_HB + 2 * (256 + 32 * (i >> 1) + 8 * q);
  f32x4 acc[8];
#pragma unroll
  for (int t = 0; t < 8; ++t) acc[t] = f32x4{0.f, 0.f, 0.f, 0.f};
#pragma unroll
  for (int ee = 0; ee < 4; ++ee) {
    const int e = eh * 4 + ee;
    v4u A[8], B[8];
#pragma unroll
    for (int k = 0; k < 8; ++k) {
      A[k] = *(const v4u*)(abase - 64 * e - 512 * k);
      B[k] = *(const v4u*)(bbase - 64 * e + 512 * k);
    }
#pragma unroll
    for (int qq = 0; qq < 8; ++qq)
#pragma unroll
      for (int pp = 0; pp + qq < 8; ++pp) acc[pp + qq] = mfma(A[qq], B[pp], acc[pp + qq]);
  }
  f32x4* red = (f32x4*)(smem + 2 * CV_CH);
  f32x4 keepv[4];
#pragma unroll
  for (int t = 0; t < 4; ++t) {
    f32x4 lo = acc[t], hi = acc[4 + t];
    asm volatile("" : "+v"(lo), "+v"(hi));
    red[(w * 4 + t) * 64 + lane] = eh ? lo : hi;
    keepv[t] = eh ? hi : lo;
  }
  __syncthreads();
  const int d = d0 + ch;
  const float dsk = bf2f(p.dsk[d]), ga = modval(p.ws, 1024 + d);
  const unsigned char* hb = smem + ch * CV_CH + 4 * CV_RK;
  u16* x1t = (u16*)(p.ws + WS_X1T) + (size_t)d * L;
  const int odd = i & 1;
#pragma unroll
  for (int t = 0; t < 4; ++t) {
    const f32x4 y = keepv[t] + red[((w ^ 1) * 4 + t) * 64 + lane];
    const int l0 = 256 * (eh * 4 + t) + 32 * (i >> 1) + 8 * q;
    const v4u hv = *(const v4u*)(hb + 512 + l0 * 2);
    const v4u uv = *(const v4u*)(hb + 2 * CV_HB + l0 * 2);
    float o[4];
#pragma unroll
    for (int r = 0; r < 4; ++r) {
      const float h = odd ? bflo(hv[r]) : bfhi(hv[r]);
      const float uu = odd ? bflo(uv[r]) : bfhi(uv[r]);
      o[r] = uu + ga * (y[r] + h * dsk);
    }
    const unsigned p01 = pk2(o[0], o[1]), p23 = pk2(o[2], o[3]);
    const unsigned snd = odd ? p01 : p23, keep = odd ? p23 : p01;
    const unsigned rcv = (unsigned)__builtin_amdgcn_update_dpp(0, (int)snd, 0xB1, 0xF, 0xF, true);
    const unsigned ev = odd ? keep : rcv, od = odd ? rcv : keep;
    v2u o2;
    o2[0] = (ev & 0xFFFFu) | (od << 16);
    o2[1] = (ev >> 16) | (od & 0xFFFF0000u);
    const int pos = (l0 & ~31) + odd * 16 + q * 4;
    *(v2u*)(x1t + pos) = o2;
  }
}

__device__ __forceinline__ void role_wp(const Params& p, unsigned char* smem, int b) {
  const int tid = threadIdx.x, lane = tid & 63, w = tid >> 6;
  const int kg = b & 3, pr = b >> 2;
  const int half = pr / 22, pp = pr % 22;
  u16* tile = (u16*)smem;
  float* red = (float*)(smem + 128 * 80);
#pragma unroll
  for (int it = 0; it < 2; ++it) {
    const int item = it * 256 + tid, r = item >> 2, chn = item & 3, d = kg * 128 + r;
    const int f = pp * 32 + chn * 8;
    v4u x = {0u, 0u, 0u, 0u};
    if (f < FH) x = *(const v4u*)(p.win + (size_t)d * FF + half * FH + f);
    const float g = rbf(1.0f + modval(p.ws, 4 * 512 + d)), sb = modval(p.ws, 3 * 512 + d);
    v4u o;
    float cs[8], bs[8];
#pragma unroll
    for (int j = 0; j < 4; ++j) {
      const float a0 = bflo(x[j]), a1 = bfhi(x[j]);
      o[j] = pk2(a0 * g, a1 * g);
      cs[2 * j] = bflo(o[j]); cs[2 * j + 1] = bfhi(o[j]);
      bs[2 * j] = a0 * sb; bs[2 * j + 1] = a1 * sb;
    }
    *(v4u*)(tile + r * 40 + chn * 8) = o;
#pragma unroll
    for (int j = 0; j < 8; ++j) {
#pragma unroll
      for (int o2 = 4; o2 < 64; o2 <<= 1) { cs[j] += __shfl_xor(cs[j], o2); bs[j] += __shfl_xor(bs[j], o2); }
    }
    if (lane < 4) {
#pragma unroll
      for (int j = 0; j < 8; ++j) { red[(it * 4 + w) * 64 + chn * 8 + j] = cs[j]; red[(it * 4 + w) * 64 + 32 + chn * 8 + j] = bs[j]; }
    }
  }
  __syncthreads();
  const int c = lane & 15, q = lane >> 4;
#pragma unroll
  for (int i = 0; i < 2; ++i) {
    const int col = perm(i, c);
    v4u o;
#pragma unroll
    for (int j = 0; j < 4; ++j)
      o[j] = (unsigned)tile[(32 * w + 8 * q + 2 * j) * 40 + col] | ((unsigned)tile[(32 * w + 8 * q + 2 * j + 1) * 40 + col] << 16);
    *(v4u*)(p.ws + WS_WP + ((size_t)(half * NU + pp * 2 + i) * 16 + kg * 4 + w) * 1024 + lane * 16) = o;
  }
  if (tid < 64) {
    const int which = tid >> 5, uc = tid & 31;
    const int col = perm(uc >> 4, uc & 15);
    float s = 0.f;
#pragma unroll
    for (int k = 0; k < 8; ++k) s += red[k * 64 + which * 32 + col];
    ((float*)(p.ws + WS_CSB))[(kg * 2 + which) * 1408 + half * 704 + pp * 32 + uc] = s;
  }
}

__global__ void __launch_bounds__(256) k1(Params p) {
  extern __shared__ __attribute__((aligned(16))) unsigned char smem[];
  int b = blockIdx.x;
  if (b < K1_CONV) { role_conv(p, smem, b); return; }
  role_wp(p, smem, b - K1_CONV);
}

__device__ __forceinline__ void tile_of(int b, int& mt, int& nt) {
  const int xcd = b & 7, idx = b >> 3;
  mt = xcd * 4 + (idx & 3);
  nt = idx >> 2;
}
__device__ __forceinline__ int pair0(int nt) { return nt < 6 ? 3 * nt : 18 + 2 * (nt - 6); }
__device__ __forceinline__ int npairs(int nt) { return nt < 6 ? 3 : 2; }
__device__ __forceinline__ v2u trread(const unsigned char* ptr) {
  typedef short v4i16_t __attribute__((ext_vector_type(4)));
  return __builtin_bit_cast(v2u, __builtin_amdgcn_ds_read_tr16_b64_v4i16((LAS v4i16_t*)ptr));
}
constexpr int K2_LDS = 65536 + 4 * 64 * 8 + 64 * 8 + 2 * 64 * 8;

__global__ void __launch_bounds__(256, 1) k2(Params p) {
  extern __shared__ __attribute__((aligned(16))) unsigned char smem[];
  const int tid = threadIdx.x, lane = tid & 63, w = __builtin_amdgcn_readfirstlane(tid >> 6);
  int mt, nt;
  tile_of(blockIdx.x, mt, nt);
  const int np = npairs(nt);
  const int half = (w < 2) ? 1 : 0;
  const int u0 = pair0(nt) * 2 + (w & 1) * np;
  v4u xs[16];
  {
    const u16* src = (const u16*)(p.ws + WS_X1T) + mt * 64 + (lane & 7) * 8;
#pragma unroll
    for (int i = 0; i < 16; ++i) xs[i] = *(const v4u*)(src + (size_t)(i * 32 + w * 8 + (lane >> 3)) * L);
  }
  v4u wf[3][16];
  {
    const v4u* src = (const v4u*)(p.ws + WS_WP + (size_t)(half * NU + u0) * 16 * 1024) + lane;
#pragma unroll
    for (int ks = 0; ks < 16; ++ks) {
      wf[0][ks] = src[ks * 64];
      wf[1][ks] = src[(16 + ks) * 64];
      wf[2][ks] = src[((np == 3 ? 32 : 16) + ks) * 64];
    }
  }
  __builtin_amdgcn_sched_barrier(0);
#pragma unroll
  for (int i = 0; i < 16; ++i) {
    const int row = i * 32 + w * 8 + (lane >> 3), chn = lane & 7;
    const int sg = ((row >> 1) & 1) | (((row >> 3) & 1) << 1);
    *(v4u*)(smem + row * 128 + (((chn >> 1) ^ sg) * 32) + (chn & 1) * 16) = xs[i];
  }
  __syncthreads();
  const int c = lane & 15, q = lane >> 4;
  const int ra = (lane >> 2) & 3, pc = lane & 3;
  const unsigned char* tb[2][4];
#pragma unroll
  for (int t = 0; t < 2; ++t)
#pragma unroll
    for (int tu = 0; tu < 4; ++tu) {
      const int row = 8 * q + 4 * t + ra;
      const int sg = ((row >> 1) & 1) | (((row >> 3) & 1) << 1);
      tb[t][tu] = smem + row * 128 + ((tu ^ sg) * 32) + pc * 8;
    }
  f32x4 acc[3][4], s1[4], s2[4];
#pragma unroll
  for (int tu = 0; tu < 4; ++tu) {
    s1[tu] = f32x4{0.f, 0.f, 0.f, 0.f}; s2[tu] = s1[tu];
#pragma unroll
    for (int k = 0; k < 3; ++k) acc[k][tu] = s1[tu];
  }
  const v4u ones = {0x3F803F80u, 0x3F803F80u, 0x3F803F80u, 0x3F803F80u};
#pragma unroll
  for (int ks = 0; ks < 16; ++ks) {
    v4u a[4];
#pragma unroll
    for (int tu = 0; tu < 4; ++tu) {
      const v2u lo = trread(tb[0][tu] + ks * 4096), hi = trread(tb[1][tu] + ks * 4096);
      a[tu] = v4u{lo[0], lo[1], hi[0], hi[1]};
    }
    if (half) {
#pragma unroll
      for (int k = 0; k < 3; ++k)
#pragma unroll
        for (int tu = 0; tu < 4; ++tu) acc[k][tu] = mfma(a[tu], wf[k][ks], acc[k][tu]);
    } else {
#pragma unroll
      for (int k = 0; k < 3; ++k)
#pragma unroll
        for (int tu = 0; tu < 4; ++tu) acc[k][tu] = mfma(wf[k][ks], a[tu], acc[k][tu]);
    }
    if ((ks & 3) == w) {
#pragma unroll
      for (int tu = 0; tu < 4; ++tu) { s1[tu] = mfma(a[tu], ones, s1[tu]); s2[tu] = mfma(a[tu], a[tu], s2[tu]); }
      if ((ks >> 1) == nt) {
#pragma unroll
        for (int tu = 0; tu < 4; ++tu) *(v4u*)(p.ws + WS_X1F + ((size_t)(mt * 4 + tu) * 16 + ks) * 1024 + lane * 16) = a[tu];
      }
    }
  }
  float* sp = (float*)(smem + 65536);
  float* fin = (float*)(smem + 65536 + 2048);
#pragma unroll
  for (int tu = 0; tu < 4; ++tu) {
    if (c == 0) *(f32x4*)(sp + w * 128 + tu * 16 + q * 4) = s1[tu];
    if ((c >> 2) == q) sp[w * 128 + 64 + tu * 16 + c] = (c & 3) == 0 ? s2[tu][0] : (c & 3) == 1 ? s2[tu][1] : (c & 3) == 2 ? s2[tu][2] : s2[tu][3];
  }
  __syncthreads();
  if (tid < 64) {
    const float a = (sp[tid] + sp[128 + tid]) + (sp[256 + tid] + sp[384 + tid]);
    const float bq = (sp[64 + tid] + sp[192 + tid]) + (sp[320 + tid] + sp[448 + tid]);
    const float m = a * (1.0f / D);
    const float var = fmaxf(bq * (1.0f / D) - m * m, 0.f);
    const float r = 1.0f / sqrtf(var + EPS);
    fin[tid] = r; fin[64 + tid] = r * m;
  }
  __syncthreads();
  const float* csb = (const float*)(p.ws + WS_CSB);
  if (half) {
    float rs[4][4], rq2[4][4];
#pragma unroll
    for (int tu = 0; tu < 4; ++tu)
#pragma unroll
      for (int r = 0; r < 4; ++r) { rs[tu][r] = 0.f; rq2[tu][r] = 0.f; }
#pragma unroll
    for (int k = 0; k < 3; ++k) {
      if (k < np) {
        const int ci = 704 + (u0 + k) * 16 + c;
        const float cs = (csb[ci] + csb[2 * 1408 + ci]) + (csb[4 * 1408 + ci] + csb[6 * 1408 + ci]);
        const float bs = (csb[1408 + ci] + csb[3 * 1408 + ci]) + (csb[5 * 1408 + ci] + csb[7 * 1408 + ci]);
        unsigned o[4][2];
#pragma unroll
        for (int tu = 0; tu < 4; ++tu) {
          const f32x4 r4 = *(const f32x4*)(fin + tu * 16 + q * 4), m4 = *(const f32x4*)(fin + 64 + tu * 16 + q * 4);
          float z[4];
#pragma unroll
          for (int r = 0; r < 4; ++r) z[r] = gelu_tanh(r4[r] * acc[k][tu][r] - m4[r] * cs + bs);
          o[tu][0] = pk2(z[0], z[1]); o[tu][1] = pk2(z[2], z[3]);
          z[0] = bflo(o[tu][0]); z[1] = bfhi(o[tu][0]); z[2] = bflo(o[tu][1]); z[3] = bfhi(o[tu][1]);
#pragma unroll
          for (int r = 0; r < 4; ++r) { rs[tu][r] += z[r]; rq2[tu][r] += z[r] * z[r]; }
        }
#pragma unroll
        for (int h2 = 0; h2 < 2; ++h2)
          *(v4u*)(p.ws + WS_Z2 + ((size_t)(u0 + k) * 64 + mt * 2 + h2) * 1024 + lane * 16) =
              v4u{o[2 * h2][0], o[2 * h2][1], o[2 * h2 + 1][0], o[2 * h2 + 1][1]};
      }
    }
    float* zs = (float*)(smem + 65536 + 2048 + 512);
#pragma unroll
    for (int tu = 0; tu < 4; ++tu)
#pragma unroll
      for (int r = 0; r < 4; ++r) {
        float a = rs[tu][r], bq = rq2[tu][r];
#pragma unroll
        for (int o2 = 1; o2 < 16; o2 <<= 1) { a += __shfl_xor(a, o2); bq += __shfl_xor(bq, o2); }
        if (c == 0) { const int s = perm(tu, 4 * q + r); zs[w * 128 + s] = a; zs[w * 128 + 64 + s] = bq; }
      }
  } else {
#pragma unroll
    for (int k = 0; k < 3; ++k) {
      if (k < np) {
        const int ci = (u0 + k) * 16 + q * 4;
        f32x4 cs = {0.f, 0.f, 0.f, 0.f}, bs = cs;
#pragma unroll
        for (int kg = 0; kg < 4; ++kg) { cs += *(const f32x4*)(csb + kg * 2 * 1408 + ci); bs += *(const f32x4*)(csb + (kg * 2 + 1) * 1408 + ci); }
#pragma unroll
        for (int tu = 0; tu < 4; ++tu) {
          const float r1 = fin[tu * 16 + c], m1 = fin[64 + tu * 16 + c];
          float z[4];
#pragma unroll
          for (int r = 0; r < 4; ++r) z[r] = gelu_tanh(r1 * acc[k][tu][r] - m1 * cs[r] + bs[r]);
          const int uu = u0 + k;
          *(v2u*)(p.ws + WS_Z1 + ((size_t)(mt * 4 + tu) * 22 + (uu >> 1)) * 1024 + lane * 16 + (uu & 1) * 8) = v2u{pk2(z[0], z[1]), pk2(z[2], z[3])};
        }
      }
    }
  }
  __syncthreads();
  if (tid < 64) {
    const float* zs = (const float*)(smem + 65536 + 2048 + 512);
    ((f32x2*)(p.ws + WS_ST2))[nt * 2048 + mt * 64 + tid] = f32x2{zs[tid] + zs[128 + tid], zs[64 + tid] + zs[192 + tid]};
  }
}

__global__ void __launch_bounds__(256) k2b(Params p) {
  const int fr = blockIdx.x * 4 + (threadIdx.x >> 6), lane = threadIdx.x & 63;
  const int u = fr >> 6, ks = fr & 63, c = lane & 15, q = lane >> 4;
  v4u* ptr = (v4u*)(p.ws + WS_Z2 + (size_t)fr * 1024 + lane * 16);
  const v4u x = *ptr;
  const bool valid = perm(u, c) < FH;
  float z[8];
#pragma unroll
  for (int j = 0; j < 8; ++j) {
    const int s = ks * 32 + q * 8 + j;
    float a = 0.f, b = 0.f;
#pragma unroll
    for (int k = 0; k < 8; ++k) { const f32x2 t = ((const f32x2*)(p.ws + WS_ST2))[k * 2048 + s]; a += t[0]; b += t[1]; }
    const float m = a * (1.0f / FH);
    const float var = fmaxf(b * (1.0f / FH) - m * m, 0.f);
    const float r = 1.0f / sqrtf(var + EPS);
    const float xv = (j & 1) ? bfhi(x[j >> 1]) : bflo(x[j >> 1]);
    z[j] = valid ? (xv - m) * r : 0.f;
  }
  *ptr = v4u{pk2(z[0], z[1]), pk2(z[2], z[3]), pk2(z[4], z[5]), pk2(z[6], z[7])};
}

constexpr int K3_LDS = 4 * 18 * 1024;
__global__ void __launch_bounds__(256, 1) k3(Params p) {
  extern __shared__ __attribute__((aligned(16))) unsigned char smem[];
  const int tid = threadIdx.x, lane = tid & 63, w = __builtin_amdgcn_readfirstlane(tid >> 6);
  int mt, nt;
  tile_of(blockIdx.x, mt, nt);
  const int np = npairs(nt), u0 = pair0(nt) * 2;
  const int c = lane & 15, q = lane >> 4;
  const u16* wsrc = p.wsp + (size_t)(mt * 64 + (lane >> 3)) * 2048 + w * 512 + (lane & 7) * 8;
  const v4u* zsrc = (const v4u*)(p.ws + WS_Z2 + ((size_t)u0 * 64 + w * 16) * 1024) + lane;
  unsigned char* slot = smem + w * 8192;
  const int wr_off = (lane >> 3) * 128 + (((lane & 7) ^ ((lane >> 4) & 1)) * 16);
  int rd_off[4];
#pragma unroll
  for (int tu = 0; tu < 4; ++tu) {
    const int row = perm(tu, c);
    rd_off[tu] = row * 128 + ((q ^ (c >> 1)) * 16);
  }
  f32x4 acc[6][4];
#pragma unroll
  for (int i = 0; i < 6; ++i)
#pragma unroll
    for (int j = 0; j < 4; ++j) acc[i][j] = f32x4{0.f, 0.f, 0.f, 0.f};
  constexpr int DP = 2;
  v4u wr[DP][8], zf[DP][2][6];
  auto load = [&](int s, int sl) {
#pragma unroll
    for (int i = 0; i < 8; ++i) wr[sl][i] = *(const v4u*)(wsrc + (size_t)i * 8 * 2048 + s * 64);
#pragma unroll
    for (int k2 = 0; k2 < 2; ++k2)
#pragma unroll
      for (int i = 0; i < 6; ++i) zf[sl][k2][i] = zsrc[((i < 2 * np ? i : 0) * 64 + s * 2 + k2) * 64];
  };
#pragma unroll
  for (int s = 0; s < DP; ++s) load(s, s);
  __builtin_amdgcn_sched_barrier(0);
#pragma unroll
  for (int s = 0; s < 8; ++s) {
    const int sl = s % DP;
#pragma unroll
    for (int i = 0; i < 8; ++i) *(v4u*)(slot + i * 1024 + (wr_off ^ ((i & 3) * 32))) = wr[sl][i];
    v4u wf[2][4];
#pragma unroll
    for (int k2 = 0; k2 < 2; ++k2)
#pragma unroll
      for (int tu = 0; tu < 4; ++tu) wf[k2][tu] = *(const v4u*)(slot + (rd_off[tu] ^ (k2 * 64)));
    v4u cz[2][6];
#pragma unroll
    for (int k2 = 0; k2 < 2; ++k2)
#pragma unroll
      for (int i = 0; i < 6; ++i) cz[k2][i] = zf[sl][k2][i];
    if (s + DP < 8) load(s + DP, sl);
#pragma unroll
    for (int k2 = 0; k2 < 2; ++k2)
#pragma unroll
      for (int i = 0; i < 6; ++i)
#pragma unroll
        for (int tu = 0; tu < 4; ++tu) acc[i][tu] = mfma(cz[k2][i], wf[k2][tu], acc[i][tu]);
    __builtin_amdgcn_sched_barrier(0);
  }
  __syncthreads();
  f32x4* red = (f32x4*)smem;
#pragma unroll
  for (int tu = 0; tu < 4; ++tu) {
    if (tu != w) {
      const int sslot = (w - tu - 1) & 3;
#pragma unroll
      for (int i = 0; i < 6; ++i) red[((tu * 3 + sslot) * 6 + i) * 64 + lane] = acc[i][tu];
    }
  }
  __syncthreads();
  const int t = mt * 64 + perm(w, c);
  const float bsp = bf2f(p.bsp[t]);
#pragma unroll
  for (int pp = 0; pp < 3; ++pp) {
    if (pp < np) {
      const size_t fo = ((size_t)(mt * 4 + w) * 22 + pair0(nt) + pp) * 1024 + lane * 16;
      const v4u z1 = *(const v4u*)(p.ws + WS_Z1 + fo);
      v4u o;
#pragma unroll
      for (int e = 0; e < 2; ++e) {
        const int i = 2 * pp + e;
        f32x4 g = w == 0 ? acc[i][0] : w == 1 ? acc[i][1] : w == 2 ? acc[i][2] : acc[i][3];
#pragma unroll
        for (int sslot = 0; sslot < 3; ++sslot) g += red[((w * 3 + sslot) * 6 + i) * 64 + lane];
        o[2 * e] = pk2(bflo(z1[2 * e]) * (g[0] + bsp), bfhi(z1[2 * e]) * (g[1] + bsp));
        o[2 * e + 1] = pk2(bflo(z1[2 * e + 1]) * (g[2] + bsp), bfhi(z1[2 * e + 1]) * (g[3] + bsp));
      }
      *(v4u*)(p.ws + WS_V + fo) = o;
    }
  }
}

constexpr int K4_LDS = 4 * 12 * 1024;
__global__ void __launch_bounds__(256, 1) k4(Params p) {
  extern __shared__ __attribute__((aligned(16))) unsigned char smem[];
  const int tid = threadIdx.x, lane = tid & 63, w = __builtin_amdgcn_readfirstlane(tid >> 6);
  int mt, nt;
  tile_of(blockIdx.x, mt, nt);
  const int c = lane & 15, q = lane >> 4;
  const int ks0 = w < 2 ? w * 6 : 12 + (w - 2) * 5, nks = w < 2 ? 6 : 5;
  const v4u* vsrc = (const v4u*)(p.ws + WS_V + ((size_t)(mt * 4) * 22 + ks0) * 1024) + lane;
  const v4u* wsrc = (const v4u*)(p.ws + WS_WOT + ((size_t)(nt * 4) * 22 + ks0) * 1024) + lane;
  v4u vf[6][4], wf[6][4];
#pragma unroll
  for (int s = 0; s < 6; ++s)
#pragma unroll
    for (int j = 0; j < 4; ++j) {
      const int ss = s < nks ? s : 0;
      vf[s][j] = vsrc[(j * 22 + ss) * 64];
      wf[s][j] = wsrc[(j * 22 + ss) * 64];
    }
  __builtin_amdgcn_sched_barrier(0);
  f32x4 acc[4][4];
#pragma unroll
  for (int i = 0; i < 4; ++i)
#pragma unroll
    for (int j = 0; j < 4; ++j) acc[i][j] = f32x4{0.f, 0.f, 0.f, 0.f};
#pragma unroll
  for (int s = 0; s < 6; ++s) {
    if (s < nks) {
#pragma unroll
      for (int i = 0; i < 4; ++i)
#pragma unroll
        for (int tu = 0; tu < 4; ++tu) acc[i][tu] = mfma(wf[s][i], vf[s][tu], acc[i][tu]);
    }
  }
  f32x4* red = (f32x4*)smem;
#pragma unroll
  for (int tu = 0; tu < 4; ++tu) {
    if (tu != w) {
      const int sslot = (w - tu - 1) & 3;
#pragma unroll
      for (int i = 0; i < 4; ++i) red[((tu * 3 + sslot) * 4 + i) * 64 + lane] = acc[i][tu];
    }
  }
  __syncthreads();
  const int t = mt * 64 + perm(w, c);
#pragma unroll
  for (int pp = 0; pp < 2; ++pp) {
    const int dbase = nt * 64 + pp * 32 + q * 8;
    const v4u x1 = *(const v4u*)(p.ws + WS_X1F + ((size_t)(mt * 4 + w) * 16 + nt * 2 + pp) * 1024 + lane * 16);
    v4u o;
#pragma unroll
    for (int e = 0; e < 2; ++e) {
      const int i = 2 * pp + e;
      f32x4 g = w == 0 ? acc[i][0] : w == 1 ? acc[i][1] : w == 2 ? acc[i][2] : acc[i][3];
#pragma unroll
      for (int sslot = 0; sslot < 3; ++sslot) g += red[((w * 3 + sslot) * 4 + i) * 64 + lane];
      float gb[4];
#pragma unroll
      for (int r = 0; r < 4; ++r) gb[r] = modval(p.ws, 5 * 512 + dbase + e * 4 + r);
      o[2 * e] = pk2(bflo(x1[2 * e]) + gb[0] * g[0], bfhi(x1[2 * e]) + gb[1] * g[1]);
      o[2 * e + 1] = pk2(bflo(x1[2 * e + 1]) + gb[2] * g[2], bfhi(x1[2 * e + 1]) + gb[3] * g[3]);
    }
    *(v4u*)(p.out + (size_t)t * D + dbase) = o;
  }
}

}

extern "C" void kernel_launch(void* const* d_in, const int* in_sizes, int n_in, void* d_out, int out_size, void* d_ws,
                              size_t ws_size, hipStream_t stream) {
  Params p;
  p.u = (const u16*)d_in[0];
  p.lar = (const u16*)d_in[1];
  p.aim = (const u16*)d_in[2];
  p.cre = (const u16*)d_in[3];
  p.cim = (const u16*)d_in[4];
  p.ldt = (const u16*)d_in[5];
  p.dsk = (const u16*)d_in[6];
  p.win = (const u16*)d_in[8];
  p.wsp = (const u16*)d_in[9];
  p.bsp = (const u16*)d_in[10];
  p.wout = (const u16*)d_in[11];
  p.c = (const u16*)d_in[12];
  p.wmod = (const u16*)d_in[13];
  p.out = (u16*)d_out;
  p.ws = (unsigned char*)d_ws;
  (void)hipFuncSetAttribute((const void*)k0, hipFuncAttributeMaxDynamicSharedMemorySize, K0_LDS);
  (void)hipFuncSetAttribute((const void*)k1, hipFuncAttributeMaxDynamicSharedMemorySize, K1_LDS);
  (void)hipFuncSetAttribute((const void*)k2, hipFuncAttributeMaxDynamicSharedMemorySize, K2_LDS);
  (void)hipFuncSetAttribute((const void*)k3, hipFuncAttributeMaxDynamicSharedMemorySize, K3_LDS);
  k0<<<K0_GRID, 256, K0_LDS, stream>>>(p);
  k1<<<K1_GRID, 256, K1_LDS, stream>>>(p);
  k2<<<256, 256, K2_LDS, stream>>>(p);
  k2b<<<44 * 64 / 4, 256, 0, stream>>>(p);
  k3<<<256, 256, K3_LDS, stream>>>(p);
  k4<<<256, 256, K4_LDS, stream>>>(p);
}
```

```cpp
#include <hip/hip_runtime.h>
#include <hip/hip_bf16.h>
#include <stdint.h>

#define LAS __attribute__((address_space(3)))
typedef unsigned v4u __attribute__((ext_vector_type(4)));
typedef unsigned v2u __attribute__((ext_vector_type(2)));
typedef float f32x4 __attribute__((ext_vector_type(4)));
typedef float f32x2 __attribute__((ext_vector_type(2)));
typedef short bf16x8 __attribute__((ext_vector_type(8)));
typedef _Float16 f16x8 __attribute__((ext_vector_type(8)));
typedef _Float16 f16x2 __attribute__((ext_vector_type(2)));
typedef __bf16 bf16x2_t __attribute__((ext_vector_type(2)));
typedef unsigned short u16;
typedef __amdgpu_buffer_rsrc_t rsrc_t;

#define STAMP(k, i) do { } while (0)

constexpr int L = 2048, D = 512, NM = 128, FH = 688, FF = 1376;
constexpr int KROW = 2112;
constexpr float EPS = 1e-5f;
constexpr int SC1 = 16;
constexpr unsigned OOB = 0x40000000u;
constexpr unsigned SPIN_CAP = 1u << 14;

constexpr unsigned WS_X1T = 0;
constexpr unsigned WS_WP = WS_X1T + 512 * 2048 * 2;
constexpr unsigned WS_ST2 = WS_WP + 88 * 16 * 1024;
constexpr unsigned WS_CSB = WS_ST2 + 32 * 2 * 16 * 32 * 8;
constexpr unsigned WS_FILL_END = 4u << 20;
static_assert(WS_CSB + 88 * 16 * 16 <= WS_FILL_END, "fill region");
constexpr unsigned WS_MODP = WS_FILL_END;
constexpr unsigned WS_ST1 = WS_MODP + 3072 * 16;
constexpr unsigned WS_UT = WS_ST1 + 2 * 2048 * 4;
constexpr unsigned WS_KREV = WS_UT + 512 * 2048 * 2;
constexpr unsigned WS_WT = WS_KREV + 512 * KROW * 2;
constexpr unsigned WS_WOT = WS_WT + 88 * 16 * 1024;
constexpr unsigned WS_Z2 = WS_WOT + 32 * 22 * 1024;
constexpr unsigned WS_V = WS_Z2 + 44 * 64 * 1024;
constexpr unsigned WS_END = WS_V + 128 * 22 * 1024;

struct Params {
  const u16 *u, *lar, *aim, *cre, *cim, *ldt, *dsk, *win, *wsp, *bsp, *wout, *c, *wmod;
  u16* out;
  unsigned char* ws;
};

#define DI __device__ __forceinline__
DI float bf2f(unsigned b) { return __builtin_bit_cast(float, b << 16); }
DI float bflo(unsigned w) { return __builtin_bit_cast(float, w << 16); }
DI float bfhi(unsigned w) { return __builtin_bit_cast(float, w & 0xFFFF0000u); }
DI f32x2 unpk(unsigned w) { return f32x2{bflo(w), bfhi(w)}; }
DI unsigned pk2(f32x2 v) { return __builtin_bit_cast(unsigned, __builtin_convertvector(v, bf16x2_t)); }
DI unsigned pk2(float lo, float hi) { return pk2(f32x2{lo, hi}); }
DI unsigned nomark(unsigned x) { return x < 0xFFFFFFFEu ? x : 0xFFFFFFFEu; }
DI float rbf(float x) { return bflo(pk2(x, 0.f)); }
DI f32x2 fma2(f32x2 a, f32x2 b, f32x2 c) { return __builtin_elementwise_fma(a, b, c); }
DI f32x2 lo2(f32x4 v) { return f32x2{v[0], v[1]}; }
DI f32x2 hi2(f32x4 v) { return f32x2{v[2], v[3]}; }
DI f32x2 bc2(float x) { return f32x2{x, x}; }
DI f32x4 mfma(v4u a, v4u b, f32x4 c) {
  return __builtin_amdgcn_mfma_f32_16x16x32_bf16(__builtin_bit_cast(bf16x8, a), __builtin_bit_cast(bf16x8, b), c, 0, 0, 0);
}
DI f32x4 mfma_h(v4u a, v4u b, f32x4 c) {
  return __builtin_amdgcn_mfma_f32_16x16x32_f16(__builtin_bit_cast(f16x8, a), __builtin_bit_cast(f16x8, b), c, 0, 0, 0);
}
DI float dot2(unsigned a, unsigned b, float acc) {
  return __builtin_amdgcn_fdot2_f32_bf16(__builtin_bit_cast(bf16x2_t, a), __builtin_bit_cast(bf16x2_t, b), acc, false);
}
constexpr unsigned ONES = 0x3F803F80u;
DI int perm(int u, int c) { return 32 * (u >> 1) + 8 * (c >> 2) + 4 * (u & 1) + (c & 3); }
template <int CTRL> DI float dpp(float x) {
  return __builtin_bit_cast(float, __builtin_amdgcn_mov_dpp(__builtin_bit_cast(int, x), CTRL, 0xf, 0xf, true));
}
DI float row16_sum(float x) {
  x += dpp<0xB1>(x); x += dpp<0x4E>(x); x += dpp<0x141>(x); x += dpp<0x128>(x);
  return x;
}
DI float row16_max(float x) {
  x = fmaxf(x, dpp<0xB1>(x)); x = fmaxf(x, dpp<0x4E>(x)); x = fmaxf(x, dpp<0x141>(x)); x = fmaxf(x, dpp<0x128>(x));
  return x;
}
DI float xrow_sum(float x) {
  auto s = __builtin_amdgcn_permlane16_swap(__float_as_uint(x), __float_as_uint(x), false, false);
  x = __uint_as_float(s[0]) + __uint_as_float(s[1]);
  auto t = __builtin_amdgcn_permlane32_swap(__float_as_uint(x), __float_as_uint(x), false, false);
  return __uint_as_float(t[0]) + __uint_as_float(t[1]);
}
DI float xrow_max(float x) {
  auto s = __builtin_amdgcn_permlane16_swap(__float_as_uint(x), __float_as_uint(x), false, false);
  x = fmaxf(__uint_as_float(s[0]), __uint_as_float(s[1]));
  auto t = __builtin_amdgcn_permlane32_swap(__float_as_uint(x), __float_as_uint(x), false, false);
  return fmaxf(__uint_as_float(t[0]), __uint_as_float(t[1]));
}
DI f32x2 gelu2(f32x2 x) {
  const float k0 = -2.0f * 0.7978845608028654f * 1.4426950408889634f, k1 = k0 * 0.044715f;
  const f32x2 a = x * fma2(x * x, bc2(k1), bc2(k0));
  const f32x2 e = f32x2{__builtin_amdgcn_exp2f(a[0]), __builtin_amdgcn_exp2f(a[1])} + bc2(1.0f);
  return x * f32x2{__builtin_amdgcn_rcpf(e[0]), __builtin_amdgcn_rcpf(e[1])};
}
DI rsrc_t mkrs(const void* ptr, unsigned bytes) { return __builtin_amdgcn_make_buffer_rsrc((void*)ptr, 0, (int)bytes, 0x00020000); }
template <int AUX> DI v4u ld128(rsrc_t rs, unsigned v, unsigned s) { return __builtin_amdgcn_raw_buffer_load_b128(rs, (int)v, (int)s, AUX); }
template <int AUX> DI v2u ld64(rsrc_t rs, unsigned v, unsigned s) { return __builtin_amdgcn_raw_buffer_load_b64(rs, (int)v, (int)s, AUX); }
template <int AUX> DI void st128(v4u x, rsrc_t rs, unsigned v, unsigned s) { __builtin_amdgcn_raw_buffer_store_b128(x, rs, (int)v, (int)s, AUX); }
template <int AUX> DI void st64(v2u x, rsrc_t rs, unsigned v, unsigned s) { __builtin_amdgcn_raw_buffer_store_b64(x, rs, (int)v, (int)s, AUX); }
DI f32x4 asf(v4u x) { return __builtin_bit_cast(f32x4, x); }
DI float sum4(f32x4 x) { return (x[0] + x[1]) + (x[2] + x[3]); }
DI unsigned umax3(unsigned a, unsigned b, unsigned c) { unsigned m = a > b ? a : b; return m > c ? m : c; }
DI unsigned macc(unsigned m, v4u x) { return umax3(umax3(m, x[0], x[1]), x[2], x[3]); }
DI bool any_mark(unsigned m) { return __builtin_amdgcn_ballot_w64(m == 0xFFFFFFFFu) != 0ull; }
#define SB() __builtin_amdgcn_sched_barrier(0)
#define PIN(x) asm volatile("" : "+v"(x))
#define MEMFENCE() asm volatile("" ::: "memory")
DI void bar() { asm volatile("s_waitcnt lgkmcnt(0)" ::: "memory"); __builtin_amdgcn_s_barrier(); asm volatile("" ::: "memory"); }

DI f32x2 cmul(f32x2 a, f32x2 b) { return fma2(bc2(a[1]), f32x2{-b[1], b[0]}, bc2(a[0]) * b); }
DI f32x2 cexp_p(float are2, float rev_hi, float rev_lo, float p) {
  const float mag = __builtin_amdgcn_exp2f(are2 * p);
  const float hi = rev_hi * p;
  const float fr = (hi - __builtin_rintf(hi)) + rev_lo * p;
  return f32x2{mag * __builtin_amdgcn_cosf(fr), mag * __builtin_amdgcn_sinf(fr)};
}
DI float exp_acc(float x) {
  const float hi = x * 1.4426950408889634f;
  const float lo = __builtin_fmaf(x, 1.4426950408889634f, -hi) + x * 1.925963033500011e-8f;
  const float e = __builtin_amdgcn_exp2f(hi);
  return __builtin_fmaf(e, lo * 0.6931471805599453f, e);
}
DI unsigned pkh(float a, float b) {
  f16x2 v = {(_Float16)a, (_Float16)b};
  return __builtin_bit_cast(unsigned, v);
}

extern "C" __global__ void __launch_bounds__(256, 1) adaln_mega(Params p);

constexpr int K0_KG = 512, K0_LN = 128, K0_MOD = 192, K0_WI = 88, K0_WO = 44;
constexpr int K0_GRID = K0_KG + K0_LN + K0_MOD + K0_WI + K0_WO;
constexpr int K0_LDS = 64 * 528 + 64;

extern "C" __global__ void __launch_bounds__(256) adaln_k0(Params p) {
  extern __shared__ __attribute__((aligned(16))) unsigned char smem[];
  const int tid = threadIdx.x, lane = tid & 63, w = __builtin_amdgcn_readfirstlane(tid >> 6);
  const int b = blockIdx.x;
  STAMP(0, 0);
  const rsrc_t rws = mkrs(p.ws, WS_END);
  unsigned long long pc;
  int rel_m, rel_e;
  asm volatile("s_getpc_b64 %0\n\ts_mov_b32 %1, adaln_k0@rel32@lo+4\n\ts_mov_b32 %2, adaln_code_end@rel32@lo+12" : "=s"(pc), "=s"(rel_m), "=s"(rel_e));
  const rsrc_t rcode = mkrs((const void*)(pc + (long long)rel_m), rel_e > rel_m ? (unsigned)(rel_e - rel_m) & ~15u : 0u);
  const v4u codepf = ld128<0>(rcode, tid * 16, ((b >> 3) & 15) * 4096);
  const int r1 = b - K0_KG, r2 = r1 - K0_LN, r3 = r2 - K0_MOD, r4 = r3 - K0_WI;
  const u16* gbase; unsigned gbytes, gsh, gpitch, gstep, goff;
  if (r2 < 0) {
    const int rg = (r1 & 7) * 16 + (r1 >> 3);
    gbase = p.u; gbytes = L * D * 2; gsh = 6; gpitch = 4096; gstep = 1024; goff = r1 < 0 ? OOB : rg * 16 * 1024;
  } else if (r3 < 0) {
    gbase = p.wmod; gbytes = D * 3072 * 2; gsh = 3; gpitch = 6144; gstep = 32 * 6144; goff = (r2 / 48) * 128 * 6144 + (r2 % 48) * 128;
  } else if (r4 < 0) {
    const int pr = r3 >> 1;
    gbase = p.win; gbytes = D * FF * 2; gsh = 2; gpitch = FF * 2; gstep = 64 * FF * 2;
    goff = (r3 & 1) * 256 * FF * 2 + ((pr / 22) * FH + (pr % 22) * 32) * 2;
  } else {
    gbase = p.wout; gbytes = FH * D * 2; gsh = 5; gpitch = 1024; gstep = 8 * 1024; goff = (r4 >> 1) * 32 * 1024 + (r4 & 1) * 512;
  }
  const rsrc_t rg_ = mkrs(gbase, gbytes);
  const unsigned gv = (tid >> gsh) * gpitch + (tid & ((1u << gsh) - 1u)) * 16;
  v4u x[4];
#pragma unroll
  for (int i = 0; i < 4; ++i) x[i] = ld128<0>(rg_, gv, goff + i * gstep);
  const int d = ((b & 7) * 64 + ((b >> 3) & 63));
  const int n = tid & 127;
  const unsigned q_ldt = p.ldt[d], q_lar = p.lar[d * NM + n], q_aim = p.aim[d * NM + n], q_cre = p.cre[d * NM + n], q_cim = p.cim[d * NM + n];
  SB();
  if (r1 < 0) {
    const v4u mk = {~0u, ~0u, ~0u, ~0u};
    st128<SC1>(mk, rws, tid * 16, b * 8192);
    st128<SC1>(mk, rws, tid * 16, b * 8192 + 4096);
    const int hf = w >> 1;
    unsigned char* tab = smem;
    float* kpos = (float*)smem;
    float* wmax = (float*)(smem + 64 * 528);
    int pos4[8];
#pragma unroll
    for (int j = 0; j < 8; j += 2) {
      const unsigned pp = pk2((float)(2047 - (tid * 8 + j)), (float)(2046 - (tid * 8 + j)));
      pos4[j] = (int)bflo(pp); pos4[j + 1] = (int)bfhi(pp);
    }
    const float dt = rbf(exp_acc(bf2f(q_ldt)));
    const float are2 = -rbf(exp_acc(bf2f(q_lar))) * dt * 1.4426950408889634f;
    const float aim = bf2f(q_aim) * dt;
    const float rev_hi = aim * 0.15915494309189535f;
    const float rev_lo = __builtin_fmaf(aim, 0.15915494309189535f, -rev_hi) + aim * 6.4206383e-9f;
    const float cre = bf2f(q_cre) * dt, cim = bf2f(q_cim) * dt;
    const float mx = xrow_max(row16_max(fmaxf(fabsf(cre), fabsf(cim))));
    if (lane == 0) wmax[w] = mx;
    STAMP(0, 2);
    const f32x2 e128 = cexp_p(are2, rev_hi, rev_lo, 128.f);
    const f32x2 ea = cexp_p(are2, rev_hi, rev_lo, hf ? 1.f : 8.f);
    const f32x2 eb = cexp_p(are2, rev_hi, rev_lo, hf ? 2.f : 64.f);
    const f32x2 e4 = cexp_p(are2, rev_hi, rev_lo, 4.f);
    unsigned char* tn = tab + n * 4;
    f32x2 t1[8];
    if (hf == 0) {
      f32x2 f0 = {1.f, 0.f}, f1 = eb;
#pragma unroll
      for (int j = 0; j < 8; ++j) {
        *(unsigned*)(tn + (48 + j) * 528) = pkh(f0[0], f0[1]);
        *(unsigned*)(tn + (56 + j) * 528) = pkh(f1[0], f1[1]);
        f0 = cmul(f0, ea); f1 = cmul(f1, ea);
      }
      t1[0] = cexp_p(are2, rev_hi, rev_lo, 512.f);
      t1[1] = cexp_p(are2, rev_hi, rev_lo, 1024.f);
    } else {
      t1[1] = ea; t1[2] = eb; t1[4] = e4;
      t1[3] = cmul(eb, ea); t1[5] = cmul(e4, ea); t1[6] = cmul(e4, eb); t1[7] = cmul(t1[6], ea);
      t1[0] = cexp_p(are2, rev_hi, rev_lo, 256.f);
    }
    bar();
    STAMP(0, 3);
    const float cm = fmaxf(wmax[0], wmax[1]);
    int ex = __builtin_amdgcn_frexp_expf(cm) + 1;
    ex = ex < -100 ? -100 : ex;
    const float scale = __builtin_amdgcn_ldexpf(1.f, -ex), unscale = __builtin_amdgcn_ldexpf(2.f, ex);
    const f32x2 c0 = {cre * scale, cim * scale};
    if (hf == 0) {
      f32x2 ca = cmul(c0, t1[0]), cb = cmul(c0, t1[1]);
#pragma unroll
      for (int uu = 0; uu < 4; ++uu) {
        const f32x2 z = cmul(ca, e4);
        *(unsigned*)(tn + (24 + uu * 2) * 528) = pkh(ca[0], -ca[1]);
        *(unsigned*)(tn + (25 + uu * 2) * 528) = pkh(z[0], -z[1]);
        ca = cmul(ca, e128);
      }
#pragma unroll
      for (int uu = 0; uu < 9; ++uu) {
        *(unsigned*)(tn + (32 + uu) * 528) = pkh(cb[0], -cb[1]);
        cb = cmul(cb, e128);
      }
    } else {
      const f32x2 c1 = cmul(c0, e128), c2 = cmul(c0, t1[0]), c3 = cmul(c2, e128);
      *(unsigned*)(tn + 0 * 528) = pkh(c0[0], -c0[1]);
      *(unsigned*)(tn + 8 * 528) = pkh(c1[0], -c1[1]);
      *(unsigned*)(tn + 16 * 528) = pkh(c2[0], -c2[1]);
      *(unsigned*)(tn + 20 * 528) = pkh(c3[0], -c3[1]);
#pragma unroll
      for (int v = 1; v < 8; ++v) {
        const f32x2 z0 = cmul(t1[v], c0), z1 = cmul(t1[v], c1);
        *(unsigned*)(tn + v * 528) = pkh(z0[0], -z0[1]);
        *(unsigned*)(tn + (8 + v) * 528) = pkh(z1[0], -z1[1]);
        if ((v & 1) == 0) {
          const f32x2 z2 = cmul(t1[v], c2), z3 = cmul(t1[v], c3);
          *(unsigned*)(tn + (16 + v / 2) * 528) = pkh(z2[0], -z2[1]);
          *(unsigned*)(tn + (20 + v / 2) * 528) = pkh(z3[0], -z3[1]);
        }
      }
    }
    bar();
    STAMP(0, 4);
    const int c = lane & 15, q = lane >> 4;
    const int wt = w < 3 ? w : 2;
    const int arow = wt * 16 + c > 40 ? 40 : wt * 16 + c;
    f32x4 acc = {0.f, 0.f, 0.f, 0.f}, acc2 = acc;
#pragma unroll
    for (int ks = 0; ks < 8; ks += 2) {
      acc = mfma_h(*(const v4u*)(tab + arow * 528 + ks * 64 + q * 16), *(const v4u*)(tab + (48 + c) * 528 + ks * 64 + q * 16), acc);
      acc2 = mfma_h(*(const v4u*)(tab + arow * 528 + ks * 64 + 64 + q * 16), *(const v4u*)(tab + (48 + c) * 528 + ks * 64 + 64 + q * 16), acc2);
    }
    int co[4];
#pragma unroll
    for (int r = 0; r < 4; ++r) {
      int i = wt * 16 + q * 4 + r;
      i = i > 40 ? 40 : i;
      co[r] = i < 16 ? 128 * (i >> 3) + (i & 7) : i < 24 ? 256 + 128 * ((i - 16) >> 2) + 2 * (i & 3) : i < 32 ? 512 + 128 * ((i - 24) >> 1) + 4 * (i & 1) : 1024 + 128 * (i - 32);
    }
    bar();
#pragma unroll
    for (int r = 0; r < 4; ++r) kpos[co[r] + 8 * c] = (acc[r] + acc2[r]) * unscale;
    bar();
    STAMP(0, 5);
    float v[8];
#pragma unroll
    for (int j = 0; j < 8; ++j) v[j] = kpos[pos4[j]];
    st128<0>(v4u{pk2(v[0], v[1]), pk2(v[2], v[3]), pk2(v[4], v[5]), pk2(v[6], v[7])}, rws, tid * 16, WS_KREV + d * (KROW * 2));
    st128<0>(v4u{0u, 0u, 0u, 0u}, rws, tid < 8 ? tid * 16 : OOB, WS_KREV + d * (KROW * 2) + 4096);
  } else if (r2 < 0) {
    const int s0 = ((r1 & 7) * 16 + (r1 >> 3)) * 16;
    u16* tile = (u16*)smem;
    float sm[4], sq[4];
#pragma unroll
    for (int i = 0; i < 4; ++i) {
      *(v4u*)(tile + (w * 4 + i) * 520 + lane * 8) = x[i];
      sm[i] = dot2(x[i][0], ONES, dot2(x[i][1], ONES, dot2(x[i][2], ONES, dot2(x[i][3], ONES, 0.f))));
      sq[i] = dot2(x[i][0], x[i][0], dot2(x[i][1], x[i][1], dot2(x[i][2], x[i][2], dot2(x[i][3], x[i][3], 0.f))));
    }
#pragma unroll
    for (int i = 0; i < 4; ++i) { sm[i] = xrow_sum(row16_sum(sm[i])); sq[i] = xrow_sum(row16_sum(sq[i])); }
    {
      const float s = lane == 0 ? sm[0] : lane == 1 ? sm[1] : lane == 2 ? sm[2] : sm[3];
      const float qq = lane == 0 ? sq[0] : lane == 1 ? sq[1] : lane == 2 ? sq[2] : sq[3];
      const float m = s * (1.0f / D);
      const float r = __builtin_amdgcn_rsqf(fmaxf(qq * (1.0f / D) - m * m, 0.f) + EPS);
      if (lane < 4) {
        ((float*)(p.ws + WS_ST1))[s0 + w * 4 + lane] = r;
        ((float*)(p.ws + WS_ST1))[2048 + s0 + w * 4 + lane] = -m * r;
      }
    }
    bar();
    typedef short v4i16_t __attribute__((ext_vector_type(4)));
#pragma unroll
    for (int it = 0; it < 4; ++it) {
      const int g = lane >> 4, cb = (it * 4 + w) * 2 + (g >> 1), hf = g & 1;
      const unsigned char* a0 = smem + (hf * 8 + ((lane >> 2) & 3)) * 1040 + cb * 32 + (lane & 3) * 8;
      const v2u lo = __builtin_bit_cast(v2u, __builtin_amdgcn_ds_read_tr16_b64_v4i16((LAS v4i16_t*)a0));
      const v2u hi = __builtin_bit_cast(v2u, __builtin_amdgcn_ds_read_tr16_b64_v4i16((LAS v4i16_t*)(a0 + 4 * 1040)));
      st128<0>(v4u{lo[0], lo[1], hi[0], hi[1]}, rws, (cb * 16 + (lane & 15)) * 4096 + hf * 16, WS_UT + s0 * 2);
    }
  } else if (r3 < 0) {
    const int cg = r2 % 48, kg = r2 / 48;
    f32x2 a[4];
#pragma unroll
    for (int j = 0; j < 4; ++j) a[j] = f32x2{0.f, 0.f};
#pragma unroll
    for (int i = 0; i < 4; ++i) {
      const float cv = bf2f(p.c[kg * 128 + i * 32 + (tid >> 3)]);
      const float sc = rbf(cv * __builtin_amdgcn_rcpf(1.0f + __builtin_amdgcn_exp2f(cv * -1.4426950408889634f)));
#pragma unroll
      for (int j = 0; j < 4; ++j) a[j] = fma2(unpk(x[i][j]), bc2(sc), a[j]);
    }
    float* red = (float*)smem;
    *(f32x4*)(red + (tid >> 3) * 68 + (tid & 7) * 8) = f32x4{a[0][0], a[0][1], a[1][0], a[1][1]};
    *(f32x4*)(red + (tid >> 3) * 68 + (tid & 7) * 8 + 4) = f32x4{a[2][0], a[2][1], a[3][0], a[3][1]};
    bar();
    {
      const int col = w * 16 + (lane & 15), g = lane >> 4;
      float s = 0.f;
#pragma unroll
      for (int i = 0; i < 8; ++i) s += red[(g * 8 + i) * 68 + col];
      s = xrow_sum(s);
      if (lane < 16) ((float*)(p.ws + WS_MODP))[(cg * 64 + col) * 4 + kg] = s;
    }
  } else {
    typedef short v4i16_t __attribute__((ext_vector_type(4)));
    const bool isin = r4 < 0;
    const int pitch = isin ? 80 : 528;
    const int rstep = isin ? 64 : 8;
    const bool padc = isin && ((r3 >> 1) % 22) == 21 && (tid & 3) >= 2;
#pragma unroll
    for (int i = 0; i < 4; ++i)
      *(v4u*)(smem + (i * rstep + (tid >> gsh)) * pitch + (tid & ((1u << gsh) - 1u)) * 16) = padc ? v4u{0u, 0u, 0u, 0u} : x[i];
    bar();
    const int q = lane >> 4;
#pragma unroll
    for (int i = 0; i < 4; ++i) {
      const int fr = w * 4 + i;
      const int row0 = isin ? (fr >> 1) * 32 : 0, col0 = isin ? 0 : (fr >> 1) * 64;
      const unsigned char* a0 = smem + (row0 + 8 * q + ((lane >> 2) & 3)) * pitch + col0 + (lane & 3) * 16 + (fr & 1) * 8;
      const v2u lo = __builtin_bit_cast(v2u, __builtin_amdgcn_ds_read_tr16_b64_v4i16((LAS v4i16_t*)a0));
      const v2u hi = __builtin_bit_cast(v2u, __builtin_amdgcn_ds_read_tr16_b64_v4i16((LAS v4i16_t*)(a0 + 4 * pitch)));
      const unsigned dst = isin ? WS_WT + (((r3 >> 1) * 2 + (fr & 1)) * 16 + (r3 & 1) * 8 + (fr >> 1)) * 1024
                                : WS_WOT + (((r4 & 1) * 16 + fr) * 22 + (r4 >> 1)) * 1024;
      st128<0>(v4u{lo[0], lo[1], hi[0], hi[1]}, rws, lane * 16, dst);
    }
  }
  asm volatile("" ::"v"(codepf));
  STAMP(0, 1);
}

constexpr int CV_RK = 4160;
constexpr int CV_CH = 4 * CV_RK + 4608;
constexpr int LDS_A = 0;
constexpr int LDS_GS = 2 * CV_CH;
constexpr int LDS_X = 73728;
constexpr int LDS_S = LDS_X + 24576;
constexpr int LDS_W = LDS_S + 4096;
constexpr int W_SLOT = 8192;
constexpr int MEGA_LDS = LDS_W + 48 * 1024;
static_assert(LDS_GS + 2560 <= 65536 && MEGA_LDS <= 160 * 1024, "lds");

DI v2u trread(const unsigned char* ptr) {
  typedef short v4i16_t __attribute__((ext_vector_type(4)));
  return __builtin_bit_cast(v2u, __builtin_amdgcn_ds_read_tr16_b64_v4i16((LAS v4i16_t*)ptr));
}

extern "C" __global__ void __launch_bounds__(256, 1) adaln_mega(Params p) {
  extern __shared__ __attribute__((aligned(16))) unsigned char smem[];
  const int tid = threadIdx.x, lane = tid & 63, w = __builtin_amdgcn_readfirstlane(tid >> 6);
  const int b = blockIdx.x, xcd = b & 7, idx = b >> 3;
  const int c = lane & 15, q = lane >> 4;
  const int d0 = xcd * 64 + idx * 2;
  const int mt = (xcd >> 1) * 8 + (idx & 7), nt = (xcd & 1) * 4 + (idx >> 3);
  const int np = nt < 6 ? 3 : 2, pr0 = nt < 6 ? 3 * nt : 6 + 2 * nt;
  const int kh = w & 1, uh = w >> 1;
  const int ub_own = pr0 * 2 + uh * np, ub_oth = pr0 * 2 + (uh ^ 1) * np;
  const unsigned oob3 = np == 3 ? 0u : OOB;
  const rsrc_t rws = mkrs(p.ws, WS_END), rwsp = mkrs(p.wsp, L * L * 2);
  const unsigned l16 = lane * 16;
  STAMP(1, 0);

  v4u ka[2], un[2], sr[2], sb_[2], wt[2];
  v2u kn[2], ue[4];
  f32x4 mg, ms;
  const int chs = tid >> 7, ns = tid & 127;
#pragma unroll
  for (int it = 0; it < 2; ++it) {
    ka[it] = ld128<0>(rws, chs * (KROW * 2) + ns * 16 + it * 2048, WS_KREV + d0 * (KROW * 2));
    kn[it] = ld64<0>(rws, chs * (KROW * 2) + ns * 16 + it * 2048 + 16, WS_KREV + d0 * (KROW * 2));
    un[it] = ld128<0>(rws, tid * 16, WS_UT + (d0 + it) * 4096);
    sr[it] = ld128<0>(rws, tid * 32 + it * 16, WS_ST1);
    sb_[it] = ld128<0>(rws, tid * 32 + it * 16, WS_ST1 + 8192);
  }
  const unsigned wp_oob = b < 176 ? 0u : OOB;
  mg = asf(ld128<0>(rws, tid * 16, WS_MODP + (4 * 512 + (b & 1) * 256) * 16));
  ms = asf(ld128<0>(rws, tid * 16, WS_MODP + (3 * 512 + (b & 1) * 256) * 16));
  const unsigned wp_fr = ((b >> 1) * 16 + (b & 1) * 8 + w * 2) * 1024 + wp_oob;
  wt[0] = ld128<0>(rws, l16, WS_WT + wp_fr);
  wt[1] = ld128<0>(rws, l16 + 1024, WS_WT + wp_fr);
  const int cch = w >> 1, eh = w & 1;
#pragma unroll
  for (int t = 0; t < 4; ++t) ue[t] = ld64<0>(rws, (c * 16 + q * 4) * 2 + t * 512, WS_UT + (d0 + cch) * 4096 + eh * 2048);
  const float* modp = (const float*)(p.ws + WS_MODP);
  float sa[2], ga_[2], g1[2];
#pragma unroll
  for (int ch = 0; ch < 2; ++ch) {
    sa[ch] = rbf(sum4(*(const f32x4*)(modp + (d0 + ch) * 4)));
    g1[ch] = rbf(1.0f + rbf(sum4(*(const f32x4*)(modp + (512 + d0 + ch) * 4))));
    ga_[ch] = rbf(sum4(*(const f32x4*)(modp + (1024 + d0 + ch) * 4)));
  }
  const float dsk = bf2f(p.dsk[d0 + cch]);
  SB();
  {
    const v4u mk = {~0u, ~0u, ~0u, ~0u};
#pragma unroll
    for (int k = 0; k < 3; ++k) {
      st128<SC1>(mk, rws, l16, WS_Z2 + ((ub_own + k) * 64 + mt * 2 + kh) * 1024 + (k == 2 ? oob3 : 0u));
      st128<SC1>(mk, rws, l16, WS_V + ((mt * 4 + w) * 22 + pr0 + k) * 1024 + (k == 2 ? oob3 : 0u));
    }
  }
  v4u wr[8][8];
  const unsigned wsp_v = (lane >> 3) * 4096 + (lane & 7) * 16, wsp_s = mt * 64 * 4096 + w * 1024;
#define LOAD_WSP(ck) _Pragma("unroll") for (int i_ = 0; i_ < 8; ++i_) wr[ck][i_] = ld128<0>(rwsp, wsp_v + (ck) * 128, wsp_s + i_ * 8 * 4096)
  LOAD_WSP(0); LOAD_WSP(1); LOAD_WSP(2); LOAD_WSP(3);
  SB();
  {
    unsigned char* rk = smem + chs * CV_CH + ns * 16;
#pragma unroll
    for (int it = 0; it < 2; ++it) {
      const v4u a = ka[it];
      const unsigned o0 = __builtin_amdgcn_alignbit(a[1], a[0], 16), o1 = __builtin_amdgcn_alignbit(a[2], a[1], 16),
                     o2 = __builtin_amdgcn_alignbit(a[3], a[2], 16), o3 = __builtin_amdgcn_alignbit(kn[it][0], a[3], 16),
                     o4 = __builtin_amdgcn_alignbit(kn[it][1], kn[it][0], 16);
      *(v4u*)(rk + 0 * CV_RK + it * 2048) = a;
      *(v4u*)(rk + 1 * CV_RK + it * 2048) = v4u{o0, o1, o2, o3};
      *(v4u*)(rk + 2 * CV_RK + it * 2048) = v4u{a[1], a[2], a[3], kn[it][0]};
      *(v4u*)(rk + 3 * CV_RK + it * 2048) = v4u{o1, o2, o3, o4};
    }
    if (ns < 48) *(v4u*)(smem + chs * CV_CH + (ns < 16 ? (ns >> 2) * CV_RK + 4096 + (ns & 3) * 16 : 4 * CV_RK + (ns - 16) * 16)) = v4u{0u, 0u, 0u, 0u};
#pragma unroll
    for (int ch = 0; ch < 2; ++ch) {
      v4u o;
#pragma unroll
      for (int j = 0; j < 4; ++j) {
        const f32x4 r4 = asf(sr[j >> 1]), b4 = asf(sb_[j >> 1]);
        const f32x2 nn = fma2(unpk(un[ch][j]), (j & 1) ? hi2(r4) : lo2(r4), (j & 1) ? hi2(b4) : lo2(b4));
        o[j] = pk2(fma2(nn, bc2(g1[ch]), bc2(sa[ch])));
      }
      *(v4u*)(smem + ch * CV_CH + 4 * CV_RK + 512 + tid * 16) = o;
    }
    ((float*)(smem + LDS_GS))[tid] = rbf(1.0f + rbf(sum4(mg)));
    ((float*)(smem + LDS_GS))[256 + tid] = rbf(sum4(ms));
  }
  bar();
  STAMP(1, 1);
  {
    float cs = 0.f;
    f32x2 bs = {0.f, 0.f};
#pragma unroll
    for (int i = 0; i < 2; ++i) {
      const float* gp = (const float*)(smem + LDS_GS) + (w * 2 + i) * 32 + q * 8;
      const f32x4 ga4 = *(const f32x4*)gp, gb4 = *(const f32x4*)(gp + 4), sa4 = *(const f32x4*)(gp + 256), sb4 = *(const f32x4*)(gp + 260);
      v4u o;
#pragma unroll
      for (int j = 0; j < 4; ++j) {
        const f32x2 x = unpk(wt[i][j]);
        const f32x2 g2 = j == 0 ? lo2(ga4) : j == 1 ? hi2(ga4) : j == 2 ? lo2(gb4) : hi2(gb4);
        const f32x2 s2 = j == 0 ? lo2(sa4) : j == 1 ? hi2(sa4) : j == 2 ? lo2(sb4) : hi2(sb4);
        o[j] = nomark(pk2(x * g2));
        cs = dot2(o[j], ONES, cs);
        bs = fma2(x, s2, bs);
      }
      st128<SC1>(o, rws, l16 + i * 1024, WS_WP + wp_fr);
    }
    const float cst = xrow_sum(cs), bst = xrow_sum(bs[0] + bs[1]);
    if (q == 0) *(f32x2*)(smem + LDS_GS + 2048 + (w * 16 + c) * 8) = f32x2{cst, bst};
  }
  f32x4 cacc[8];
  {
    const unsigned char* abase = smem + cch * CV_CH + (3 - (c & 3)) * CV_RK + 2 * (2028 - 4 * (c >> 2) + 8 * q) - eh * 256 - 192 - 3584;
    const unsigned char* bbase = smem + cch * CV_CH + 4 * CV_RK + 2 * (256 - 16 + 16 * c + 8 * q) - eh * 256 - 192;
#pragma unroll
    for (int t = 0; t < 8; ++t) cacc[t] = f32x4{0.f, 0.f, 0.f, 0.f};
#pragma unroll
    for (int ee = 0; ee < 4; ++ee) {
      v4u A[8], B[8];
#pragma unroll
      for (int k = 0; k < 8; ++k) {
        const v2u lo = *(const v2u*)(abase + 192 - 64 * ee + 3584 - 512 * k), hi = *(const v2u*)(abase + 192 - 64 * ee + 3584 - 512 * k + 8);
        A[k] = v4u{lo[0], lo[1], hi[0], hi[1]};
        B[k] = *(const v4u*)(bbase + 192 - 64 * ee + 512 * k);
      }
#pragma unroll
      for (int qq = 0; qq < 8; ++qq)
#pragma unroll
        for (int pp = 0; pp + qq < 8; ++pp) cacc[pp + qq] = mfma(A[qq], B[pp], cacc[pp + qq]);
    }
  }
  STAMP(1, 2);
  {
    f32x4* red = (f32x4*)(smem + LDS_X);
    f32x4 keep[4];
#pragma unroll
    for (int t = 0; t < 4; ++t) {
      f32x4 lo = cacc[t], hi = cacc[4 + t];
      asm volatile("" : "+v"(lo), "+v"(hi));
      red[(w * 4 + t) * 64 + lane] = eh ? lo : hi;
      keep[t] = eh ? hi : lo;
    }
    asm volatile("s_waitcnt vmcnt(0)" ::: "memory");
    bar();
    const float ga = cch ? ga_[1] : ga_[0];
    const unsigned xoff = ((c >> 1) * 32 + ((q & 1) * 4 + 2 * (c & 1) + (q >> 1)) * 4) * 2;
#pragma unroll
    for (int t = 0; t < 4; ++t) {
      const f32x4 y = keep[t] + red[((w ^ 1) * 4 + t) * 64 + lane];
      const v2u hv = *(const v2u*)(smem + cch * CV_CH + 4 * CV_RK + 512 + (eh * 4 + t) * 512 + (c * 16 + q * 4) * 2);
      v2u o;
      o[0] = nomark(pk2(fma2(fma2(unpk(hv[0]), bc2(dsk), lo2(y)), bc2(ga), unpk(ue[t][0]))));
      o[1] = nomark(pk2(fma2(fma2(unpk(hv[1]), bc2(dsk), hi2(y)), bc2(ga), unpk(ue[t][1]))));
      st64<SC1>(o, rws, xoff + t * 512, WS_X1T + (d0 + cch) * 4096 + eh * 2048);
    }
    if (tid < 16) {
      const f32x2* pp = (const f32x2*)(smem + LDS_GS + 2048) + tid;
      const f32x2 s = (pp[0] + pp[16]) + (pp[32] + pp[48]);
      st64<SC1>(v2u{nomark(__float_as_uint(s[0])), nomark(__float_as_uint(s[1]))}, rws, tid * 16 + (b & 1) * 8, WS_CSB + (b >> 1) * 256 + wp_oob);
    }
  }
  STAMP(1, 3);
  SB();
  unsigned char* wslot = smem + LDS_W + w * W_SLOT;
  const int wr_off = (lane >> 3) * 128 + (((lane & 7) ^ ((lane >> 4) & 1)) * 16);
#define PARK_WSP(ck, slot) _Pragma("unroll") for (int i_ = 0; i_ < 8; ++i_) *(v4u*)((slot) + i_ * 1024 + (wr_off ^ ((i_ & 3) * 32))) = wr[ck][i_]
  PARK_WSP(0, wslot);
  v4u wf[3][8];
  const unsigned rot = uh * 4;
#define LOAD_WP(half, k, i) ld128<SC1>(rws, l16 + ((i) & 3) * 1024, WS_WP + (((half) * 44 + ub_own + (k)) * 16 + kh * 8 + (((i) & 4) ^ rot)) * 1024 + ((k) == 2 ? oob3 : 0u))
  v4u xs[16];
  f32x4 cz[3];
  unsigned spins = 0;
  for (;;) {
    MEMFENCE();
#pragma unroll
    for (int i = 0; i < 16; ++i) xs[i] = ld128<SC1>(rws, (lane >> 3) * 4096 + (lane & 7) * 16, WS_X1T + (i * 32 + w * 8) * 4096 + mt * 128);
#pragma unroll
    for (int i = 0; i < 8; ++i)
#pragma unroll
      for (int k = 0; k < 3; ++k) wf[k][i] = LOAD_WP(1, k, i);
#pragma unroll
    for (int k = 0; k < 3; ++k) cz[k] = asf(ld128<SC1>(rws, c * 16, WS_CSB + (44 + ub_own + k) * 256 + (k == 2 ? oob3 : 0u)));
    SB();
    unsigned m = 0;
#pragma unroll
    for (int i = 0; i < 16; ++i) m = macc(m, xs[i]);
#pragma unroll
    for (int i = 0; i < 8; ++i)
#pragma unroll
      for (int k = 0; k < 3; ++k) m = macc(m, wf[k][i]);
#pragma unroll
    for (int k = 0; k < 3; ++k) m = macc(m, __builtin_bit_cast(v4u, cz[k]));
    if (!any_mark(m) || ++spins > SPIN_CAP) break;
  }
  STAMP(1, 4);
  bar();
#pragma unroll
  for (int i = 0; i < 16; ++i) {
    const int rl = w * 8 + (lane >> 3), chn = lane & 7;
    const int sg = ((rl >> 1) & 1) | (((rl >> 3) & 1) << 1);
    *(v4u*)(smem + LDS_A + i * 4096 + rl * 128 + (((chn >> 1) ^ sg) * 32) + (chn & 1) * 16) = xs[i];
  }
  LOAD_WSP(4); LOAD_WSP(5);
  bar();
  STAMP(1, 5);
  const unsigned char* tb[4];
  {
    const int row = 8 * q + ((lane >> 2) & 3);
    const int sg = ((row >> 1) & 1) | (((row >> 3) & 1) << 1);
#pragma unroll
    for (int tu = 0; tu < 4; ++tu) tb[tu] = smem + LDS_A + kh * 32768 + row * 128 + ((tu ^ (2 * kh) ^ sg) * 32) + (lane & 3) * 8;
  }
  const int rot_off = uh * 16384;
#define AFRAG(tu, i) ({ const unsigned char* p_ = tb[tu] + ((((i) & 4) * 4096) ^ rot_off) + ((i) & 3) * 4096; const v2u lo_ = trread(p_), hi_ = trread(p_ + 512); v4u{lo_[0], lo_[1], hi_[0], hi_[1]}; })
  f32x4 acc[3][4];
  float s1[4], s2[4];
#pragma unroll
  for (int tu = 0; tu < 4; ++tu) {
    s1[tu] = 0.f; s2[tu] = 0.f;
#pragma unroll
    for (int k = 0; k < 3; ++k) acc[k][tu] = f32x4{0.f, 0.f, 0.f, 0.f};
  }
  v4u wg[3][8];
#pragma unroll
  for (int i = 0; i < 8; ++i) {
    v4u a[4];
#pragma unroll
    for (int tu = 0; tu < 4; ++tu) a[tu] = AFRAG(tu, i);
#pragma unroll
    for (int k = 0; k < 3; ++k)
#pragma unroll
      for (int tu = 0; tu < 4; ++tu) acc[k][tu] = mfma(a[tu], wf[k][i], acc[k][tu]);
    if (i < 4) {
#pragma unroll
      for (int tu = 0; tu < 4; ++tu)
#pragma unroll
        for (int j = 0; j < 4; ++j) { s1[tu] = dot2(a[tu][j], ONES, s1[tu]); s2[tu] = dot2(a[tu][j], a[tu][j], s2[tu]); }
    }
#pragma unroll
    for (int k = 0; k < 3; ++k) wg[k][i] = LOAD_WP(0, k, i);
  }
  STAMP(1, 6);
  f32x4* xch = (f32x4*)(smem + LDS_X);
  float* st = (float*)(smem + LDS_S);
#pragma unroll
  for (int k = 0; k < 3; ++k)
#pragma unroll
    for (int t = 0; t < 2; ++t) xch[(w * 6 + k * 2 + t) * 64 + lane] = acc[k][2 + t];
#pragma unroll
  for (int tu = 0; tu < 4; ++tu) {
    const float a = xrow_sum(s1[tu]), bq = xrow_sum(s2[tu]);
    if (q < 2) st[w * 128 + q * 64 + (tu ^ (2 * kh)) * 16 + c] = q ? bq : a;
  }
  bar();
  float* fin = st + 512 + w * 64;
  {
    const int row = kh * 32 + (lane & 31);
    const float a = (st[row] + st[128 + row]) + (st[256 + row] + st[384 + row]);
    const float bq = (st[64 + row] + st[192 + row]) + (st[320 + row] + st[448 + row]);
    const float m = a * (1.0f / D);
    const float r = __builtin_amdgcn_rsqf(fmaxf(bq * (1.0f / D) - m * m, 0.f) + EPS);
    fin[lane] = lane < 32 ? r : -r * m;
  }
  f32x4 z2v[3][2];
  {
    f32x4 rs[2], rq[2];
#pragma unroll
    for (int t = 0; t < 2; ++t) {
      rs[t] = f32x4{0.f, 0.f, 0.f, 0.f}; rq[t] = rs[t];
      const f32x4 r4 = *(const f32x4*)(fin + t * 16 + q * 4), m4 = *(const f32x4*)(fin + 32 + t * 16 + q * 4);
#pragma unroll
      for (int k = 0; k < 3; ++k) {
        const f32x4 a = acc[k][t] + xch[((w ^ 1) * 6 + k * 2 + t) * 64 + lane];
        const float cs = cz[k][0] + cz[k][2], bs = cz[k][1] + cz[k][3];
        const f32x2 zl = gelu2(fma2(lo2(a), lo2(r4), fma2(lo2(m4), bc2(cs), bc2(bs))));
        const f32x2 zh = gelu2(fma2(hi2(a), hi2(r4), fma2(hi2(m4), bc2(cs), bc2(bs))));
        z2v[k][t] = f32x4{zl[0], zl[1], zh[0], zh[1]};
        rs[t] += z2v[k][t];
        rq[t] += z2v[k][t] * z2v[k][t];
      }
    }
    float outv = 0.f;
#pragma unroll
    for (int t = 0; t < 2; ++t)
#pragma unroll
      for (int r = 0; r < 4; ++r) {
        const float a = row16_sum(rs[t][r]), bq = row16_sum(rq[t][r]);
        outv = c == (t * 4 + r) * 2 ? a : c == (t * 4 + r) * 2 + 1 ? bq : outv;
      }
    __builtin_amdgcn_raw_buffer_store_b32(nomark(__float_as_uint(outv)), rws, lane * 4, WS_ST2 + ((mt * 2 + kh) * 16 + nt * 2 + uh) * 256, SC1);
  }
  STAMP(1, 7);
  SB();
  f32x4 c1[3][4];
  spins = 0;
  for (;;) {
    MEMFENCE();
#pragma unroll
    for (int k = 0; k < 3; ++k)
#pragma unroll
      for (int r = 0; r < 4; ++r) c1[k][r] = asf(ld128<SC1>(rws, (q * 4 + r) * 16, WS_CSB + (ub_own + k) * 256 + (k == 2 ? oob3 : 0u)));
    unsigned m = 0;
#pragma unroll
    for (int i = 0; i < 8; ++i)
#pragma unroll
      for (int k = 0; k < 3; ++k) m = macc(m, wg[k][i]);
#pragma unroll
    for (int k = 0; k < 3; ++k)
#pragma unroll
      for (int r = 0; r < 4; ++r) m = macc(m, __builtin_bit_cast(v4u, c1[k][r]));
    if (!any_mark(m) || ++spins > SPIN_CAP) break;
#pragma unroll
    for (int i = 0; i < 8; ++i)
#pragma unroll
      for (int k = 0; k < 3; ++k) wg[k][i] = LOAD_WP(0, k, i);
  }
  f32x4 cs1[3], bs1[3];
#pragma unroll
  for (int k = 0; k < 3; ++k)
#pragma unroll
    for (int r = 0; r < 4; ++r) { cs1[k][r] = c1[k][r][0] + c1[k][r][2]; bs1[k][r] = c1[k][r][1] + c1[k][r][3]; }
#pragma unroll
  for (int k = 0; k < 3; ++k) { PIN(cs1[k]); PIN(bs1[k]); }
#pragma unroll
  for (int tu = 0; tu < 4; ++tu)
#pragma unroll
    for (int k = 0; k < 3; ++k) acc[k][tu] = f32x4{0.f, 0.f, 0.f, 0.f};
#pragma unroll
  for (int i = 0; i < 8; ++i) {
    v4u a[4];
#pragma unroll
    for (int tu = 0; tu < 4; ++tu) a[tu] = AFRAG(tu, i);
#pragma unroll
    for (int k = 0; k < 3; ++k)
#pragma unroll
      for (int tu = 0; tu < 4; ++tu) acc[k][tu] = mfma(wg[k][i], a[tu], acc[k][tu]);
  }
  const int th = xcd & 1, dq = idx >> 3;
  v4u x1r[2];
  {
    const int row = 8 * q + ((lane >> 2) & 3);
    const int sg = ((row >> 1) & 1) | (((row >> 3) & 1) << 1);
#pragma unroll
    for (int t = 0; t < 2; ++t) {
      const unsigned char* p_ = smem + LDS_A + (dq * 4 + w) * 4096 + row * 128 + (((2 * th + t) ^ sg) * 32) + (lane & 3) * 8;
      const v2u lo = trread(p_), hi = trread(p_ + 512);
      x1r[t] = v4u{lo[0], lo[1], hi[0], hi[1]};
    }
  }
  STAMP(1, 8);
  SB();
  {
    f32x4 pt[4];
    spins = 0;
    for (;;) {
      MEMFENCE();
#pragma unroll
      for (int i = 0; i < 4; ++i) pt[i] = asf(ld128<SC1>(rws, l16 + i * 1024, WS_ST2 + (mt * 2 + kh) * 4096));
      unsigned m = 0;
#pragma unroll
      for (int i = 0; i < 4; ++i) m = macc(m, __builtin_bit_cast(v4u, pt[i]));
      if (!any_mark(m) || ++spins > SPIN_CAP) break;
    }
    const f32x4 sx = (pt[0] + pt[1]) + (pt[2] + pt[3]);
    const float a0 = xrow_sum(sx[0]), b0 = xrow_sum(sx[1]), a1 = xrow_sum(sx[2]), b1 = xrow_sum(sx[3]);
    const float a = (q & 1) ? a1 : a0, bq = (q & 1) ? b1 : b0;
    const float m = a * (1.0f / FH);
    const float r = __builtin_amdgcn_rsqf(fmaxf(bq * (1.0f / FH) - m * m, 0.f) + EPS);
    float* f2 = st + 768 + w * 64;
    f2[(q >> 1) * 32 + 2 * c + (q & 1)] = (q >> 1) ? -r * m : r;
    v4u o[3];
#pragma unroll
    for (int t = 0; t < 2; ++t) {
      const f32x4 r4 = *(const f32x4*)(f2 + q * 8 + t * 4), m4 = *(const f32x4*)(f2 + 32 + q * 8 + t * 4);
#pragma unroll
      for (int k = 0; k < 3; ++k) {
        o[k][2 * t] = nomark(pk2(fma2(lo2(z2v[k][t]), lo2(r4), lo2(m4))));
        o[k][2 * t + 1] = nomark(pk2(fma2(hi2(z2v[k][t]), hi2(r4), hi2(m4))));
      }
    }
#pragma unroll
    for (int k = 0; k < 3; ++k) st128<SC1>(o[k], rws, l16, WS_Z2 + ((ub_own + k) * 64 + mt * 2 + kh) * 1024 + (k == 2 ? oob3 : 0u));
  }
  STAMP(1, 9);
  SB();
  LOAD_WSP(6); LOAD_WSP(7);
  v2u z1p[3][2];
  bar();
#pragma unroll
  for (int k = 0; k < 3; ++k)
#pragma unroll
    for (int t = 0; t < 2; ++t) xch[(w * 6 + k * 2 + t) * 64 + lane] = acc[k][2 + t];
  unsigned char* wslot2 = smem + LDS_A + w * 16384;
  PARK_WSP(1, wslot2);
  PARK_WSP(2, wslot2 + 8192);
  bar();
#pragma unroll
  for (int t = 0; t < 2; ++t) {
    const float r1 = fin[t * 16 + c], m1 = fin[32 + t * 16 + c];
#pragma unroll
    for (int k = 0; k < 3; ++k) {
      const f32x4 a = acc[k][t] + xch[((w ^ 1) * 6 + k * 2 + t) * 64 + lane];
      z1p[k][t][0] = pk2(gelu2(fma2(lo2(a), bc2(r1), fma2(lo2(cs1[k]), bc2(m1), lo2(bs1[k])))));
      z1p[k][t][1] = pk2(gelu2(fma2(hi2(a), bc2(r1), fma2(hi2(cs1[k]), bc2(m1), hi2(bs1[k])))));
      PIN(z1p[k][t]);
    }
  }
  float bsp[2];
#pragma unroll
  for (int t = 0; t < 2; ++t) bsp[t] = bf2f(p.bsp[mt * 64 + perm(2 * kh + t, c)]);
  STAMP(1, 10);
  SB();
  f32x4 ac2[6][4];
#pragma unroll
  for (int i = 0; i < 6; ++i)
#pragma unroll
    for (int j = 0; j < 4; ++j) ac2[i][j] = f32x4{0.f, 0.f, 0.f, 0.f};
  int rd_off[4];
#pragma unroll
  for (int tu = 0; tu < 4; ++tu) rd_off[tu] = perm(tu ^ (2 * kh), c) * 128 + ((q ^ (c >> 1)) * 16);
  constexpr int ZD = 4;
  v4u zf[ZD][6];
#define LOAD_Z(ks, i) ld128<SC1>(rws, l16 + ((ks) & 3) * 1024, WS_Z2 + ((((i) < 3 ? ub_own : ub_oth) + (i) % 3) * 64 + w * 16 + ((ks) & ~3)) * 1024 + ((i) % 3 == 2 ? oob3 : 0u))
  spins = 0;
  for (;;) {
    MEMFENCE();
#pragma unroll
    for (int i = 0; i < 6; ++i) { zf[0][i] = LOAD_Z(0, i); zf[1][i] = LOAD_Z(1, i); }
    const v4u smp = ld128<SC1>(rws, (q * 64 + c) * 1024 + 1008, WS_Z2 + (pr0 * 2 * 64 + w * 16) * 1024);
    unsigned m = macc(0u, smp);
#pragma unroll
    for (int i = 0; i < 6; ++i) { m = macc(m, zf[0][i]); m = macc(m, zf[1][i]); }
    if (!any_mark(m) || ++spins > SPIN_CAP) break;
  }
  STAMP(1, 11);
#pragma unroll
  for (int s = 2; s < ZD; ++s)
#pragma unroll
    for (int i = 0; i < 6; ++i) zf[s][i] = LOAD_Z(s, i);
  v4u wo[2][22];
  v4u wfr[2][4];
#pragma unroll
  for (int k2 = 0; k2 < 2; ++k2)
#pragma unroll
    for (int tu = 0; tu < 4; ++tu) wfr[k2][tu] = *(const v4u*)(wslot + (rd_off[tu] ^ (k2 * 64)));
  SB();
#pragma unroll
  for (int ck = 0; ck < 8; ++ck) {
    v4u wcur[2][4];
#pragma unroll
    for (int k2 = 0; k2 < 2; ++k2)
#pragma unroll
      for (int tu = 0; tu < 4; ++tu) wcur[k2][tu] = wfr[k2][tu];
#pragma unroll
    for (int k2 = 0; k2 < 2; ++k2) {
      const int ks = ck * 2 + k2;
      if (ks >= 2) {
        unsigned m = 0;
#pragma unroll
        for (int i = 0; i < 6; ++i) m = macc(m, zf[ks % ZD][i]);
        if (__builtin_expect(any_mark(m), 0)) {
          spins = 0;
          do {
            MEMFENCE();
            m = 0;
#pragma unroll
            for (int i = 0; i < 6; ++i) { zf[ks % ZD][i] = LOAD_Z(ks, i); m = macc(m, zf[ks % ZD][i]); }
          } while (any_mark(m) && ++spins <= SPIN_CAP);
        }
      }
      if (k2 == 0 && ck >= 2 && ck < 7) { PARK_WSP(ck + 1, wslot); }
      if (k2 == 1 && ck < 7) {
        const unsigned char* src = ck < 2 ? wslot2 + ck * 8192 : wslot;
#pragma unroll
        for (int k3 = 0; k3 < 2; ++k3)
#pragma unroll
          for (int tu = 0; tu < 4; ++tu) wfr[k3][tu] = *(const v4u*)(src + (rd_off[tu] ^ (k3 * 64)));
      }
      v4u cur[6];
#pragma unroll
      for (int i = 0; i < 6; ++i) cur[i] = zf[ks % ZD][i];
#pragma unroll
      for (int i = 0; i < 6; ++i)
#pragma unroll
        for (int tu = 0; tu < 4; ++tu) ac2[i][tu] = mfma(cur[i], wcur[k2][tu], ac2[i][tu]);
      if (ks + ZD < 16) {
#pragma unroll
        for (int i = 0; i < 6; ++i) zf[ks % ZD][i] = LOAD_Z(ks + ZD, i);
      } else {
#pragma unroll
        for (int i = 0; i < 11; ++i) {
          const int f = (ks + ZD - 16) * 11 + i;
          wo[f & 1][f >> 1] = ld128<0>(rws, l16 + ((f >> 1) & 3) * 1024, WS_WOT + (((dq * 4 + w) * 2 + (f & 1)) * 22 + ((f >> 1) & ~3)) * 1024);
        }
      }
      SB();
    }
  }
  STAMP(1, 12);
  f32x4 gbp[8];
#pragma unroll
  for (int j = 0; j < 8; ++j) gbp[j] = asf(ld128<0>(rws, (q * 8 + j) * 16, WS_MODP + (5 * 512 + (dq * 4 + w) * 32) * 16));
  {
    f32x4* red = (f32x4*)(smem + LDS_A);
    bar();
#pragma unroll
    for (int i = 0; i < 6; ++i)
#pragma unroll
      for (int tu = 0; tu < 4; ++tu) {
        const int rel = (tu >> 1) | ((i / 3) << 1);
        if (rel) red[(((w ^ rel) * 3 + rel - 1) * 6 + (i % 3) * 2 + (tu & 1)) * 64 + lane] = ac2[i][tu];
      }
    bar();
#pragma unroll
    for (int k = 0; k < 3; ++k)
#pragma unroll
      for (int t = 0; t < 2; ++t) {
        f32x4 g = ac2[k][t];
#pragma unroll
        for (int rel = 0; rel < 3; ++rel) g += red[((w * 3 + rel) * 6 + k * 2 + t) * 64 + lane];
        v2u o;
        o[0] = nomark(pk2(unpk(z1p[k][t][0]) * (lo2(g) + bc2(bsp[t]))));
        o[1] = nomark(pk2(unpk(z1p[k][t][1]) * (hi2(g) + bc2(bsp[t]))));
        const int uu = ub_own + k;
        st64<SC1>(o, rws, l16, WS_V + ((mt * 4 + 2 * kh + t) * 22 + (uu >> 1)) * 1024 + (uu & 1) * 8 + (k == 2 ? oob3 : 0u));
      }
  }
  STAMP(1, 13);
  SB();
  f32x2 gb2[4];
#pragma unroll
  for (int j = 0; j < 4; ++j) { gb2[j] = f32x2{rbf(sum4(gbp[2 * j])), rbf(sum4(gbp[2 * j + 1]))}; PIN(gb2[j]); }
  {
    v4u vs[11];
    spins = 0;
    for (;;) {
      MEMFENCE();
#pragma unroll
      for (int i = 0; i < 11; ++i) vs[i] = ld128<SC1>(rws, l16 + (i & 3) * 1024, WS_V + ((mt * 4 + 2 * th) * 22 + w * 11 + (i & ~3)) * 1024);
      unsigned m = 0;
#pragma unroll
      for (int i = 0; i < 11; ++i) m = macc(m, vs[i]);
      if (!any_mark(m) || ++spins > SPIN_CAP) break;
    }
    STAMP(1, 14);
#pragma unroll
    for (int i = 0; i < 11; ++i) *(v4u*)(smem + LDS_W + (w * 11 + i) * 1024 + l16) = vs[i];
  }
  bar();
  f32x4 a3[2][2];
#pragma unroll
  for (int e = 0; e < 2; ++e)
#pragma unroll
    for (int t = 0; t < 2; ++t) a3[e][t] = f32x4{0.f, 0.f, 0.f, 0.f};
#pragma unroll
  for (int ks = 0; ks < 22; ++ks)
#pragma unroll
    for (int t = 0; t < 2; ++t) {
      const v4u vf = *(const v4u*)(smem + LDS_W + (t * 22 + ks) * 1024 + l16);
#pragma unroll
      for (int e = 0; e < 2; ++e) a3[e][t] = mfma(wo[e][ks], vf, a3[e][t]);
    }
  const rsrc_t rout = mkrs(p.out, L * D * 2);
#pragma unroll
  for (int t = 0; t < 2; ++t) {
    v4u o;
    o[0] = pk2(fma2(lo2(a3[0][t]), gb2[0], unpk(x1r[t][0])));
    o[1] = pk2(fma2(hi2(a3[0][t]), gb2[1], unpk(x1r[t][1])));
    o[2] = pk2(fma2(lo2(a3[1][t]), gb2[2], unpk(x1r[t][2])));
    o[3] = pk2(fma2(hi2(a3[1][t]), gb2[3], unpk(x1r[t][3])));
    st128<0>(o, rout, perm(t, c) * 1024 + q * 16, (mt * 64 + th * 32) * 1024 + (dq * 4 + w) * 64);
  }
  STAMP(1, 15);
  asm volatile("adaln_code_end:");
}

extern "C" void kernel_launch(void* const* d_in, const int* in_sizes, int n_in, void* d_out, int out_size, void* d_ws,
                              size_t ws_size, hipStream_t stream) {
  Params p;
  p.u = (const u16*)d_in[0];
  p.lar = (const u16*)d_in[1];
  p.aim = (const u16*)d_in[2];
  p.cre = (const u16*)d_in[3];
  p.cim = (const u16*)d_in[4];
  p.ldt = (const u16*)d_in[5];
  p.dsk = (const u16*)d_in[6];
  p.win = (const u16*)d_in[8];
  p.wsp = (const u16*)d_in[9];
  p.bsp = (const u16*)d_in[10];
  p.wout = (const u16*)d_in[11];
  p.c = (const u16*)d_in[12];
  p.wmod = (const u16*)d_in[13];
  p.out = (u16*)d_out;
  p.ws = (unsigned char*)d_ws;
  (void)hipFuncSetAttribute((const void*)adaln_mega, hipFuncAttributeMaxDynamicSharedMemorySize, MEGA_LDS);
  adaln_k0<<<K0_GRID, 256, K0_LDS, stream>>>(p);
  adaln_mega<<<256, 256, MEGA_LDS, stream>>>(p);
}
```

```cpp
#include <hip/hip_runtime.h>
#include <hip/hip_bf16.h>
#include <stdint.h>

#define LAS __attribute__((address_space(3)))
typedef unsigned v4u __attribute__((ext_vector_type(4)));
typedef unsigned v2u __attribute__((ext_vector_type(2)));
typedef float f32x4 __attribute__((ext_vector_type(4)));
typedef float f32x2 __attribute__((ext_vector_type(2)));
typedef short bf16x8 __attribute__((ext_vector_type(8)));
typedef _Float16 f16x8 __attribute__((ext_vector_type(8)));
typedef _Float16 f16x2 __attribute__((ext_vector_type(2)));
typedef __bf16 bf16x2_t __attribute__((ext_vector_type(2)));
typedef unsigned short u16;
typedef __amdgpu_buffer_rsrc_t rsrc_t;

#define STAMP_DECL do { } while (0)
#define STAMP(k, i) do { } while (0)
#define STAMP_FLUSH(k) do { } while (0)

constexpr int L = 2048, D = 512, NM = 128, FH = 688, FF = 1376;
constexpr float EPS = 1e-5f;
constexpr int SC1 = 16;
constexpr unsigned OOB = 0x40000000u;
constexpr unsigned SPIN_CAP = 1u << 14;

constexpr unsigned WS_EPOCH = 0;
constexpr unsigned WS_CNT = 4096;
constexpr unsigned WS_SR = WS_CNT + 256 * 128;
constexpr unsigned WS_SB = WS_SR + 2048 * 8;
constexpr unsigned WS_MODG = WS_SB + 2048 * 8;
constexpr unsigned WS_CSB = WS_MODG + 3072 * 32;
constexpr unsigned WS_ST2 = WS_CSB + 88 * 16 * 32;
constexpr unsigned WS_UG = 1u << 20;
static_assert(WS_ST2 + 32 * 2 * 16 * 32 * 8 <= WS_UG, "small buffers");
constexpr unsigned WS_X1T = WS_UG + 256 * 2048 * 8;
constexpr unsigned WS_WP = WS_X1T + 512 * 2048 * 2;
constexpr unsigned WS_WOT = WS_WP + 88 * 16 * 1024;
constexpr unsigned WS_Z2 = WS_WOT + 32 * 22 * 1024;
constexpr unsigned WS_V = WS_Z2 + 44 * 64 * 1024;
constexpr unsigned WS_END = WS_V + 128 * 22 * 1024;

struct Params {
  const u16 *u, *lar, *aim, *cre, *cim, *ldt, *dsk, *win, *wsp, *bsp, *wout, *c, *wmod;
  u16* out;
  unsigned char* ws;
};

#define DI __device__ __forceinline__
template <int V> struct IC { static constexpr int value = V; };
template <int I, int N, class F> DI void sfor(F&& f) {
  if constexpr (I < N) { f(IC<I>{}); sfor<I + 1, N>(f); }
}
DI float bf2f(unsigned b) { return __builtin_bit_cast(float, b << 16); }
DI float bflo(unsigned w) { return __builtin_bit_cast(float, w << 16); }
DI float bfhi(unsigned w) { return __builtin_bit_cast(float, w & 0xFFFF0000u); }
DI f32x2 unpk(unsigned w) { return f32x2{bflo(w), bfhi(w)}; }
DI unsigned pk2(f32x2 v) { return __builtin_bit_cast(unsigned, __builtin_convertvector(v, bf16x2_t)); }
DI unsigned pk2(float lo, float hi) { return pk2(f32x2{lo, hi}); }
DI unsigned nomark(unsigned x) { return x < 0xFFFFFFFEu ? x : 0xFFFFFFFEu; }
DI float rbf(float x) { return bflo(pk2(x, 0.f)); }
DI f32x2 fma2(f32x2 a, f32x2 b, f32x2 c) { return __builtin_elementwise_fma(a, b, c); }
DI f32x2 lo2(f32x4 v) { return f32x2{v[0], v[1]}; }
DI f32x2 hi2(f32x4 v) { return f32x2{v[2], v[3]}; }
DI f32x2 bc2(float x) { return f32x2{x, x}; }
DI f32x4 mfma(v4u a, v4u b, f32x4 c) {
  return __builtin_amdgcn_mfma_f32_16x16x32_bf16(__builtin_bit_cast(bf16x8, a), __builtin_bit_cast(bf16x8, b), c, 0, 0, 0);
}
DI f32x4 mfma_h(v4u a, v4u b, f32x4 c) {
  return __builtin_amdgcn_mfma_f32_16x16x32_f16(__builtin_bit_cast(f16x8, a), __builtin_bit_cast(f16x8, b), c, 0, 0, 0);
}
DI float dot2(unsigned a, unsigned b, float acc) {
  return __builtin_amdgcn_fdot2_f32_bf16(__builtin_bit_cast(bf16x2_t, a), __builtin_bit_cast(bf16x2_t, b), acc, false);
}
constexpr unsigned ONES = 0x3F803F80u;
DI int perm(int u, int c) { return 32 * (u >> 1) + 8 * (c >> 2) + 4 * (u & 1) + (c & 3); }
template <int CTRL> DI float dpp(float x) {
  return __builtin_bit_cast(float, __builtin_amdgcn_mov_dpp(__builtin_bit_cast(int, x), CTRL, 0xf, 0xf, true));
}
DI float row16_sum(float x) {
  x += dpp<0xB1>(x); x += dpp<0x4E>(x); x += dpp<0x141>(x); x += dpp<0x128>(x);
  return x;
}
DI float row16_max(float x) {
  x = fmaxf(x, dpp<0xB1>(x)); x = fmaxf(x, dpp<0x4E>(x)); x = fmaxf(x, dpp<0x141>(x)); x = fmaxf(x, dpp<0x128>(x));
  return x;
}
DI float xrow_sum(float x) {
  auto s = __builtin_amdgcn_permlane16_swap(__float_as_uint(x), __float_as_uint(x), false, false);
  x = __uint_as_float(s[0]) + __uint_as_float(s[1]);
  auto t = __builtin_amdgcn_permlane32_swap(__float_as_uint(x), __float_as_uint(x), false, false);
  return __uint_as_float(t[0]) + __uint_as_float(t[1]);
}
DI float xrow_sum4(float a, float b, float c, float d) {
  auto s = __builtin_amdgcn_permlane16_swap(__float_as_uint(a), __float_as_uint(b), false, false);
  auto t = __builtin_amdgcn_permlane16_swap(__float_as_uint(c), __float_as_uint(d), false, false);
  const float x = __uint_as_float(s[0]) + __uint_as_float(s[1]), y = __uint_as_float(t[0]) + __uint_as_float(t[1]);
  auto z = __builtin_amdgcn_permlane32_swap(__float_as_uint(x), __float_as_uint(y), false, false);
  return __uint_as_float(z[0]) + __uint_as_float(z[1]);
}
DI float xrow_max(float x) {
  auto s = __builtin_amdgcn_permlane16_swap(__float_as_uint(x), __float_as_uint(x), false, false);
  x = fmaxf(__uint_as_float(s[0]), __uint_as_float(s[1]));
  auto t = __builtin_amdgcn_permlane32_swap(__float_as_uint(x), __float_as_uint(x), false, false);
  return fmaxf(__uint_as_float(t[0]), __uint_as_float(t[1]));
}
DI f32x2 gelu2(f32x2 x) {
  const float k0 = -2.0f * 0.7978845608028654f * 1.4426950408889634f, k1 = k0 * 0.044715f;
  const f32x2 a = x * fma2(x * x, bc2(k1), bc2(k0));
  const f32x2 e = f32x2{__builtin_amdgcn_exp2f(a[0]), __builtin_amdgcn_exp2f(a[1])} + bc2(1.0f);
  return x * f32x2{__builtin_amdgcn_rcpf(e[0]), __builtin_amdgcn_rcpf(e[1])};
}
DI rsrc_t mkrs(const void* ptr, unsigned bytes) { return __builtin_amdgcn_make_buffer_rsrc((void*)ptr, 0, (int)bytes, 0x00020000); }
template <int AUX> DI v4u ld128(rsrc_t rs, unsigned v, unsigned s) { return __builtin_amdgcn_raw_buffer_load_b128(rs, (int)v, (int)s, AUX); }
template <int AUX> DI v2u ld64(rsrc_t rs, unsigned v, unsigned s) { return __builtin_amdgcn_raw_buffer_load_b64(rs, (int)v, (int)s, AUX); }
template <int AUX> DI void st128(v4u x, rsrc_t rs, unsigned v, unsigned s) { __builtin_amdgcn_raw_buffer_store_b128(x, rs, (int)(v + s), 0, AUX); }
template <int AUX> DI void st64(v2u x, rsrc_t rs, unsigned v, unsigned s) { __builtin_amdgcn_raw_buffer_store_b64(x, rs, (int)v, (int)s, AUX); }
DI f32x4 asf(v4u x) { return __builtin_bit_cast(f32x4, x); }
DI float sum4(f32x4 x) { return (x[0] + x[1]) + (x[2] + x[3]); }
DI unsigned umax3(unsigned a, unsigned b, unsigned c) { unsigned m = a > b ? a : b; return m > c ? m : c; }
DI unsigned macc(unsigned m, v4u x) { return umax3(umax3(m, x[0], x[1]), x[2], x[3]); }
DI bool any_mark(unsigned m) { return __builtin_amdgcn_ballot_w64(m == 0xFFFFFFFFu) != 0ull; }
#define SB() __builtin_amdgcn_sched_barrier(0)
#define PIN(x) asm volatile("" : "+v"(x))
#define RETRY_BEGIN(m) if (__builtin_expect(any_mark(m), 0)) { unsigned sp_ = 0; do { MEMFENCE(); m = 0;
#define RETRY_END(m) } while (any_mark(m) & (++sp_ <= SPIN_CAP)); }
#define MEMFENCE() asm volatile("" ::: "memory")
DI void bar() { asm volatile("s_waitcnt lgkmcnt(0)" ::: "memory"); __builtin_amdgcn_s_barrier(); asm volatile("" ::: "memory"); }

DI f32x2 cmul(f32x2 a, f32x2 b) { return fma2(bc2(a[1]), f32x2{-b[1], b[0]}, bc2(a[0]) * b); }
DI f32x2 cexp_p(float are2, float rev_hi, float rev_lo, float p) {
  const float mag = __builtin_amdgcn_exp2f(are2 * p);
  const float hi = rev_hi * p;
  const float fr = (hi - __builtin_rintf(hi)) + rev_lo * p;
  return f32x2{mag * __builtin_amdgcn_cosf(fr), mag * __builtin_amdgcn_sinf(fr)};
}
DI float exp_acc(float x) {
  const float hi = x * 1.4426950408889634f;
  const float lo = __builtin_fmaf(x, 1.4426950408889634f, -hi) + x * 1.925963033500011e-8f;
  const float e = __builtin_amdgcn_exp2f(hi);
  return __builtin_fmaf(e, lo * 0.6931471805599453f, e);
}
DI unsigned pkh(float a, float b) {
  f16x2 v = {(_Float16)a, (_Float16)b};
  return __builtin_bit_cast(unsigned, v);
}

constexpr int CV_RK = 4160;
constexpr int CV_H = 2 * CV_RK;
constexpr int CV_CH = CV_H + 4608;
constexpr int LDS_A = 0;
constexpr int LDS_U = 2 * CV_CH;
constexpr int LDS_GS = LDS_U + 8192;
constexpr int LDS_TAB = 53248;
constexpr int KP_CH = 12304;
constexpr int LDS_KP = LDS_TAB + 2 * 64 * 528 + 16;
constexpr int LDS_LN = 0, LDS_MR = 8448;
constexpr int LDS_WI = 145472, LDS_WO = LDS_WI + 10240;
constexpr int LDS_X = 65536;
constexpr int LDS_S = LDS_X + 24576;
constexpr int LDS_W = LDS_S + 6144;
constexpr int MEGA_LDS = LDS_W + 65536;
static_assert(LDS_WO + 4608 <= MEGA_LDS && LDS_KP + 2 * KP_CH <= LDS_WI && LDS_S >= 73728, "lds");
static_assert(LDS_GS + 1280 <= LDS_TAB && LDS_KP + 2 * KP_CH <= MEGA_LDS && MEGA_LDS <= 160 * 1024, "lds");
#define FLAG_AT 110
static_assert(FLAG_AT > 57 && FLAG_AT < 144 && FLAG_AT % 6 != 3, "flag position");
#define WF_EARLY 3
#define WO0 16
#define WSP_P0 4
#define HINT4_KS 15
#define IN_AUX 0
#define WSP_AUX 0

DI v2u trread(const unsigned char* ptr) {
  typedef short v4i16_t __attribute__((ext_vector_type(4)));
  return __builtin_bit_cast(v2u, __builtin_amdgcn_ds_read_tr16_b64_v4i16((LAS v4i16_t*)ptr));
}
struct CvOrder { int qq[36], pp[36]; };
constexpr CvOrder cv_order() {
  CvOrder o{};
  int n = 0;
  for (int m = 0; m < 8; ++m) {
    for (int j = 0; j < m; ++j) if (m + j < 8) { o.qq[n] = m; o.pp[n] = j; ++n; }
    for (int j = 0; j <= m; ++j) if (m + j < 8) { o.qq[n] = j; o.pp[n] = m; ++n; }
  }
  return o;
}
constexpr CvOrder CVO = cv_order();

typedef __attribute__((address_space(1))) unsigned gu32;
DI unsigned tacc(unsigned m, v4u x, unsigned tag) { return m | (x[1] ^ tag) | (x[3] ^ tag); }
DI bool any_nz(unsigned m) { return __builtin_amdgcn_ballot_w64(m != 0u) != 0ull; }
DI void hint_add(unsigned char* ws, int line) { (void)__hip_atomic_fetch_add((gu32*)(ws + WS_CNT + line * 128), 1u, __ATOMIC_RELAXED, __HIP_MEMORY_SCOPE_AGENT); }
DI void hint_wait(rsrc_t rws, unsigned voff, unsigned soff, unsigned want) {
  unsigned sp = 0;
  for (;;) {
    MEMFENCE();
    const unsigned v = __builtin_amdgcn_raw_buffer_load_b32(rws, (int)voff, (int)soff, SC1);
    if (!((__builtin_amdgcn_ballot_w64(v < want) != 0ull) & (++sp <= SPIN_CAP))) break;
    __builtin_amdgcn_s_sleep(2);
  }
  MEMFENCE();
}

extern "C" __global__ void __launch_bounds__(256, 1) adaln_mega(Params p) {
  extern __shared__ __attribute__((aligned(16))) unsigned char smem[];
  const int tid = threadIdx.x, lane = tid & 63, w = __builtin_amdgcn_readfirstlane(tid >> 6);
  const int b = blockIdx.x, xcd = b & 7, idx = b >> 3;
  const int pb = xcd * 32 + idx;
  const unsigned l16 = lane * 16;
  STAMP_DECL;
  STAMP(1, 0);
  const rsrc_t ru = mkrs(p.u, L * D * 2);
  v4u xr[2];
  xr[0] = ld128<IN_AUX>(ru, l16, (pb * 8 + w * 2) * 1024);
  xr[1] = ld128<IN_AUX>(ru, l16 + 1024, (pb * 8 + w * 2) * 1024);
  const int mkg = b & 3, mcg = b >> 2;
  const rsrc_t rmod = mkrs(p.wmod, D * 3072 * 2);
  v4u xm[4];
#pragma unroll
  for (int i = 0; i < 4; ++i) xm[i] = ld128<IN_AUX>(rmod, (tid >> 3) * 6144 + (tid & 7) * 16, (mkg * 128 + i * 32) * 6144 + mcg * 128 + (b < 192 ? 0u : OOB));
  const unsigned cq = p.c[mkg * 128 + (tid & 3) * 32 + (tid >> 3)];
  const unsigned epoch = *(const unsigned*)(p.ws + WS_EPOCH);
  const rsrc_t rws = mkrs(p.ws, WS_END);
  __builtin_amdgcn_raw_buffer_store_b32(0u, rws, tid == 0 ? 0u : OOB, WS_CNT + b * 128, SC1);
  SB();
  const int cch = w >> 1, eh = w & 1, ns = tid & 127;
  const unsigned q_cre = p.cre[pb * 256 + tid], q_cim = p.cim[pb * 256 + tid], q_lar = p.lar[pb * 256 + tid], q_aim = p.aim[pb * 256 + tid];
  const unsigned q_ldt = p.ldt[pb * 2 + cch], q_dsk = p.dsk[pb * 2 + cch];
  v2u codepf;
  {
    unsigned long long pc;
    int rel_m, rel_e;
    asm volatile("s_getpc_b64 %0\n\ts_mov_b32 %1, adaln_mega@rel32@lo+4\n\ts_mov_b32 %2, adaln_code_end@rel32@lo+12" : "=s"(pc), "=s"(rel_m), "=s"(rel_e));
    const rsrc_t rcode = mkrs((const void*)(pc + (long long)rel_m), rel_e > rel_m ? (unsigned)(rel_e - rel_m) & ~15u : 0u);
    codepf = ld64<0>(rcode, tid * 8, idx * 2048);
  }
  SB();
  STAMP(1, 24);
  const int c = lane & 15, q = lane >> 4;
  int pos4[9];
  {
    const float f4 = (float)(8188 - tid * 32);
#pragma unroll
    for (int j = 0; j < 10; j += 2) {
      const unsigned pp = pk2(bc2(f4) - f32x2{4.f * j, 4.f * j + 4.f});
      pos4[j] = (int)bflo(pp);
      PIN(pos4[j]);
      if (j + 1 < 9) { pos4[j + 1] = (int)bfhi(pp); PIN(pos4[j + 1]); }
    }
  }
  int cob[2], cos_[2];
  cob[0] = (eh ? 1024 + 512 * q : 128 * (q >> 1) + 4 * (q & 1)) * 4 + 32 * c + LDS_KP + cch * KP_CH;
  cob[1] = (q < 2 ? 256 + 128 * q : 504 + 4 * q) * 4 + 32 * c + LDS_KP + cch * KP_CH;
  cos_[0] = eh ? 512 : 4;
  cos_[1] = q < 2 ? 8 : 512;
  PIN(cob[0]); PIN(cob[1]); PIN(cos_[1]);
  SB();
  STAMP(1, 26);
  {
    *(v4u*)(smem + LDS_LN + (w * 2) * 1040 + l16) = xr[0];
    *(v4u*)(smem + LDS_LN + (w * 2 + 1) * 1040 + l16) = xr[1];
    float sm[2], sq[2];
#pragma unroll
    for (int i = 0; i < 2; ++i) {
      sm[i] = dot2(xr[i][0], ONES, dot2(xr[i][1], ONES, dot2(xr[i][2], ONES, dot2(xr[i][3], ONES, 0.f))));
      sq[i] = dot2(xr[i][0], xr[i][0], dot2(xr[i][1], xr[i][1], dot2(xr[i][2], xr[i][2], dot2(xr[i][3], xr[i][3], 0.f))));
    }
    const float tv = row16_sum(xrow_sum4(sm[0], sm[1], sq[0], sq[1]));
    auto sw = __builtin_amdgcn_permlane32_swap(__float_as_uint(tv), __float_as_uint(tv), false, false);
    const float m = __uint_as_float(sw[0]) * (1.0f / D);
    const float r = __builtin_amdgcn_rsqf(fmaxf(__uint_as_float(sw[1]) * (1.0f / D) - m * m, 0.f) + EPS);
    ((float*)(smem + LDS_GS + 1088))[(q >> 1) * 8 + w * 2 + (q & 1)] = (q >> 1) ? -m * r : r;
    const float cv = bf2f(cq);
    const float sl = rbf(cv * __builtin_amdgcn_rcpf(1.0f + __builtin_amdgcn_exp2f(cv * -1.4426950408889634f)));
    const float sc[4] = {dpp<0x00>(sl), dpp<0x55>(sl), dpp<0xAA>(sl), dpp<0xFF>(sl)};
    f32x2 a[4];
#pragma unroll
    for (int i = 0; i < 4; ++i)
#pragma unroll
      for (int j = 0; j < 4; ++j) a[j] = i == 0 ? unpk(xm[i][j]) * bc2(sc[i]) : fma2(unpk(xm[i][j]), bc2(sc[i]), a[j]);
    float* red = (float*)(smem + LDS_MR) + (tid >> 3) * 68 + (tid & 7) * 8;
    *(f32x4*)red = f32x4{a[0][0], a[0][1], a[1][0], a[1][1]};
    *(f32x4*)(red + 4) = f32x4{a[2][0], a[2][1], a[3][0], a[3][1]};
  }
  const unsigned tag = epoch * 2654435761u + 0x9E3779B9u;
  STAMP(1, 28);
  asm volatile("s_waitcnt vmcnt(3)" ::: "memory");
  bar();
  STAMP(1, 16);
  {
    unsigned ua[4][2];
#pragma unroll
    for (int i = 0; i < 4; ++i) {
      const unsigned char* src = smem + LDS_LN + (tid & 3) * 2080 + (i * 64 + (tid >> 2)) * 4;
      ua[i][0] = *(const unsigned*)src; ua[i][1] = *(const unsigned*)(src + 1040);
    }
    float ms8[8];
#pragma unroll
    for (int i = 0; i < 8; ++i) ms8[i] = ((const float*)(smem + LDS_MR))[(q * 8 + i) * 68 + w * 16 + c];
    const unsigned lnv = ((const unsigned*)(smem + LDS_GS + 1088))[c];
    SB();
#pragma unroll
    for (int i = 0; i < 4; ++i) st128<SC1>(v4u{ua[i][0], tag, ua[i][1], tag}, rws, (tid >> 2) * 16384 + (tid & 3) * 16, WS_UG + i * 64 * 16384 + pb * 64);
    const float s = xrow_sum(((ms8[0] + ms8[1]) + (ms8[2] + ms8[3])) + ((ms8[4] + ms8[5]) + (ms8[6] + ms8[7])));
    st64<SC1>(v2u{__float_as_uint(s), tag}, rws, q == 0 ? c * 8 : OOB, WS_MODG + (mkg * 3072 + mcg * 64 + w * 16) * 8 + (b < 192 ? 0u : OOB));
    st64<SC1>(v2u{lnv, tag}, rws, tid < 16 ? (c >> 3) * 16384 + (c & 7) * 8 : OOB, WS_SR + pb * 64);
  }
  STAMP(1, 17);
  SB();
  const unsigned wp_oob = b < 176 ? 0u : OOB;
  const int wpr = b >> 2, wkg = b & 3;
  v4u xi[2], xo;
  {
    const rsrc_t rwin = mkrs(p.win, D * FF * 2), rwout = mkrs(p.wout, FH * D * 2);
    const int hf = wpr >= 22, pp = wpr - 22 * hf;
    const unsigned v = ((tid >> 2) * (FF * 2) + (tid & 3) * 16) | ((pp == 21 ? tid & 2 : 0) << 29);
#pragma unroll
    for (int i = 0; i < 2; ++i) xi[i] = ld128<IN_AUX>(rwin, v, (wkg * 128 + i * 64) * (FF * 2) + (hf * FH + pp * 32) * 2 + wp_oob);
    xo = ld128<IN_AUX>(rwout, (tid >> 3) * 1024 + (tid & 7) * 16, idx * 32 * 1024 + xcd * 128);
  }
  SB();
  const int d0 = pb * 2, dk = d0 + cch;
  const int mt = (xcd >> 1) * 8 + (idx & 7), nt = (xcd & 1) * 4 + (idx >> 3);
  const int big = (nt + 2) >> 3;
  const int np = 3 - big, pr0 = 3 * nt - big * (nt - 6);
  const int kh = w & 1, uh = w >> 1;
  const int ub_own = pr0 * 2 + uh * np, ub_oth = pr0 * 2 + (uh ^ 1) * np;
  const unsigned oob3 = (unsigned)big << 30;
  const int kr = 2 * (idx >> 3) + (xcd & 1) + (idx & 7);
  const rsrc_t rwsp = mkrs(p.wsp, L * L * 2);
  const unsigned wp_fr = (wpr * 2 * 16 + wkg * 4 + w) * 1024 + wp_oob;
  v4u wr[8][8];
  const unsigned wsp_v = (lane >> 3) * 4096 + (lane & 7) * 16, wsp_s = mt * 64 * 4096 + w * 1024;
#define LOAD_WSP1(ck, i) wr[ck][i] = ld128<WSP_AUX>(rwsp, wsp_v + (((ck) + kr) & 7) * 128, wsp_s + (i) * 8 * 4096)
#define LOAD_WSPN(n) LOAD_WSP1((n) >> 3, (n) & 7)
  const v4u mk = {~0u, ~0u, ~0u, ~0u};
  {
    const float dt = rbf(exp_acc(bf2f(q_ldt)));
    const float cre = bf2f(q_cre) * dt, cim = bf2f(q_cim) * dt;
    const float are2 = -rbf(exp_acc(bf2f(q_lar))) * dt * 1.4426950408889634f;
    const float aim = bf2f(q_aim) * dt;
    const float rev_hi = aim * 0.15915494309189535f;
    const float rev_lo = __builtin_fmaf(aim, 0.15915494309189535f, -rev_hi) + aim * 6.4206383e-9f;
    int ex = __builtin_amdgcn_frexp_expf(xrow_max(row16_max(fmaxf(fabsf(cre), fabsf(cim))))) + 1;
    ex = ex < -100 ? -100 : ex;
    const float scale = __builtin_amdgcn_ldexpf(1.f, -ex);
    ((float*)(smem + LDS_GS + 1024))[w] = __builtin_amdgcn_ldexpf(2.f, ex);
    const f32x2 c0 = {cre * scale, cim * scale};
    const f32x2 e1 = cexp_p(are2, rev_hi, rev_lo, 1.f), e8 = cexp_p(are2, rev_hi, rev_lo, 8.f), e64 = cexp_p(are2, rev_hi, rev_lo, 64.f);
    unsigned char* tn = smem + LDS_TAB + cch * (64 * 528) + ns * 4;
#define TROW(r, z) *(unsigned*)(tn + (r) * 528) = pkh((z)[0], -(z)[1])
    SB();
    const f32x2 e128 = cmul(e64, e64), e2 = cmul(e1, e1), e256 = cmul(e128, e128), e4 = cmul(e2, e2), e512 = cmul(e256, e256);
    f32x2 f0 = {1.f, 0.f}, f1 = e64;
    f32x2 ca = cmul(c0, e512), cb = cmul(ca, e512);
#pragma unroll
    for (int j = 0; j < 8; ++j) {
      *(unsigned*)(tn + (48 + j) * 528) = pkh(f0[0], f0[1]);
      *(unsigned*)(tn + (56 + j) * 528) = pkh(f1[0], f1[1]);
      TROW(32 + j, cb);
      f0 = j == 0 ? e8 : cmul(f0, e8); f1 = cmul(f1, e8); cb = cmul(cb, e128);
      if (j < 4) {
        const f32x2 z = cmul(ca, e4);
        TROW(24 + j, ca); TROW(28 + j, z);
        ca = cmul(ca, e128);
      }
      if (j == 3 || j == 7) {
        PIN(f0); PIN(f1); PIN(cb);
        if (WSP_P0 >= 4) { LOAD_WSPN(j >> 2 << 1); LOAD_WSPN((j >> 2 << 1) + 1); }
        SB();
      }
    }
    TROW(40, cb);
    f32x2 t1[8];
    t1[1] = e1; t1[2] = e2; t1[4] = e4;
    t1[3] = cmul(e2, e1); t1[5] = cmul(e4, e1); t1[6] = cmul(e2, e4); t1[7] = cmul(t1[6], e1);
    const f32x2 c1 = cmul(c0, e128), c2 = cmul(c0, e256), c3 = cmul(c2, e128);
    TROW(0, c0); TROW(8, c1); TROW(16, c2); TROW(20, c3);
#pragma unroll
    for (int v = 1; v < 8; ++v) {
      const f32x2 z0 = cmul(t1[v], c0), z1 = cmul(t1[v], c1);
      TROW(v, z0); TROW(8 + v, z1);
      if ((v & 1) == 0) {
        const f32x2 z2 = cmul(t1[v], c2), z3 = cmul(t1[v], c3);
        TROW(16 + v / 2, z2); TROW(20 + v / 2, z3);
        if (v < 6 && WSP_P0 >= 8) { LOAD_WSPN(v + 2); LOAD_WSPN(v + 3); }
        SB();
      }
    }
  }
  STAMP(1, 29);
  bar();
  STAMP(1, 18);
  v4u gr[4], gb_[4];
  v2u gm[4], gc[4];
  const unsigned gm_off = ((cch ? 3 * 512 : 4 * 512) + wkg * 128 + ns) * 8;
  const unsigned gc_off = (((lane & 7) > 5 ? 0 : ((lane & 7) >> 1)) * 512 + d0 + (lane & 1)) * 8;
#define LOAD_GS() _Pragma("unroll") for (int i_ = 0; i_ < 4; ++i_) { gr[i_] = ld128<SC1>(rws, tid * 16, WS_SR + i_ * 4096); gb_[i_] = ld128<SC1>(rws, tid * 16, WS_SB + i_ * 4096); }
#define LOAD_GM() _Pragma("unroll") for (int i_ = 0; i_ < 4; ++i_) { gm[i_] = ld64<SC1>(rws, gm_off, WS_MODG + i_ * 24576); gc[i_] = ld64<SC1>(rws, gc_off, WS_MODG + i_ * 24576); }
  {
    const unsigned char* tab = smem + LDS_TAB + cch * (64 * 528) + c * 528 + q * 16;
    v4u fa[2][8], fb[8];
#pragma unroll
    for (int ks = 0; ks < 4; ++ks) {
      fb[ks] = *(const v4u*)(tab + 48 * 528 + ks * 64);
      fa[0][ks] = *(const v4u*)(tab + eh * (32 * 528) + ks * 64);
      fa[1][ks] = *(const v4u*)(tab + 16 * 528 + ks * 64);
    }
    const f32x2 us = *(const f32x2*)(smem + LDS_GS + 1024 + cch * 8);
    SB();
    f32x4 kk[2][2];
#pragma unroll
    for (int i = 0; i < 4; ++i) kk[i >> 1][i & 1] = f32x4{0.f, 0.f, 0.f, 0.f};
#pragma unroll
    for (int ks = 0; ks < 8; ++ks) {
      if (ks < 4) {
        fb[4 + ks] = *(const v4u*)(tab + 48 * 528 + (4 + ks) * 64);
        fa[0][4 + ks] = *(const v4u*)(tab + eh * (32 * 528) + (4 + ks) * 64);
        fa[1][4 + ks] = *(const v4u*)(tab + 16 * 528 + (4 + ks) * 64);
      }
      kk[0][ks >> 2] = mfma_h(fa[0][ks], fb[ks], kk[0][ks >> 2]); kk[1][ks >> 2] = mfma_h(fa[1][ks], fb[ks], kk[1][ks >> 2]);
      SB();
    }
    STAMP(1, 37);
    *(v4u*)(smem + (lane == 63 ? LDS_KP - 16 + cch * KP_CH : cch * CV_CH + (lane < 32 ? CV_H + l16 : ((lane >> 1) & 1) * CV_RK + 4096 + (lane & 1) * 16))) = v4u{0u, 0u, 0u, 0u};
#pragma unroll
    for (int i = 0; i < 2; ++i) *(v4u*)(smem + LDS_WI + (i * 64 + (tid >> 2)) * 80 + (tid & 3) * 16) = xi[i];
    *(v4u*)(smem + LDS_WO + (tid >> 3) * 144 + (tid & 7) * 16) = xo;
    f32x4 k0 = kk[0][0] * us[0] + kk[0][1] * us[1];
    const f32x4 k1 = kk[1][0] * us[0] + kk[1][1] * us[1];
    k0[0] += ns == 0 ? bf2f(q_dsk) : 0.f;
#pragma unroll
    for (int r = 0; r < 4; ++r) { *(float*)(smem + cob[0] + r * cos_[0]) = k0[r]; *(float*)(smem + cob[1] + r * cos_[1]) = k1[r]; }
  }
  v4u ug[4];
#define LOAD_UG() _Pragma("unroll") for (int i_ = 0; i_ < 4; ++i_) ug[i_] = ld128<SC1>(rws, tid * 16, WS_UG + pb * 16384 + i_ * 4096)
  STAMP(1, 31);
  LOAD_GM();
  LOAD_UG();
  bar();
  STAMP(1, 19);
  v4u wt[2];
  {
    float v[2][9];
#pragma unroll
    for (int j = 0; j < 9; ++j) v[0][j] = *(const float*)(smem + LDS_KP + pos4[j]);
    LOAD_GS();
#pragma unroll
    for (int j = 0; j < 9; ++j) v[1][j] = *(const float*)(smem + LDS_KP + KP_CH + pos4[j]);
    SB();
    STAMP(1, 36);
#pragma unroll
    for (int ch = 0; ch < 2; ++ch) {
      unsigned char* rk = smem + ch * CV_CH + tid * 16;
#pragma unroll
      for (int sft = 0; sft < 2; ++sft)
        *(v4u*)(rk + sft * CV_RK) = v4u{pk2(v[ch][sft], v[ch][sft + 1]), pk2(v[ch][sft + 2], v[ch][sft + 3]), pk2(v[ch][sft + 4], v[ch][sft + 5]), pk2(v[ch][sft + 6], v[ch][sft + 7])};
    }
    const unsigned char* a0 = smem + LDS_WI + (w * 32 + 8 * q + ((lane >> 2) & 3)) * 80 + (lane & 3) * 16;
#pragma unroll
    for (int e = 0; e < 2; ++e) {
      const v2u lo = trread(a0 + e * 8), hi = trread(a0 + e * 8 + 320);
      wt[e] = v4u{lo[0], lo[1], hi[0], hi[1]};
    }
    const unsigned char* o0 = smem + LDS_WO + (8 * q + ((lane >> 2) & 3)) * 144 + (w >> 1) * 64 + (lane & 3) * 16 + (w & 1) * 8;
    const v2u olo = trread(o0), ohi = trread(o0 + 576);
    st128<SC1>(v4u{olo[0], olo[1], ohi[0], ohi[1]}, rws, l16, WS_WOT + ((xcd * 4 + w) * 22 + idx) * 1024 + wp_oob);
    SB();
    STAMP(1, 32);
    unsigned m = 0;
#pragma unroll
    for (int i = 0; i < 4; ++i) m = m | (gm[i][1] ^ tag) | (gc[i][1] ^ tag);
    if (__builtin_expect(any_nz(m), 0)) {
      unsigned sp = 0;
      do {
        MEMFENCE(); LOAD_GM(); m = 0;
#pragma unroll
        for (int i = 0; i < 4; ++i) m = m | (gm[i][1] ^ tag) | (gc[i][1] ^ tag);
      } while (any_nz(m) & (++sp <= SPIN_CAP));
    }
    const float mv = rbf((__uint_as_float(gm[0][0]) + __uint_as_float(gm[1][0])) + (__uint_as_float(gm[2][0]) + __uint_as_float(gm[3][0])));
    ((float*)(smem + LDS_GS))[tid] = cch ? mv : rbf(1.0f + mv);
    const float cv = rbf((__uint_as_float(gc[0][0]) + __uint_as_float(gc[1][0])) + (__uint_as_float(gc[2][0]) + __uint_as_float(gc[3][0])));
    const float gav = dpp<0x104>(cv), gav2 = dpp<0x102>(cv);
    const float hbv = cv * gav, hsv = rbf(1.0f + cv) * gav2;
    const f32x2 hb = {__builtin_bit_cast(float, __builtin_amdgcn_readlane(__builtin_bit_cast(int, hbv), 0)), __builtin_bit_cast(float, __builtin_amdgcn_readlane(__builtin_bit_cast(int, hbv), 1))};
    const f32x2 hs = {__builtin_bit_cast(float, __builtin_amdgcn_readlane(__builtin_bit_cast(int, hsv), 2)), __builtin_bit_cast(float, __builtin_amdgcn_readlane(__builtin_bit_cast(int, hsv), 3))};
    SB();
    STAMP(1, 33);
    m = 0;
#pragma unroll
    for (int i = 0; i < 4; ++i) { m = tacc(m, ug[i], tag); m = tacc(m, gr[i], tag); m = tacc(m, gb_[i], tag); }
    if (__builtin_expect(any_nz(m), 0)) {
      unsigned sp = 0;
      do {
        MEMFENCE(); LOAD_UG(); LOAD_GS(); m = 0;
#pragma unroll
        for (int i = 0; i < 4; ++i) { m = tacc(m, ug[i], tag); m = tacc(m, gr[i], tag); m = tacc(m, gb_[i], tag); }
      } while (any_nz(m) & (++sp <= SPIN_CAP));
    }
#pragma unroll
    for (int i = WSP_P0; i < WSP_P0 + 4; ++i) LOAD_WSPN(i);
    STAMP(1, 38);
#pragma unroll
    for (int i = 0; i < 4; ++i) {
      const f32x2 h0 = fma2(fma2(unpk(ug[i][0]), bc2(__uint_as_float(gr[i][0])), bc2(__uint_as_float(gb_[i][0]))), hs, hb);
      const f32x2 h1 = fma2(fma2(unpk(ug[i][2]), bc2(__uint_as_float(gr[i][2])), bc2(__uint_as_float(gb_[i][2]))), hs, hb);
      *(unsigned*)(smem + CV_H + 512 + i * 1024 + tid * 4) = pk2(h0[0], h1[0]);
      *(unsigned*)(smem + CV_CH + CV_H + 512 + i * 1024 + tid * 4) = pk2(h0[1], h1[1]);
      *(unsigned*)(smem + LDS_U + i * 1024 + tid * 4) = __builtin_amdgcn_perm(ug[i][2], ug[i][0], 0x05040100u);
      *(unsigned*)(smem + LDS_U + 4096 + i * 1024 + tid * 4) = __builtin_amdgcn_perm(ug[i][2], ug[i][0], 0x07060302u);
    }
  }
  asm volatile("" ::"v"(codepf));
  const unsigned char* abase = smem + cch * CV_CH + (1 - (c & 1)) * CV_RK + 2 * (2030 - 2 * (c >> 1) + 8 * q) - eh * 256 - 192 - 3584;
  const unsigned char* bbase = smem + cch * CV_CH + CV_H + 2 * (256 - 16 + 16 * c + 8 * q) - eh * 256 - 192;
  STAMP(1, 39);
  bar();
  STAMP(1, 1);
#define CV_A(ee, k) ({ const unsigned* p_ = (const unsigned*)(abase + 192 - 64 * (ee) + 3584 - 512 * (k)); v4u{p_[0], p_[1], p_[2], p_[3]}; })
#define CV_B(ee, k) (*(const v4u*)(bbase + 192 - 64 * (ee) + 512 * (k)))
  v4u cA[2][8], cB[2][8];
  f32x4 gs[4];
#pragma unroll
  for (int k = 0; k < 2; ++k) { cA[0][k] = CV_A(0, k); cB[0][k] = CV_B(0, k); }
  {
    const float* gp = (const float*)(smem + LDS_GS) + w * 32 + q * 8;
    gs[0] = *(const f32x4*)gp; gs[1] = *(const f32x4*)(gp + 4); gs[2] = *(const f32x4*)(gp + 128); gs[3] = *(const f32x4*)(gp + 132);
  }
  SB();
  f32x4 cacc[8];
  v2u ue[4];
  {
    f32x2 cs[2], bs[2];
    v4u wo_;
    f32x2 cp[4];
#pragma unroll
    for (int t = 0; t < 8; ++t) cacc[t] = f32x4{0.f, 0.f, 0.f, 0.f};
    sfor<0, 144>([&](auto G) __attribute__((always_inline)) {
      constexpr int g = decltype(G)::value, ee = g / 36, j = g % 36;
      if constexpr (ee == 0 && j < 6) { cA[0][2 + j] = CV_A(0, 2 + j); cB[0][2 + j] = CV_B(0, 2 + j); }
      cacc[CVO.pp[j] + CVO.qq[j]] = mfma(cA[ee & 1][CVO.qq[j]], cB[ee & 1][CVO.pp[j]], cacc[CVO.pp[j] + CVO.qq[j]]);
      constexpr int jn = ee == 0 ? j - 20 : (j & 1) ? j >> 1 : -1;
      if constexpr (ee < 3 && jn >= 0 && jn < 16) {
        if constexpr (jn & 1) cB[(ee + 1) & 1][jn >> 1] = CV_B(ee + 1, jn >> 1);
        else cA[(ee + 1) & 1][jn >> 1] = CV_A(ee + 1, jn >> 1);
      }
      if constexpr (ee == 3 && j < 4) ue[j] = *(const v2u*)(smem + LDS_U + cch * 4096 + (eh * 4 + j) * 512 + (c * 16 + q * 4) * 2);
      if constexpr (g >= 4 && g < 36 && (g & 3) == 0) {
        constexpr int i = (g - 4) >> 4, jj = ((g - 4) >> 2) & 3;
        const f32x2 x = unpk(wt[i][jj]);
        const f32x2 g2 = (jj & 1) ? hi2(gs[jj >> 1]) : lo2(gs[jj >> 1]);
        const f32x2 s2 = (jj & 1) ? hi2(gs[2 + (jj >> 1)]) : lo2(gs[2 + (jj >> 1)]);
        wo_[jj] = pk2(x * g2);
        if constexpr (jj == 0) { cs[i] = unpk(wo_[jj]); bs[i] = x * s2; }
        else { cs[i] += unpk(wo_[jj]); bs[i] = fma2(x, s2, bs[i]); }
        PIN(cs[i]); PIN(bs[i]);
        if constexpr (jj == 3) st128<SC1>(wo_, rws, l16 + i * 16384, WS_WP + wp_fr);
      }
      if constexpr (g == 38) {
        const float t2 = xrow_sum4(cs[0][0] + cs[0][1], bs[0][0] + bs[0][1], cs[1][0] + cs[1][1], bs[1][0] + bs[1][1]);
        ((float*)(smem + LDS_S + 4096))[(w * 16 + c) * 4 + q] = t2;
      }
      if constexpr (g == 46) bar();
      if constexpr (g == 47) {
#pragma unroll
        for (int k = 0; k < 4; ++k) cp[k] = *(const f32x2*)(smem + LDS_S + 4096 + (k * 16 + c) * 16 + (q & 1) * 8);
      }
      if constexpr (g == 54) {
        const f32x2 sx = (cp[0] + cp[1]) + (cp[2] + cp[3]);
        st64<SC1>(v2u{__float_as_uint(sx[0]), __float_as_uint(sx[1])}, rws, tid < 32 ? tid * 32 : OOB, WS_CSB + wpr * 1024 + wkg * 8 + wp_oob);
      }
      if constexpr (g == 1 || g == 5) st128<SC1>(mk, rws, l16 + (g == 5) * 1024, WS_X1T + dk * 4096 + eh * 2048);
      if constexpr (g == 9) __builtin_amdgcn_raw_buffer_store_b32(~0u, rws, lane * 4, WS_ST2 + ((mt * 2 + kh) * 16 + nt * 2 + uh) * 256, SC1);
      if constexpr (g == 42) { if (b == 0 && tid == 0) *(unsigned*)(p.ws + WS_EPOCH) = tag; }
      if constexpr (g == 36) STAMP(1, 40);
      if constexpr (g == 72) STAMP(1, 41);
      if constexpr (g == FLAG_AT) {
        STAMP(1, 42);
        asm volatile("s_waitcnt vmcnt(%0)" ::"i"((FLAG_AT - 57 + 5) / 6) : "memory");
        __builtin_amdgcn_s_barrier();
        if (tid < 8) hint_add(p.ws, tid * 8 + (idx & 7));
        STAMP(1, 43);
      }
      if constexpr (g % 6 == 3) LOAD_WSPN(WSP_P0 + 4 + g / 6);
      SB();
    });
  }
  STAMP(1, 2);
  {
    f32x4* red = (f32x4*)(smem + LDS_X) + w * 256 + lane;
    f32x4 keep[4];
    if (eh) {
#pragma unroll
      for (int t = 0; t < 4; ++t) { red[t * 64] = cacc[t]; keep[t] = cacc[4 + t]; PIN(keep[t]); }
    } else {
#pragma unroll
      for (int t = 0; t < 4; ++t) { red[t * 64] = cacc[4 + t]; keep[t] = cacc[t]; PIN(keep[t]); }
    }
    f32x2 pre[4][2];
#pragma unroll
    for (int t = 0; t < 4; ++t) {
      pre[t][0] = unpk(ue[t][0]) + lo2(keep[t]);
      pre[t][1] = unpk(ue[t][1]) + hi2(keep[t]);
      PIN(pre[t][0]); PIN(pre[t][1]);
    }
    const unsigned xoff = ((c >> 1) * 32 + ((q & 1) * 4 + 2 * (c & 1) + (q >> 1)) * 4) * 2;
    bar();
    const f32x4* rd = (const f32x4*)(smem + LDS_X) + (w ^ 1) * 256 + lane;
    f32x4 oth[4];
#pragma unroll
    for (int t = 0; t < 4; ++t) oth[t] = rd[t * 64];
#pragma unroll
    for (int t = 0; t < 4; ++t) {
      v2u o;
      o[0] = nomark(pk2(pre[t][0] + lo2(oth[t])));
      o[1] = nomark(pk2(pre[t][1] + hi2(oth[t])));
      st64<SC1>(o, rws, xoff + t * 512, WS_X1T + (d0 + cch) * 4096 + eh * 2048);
    }
  }
  STAMP(1, 3);
  SB();
  v4u wf[3][8];
  const unsigned rot = uh * 4;
#define LOAD_WP(half, k, i) ld128<SC1>(rws, l16 + ((i) & 3) * 1024, WS_WP + (((half) * 44 + ub_own + (k)) * 16 + kh * 8 + (((i) & 4) ^ rot)) * 1024 + ((k) == 2 ? oob3 : 0u))
#define LOAD_XS(i) ld128<SC1>(rws, wsp_v, WS_X1T + ((i) * 32 + w * 8) * 4096 + mt * 128)
  unsigned char* wslot = smem + LDS_W + w * 16384;
  const int wr_off = (lane >> 3) * 128 + (((lane & 7) ^ ((lane >> 4) & 1)) * 16);
#define PARK1(ck, slot, i) *(v4u*)((slot) + (i) * 1024 + (wr_off ^ (((i) & 3) * 32))) = wr[ck][i]
#define PARK_WSP(ck, slot) _Pragma("unroll") for (int i_ = 0; i_ < 8; ++i_) PARK1(ck, slot, i_)
  PARK_WSP(0, wslot);
  PARK_WSP(1, wslot + 8192);
  v4u xs[16];
  const unsigned char* tb[2][4];
  unsigned char* xdst;
  {
    const int row = 8 * q + ((lane >> 2) & 3);
    const int sg = ((row >> 1) & 1) | (((row >> 3) & 1) << 1);
#pragma unroll
    for (int tu = 0; tu < 4; ++tu) {
      const unsigned char* t0 = smem + LDS_A + kh * 32768 + row * 128 + ((tu ^ (2 * kh) ^ sg) * 32) + (lane & 3) * 8;
      tb[0][tu] = t0 + uh * 16384;
      tb[1][tu] = t0 + (uh ^ 1) * 16384;
    }
    const int rl = w * 8 + (lane >> 3), chn = lane & 7;
    const int sg2 = ((rl >> 1) & 1) | (((rl >> 3) & 1) << 1);
    xdst = smem + LDS_A + rl * 128 + (((chn >> 1) ^ sg2) * 32) + (chn & 1) * 16;
  }
  SB();
  f32x4 cz[2][3][2];
  {
    STAMP(1, 20);
    hint_wait(rws, (lane & 7) * 128, WS_CNT + xcd * 1024, 32u);
    STAMP(1, 21);
#pragma unroll
    for (int i = 0; i < WF_EARLY; ++i)
#pragma unroll
      for (int k = 0; k < 3; ++k) wf[k][i] = LOAD_WP(1, k, i);
#pragma unroll
    for (int i = 0; i < 16; ++i) xs[i] = LOAD_XS(i);
    SB();
    STAMP(1, 44);
    unsigned m = 0;
#pragma unroll
    for (int i = 0; i < 16; ++i) {
      m = macc(m, xs[i]); *(v4u*)(xdst + i * 4096) = xs[i];
      if (WF_EARLY * 3 + i < 24) wf[(WF_EARLY * 3 + i) % 3][(WF_EARLY * 3 + i) / 3] = LOAD_WP(1, (WF_EARLY * 3 + i) % 3, (WF_EARLY * 3 + i) / 3);
      SB();
    }
    PIN(m);
    STAMP(1, 45);
    RETRY_BEGIN(m)
#pragma unroll
      for (int i = 0; i < 16; ++i) xs[i] = LOAD_XS(i);
      SB();
#pragma unroll
      for (int i = 0; i < 16; ++i) { m = macc(m, xs[i]); *(v4u*)(xdst + i * 4096) = xs[i]; }
    RETRY_END(m)
  }
  STAMP(1, 4);
  bar();
  STAMP(1, 5);
#define AHALF(tu, i, h) trread(tb[(i) >> 2][tu] + ((i) & 3) * 4096 + (h) * 512)
  f32x4 acc[3][4], s1[4], s2[4];
#pragma unroll
  for (int tu = 0; tu < 4; ++tu) {
    s1[tu] = f32x4{0.f, 0.f, 0.f, 0.f}; s2[tu] = s1[tu];
#pragma unroll
    for (int k = 0; k < 3; ++k) acc[k][tu] = s1[tu];
  }
  v4u wg[3][8];
  v2u ah[2][4][2];
#pragma unroll
  for (int tu = 0; tu < 4; ++tu) { ah[0][tu][0] = AHALF(tu, 0, 0); ah[0][tu][1] = AHALF(tu, 0, 1); }
  float* st = (float*)(smem + LDS_S);
  const v4u ones4 = {ONES, ONES, ONES, ONES};
  f32x2 csb[2][3];
  SB();
#pragma unroll
  for (int i = 0; i < 8; ++i) {
#pragma unroll
    for (int j = 0; j < 12; ++j) {
      const int tu = j / 3, k = j % 3;
      const v4u a = {ah[i & 1][tu][0][0], ah[i & 1][tu][0][1], ah[i & 1][tu][1][0], ah[i & 1][tu][1][1]};
      acc[k][tu] = mfma(a, wf[k][i], acc[k][tu]);
      if (j < 8) ah[(i + 1) & 1][j >> 1][j & 1] = AHALF(j >> 1, (i + 1) & 7, j & 1);
      if (i < 4 && k == 0) s1[tu] = mfma(ones4, a, s1[tu]);
      if (i < 4 && k == 1) s2[tu] = mfma(a, a, s2[tu]);
      if ((i == 5 || i == 6) && (j == 8 || j == 10)) {
        const int t0 = (i - 5) * 2 + ((j - 8) >> 1);
        const float dg = (c & 2) ? ((c & 1) ? s2[t0][3] : s2[t0][2]) : ((c & 1) ? s2[t0][1] : s2[t0][0]);
        float* dst = st + ((c >> 2) == q ? w * 128 + (t0 ^ (2 * kh)) * 16 + c : 1280);
        dst[0] = s1[t0][0]; dst[64] = dg;
      }
      if (i < 2 && (j & 1)) {
        const int kc = (j >> 1) % 3, ic = (j >> 1) / 3;
        cz[1 - i][kc][ic] = asf(ld128<SC1>(rws, c * 32 + ic * 16, WS_CSB + ((1 - i) * 44 + ub_own + kc) * 512 + (kc == 2 ? oob3 : 0u)));
      }
      if ((i == 2 || i == 3) && (j & 3) == 2) LOAD_WSPN(WSP_P0 + 28 + (i - 2) * 3 + (j >> 2));
      if (i == 4 && j >= 6) {
        const int hf = (j - 6) / 3;
        const f32x4 t4 = cz[hf][k][0] + cz[hf][k][1];
        csb[hf][k] = lo2(t4) + hi2(t4);
        PIN(csb[hf][k]);
      }
      if (i == 5 && (j & 3) == 1) st128<SC1>(mk, rws, l16, WS_Z2 + ((ub_own + (j >> 2)) * 64 + mt * 2 + kh) * 1024 + ((j >> 2) == 2 ? oob3 : 0u));
      if (i >= 6 && (j & 3) == 3) wg[j >> 2][i - 6] = LOAD_WP(0, j >> 2, i - 6);
      if (i == 0 && j == 11) STAMP(1, 46);
      if (i == 3 && j == 11) STAMP(1, 47);
      SB();
    }
  }
  STAMP(1, 6);
  f32x4* xch = (f32x4*)(smem + LDS_X);
#pragma unroll
  for (int k = 0; k < 3; ++k)
#pragma unroll
    for (int t = 0; t < 2; ++t) xch[(w * 6 + k * 2 + t) * 64 + lane] = acc[k][2 + t];
#pragma unroll
  for (int k = 0; k < 3; ++k) { PIN(acc[k][0]); PIN(acc[k][1]); }
  STAMP(1, 48);
  bar();
  STAMP(1, 49);
  float* fin = st + 512 + w * 64;
  f32x4 z2v[3][2];
  {
    const int row = kh * 32 + (lane & 31);
    float pa[4], pb[4];
#pragma unroll
    for (int k = 0; k < 4; ++k) { pa[k] = st[k * 128 + row]; pb[k] = st[k * 128 + 64 + row]; }
    f32x4 oth[3][2];
#pragma unroll
    for (int k = 0; k < 3; ++k)
#pragma unroll
      for (int t = 0; t < 2; ++t) oth[k][t] = xch[((w ^ 1) * 6 + k * 2 + t) * 64 + lane];
    SB();
    const float m = ((pa[0] + pa[1]) + (pa[2] + pa[3])) * (1.0f / D);
    const float r = __builtin_amdgcn_rsqf(fmaxf(((pb[0] + pb[1]) + (pb[2] + pb[3])) * (1.0f / D) - m * m, 0.f) + EPS);
    fin[lane] = lane < 32 ? r : -r * m;
    f32x4 r4[2], m4[2];
#pragma unroll
    for (int t = 0; t < 2; ++t) { r4[t] = *(const f32x4*)(fin + t * 16 + q * 4); m4[t] = *(const f32x4*)(fin + 32 + t * 16 + q * 4); }
    SB();
#pragma unroll
    for (int k = 0; k < 3; ++k)
#pragma unroll
      for (int t = 0; t < 2; ++t) { oth[k][t] += acc[k][t]; PIN(oth[k][t]); }
    SB();
    f32x4 rs[2], rq[2];
#pragma unroll
    for (int t = 0; t < 2; ++t) {
#pragma unroll
      for (int k = 0; k < 3; ++k) {
        const f32x4 a = oth[k][t];
        const f32x2 zl = gelu2(fma2(lo2(a), lo2(r4[t]), fma2(lo2(m4[t]), bc2(csb[1][k][0]), bc2(csb[1][k][1]))));
        const f32x2 zh = gelu2(fma2(hi2(a), hi2(r4[t]), fma2(hi2(m4[t]), bc2(csb[1][k][0]), bc2(csb[1][k][1]))));
        z2v[k][t] = f32x4{zl[0], zl[1], zh[0], zh[1]};
        if (k == 0) { rs[t] = z2v[k][t]; rq[t] = z2v[k][t] * z2v[k][t]; }
        else { rs[t] += z2v[k][t]; rq[t] += z2v[k][t] * z2v[k][t]; }
        PIN(rs[t]); PIN(rq[t]);
#pragma unroll
        for (int kk = 0; kk < 3; ++kk) wg[kk][2 + t * 3 + k] = LOAD_WP(0, kk, 2 + t * 3 + k);
        SB();
      }
    }
    STAMP(1, 50);
    float v8[8], v4[4], v2[2];
    const bool b3 = c & 8, b2 = c & 4, b1 = c & 2, b0 = c & 1;
#pragma unroll
    for (int r_ = 0; r_ < 4; ++r_) {
      const float a0 = rs[0][r_] + dpp<0x128>(rs[0][r_]), a1 = rs[1][r_] + dpp<0x128>(rs[1][r_]);
      const float q0 = rq[0][r_] + dpp<0x128>(rq[0][r_]), q1 = rq[1][r_] + dpp<0x128>(rq[1][r_]);
      v8[2 * r_] = b3 ? a1 : a0; v8[2 * r_ + 1] = b3 ? q1 : q0;
    }
#pragma unroll
    for (int j = 0; j < 4; ++j) {
      const float x0 = v8[j] + dpp<0x141>(v8[j]), x1 = v8[4 + j] + dpp<0x141>(v8[4 + j]);
      v4[j] = b2 ? x1 : x0;
    }
#pragma unroll
    for (int j = 0; j < 2; ++j) {
      const float x0 = v4[j] + dpp<0x4E>(v4[j]), x1 = v4[2 + j] + dpp<0x4E>(v4[2 + j]);
      v2[j] = b1 ? x1 : x0;
    }
    const float x0 = v2[0] + dpp<0xB1>(v2[0]), x1 = v2[1] + dpp<0xB1>(v2[1]);
    const float outv = b0 ? x1 : x0;
    __builtin_amdgcn_raw_buffer_store_b32(nomark(__float_as_uint(outv)), rws, lane * 4, WS_ST2 + ((mt * 2 + kh) * 16 + nt * 2 + uh) * 256, SC1);
  }
  STAMP(1, 7);
  SB();
#pragma unroll
  for (int tu = 0; tu < 4; ++tu)
#pragma unroll
    for (int k = 0; k < 3; ++k) acc[k][tu] = f32x4{0.f, 0.f, 0.f, 0.f};
  const int th = xcd & 1, dq = idx >> 3;
  v2u x1h[2][2];
  int x1o;
  {
    const int row = 8 * q + ((lane >> 2) & 3);
    const int sg = ((row >> 1) & 1) | (((row >> 3) & 1) << 1);
    x1o = LDS_A + (dq * 4 + w) * 4096 + row * 128 + (((2 * th) ^ sg) * 32) + (lane & 3) * 8;
  }
  f32x4 pt[4];
#define LOAD_PT(i) asf(ld128<SC1>(rws, l16 + (i) * 1024, WS_ST2 + (mt * 2 + kh) * 4096))
  SB();
#pragma unroll
  for (int i = 0; i < 8; ++i) {
#pragma unroll
    for (int j = 0; j < 12; ++j) {
      const int tu = j / 3, k = j % 3;
      const v4u a = {ah[i & 1][tu][0][0], ah[i & 1][tu][0][1], ah[i & 1][tu][1][0], ah[i & 1][tu][1][1]};
      acc[k][tu] = mfma(wg[k][i], a, acc[k][tu]);
      if (j < 8 && i < 7) ah[(i + 1) & 1][j >> 1][j & 1] = AHALF(j >> 1, i + 1, j & 1);
      if (j < 4 && i == 7) x1h[j >> 1][j & 1] = trread(smem + (x1o ^ ((j >> 1) * 32)) + (j & 1) * 512);
      if (i < 6 && (j == 1 || j == 5 || j == 9)) LOAD_WSPN(WSP_P0 + 34 + i * 3 + (j >> 2));
      if (j >= 8 && i == 6) pt[j - 8] = LOAD_PT(j - 8);
      SB();
    }
  }
  STAMP(1, 8);
  {
    unsigned m = 0;
#pragma unroll
    for (int i = 0; i < 4; ++i) m = macc(m, __builtin_bit_cast(v4u, pt[i]));
    RETRY_BEGIN(m)
#pragma unroll
      for (int i = 0; i < 4; ++i) { pt[i] = LOAD_PT(i); m = macc(m, __builtin_bit_cast(v4u, pt[i])); }
    RETRY_END(m)
    bar();
    if (tid < 4) hint_add(p.ws, 64 + (nt * 4 + (mt >> 3)) * 4 + tid);
    const f32x4 sx = (pt[0] + pt[1]) + (pt[2] + pt[3]);
    const float tv = xrow_sum4(sx[0], sx[2], sx[1], sx[3]);
    auto sw = __builtin_amdgcn_permlane32_swap(__float_as_uint(tv), __float_as_uint(tv), false, false);
    const float mm = __uint_as_float(sw[0]) * (1.0f / FH);
    const float r = __builtin_amdgcn_rsqf(fmaxf(__uint_as_float(sw[1]) * (1.0f / FH) - mm * mm, 0.f) + EPS);
    float* f2 = st + 768 + w * 64;
    f2[(q >> 1) * 32 + 2 * c + (q & 1)] = (q >> 1) ? -r * mm : r;
    f32x4 r4[2], m4[2];
#pragma unroll
    for (int t = 0; t < 2; ++t) { r4[t] = *(const f32x4*)(f2 + q * 8 + t * 4); m4[t] = *(const f32x4*)(f2 + 32 + q * 8 + t * 4); }
#pragma unroll
    for (int k = 0; k < 3; ++k) {
      v4u o;
#pragma unroll
      for (int t = 0; t < 2; ++t) {
        o[2 * t] = nomark(pk2(fma2(lo2(z2v[k][t]), lo2(r4[t]), lo2(m4[t]))));
        o[2 * t + 1] = nomark(pk2(fma2(hi2(z2v[k][t]), hi2(r4[t]), hi2(m4[t]))));
      }
      st128<SC1>(o, rws, l16, WS_Z2 + ((ub_own + k) * 64 + mt * 2 + kh) * 1024 + (k == 2 ? oob3 : 0u));
    }
  }
  STAMP(1, 9);
  SB();
  unsigned z1p[3][2][2];
  unsigned char* wslot2 = smem + LDS_A + w * 18432;
  v2u gq[4];
  unsigned bspq[2];
  v4u wo[2][22];
#define LOAD_WO(f) wo[(f) & 1][(f) >> 1] = ld128<SC1>(rws, l16 + (((f) >> 1) & 3) * 1024, WS_WOT + (((dq * 4 + w) * 2 + ((f) & 1)) * 22 + (((f) >> 1) & ~3)) * 1024)
  f32x4 z1a[3][2];
  {
#pragma unroll
    for (int i = WSP_P0 + 52; i < 64; ++i) LOAD_WSPN(i);
#pragma unroll
    for (int i = 0; i < 4; ++i) gq[i] = ld64<SC1>(rws, (lane & 31) * 8, WS_MODG + (i * 3072 + 5 * 512 + (dq * 4 + w) * 32) * 8);
#pragma unroll
    for (int t = 0; t < 2; ++t) bspq[t] = p.bsp[mt * 64 + perm(2 * kh + t, c)];
    float* c1s = st + 1024 + w * 96;
#pragma unroll
    for (int k = 0; k < 3; ++k) c1s[k * 32 + (q & 1) * 16 + c] = (q & 1) ? csb[0][k][1] : csb[0][k][0];
#pragma unroll
    for (int k = 0; k < 3; ++k)
#pragma unroll
      for (int t = 0; t < 2; ++t) xch[(w * 6 + k * 2 + t) * 64 + lane] = acc[k][2 + t];
    SB();
    bar();
    STAMP(1, 34);
    f32x4 oth[3][2], cs1[3], bs1[3];
    float r1[2], m1[2];
#pragma unroll
    for (int t = 0; t < 2; ++t) { r1[t] = fin[t * 16 + c]; m1[t] = fin[32 + t * 16 + c]; }
#pragma unroll
    for (int k = 0; k < 3; ++k) {
      cs1[k] = *(const f32x4*)(c1s + k * 32 + q * 4); bs1[k] = *(const f32x4*)(c1s + k * 32 + 16 + q * 4);
#pragma unroll
      for (int t = 0; t < 2; ++t) oth[k][t] = xch[((w ^ 1) * 6 + k * 2 + t) * 64 + lane];
    }
    SB();
    PARK_WSP(2, wslot2);
#pragma unroll
    for (int k = 0; k < 3; ++k)
#pragma unroll
      for (int t = 0; t < 2; ++t) {
        const f32x4 a = acc[k][t] + oth[k][t];
        const f32x2 lo = fma2(lo2(a), bc2(r1[t]), fma2(lo2(cs1[k]), bc2(m1[t]), lo2(bs1[k])));
        const f32x2 hi = fma2(hi2(a), bc2(r1[t]), fma2(hi2(cs1[k]), bc2(m1[t]), hi2(bs1[k])));
        z1a[k][t] = f32x4{lo[0], lo[1], hi[0], hi[1]};
        PIN(z1a[k][t]);
      }
  }
  STAMP(1, 10);
  SB();
  f32x4 ac2[6][4];
#pragma unroll
  for (int i = 0; i < 6; ++i)
#pragma unroll
    for (int j = 0; j < 4; ++j) { const float b0 = (i < 3 && j < 2) ? bf2f(bspq[j & 1]) : 0.f; ac2[i][j] = f32x4{b0, b0, b0, b0}; }
  int rd_off[4];
#pragma unroll
  for (int tu = 0; tu < 4; ++tu) rd_off[tu] = perm(tu ^ (2 * kh), c) * 128 + ((q ^ (c >> 1)) * 16);
  constexpr int ZD = 3;
  v4u zf[ZD][6];
  v4u wfr[2][2][4];
#define LOAD_Z(ks, i) ld128<SC1>(rws, l16 + ((((ks) >> 1) + kr) & 7) * 2048 + ((ks) & 1) * 1024, WS_Z2 + ((((i) < 3 ? ub_own : ub_oth) + (i) % 3) * 64 + w * 16) * 1024 + ((i) % 3 == 2 ? oob3 : 0u))
#pragma unroll
  for (int k2 = 0; k2 < 2; ++k2)
#pragma unroll
    for (int tu = 0; tu < 4; ++tu) wfr[0][k2][tu] = *(const v4u*)(wslot + (rd_off[tu] ^ (k2 * 64)));
  PARK_WSP(3, wslot);
#define WSLOT(ck) ((ck) % 3 == 2 ? wslot2 : wslot + ((ck) % 3) * 8192)
  hint_wait(rws, 0u, WS_CNT + (64 + (nt * 4 + w) * 4 + (xcd >> 1)) * 128, 8u);
  STAMP(1, 22);
#pragma unroll
  for (int s_ = 0; s_ < ZD; ++s_)
#pragma unroll
    for (int i = 0; i < 6; ++i) zf[s_][i] = LOAD_Z(s_, i);
  SB();
  STAMP(1, 11);
  unsigned zm = 0;
  sfor<0, 16 * 24>([&](auto G) __attribute__((always_inline)) {
    constexpr int ks = decltype(G)::value / 24, j = decltype(G)::value % 24, ck = ks >> 1, k2 = ks & 1, i = j >> 2, tu = j & 3;
    ac2[i][tu] = mfma(zf[ks % ZD][i], wfr[ck & 1][k2][tu], ac2[i][tu]);
    if constexpr (j == 0 && (ks == 1 || ks == 4 || ks == 8 || ks == 12)) STAMP(1, 56 + (ks >> 2));
    if constexpr (tu == 1) { zm = macc(zm, zf[ks % ZD][i]); PIN(zm); }
    if constexpr (ks < 12 && j == 19) {
      constexpr int k = ks >> 2, t = (ks >> 1) & 1;
      z1p[k][t][ks & 1] = pk2(gelu2((ks & 1) ? hi2(z1a[k][t]) : lo2(z1a[k][t])));
      PIN(z1p[k][t][ks & 1]);
    }
    if constexpr (ks == 12 && j == 19)
      fin[lane & 31] = rbf((__uint_as_float(gq[0][0]) + __uint_as_float(gq[1][0])) + (__uint_as_float(gq[2][0]) + __uint_as_float(gq[3][0])));
    if constexpr (ks >= 5 && ks < 5 + WO0 / 2 && (j == 9 || j == 21)) LOAD_WO((ks - 5) * 2 + (j == 21));
    if constexpr (j == 17 && ks >= 2 && ks < 5) st128<SC1>(mk, rws, l16, WS_V + ((mt * 4 + w) * 22 + pr0 + ks - 2) * 1024 + (ks == 4 ? oob3 : 0u));
    if constexpr (j == 1 && ks == HINT4_KS) {
      asm volatile("s_waitcnt vmcnt(20)" ::: "memory");
      __builtin_amdgcn_s_barrier();
      if (tid < 2) hint_add(p.ws, 192 + mt * 2 + tid);
    }
    if constexpr (tu == 3) {
      if constexpr (ks + ZD < 16) zf[ks % ZD][i] = LOAD_Z(ks + ZD, i);
      else if constexpr (ks > 16 - ZD) LOAD_WO(WO0 + (ks + ZD - 17) * 6 + i);
    }
    if constexpr ((j & 1) == 0 && j < 16 && ck < 7) {
      constexpr int jj = j >> 1;
      if constexpr (k2 == 0) { if constexpr (ck >= 1 && ck + 3 < 8) PARK1(ck + 3, WSLOT(ck), jj); }
      else wfr[(ck + 1) & 1][jj >> 2][jj & 3] = *(const v4u*)(WSLOT(ck + 1) + (rd_off[jj & 3] ^ ((jj >> 2) * 64)));
    }
    SB();
  });
  if (__builtin_expect(any_mark(zm), 0)) {
    MEMFENCE();
#pragma unroll
    for (int i = 0; i < 6; ++i)
#pragma unroll
      for (int j = 0; j < 4; ++j) { const float b0 = (i < 3 && j < 2) ? bf2f(p.bsp[mt * 64 + perm(2 * kh + (j & 1), c)]) : 0.f; ac2[i][j] = f32x4{b0, b0, b0, b0}; }
#pragma unroll 1
    for (int ks = 0; ks < 16; ++ks) {
      v4u zz[6], ww[4];
      unsigned sp = 0;
      do {
        MEMFENCE();
        zm = 0;
#pragma unroll
        for (int i = 0; i < 6; ++i) {
          zz[i] = ld128<SC1>(rws, l16 + ks * 1024, WS_Z2 + (((i < 3 ? ub_own : ub_oth) + i % 3) * 64 + w * 16) * 1024 + (i % 3 == 2 ? oob3 : 0u));
          zm = macc(zm, zz[i]);
        }
      } while (any_mark(zm) & (++sp <= SPIN_CAP));
#pragma unroll
      for (int tu = 0; tu < 4; ++tu) ww[tu] = ld128<0>(rwsp, perm(tu ^ (2 * kh), c) * 4096 + q * 16 + ks * 64, wsp_s);
#pragma unroll
      for (int i = 0; i < 6; ++i)
#pragma unroll
        for (int tu = 0; tu < 4; ++tu) ac2[i][tu] = mfma(zz[i], ww[tu], ac2[i][tu]);
    }
  }
  STAMP(1, 12);
  constexpr int WO1 = WO0 + (ZD - 1) * 6;
  {
    f32x4* red = (f32x4*)(smem + LDS_A);
#pragma unroll
    for (int i = 0; i < 6; ++i)
#pragma unroll
      for (int tu = 0; tu < 4; ++tu) {
        const int rel = (tu >> 1) | ((i / 3) << 1);
        if (rel) red[((w * 3 + rel - 1) * 6 + (i % 3) * 2 + (tu & 1)) * 64 + lane] = ac2[i][tu];
      }
#pragma unroll
    for (int f = WO1; f < WO1 + 10 && f < 44; ++f) LOAD_WO(f);
    f32x4 own[3][2];
#pragma unroll
    for (int k = 0; k < 3; ++k)
#pragma unroll
      for (int t = 0; t < 2; ++t) { own[k][t] = ac2[k][t]; PIN(own[k][t]); }
    STAMP(1, 51);
    bar();
    STAMP(1, 52);
    f32x4 o3[3][2][3];
#pragma unroll
    for (int k = 0; k < 3; ++k)
#pragma unroll
      for (int t = 0; t < 2; ++t)
#pragma unroll
        for (int rel = 1; rel < 4; ++rel) o3[k][t][rel - 1] = red[(((w ^ rel) * 3 + rel - 1) * 6 + k * 2 + t) * 64 + lane];
    SB();
#pragma unroll
    for (int k = 0; k < 3; ++k) {
#pragma unroll
      for (int t = 0; t < 2; ++t) {
        const f32x4 g = (own[k][t] + o3[k][t][0]) + (o3[k][t][1] + o3[k][t][2]);
        v2u o;
        o[0] = nomark(pk2(unpk(z1p[k][t][0]) * lo2(g)));
        o[1] = nomark(pk2(unpk(z1p[k][t][1]) * hi2(g)));
        const int uu = ub_own + k;
        st64<SC1>(o, rws, l16, WS_V + ((mt * 4 + 2 * kh + t) * 22 + (uu >> 1)) * 1024 + (uu & 1) * 8 + (k == 2 ? oob3 : 0u));
      }
    }
  }
  STAMP(1, 13);
  SB();
#pragma unroll
  for (int f = WO1 + 10; f < 44; ++f) LOAD_WO(f);
  SB();
  STAMP(1, 53);
  hint_wait(rws, 0u, WS_CNT + (192 + mt * 2 + th) * 128, 8u);
  STAMP(1, 23);
  {
    v4u vs[11];
    unsigned spins = 0;
    for (;;) {
      MEMFENCE();
#pragma unroll
      for (int i = 0; i < 11; ++i) vs[i] = ld128<SC1>(rws, l16 + (i & 3) * 1024, WS_V + ((mt * 4 + 2 * th) * 22 + w * 11 + (i & ~3)) * 1024);
      SB();
      unsigned m = 0;
#pragma unroll
      for (int i = 0; i < 11; ++i) { m = macc(m, vs[i]); *(v4u*)(smem + LDS_W + (w * 11 + i) * 1024 + l16) = vs[i]; }
      if (!(any_mark(m) & (++spins <= SPIN_CAP))) break;
    }
  }
  STAMP(1, 14);
  bar();
  f32x4 a3[2][2];
#pragma unroll
  for (int e = 0; e < 2; ++e)
#pragma unroll
    for (int t = 0; t < 2; ++t) a3[e][t] = f32x4{0.f, 0.f, 0.f, 0.f};
  STAMP(1, 54);
  constexpr int VD = 8;
  v4u vf[VD];
#define VFRAG(f) (*(const v4u*)(smem + LDS_W + (((f) & 1) * 22 + ((f) >> 1)) * 1024 + l16))
#pragma unroll
  for (int f = 0; f < VD; ++f) vf[f] = VFRAG(f);
  const f32x4 gbl = *(const f32x4*)(fin + q * 8), gbh = *(const f32x4*)(fin + q * 8 + 4);
  f32x2 x1f[2][4];
  SB();
#pragma unroll
  for (int f = 0; f < 44; ++f) {
    const v4u cur = vf[f % VD];
    a3[0][f & 1] = mfma(wo[0][f >> 1], cur, a3[0][f & 1]);
    a3[1][f & 1] = mfma(wo[1][f >> 1], cur, a3[1][f & 1]);
    if (f + VD < 44) vf[f % VD] = VFRAG(f + VD);
    if (f >= 36) { x1f[(f - 36) >> 2][f & 3] = unpk(x1h[(f - 36) >> 2][(f >> 1) & 1][f & 1]); PIN(x1f[(f - 36) >> 2][f & 3]); }
    SB();
  }
  STAMP(1, 55);
  const rsrc_t rout = mkrs(p.out, L * D * 2);
#pragma unroll
  for (int t = 0; t < 2; ++t) {
    v4u o;
    o[0] = pk2(fma2(lo2(a3[0][t]), lo2(gbl), x1f[t][0]));
    o[1] = pk2(fma2(hi2(a3[0][t]), hi2(gbl), x1f[t][1]));
    o[2] = pk2(fma2(lo2(a3[1][t]), lo2(gbh), x1f[t][2]));
    o[3] = pk2(fma2(hi2(a3[1][t]), hi2(gbh), x1f[t][3]));
    st128<0>(o, rout, perm(t, c) * 1024 + q * 16, (mt * 64 + th * 32) * 1024 + (dq * 4 + w) * 64);
  }
  STAMP(1, 15);
  STAMP_FLUSH(1);
  asm volatile("adaln_code_end:");
}

extern "C" void kernel_launch(void* const* d_in, const int* in_sizes, int n_in, void* d_out, int out_size, void* d_ws,
                              size_t ws_size, hipStream_t stream) {
  Params p;
  p.u = (const u16*)d_in[0];
  p.lar = (const u16*)d_in[1];
  p.aim = (const u16*)d_in[2];
  p.cre = (const u16*)d_in[3];
  p.cim = (const u16*)d_in[4];
  p.ldt = (const u16*)d_in[5];
  p.dsk = (const u16*)d_in[6];
  p.win = (const u16*)d_in[8];
  p.wsp = (const u16*)d_in[9];
  p.bsp = (const u16*)d_in[10];
  p.wout = (const u16*)d_in[11];
  p.c = (const u16*)d_in[12];
  p.wmod = (const u16*)d_in[13];
  p.out = (u16*)d_out;
  p.ws = (unsigned char*)d_ws;
  (void)hipFuncSetAttribute((const void*)adaln_mega, hipFuncAttributeMaxDynamicSharedMemorySize, MEGA_LDS);
  adaln_mega<<<256, 256, MEGA_LDS, stream>>>(p);
}
```

```cpp
#define OPT_B 1
#include <hip/hip_runtime.h>
#include <hip/hip_bf16.h>
#include <stdint.h>

#define LAS __attribute__((address_space(3)))
typedef unsigned v4u __attribute__((ext_vector_type(4)));
typedef unsigned v2u __attribute__((ext_vector_type(2)));
typedef float f32x4 __attribute__((ext_vector_type(4)));
typedef float f32x2 __attribute__((ext_vector_type(2)));
typedef short bf16x8 __attribute__((ext_vector_type(8)));
typedef _Float16 f16x8 __attribute__((ext_vector_type(8)));
typedef _Float16 f16x2 __attribute__((ext_vector_type(2)));
typedef __bf16 bf16x2_t __attribute__((ext_vector_type(2)));
typedef unsigned short u16;
typedef __amdgpu_buffer_rsrc_t rsrc_t;

#define STAMP_DECL do { } while (0)
#define STAMP(k, i) do { } while (0)
#define STAMP_FLUSH(k) do { } while (0)

#define OPT_A 0
#define OPT_C 0
#define OPT_D 0
#define OPT_E 2
#define OPT_F 0
constexpr int L = 2048, D = 512, NM = 128, FH = 688, FF = 1376;
constexpr float EPS = 1e-5f;
constexpr int SC1 = 16;
constexpr unsigned OOB = 0x40000000u;
constexpr unsigned SPIN_CAP = 1u << 14;

constexpr unsigned WS_EPOCH = 0;
constexpr unsigned WS_CNT = 4096;
constexpr unsigned WS_SR = WS_CNT + 256 * 128;
constexpr unsigned WS_SB = WS_SR + 2048 * 8;
constexpr unsigned WS_MODG = WS_SB + 2048 * 8;
constexpr unsigned WS_CSB = WS_MODG + 3072 * 32;
constexpr unsigned WS_ST2 = WS_CSB + 88 * 16 * 32;
constexpr unsigned WS_UG = 1u << 20;
static_assert(WS_ST2 + 32 * 2 * 16 * 32 * 8 <= WS_UG, "small buffers");
constexpr unsigned WS_X1T = WS_UG + 256 * 2048 * 8;
constexpr unsigned WS_WP = WS_X1T + 512 * 2048 * 2;
constexpr unsigned WS_WOT = WS_WP + 88 * 16 * 1024;
constexpr unsigned WS_Z2 = WS_WOT + 32 * 22 * 1024;
constexpr unsigned WS_V = WS_Z2 + 44 * 64 * 1024;
constexpr unsigned WS_END = WS_V + 128 * 22 * 1024;

struct Params {
  const u16 *u, *lar, *aim, *cre, *cim, *ldt, *dsk, *win, *wsp, *bsp, *wout, *c, *wmod;
  u16* out;
  unsigned char* ws;
};

#define DI __device__ __forceinline__
template <int V> struct IC { static constexpr int value = V; };
template <int I, int N, class F> DI void sfor(F&& f) {
  if constexpr (I < N) { f(IC<I>{}); sfor<I + 1, N>(f); }
}
DI float bf2f(unsigned b) { return __builtin_bit_cast(float, b << 16); }
DI float bflo(unsigned w) { return __builtin_bit_cast(float, w << 16); }
DI float bfhi(unsigned w) { return __builtin_bit_cast(float, w & 0xFFFF0000u); }
DI f32x2 unpk(unsigned w) { return f32x2{bflo(w), bfhi(w)}; }
DI unsigned pk2(f32x2 v) { return __builtin_bit_cast(unsigned, __builtin_convertvector(v, bf16x2_t)); }
DI unsigned pk2(float lo, float hi) { return pk2(f32x2{lo, hi}); }
DI unsigned nomark(unsigned x) { return x < 0xFFFFFFFEu ? x : 0xFFFFFFFEu; }
DI float rbf(float x) { return bflo(pk2(x, 0.f)); }
DI f32x2 fma2(f32x2 a, f32x2 b, f32x2 c) { return __builtin_elementwise_fma(a, b, c); }
DI f32x2 lo2(f32x4 v) { return f32x2{v[0], v[1]}; }
DI f32x2 hi2(f32x4 v) { return f32x2{v[2], v[3]}; }
DI f32x2 bc2(float x) { return f32x2{x, x}; }
DI f32x4 mfma(v4u a, v4u b, f32x4 c) {
  return __builtin_amdgcn_mfma_f32_16x16x32_bf16(__builtin_bit_cast(bf16x8, a), __builtin_bit_cast(bf16x8, b), c, 0, 0, 0);
}
DI f32x4 mfma_h(v4u a, v4u b, f32x4 c) {
  return __builtin_amdgcn_mfma_f32_16x16x32_f16(__builtin_bit_cast(f16x8, a), __builtin_bit_cast(f16x8, b), c, 0, 0, 0);
}
DI float dot2(unsigned a, unsigned b, float acc) {
  return __builtin_amdgcn_fdot2_f32_bf16(__builtin_bit_cast(bf16x2_t, a), __builtin_bit_cast(bf16x2_t, b), acc, false);
}
constexpr unsigned ONES = 0x3F803F80u;
DI int perm(int u, int c) { return 32 * (u >> 1) + 8 * (c >> 2) + 4 * (u & 1) + (c & 3); }
template <int CTRL> DI float dpp(float x) {
  return __builtin_bit_cast(float, __builtin_amdgcn_mov_dpp(__builtin_bit_cast(int, x), CTRL, 0xf, 0xf, true));
}
DI float row16_sum(float x) {
  x += dpp<0xB1>(x); x += dpp<0x4E>(x); x += dpp<0x141>(x); x += dpp<0x128>(x);
  return x;
}
DI float row16_max(float x) {
  x = fmaxf(x, dpp<0xB1>(x)); x = fmaxf(x, dpp<0x4E>(x)); x = fmaxf(x, dpp<0x141>(x)); x = fmaxf(x, dpp<0x128>(x));
  return x;
}
DI float xrow_sum(float x) {
  auto s = __builtin_amdgcn_permlane16_swap(__float_as_uint(x), __float_as_uint(x), false, false);
  x = __uint_as_float(s[0]) + __uint_as_float(s[1]);
  auto t = __builtin_amdgcn_permlane32_swap(__float_as_uint(x), __float_as_uint(x), false, false);
  return __uint_as_float(t[0]) + __uint_as_float(t[1]);
}
DI float xrow_sum4(float a, float b, float c, float d) {
  auto s = __builtin_amdgcn_permlane16_swap(__float_as_uint(a), __float_as_uint(b), false, false);
  auto t = __builtin_amdgcn_permlane16_swap(__float_as_uint(c), __float_as_uint(d), false, false);
  const float x = __uint_as_float(s[0]) + __uint_as_float(s[1]), y = __uint_as_float(t[0]) + __uint_as_float(t[1]);
  auto z = __builtin_amdgcn_permlane32_swap(__float_as_uint(x), __float_as_uint(y), false, false);
  return __uint_as_float(z[0]) + __uint_as_float(z[1]);
}
DI float xrow_max(float x) {
  auto s = __builtin_amdgcn_permlane16_swap(__float_as_uint(x), __float_as_uint(x), false, false);
  x = fmaxf(__uint_as_float(s[0]), __uint_as_float(s[1]));
  auto t = __builtin_amdgcn_permlane32_swap(__float_as_uint(x), __float_as_uint(x), false, false);
  return fmaxf(__uint_as_float(t[0]), __uint_as_float(t[1]));
}
DI f32x2 gelu2(f32x2 x) {
  const float k0 = -2.0f * 0.7978845608028654f * 1.4426950408889634f, k1 = k0 * 0.044715f;
  const f32x2 a = x * fma2(x * x, bc2(k1), bc2(k0));
  const f32x2 e = f32x2{__builtin_amdgcn_exp2f(a[0]), __builtin_amdgcn_exp2f(a[1])} + bc2(1.0f);
  return x * f32x2{__builtin_amdgcn_rcpf(e[0]), __builtin_amdgcn_rcpf(e[1])};
}
DI rsrc_t mkrs(const void* ptr, unsigned bytes) { return __builtin_amdgcn_make_buffer_rsrc((void*)ptr, 0, (int)bytes, 0x00020000); }
template <int AUX> DI v4u ld128(rsrc_t rs, unsigned v, unsigned s) { return __builtin_amdgcn_raw_buffer_load_b128(rs, (int)v, (int)s, AUX); }
template <int AUX> DI v2u ld64(rsrc_t rs, unsigned v, unsigned s) { return __builtin_amdgcn_raw_buffer_load_b64(rs, (int)v, (int)s, AUX); }
template <int AUX> DI void st128(v4u x, rsrc_t rs, unsigned v, unsigned s) { __builtin_amdgcn_raw_buffer_store_b128(x, rs, (int)(v + s), 0, AUX); }
template <int AUX> DI void st64(v2u x, rsrc_t rs, unsigned v, unsigned s) { __builtin_amdgcn_raw_buffer_store_b64(x, rs, (int)v, (int)s, AUX); }
DI f32x4 asf(v4u x) { return __builtin_bit_cast(f32x4, x); }
DI float sum4(f32x4 x) { return (x[0] + x[1]) + (x[2] + x[3]); }
DI unsigned umax3(unsigned a, unsigned b, unsigned c) { unsigned m = a > b ? a : b; return m > c ? m : c; }
DI unsigned macc(unsigned m, v4u x) { return umax3(umax3(m, x[0], x[1]), x[2], x[3]); }
DI bool any_mark(unsigned m) { return __builtin_amdgcn_ballot_w64(m == 0xFFFFFFFFu) != 0ull; }
#define SB() __builtin_amdgcn_sched_barrier(0)
#define PIN(x) asm volatile("" : "+v"(x))
#define RETRY_BEGIN(m) if (__builtin_expect(any_mark(m), 0)) { unsigned sp_ = 0; do { MEMFENCE(); m = 0;
#define RETRY_END(m) } while (any_mark(m) & (++sp_ <= SPIN_CAP)); }
#define MEMFENCE() asm volatile("" ::: "memory")
DI void bar() { asm volatile("s_waitcnt lgkmcnt(0)" ::: "memory"); __builtin_amdgcn_s_barrier(); asm volatile("" ::: "memory"); }

DI f32x2 cmul(f32x2 a, f32x2 b) { return fma2(bc2(a[1]), f32x2{-b[1], b[0]}, bc2(a[0]) * b); }
DI f32x2 cexp_p(float are2, float rev_hi, float rev_lo, float p) {
  const float mag = __builtin_amdgcn_exp2f(are2 * p);
  const float hi = rev_hi * p;
  const float fr = (hi - __builtin_rintf(hi)) + rev_lo * p;
  return f32x2{mag * __builtin_amdgcn_cosf(fr), mag * __builtin_amdgcn_sinf(fr)};
}
DI float exp_acc(float x) {
  const float hi = x * 1.4426950408889634f;
  const float lo = __builtin_fmaf(x, 1.4426950408889634f, -hi) + x * 1.925963033500011e-8f;
  const float e = __builtin_amdgcn_exp2f(hi);
  return __builtin_fmaf(e, lo * 0.6931471805599453f, e);
}
DI unsigned pkh(float a, float b) {
  f16x2 v = {(_Float16)a, (_Float16)b};
  return __builtin_bit_cast(unsigned, v);
}

constexpr int CV_RK = 4160;
constexpr int CV_H = 2 * CV_RK;
constexpr int CV_CH = CV_H + 4608;
constexpr int LDS_A = 0;
constexpr int LDS_U = 2 * CV_CH;
constexpr int LDS_GS = LDS_U + 8192;
constexpr int LDS_TAB = 36864;
constexpr int KP_CH = 13840;
constexpr int LDS_KP = LDS_TAB + 2 * 64 * 528 + 16;
constexpr int LDS_LN = 0, LDS_MR = 8448;
constexpr int LDS_WI = 145472, LDS_WO = LDS_WI + 10240;
constexpr int LDS_X = 65536;
constexpr int LDS_S = LDS_X + 24576;
constexpr int LDS_W = LDS_S + 6144;
constexpr int MEGA_LDS = LDS_W + 65536;
static_assert(LDS_WO + 4608 <= MEGA_LDS && LDS_KP + 2 * KP_CH <= LDS_WI && LDS_S >= 73728, "lds");
static_assert(LDS_GS + 1280 <= LDS_TAB && LDS_KP + 2 * KP_CH <= MEGA_LDS && MEGA_LDS <= 160 * 1024, "lds");
#define FLAG_AT 110
static_assert(FLAG_AT > 57 && FLAG_AT < 144 && FLAG_AT % 6 != 3, "flag position");
#define WF_EARLY 3
#define WO0 16
#define WSP_P0 4
#define HINT4_KS 15
#define IN_AUX 0
#define WSP_AUX 0

DI v2u trread(const unsigned char* ptr) {
  typedef short v4i16_t __attribute__((ext_vector_type(4)));
  return __builtin_bit_cast(v2u, __builtin_amdgcn_ds_read_tr16_b64_v4i16((LAS v4i16_t*)ptr));
}
struct CvOrder { int qq[36], pp[36]; };
constexpr CvOrder cv_order() {
  CvOrder o{};
  int n = 0;
  for (int m = 0; m < 8; ++m) {
    for (int j = 0; j < m; ++j) if (m + j < 8) { o.qq[n] = m; o.pp[n] = j; ++n; }
    for (int j = 0; j <= m; ++j) if (m + j < 8) { o.qq[n] = j; o.pp[n] = m; ++n; }
  }
  return o;
}
constexpr CvOrder CVO = cv_order();

typedef __attribute__((address_space(1))) unsigned gu32;
DI unsigned tacc(unsigned m, v4u x, unsigned tag) { return m | (x[1] ^ tag) | (x[3] ^ tag); }
DI bool any_nz(unsigned m) { return __builtin_amdgcn_ballot_w64(m != 0u) != 0ull; }
DI void hint_add(unsigned char* ws, int line) { (void)__hip_atomic_fetch_add((gu32*)(ws + WS_CNT + line * 128), 1u, __ATOMIC_RELAXED, __HIP_MEMORY_SCOPE_AGENT); }
DI void hint_wait(rsrc_t rws, unsigned voff, unsigned soff, unsigned want) {
  unsigned sp = 0;
  for (;;) {
    MEMFENCE();
    const unsigned v = __builtin_amdgcn_raw_buffer_load_b32(rws, (int)voff, (int)soff, SC1);
    if (!((__builtin_amdgcn_ballot_w64(v < want) != 0ull) & (++sp <= SPIN_CAP))) break;
    __builtin_amdgcn_s_sleep(OPT_E);
  }
  MEMFENCE();
}

extern "C" __global__ void __launch_bounds__(256, 1) adaln_mega(Params p) {
  extern __shared__ __attribute__((aligned(16))) unsigned char smem[];
  const int tid = threadIdx.x, lane = tid & 63, w = __builtin_amdgcn_readfirstlane(tid >> 6);
  const int b = blockIdx.x, xcd = b & 7, idx = b >> 3;
  const int pb = xcd * 32 + idx;
  const unsigned l16 = lane * 16;
  STAMP_DECL;
  STAMP(1, 0);
  v2u codepf;
  const rsrc_t ru = mkrs(p.u, L * D * 2);
  v4u xr[2];
  xr[0] = ld128<IN_AUX>(ru, l16, (pb * 8 + w * 2) * 1024);
  xr[1] = ld128<IN_AUX>(ru, l16 + 1024, (pb * 8 + w * 2) * 1024);
  const int mkg = b & 3, mcg = b >> 2;
  const rsrc_t rmod = mkrs(p.wmod, D * 3072 * 2);
  v4u xm[4];
#pragma unroll
  for (int i = 0; i < 4; ++i) xm[i] = ld128<IN_AUX>(rmod, (tid >> 3) * 6144 + (tid & 7) * 16, (mkg * 128 + i * 32) * 6144 + mcg * 128 + (b < 192 ? 0u : OOB));
  const unsigned cq = p.c[mkg * 128 + (tid & 3) * 32 + (tid >> 3)];
  const unsigned epoch = *(const unsigned*)(p.ws + WS_EPOCH);
  const rsrc_t rws = mkrs(p.ws, WS_END);
  __builtin_amdgcn_raw_buffer_store_b32(0u, rws, tid == 0 ? 0u : OOB, WS_CNT + b * 128, SC1);
  SB();
  const int cch = w >> 1, eh = w & 1, ns = tid & 127;
  const unsigned q_cre = p.cre[pb * 256 + tid], q_cim = p.cim[pb * 256 + tid], q_lar = p.lar[pb * 256 + tid], q_aim = p.aim[pb * 256 + tid];
  const unsigned q_ldt = p.ldt[pb * 2 + cch], q_dsk = p.dsk[pb * 2 + cch];
  {
    unsigned long long pc;
    int rel_m, rel_e;
    asm volatile("s_getpc_b64 %0\n\ts_mov_b32 %1, adaln_mega@rel32@lo+4\n\ts_mov_b32 %2, adaln_code_end@rel32@lo+12" : "=s"(pc), "=s"(rel_m), "=s"(rel_e));
    const rsrc_t rcode = mkrs((const void*)(pc + (long long)rel_m), rel_e > rel_m ? (unsigned)(rel_e - rel_m) & ~15u : 0u);
    codepf = ld64<0>(rcode, tid * 8, idx * 2048);
  }
  SB();
  STAMP(1, 24);
  const int c = lane & 15, q = lane >> 4;
  int pos4[9];
  {
    const float f4 = (float)(8188 - tid * 32);
#pragma unroll
    for (int j = 0; j < 10; j += 2) {
      const unsigned pp = pk2(bc2(f4) - f32x2{4.f * j, 4.f * j + 4.f});
      pos4[j] = (int)bflo(pp);
      if (OPT_A) pos4[j] += (pos4[j] >> 5) << 2;
      PIN(pos4[j]);
      if (j + 1 < 9) { pos4[j + 1] = (int)bfhi(pp); if (OPT_A) pos4[j + 1] += (pos4[j + 1] >> 5) << 2; PIN(pos4[j + 1]); }
    }
  }
  int cob[2], cos_[2];
  cob[0] = (eh ? 1024 + 512 * q : 128 * (q >> 1) + 4 * (q & 1)) * 4 + 32 * c + LDS_KP + cch * KP_CH;
  cob[1] = (q < 2 ? 256 + 128 * q : 504 + 4 * q) * 4 + 32 * c + LDS_KP + cch * KP_CH;
  cos_[0] = eh ? 512 : 4;
  cos_[1] = q < 2 ? 8 : 512;
  if (OPT_A) {
    cob[0] = (eh ? 1152 + 576 * q : 144 * (q >> 1) + 4 * (q & 1)) * 4 + 36 * c + LDS_KP + cch * KP_CH;
    cob[1] = (q < 2 ? 288 + 144 * q : 568 + 4 * q) * 4 + 36 * c + LDS_KP + cch * KP_CH;
    cos_[0] = eh ? 576 : 4;
    cos_[1] = q < 2 ? 8 : 576;
  }
  PIN(cob[0]); PIN(cob[1]); PIN(cos_[1]);
  SB();
  STAMP(1, 26);
  {
    *(v4u*)(smem + LDS_LN + (w * 2) * 1040 + l16) = xr[0];
    *(v4u*)(smem + LDS_LN + (w * 2 + 1) * 1040 + l16) = xr[1];
    float sm[2], sq[2];
#pragma unroll
    for (int i = 0; i < 2; ++i) {
      sm[i] = dot2(xr[i][0], ONES, dot2(xr[i][1], ONES, dot2(xr[i][2], ONES, dot2(xr[i][3], ONES, 0.f))));
      sq[i] = dot2(xr[i][0], xr[i][0], dot2(xr[i][1], xr[i][1], dot2(xr[i][2], xr[i][2], dot2(xr[i][3], xr[i][3], 0.f))));
    }
    const float tv = row16_sum(xrow_sum4(sm[0], sm[1], sq[0], sq[1]));
    auto sw = __builtin_amdgcn_permlane32_swap(__float_as_uint(tv), __float_as_uint(tv), false, false);
    const float m = __uint_as_float(sw[0]) * (1.0f / D);
    const float r = __builtin_amdgcn_rsqf(fmaxf(__uint_as_float(sw[1]) * (1.0f / D) - m * m, 0.f) + EPS);
    ((float*)(smem + LDS_GS + 1088))[(q >> 1) * 8 + w * 2 + (q & 1)] = (q >> 1) ? -m * r : r;
    const float cv = bf2f(cq);
    const float sl = rbf(cv * __builtin_amdgcn_rcpf(1.0f + __builtin_amdgcn_exp2f(cv * -1.4426950408889634f)));
    const float sc[4] = {dpp<0x00>(sl), dpp<0x55>(sl), dpp<0xAA>(sl), dpp<0xFF>(sl)};
    f32x2 a[4];
#pragma unroll
    for (int i = 0; i < 4; ++i)
#pragma unroll
      for (int j = 0; j < 4; ++j) a[j] = i == 0 ? unpk(xm[i][j]) * bc2(sc[i]) : fma2(unpk(xm[i][j]), bc2(sc[i]), a[j]);
    float* red = (float*)(smem + LDS_MR) + (tid >> 3) * 68 + (tid & 7) * 8;
    *(f32x4*)red = f32x4{a[0][0], a[0][1], a[1][0], a[1][1]};
    *(f32x4*)(red + 4) = f32x4{a[2][0], a[2][1], a[3][0], a[3][1]};
  }
  const unsigned tag = epoch * 2654435761u + 0x9E3779B9u;
  STAMP(1, 28);
  asm volatile("s_waitcnt vmcnt(3)" ::: "memory");
  bar();
  STAMP(1, 16);
  {
    unsigned ua[4][2];
#pragma unroll
    for (int i = 0; i < 4; ++i) {
      const unsigned char* src = smem + LDS_LN + (tid & 3) * 2080 + (i * 64 + (tid >> 2)) * 4;
      ua[i][0] = *(const unsigned*)src; ua[i][1] = *(const unsigned*)(src + 1040);
    }
    float ms8[8];
#pragma unroll
    for (int i = 0; i < 8; ++i) ms8[i] = ((const float*)(smem + LDS_MR))[(q * 8 + i) * 68 + w * 16 + c];
    const unsigned lnv = ((const unsigned*)(smem + LDS_GS + 1088))[c];
    SB();
#pragma unroll
    for (int i = 0; i < 4; ++i) st128<SC1>(v4u{ua[i][0], tag, ua[i][1], tag}, rws, (tid >> 2) * 16384 + (tid & 3) * 16, WS_UG + i * 64 * 16384 + pb * 64);
    const float s = xrow_sum(((ms8[0] + ms8[1]) + (ms8[2] + ms8[3])) + ((ms8[4] + ms8[5]) + (ms8[6] + ms8[7])));
    st64<SC1>(v2u{__float_as_uint(s), tag}, rws, q == 0 ? c * 8 : OOB, WS_MODG + (mkg * 3072 + mcg * 64 + w * 16) * 8 + (b < 192 ? 0u : OOB));
    st64<SC1>(v2u{lnv, tag}, rws, tid < 16 ? (c >> 3) * 16384 + (c & 7) * 8 : OOB, WS_SR + pb * 64);
  }
  STAMP(1, 17);
  SB();
  const unsigned wp_oob = b < 176 ? 0u : OOB;
  const int wpr = b >> 2, wkg = b & 3;
  v4u xi[2], xo;
  {
    const rsrc_t rwin = mkrs(p.win, D * FF * 2), rwout = mkrs(p.wout, FH * D * 2);
    const int hf = wpr >= 22, pp = wpr - 22 * hf;
    const unsigned v = ((tid >> 2) * (FF * 2) + (tid & 3) * 16) | ((pp == 21 ? tid & 2 : 0) << 29);
#pragma unroll
    for (int i = 0; i < 2; ++i) xi[i] = ld128<IN_AUX>(rwin, v, (wkg * 128 + i * 64) * (FF * 2) + (hf * FH + pp * 32) * 2 + wp_oob);
    xo = ld128<IN_AUX>(rwout, (tid >> 3) * 1024 + (tid & 7) * 16, idx * 32 * 1024 + xcd * 128);
  }
  SB();
  const int d0 = pb * 2, dk = d0 + cch;
  const int mt = (xcd >> 1) * 8 + (idx & 7), nt = (xcd & 1) * 4 + (idx >> 3);
  const int big = (nt + 2) >> 3;
  const int np = 3 - big, pr0 = 3 * nt - big * (nt - 6);
  const int kh = w & 1, uh = w >> 1;
  const int ub_own = pr0 * 2 + uh * np, ub_oth = pr0 * 2 + (uh ^ 1) * np;
  const unsigned oob3 = (unsigned)big << 30;
  const int kr = 2 * (idx >> 3) + (xcd & 1) + (OPT_B == 0 ? (idx & 7) : OPT_B == 1 ? 4 * (idx & 1) : OPT_B == 2 ? 2 * (idx & 3) : 0);
  const rsrc_t rwsp = mkrs(p.wsp, L * L * 2);
  const unsigned wp_fr = (wpr * 2 * 16 + wkg * 4 + w) * 1024 + wp_oob;
  v4u wr[8][8];
  const unsigned wsp_v = (lane >> 3) * 4096 + (lane & 7) * 16, wsp_s = mt * 64 * 4096 + w * 1024;
#define LOAD_WSP1(ck, i) wr[ck][i] = ld128<WSP_AUX>(rwsp, wsp_v + (((ck) + kr) & 7) * 128, wsp_s + (i) * 8 * 4096)
#define LOAD_WSPN(n) LOAD_WSP1((n) >> 3, (n) & 7)
  const v4u mk = {~0u, ~0u, ~0u, ~0u};
  {
    const float dt = rbf(exp_acc(bf2f(q_ldt)));
    const float cre = bf2f(q_cre) * dt, cim = bf2f(q_cim) * dt;
    const float are2 = -rbf(exp_acc(bf2f(q_lar))) * dt * 1.4426950408889634f;
    const float aim = bf2f(q_aim) * dt;
    const float rev_hi = aim * 0.15915494309189535f;
    const float rev_lo = __builtin_fmaf(aim, 0.15915494309189535f, -rev_hi) + aim * 6.4206383e-9f;
    int ex = __builtin_amdgcn_frexp_expf(xrow_max(row16_max(fmaxf(fabsf(cre), fabsf(cim))))) + 1;
    ex = ex < -100 ? -100 : ex;
    const float scale = __builtin_amdgcn_ldexpf(1.f, -ex);
    ((float*)(smem + LDS_GS + 1024))[w] = __builtin_amdgcn_ldexpf(2.f, ex);
    const f32x2 c0 = {cre * scale, cim * scale};
    const f32x2 e1 = cexp_p(are2, rev_hi, rev_lo, 1.f), e8 = cexp_p(are2, rev_hi, rev_lo, 8.f), e64 = cexp_p(are2, rev_hi, rev_lo, 64.f);
    unsigned char* tn = smem + LDS_TAB + cch * (64 * 528) + ns * 4;
#define TROW(r, z) *(unsigned*)(tn + (r) * 528) = pkh((z)[0], -(z)[1])
    SB();
    const f32x2 e128 = cmul(e64, e64), e2 = cmul(e1, e1), e256 = cmul(e128, e128), e4 = cmul(e2, e2), e512 = cmul(e256, e256);
    f32x2 f0 = {1.f, 0.f}, f1 = e64;
    f32x2 ca = cmul(c0, e512), cb = cmul(ca, e512);
#pragma unroll
    for (int j = 0; j < 8; ++j) {
      *(unsigned*)(tn + (48 + j) * 528) = pkh(f0[0], f0[1]);
      *(unsigned*)(tn + (56 + j) * 528) = pkh(f1[0], f1[1]);
      TROW(32 + j, cb);
      f0 = j == 0 ? e8 : cmul(f0, e8); f1 = cmul(f1, e8); cb = cmul(cb, e128);
      if (j < 4) {
        const f32x2 z = cmul(ca, e4);
        TROW(24 + j, ca); TROW(28 + j, z);
        ca = cmul(ca, e128);
      }
      if (j == 3 || j == 7) {
        PIN(f0); PIN(f1); PIN(cb);
        if (WSP_P0 >= 4) { LOAD_WSPN(j >> 2 << 1); LOAD_WSPN((j >> 2 << 1) + 1); }
        SB();
      }
    }
    TROW(40, cb);
    f32x2 t1[8];
    t1[1] = e1; t1[2] = e2; t1[4] = e4;
    t1[3] = cmul(e2, e1); t1[5] = cmul(e4, e1); t1[6] = cmul(e2, e4); t1[7] = cmul(t1[6], e1);
    const f32x2 c1 = cmul(c0, e128), c2 = cmul(c0, e256), c3 = cmul(c2, e128);
    TROW(0, c0); TROW(8, c1); TROW(16, c2); TROW(20, c3);
#pragma unroll
    for (int v = 1; v < 8; ++v) {
      const f32x2 z0 = cmul(t1[v], c0), z1 = cmul(t1[v], c1);
      TROW(v, z0); TROW(8 + v, z1);
      if ((v & 1) == 0) {
        const f32x2 z2 = cmul(t1[v], c2), z3 = cmul(t1[v], c3);
        TROW(16 + v / 2, z2); TROW(20 + v / 2, z3);
        if (v < 6 && WSP_P0 >= 8) { LOAD_WSPN(v + 2); LOAD_WSPN(v + 3); }
        SB();
      }
    }
  }
  STAMP(1, 29);
  bar();
  STAMP(1, 18);
  v4u gr[4], gb_[4];
  v2u gm[4], gc[4];
  const unsigned gm_off = ((cch ? 3 * 512 : 4 * 512) + wkg * 128 + ns) * 8;
  const unsigned gc_off = (((lane & 7) > 5 ? 0 : ((lane & 7) >> 1)) * 512 + d0 + (lane & 1)) * 8;
#define LOAD_GS() _Pragma("unroll") for (int i_ = 0; i_ < 4; ++i_) { gr[i_] = ld128<SC1>(rws, tid * 16, WS_SR + i_ * 4096); gb_[i_] = ld128<SC1>(rws, tid * 16, WS_SB + i_ * 4096); }
#define LOAD_GM() _Pragma("unroll") for (int i_ = 0; i_ < 4; ++i_) { gm[i_] = ld64<SC1>(rws, gm_off, WS_MODG + i_ * 24576); gc[i_] = ld64<SC1>(rws, gc_off, WS_MODG + i_ * 24576); }
  {
    const unsigned char* tab = smem + LDS_TAB + cch * (64 * 528) + c * 528 + q * 16;
    v4u fa[2][8], fb[8];
#pragma unroll
    for (int ks = 0; ks < 4; ++ks) {
      fb[ks] = *(const v4u*)(tab + 48 * 528 + ks * 64);
      fa[0][ks] = *(const v4u*)(tab + eh * (32 * 528) + ks * 64);
      fa[1][ks] = *(const v4u*)(tab + 16 * 528 + ks * 64);
    }
    const f32x2 us = *(const f32x2*)(smem + LDS_GS + 1024 + cch * 8);
    SB();
    f32x4 kk[2][2];
#pragma unroll
    for (int i = 0; i < 4; ++i) kk[i >> 1][i & 1] = f32x4{0.f, 0.f, 0.f, 0.f};
#pragma unroll
    for (int ks = 0; ks < 8; ++ks) {
      if (ks < 4) {
        fb[4 + ks] = *(const v4u*)(tab + 48 * 528 + (4 + ks) * 64);
        fa[0][4 + ks] = *(const v4u*)(tab + eh * (32 * 528) + (4 + ks) * 64);
        fa[1][4 + ks] = *(const v4u*)(tab + 16 * 528 + (4 + ks) * 64);
      }
      kk[0][ks >> 2] = mfma_h(fa[0][ks], fb[ks], kk[0][ks >> 2]); kk[1][ks >> 2] = mfma_h(fa[1][ks], fb[ks], kk[1][ks >> 2]);
      SB();
    }
    STAMP(1, 37);
    *(v4u*)(smem + (lane == 63 ? LDS_KP - 16 + cch * KP_CH : cch * CV_CH + (lane < 32 ? CV_H + l16 : ((lane >> 1) & 1) * CV_RK + 4096 + (lane & 1) * 16))) = v4u{0u, 0u, 0u, 0u};
#pragma unroll
    for (int i = 0; i < 2; ++i) *(v4u*)(smem + LDS_WI + (i * 64 + (tid >> 2)) * 80 + (tid & 3) * 16) = xi[i];
    *(v4u*)(smem + LDS_WO + (tid >> 3) * 144 + (tid & 7) * 16) = xo;
    f32x4 k0 = kk[0][0] * us[0] + kk[0][1] * us[1];
    const f32x4 k1 = kk[1][0] * us[0] + kk[1][1] * us[1];
    k0[0] += ns == 0 ? bf2f(q_dsk) : 0.f;
#pragma unroll
    for (int r = 0; r < 4; ++r) { *(float*)(smem + cob[0] + r * cos_[0]) = k0[r]; *(float*)(smem + cob[1] + r * cos_[1]) = k1[r]; }
  }
  v4u ug[4];
#define LOAD_UG() _Pragma("unroll") for (int i_ = 0; i_ < 4; ++i_) ug[i_] = ld128<SC1>(rws, tid * 16, WS_UG + pb * 16384 + i_ * 4096)
  STAMP(1, 31);
  LOAD_GM();
  LOAD_UG();
  bar();
  STAMP(1, 19);
  v4u wt[2];
  {
    float v[2][9];
#pragma unroll
    for (int j = 0; j < 9; ++j) v[0][j] = *(const float*)(smem + LDS_KP + pos4[j]);
    LOAD_GS();
#pragma unroll
    for (int j = 0; j < 9; ++j) v[1][j] = *(const float*)(smem + LDS_KP + KP_CH + pos4[j]);
    SB();
    STAMP(1, 36);
#pragma unroll
    for (int ch = 0; ch < 2; ++ch) {
      unsigned char* rk = smem + ch * CV_CH + tid * 16;
#pragma unroll
      for (int sft = 0; sft < 2; ++sft)
        *(v4u*)(rk + sft * CV_RK) = v4u{pk2(v[ch][sft], v[ch][sft + 1]), pk2(v[ch][sft + 2], v[ch][sft + 3]), pk2(v[ch][sft + 4], v[ch][sft + 5]), pk2(v[ch][sft + 6], v[ch][sft + 7])};
    }
    const unsigned char* a0 = smem + LDS_WI + (w * 32 + 8 * q + ((lane >> 2) & 3)) * 80 + (lane & 3) * 16;
#pragma unroll
    for (int e = 0; e < 2; ++e) {
      const v2u lo = trread(a0 + e * 8), hi = trread(a0 + e * 8 + 320);
      wt[e] = v4u{lo[0], lo[1], hi[0], hi[1]};
    }
    const unsigned char* o0 = smem + LDS_WO + (8 * q + ((lane >> 2) & 3)) * 144 + (w >> 1) * 64 + (lane & 3) * 16 + (w & 1) * 8;
    const v2u olo = trread(o0), ohi = trread(o0 + 576);
    st128<SC1>(v4u{olo[0], olo[1], ohi[0], ohi[1]}, rws, l16, WS_WOT + ((xcd * 4 + w) * 22 + idx) * 1024 + wp_oob);
    SB();
    STAMP(1, 32);
    unsigned m = 0;
#pragma unroll
    for (int i = 0; i < 4; ++i) m = m | (gm[i][1] ^ tag) | (gc[i][1] ^ tag);
    if (__builtin_expect(any_nz(m), 0)) {
      unsigned sp = 0;
      do {
        MEMFENCE(); LOAD_GM(); m = 0;
#pragma unroll
        for (int i = 0; i < 4; ++i) m = m | (gm[i][1] ^ tag) | (gc[i][1] ^ tag);
      } while (any_nz(m) & (++sp <= SPIN_CAP));
    }
    const float mv = rbf((__uint_as_float(gm[0][0]) + __uint_as_float(gm[1][0])) + (__uint_as_float(gm[2][0]) + __uint_as_float(gm[3][0])));
    ((float*)(smem + LDS_GS))[tid] = cch ? mv : rbf(1.0f + mv);
    const float cv = rbf((__uint_as_float(gc[0][0]) + __uint_as_float(gc[1][0])) + (__uint_as_float(gc[2][0]) + __uint_as_float(gc[3][0])));
    const float gav = dpp<0x104>(cv), gav2 = dpp<0x102>(cv);
    const float hbv = cv * gav, hsv = rbf(1.0f + cv) * gav2;
    const f32x2 hb = {__builtin_bit_cast(float, __builtin_amdgcn_readlane(__builtin_bit_cast(int, hbv), 0)), __builtin_bit_cast(float, __builtin_amdgcn_readlane(__builtin_bit_cast(int, hbv), 1))};
    const f32x2 hs = {__builtin_bit_cast(float, __builtin_amdgcn_readlane(__builtin_bit_cast(int, hsv), 2)), __builtin_bit_cast(float, __builtin_amdgcn_readlane(__builtin_bit_cast(int, hsv), 3))};
    SB();
    STAMP(1, 33);
    m = 0;
#pragma unroll
    for (int i = 0; i < 4; ++i) { m = tacc(m, ug[i], tag); m = tacc(m, gr[i], tag); m = tacc(m, gb_[i], tag); }
    if (__builtin_expect(any_nz(m), 0)) {
      unsigned sp = 0;
      do {
        MEMFENCE(); LOAD_UG(); LOAD_GS(); m = 0;
#pragma unroll
        for (int i = 0; i < 4; ++i) { m = tacc(m, ug[i], tag); m = tacc(m, gr[i], tag); m = tacc(m, gb_[i], tag); }
      } while (any_nz(m) & (++sp <= SPIN_CAP));
    }
#pragma unroll
    for (int i = WSP_P0; i < WSP_P0 + 4; ++i) LOAD_WSPN(i);
    STAMP(1, 38);
#pragma unroll
    for (int i = 0; i < 4; ++i) {
      const f32x2 h0 = fma2(fma2(unpk(ug[i][0]), bc2(__uint_as_float(gr[i][0])), bc2(__uint_as_float(gb_[i][0]))), hs, hb);
      const f32x2 h1 = fma2(fma2(unpk(ug[i][2]), bc2(__uint_as_float(gr[i][2])), bc2(__uint_as_float(gb_[i][2]))), hs, hb);
      *(unsigned*)(smem + CV_H + 512 + i * 1024 + tid * 4) = pk2(h0[0], h1[0]);
      *(unsigned*)(smem + CV_CH + CV_H + 512 + i * 1024 + tid * 4) = pk2(h0[1], h1[1]);
      *(unsigned*)(smem + LDS_U + i * 1024 + tid * 4) = __builtin_amdgcn_perm(ug[i][2], ug[i][0], 0x05040100u);
      *(unsigned*)(smem + LDS_U + 4096 + i * 1024 + tid * 4) = __builtin_amdgcn_perm(ug[i][2], ug[i][0], 0x07060302u);
    }
  }
  asm volatile("" ::"v"(codepf));
  const unsigned char* abase = smem + cch * CV_CH + (1 - (c & 1)) * CV_RK + 2 * (2030 - 2 * (c >> 1) + 8 * q) - eh * 256 - 192 - 3584;
  const unsigned char* bbase = smem + cch * CV_CH + CV_H + 2 * (256 - 16 + 16 * c + 8 * q) - eh * 256 - 192;
  STAMP(1, 39);
  bar();
  STAMP(1, 1);
#define CV_A(ee, k) ({ const unsigned* p_ = (const unsigned*)(abase + 192 - 64 * (ee) + 3584 - 512 * (k)); v4u{p_[0], p_[1], p_[2], p_[3]}; })
#define CV_B(ee, k) (*(const v4u*)(bbase + 192 - 64 * (ee) + 512 * (k)))
  v4u cA[2][8], cB[2][8];
  f32x4 gs[4];
#pragma unroll
  for (int k = 0; k < 2; ++k) { cA[0][k] = CV_A(0, k); cB[0][k] = CV_B(0, k); }
  {
    const float* gp = (const float*)(smem + LDS_GS) + w * 32 + q * 8;
    gs[0] = *(const f32x4*)gp; gs[1] = *(const f32x4*)(gp + 4); gs[2] = *(const f32x4*)(gp + 128); gs[3] = *(const f32x4*)(gp + 132);
  }
  SB();
  f32x4 cacc[8];
  v2u ue[4];
  {
    f32x2 cs[2], bs[2];
    v4u wo_;
    f32x2 cp[4];
#pragma unroll
    for (int t = 0; t < 8; ++t) cacc[t] = f32x4{0.f, 0.f, 0.f, 0.f};
    sfor<0, 144>([&](auto G) __attribute__((always_inline)) {
      constexpr int g = decltype(G)::value, ee = g / 36, j = g % 36;
      if constexpr (ee == 0 && j < 6) { cA[0][2 + j] = CV_A(0, 2 + j); cB[0][2 + j] = CV_B(0, 2 + j); }
      cacc[CVO.pp[j] + CVO.qq[j]] = mfma(cA[ee & 1][CVO.qq[j]], cB[ee & 1][CVO.pp[j]], cacc[CVO.pp[j] + CVO.qq[j]]);
      constexpr int jn = ee == 0 ? j - 20 : (j & 1) ? j >> 1 : -1;
      if constexpr (ee < 3 && jn >= 0 && jn < 16) {
        if constexpr (jn & 1) cB[(ee + 1) & 1][jn >> 1] = CV_B(ee + 1, jn >> 1);
        else cA[(ee + 1) & 1][jn >> 1] = CV_A(ee + 1, jn >> 1);
      }
      if constexpr (ee == 3 && j < 4) ue[j] = *(const v2u*)(smem + LDS_U + cch * 4096 + (eh * 4 + j) * 512 + (c * 16 + q * 4) * 2);
      if constexpr (g >= 4 && g < 36 && (g & 3) == 0) {
        constexpr int i = (g - 4) >> 4, jj = ((g - 4) >> 2) & 3;
        const f32x2 x = unpk(wt[i][jj]);
        const f32x2 g2 = (jj & 1) ? hi2(gs[jj >> 1]) : lo2(gs[jj >> 1]);
        const f32x2 s2 = (jj & 1) ? hi2(gs[2 + (jj >> 1)]) : lo2(gs[2 + (jj >> 1)]);
        wo_[jj] = pk2(x * g2);
        if constexpr (jj == 0) { cs[i] = unpk(wo_[jj]); bs[i] = x * s2; }
        else { cs[i] += unpk(wo_[jj]); bs[i] = fma2(x, s2, bs[i]); }
        PIN(cs[i]); PIN(bs[i]);
        if constexpr (jj == 3) st128<SC1>(wo_, rws, l16 + i * 16384, WS_WP + wp_fr);
      }
      if constexpr (g == 38) {
        const float t2 = xrow_sum4(cs[0][0] + cs[0][1], bs[0][0] + bs[0][1], cs[1][0] + cs[1][1], bs[1][0] + bs[1][1]);
        ((float*)(smem + LDS_S + 4096))[(w * 16 + c) * 4 + q] = t2;
      }
      if constexpr (g == 46) bar();
      if constexpr (g == 47) {
#pragma unroll
        for (int k = 0; k < 4; ++k) cp[k] = *(const f32x2*)(smem + LDS_S + 4096 + (k * 16 + c) * 16 + (q & 1) * 8);
      }
      if constexpr (g == 54) {
        const f32x2 sx = (cp[0] + cp[1]) + (cp[2] + cp[3]);
        st64<SC1>(v2u{__float_as_uint(sx[0]), __float_as_uint(sx[1])}, rws, tid < 32 ? tid * 32 : OOB, WS_CSB + wpr * 1024 + wkg * 8 + wp_oob);
      }
      if constexpr (g == 1 || g == 5) st128<SC1>(mk, rws, l16 + (g == 5) * 1024, WS_X1T + dk * 4096 + eh * 2048);
      if constexpr (g == 9) __builtin_amdgcn_raw_buffer_store_b32(~0u, rws, lane * 4, WS_ST2 + ((mt * 2 + kh) * 16 + nt * 2 + uh) * 256, SC1);
      if constexpr (g == 42) { if (b == 0 && tid == 0) *(unsigned*)(p.ws + WS_EPOCH) = tag; }
      if constexpr (g == 36) STAMP(1, 40);
      if constexpr (g == 72) STAMP(1, 41);
      if constexpr (g == FLAG_AT) {
        STAMP(1, 42);
        asm volatile("s_waitcnt vmcnt(%0)" ::"i"((FLAG_AT - 57 + 5) / 6) : "memory");
        __builtin_amdgcn_s_barrier();
        if (tid < 8) hint_add(p.ws, tid * 8 + (idx & 7));
        STAMP(1, 43);
      }
      if constexpr (g % 6 == 3) LOAD_WSPN(WSP_P0 + 4 + g / 6);
      SB();
    });
  }
  STAMP(1, 2);
  {
    f32x4* red = (f32x4*)(smem + LDS_X) + w * 256 + lane;
    f32x4 keep[4];
    if (eh) {
#pragma unroll
      for (int t = 0; t < 4; ++t) { red[t * 64] = cacc[t]; keep[t] = cacc[4 + t]; PIN(keep[t]); }
    } else {
#pragma unroll
      for (int t = 0; t < 4; ++t) { red[t * 64] = cacc[4 + t]; keep[t] = cacc[t]; PIN(keep[t]); }
    }
    f32x2 pre[4][2];
#pragma unroll
    for (int t = 0; t < 4; ++t) {
      pre[t][0] = unpk(ue[t][0]) + lo2(keep[t]);
      pre[t][1] = unpk(ue[t][1]) + hi2(keep[t]);
      PIN(pre[t][0]); PIN(pre[t][1]);
    }
    const unsigned xoff = ((c >> 1) * 32 + ((q & 1) * 4 + 2 * (c & 1) + (q >> 1)) * 4) * 2;
    bar();
    const f32x4* rd = (const f32x4*)(smem + LDS_X) + (w ^ 1) * 256 + lane;
    f32x4 oth[4];
#pragma unroll
    for (int t = 0; t < 4; ++t) oth[t] = rd[t * 64];
#pragma unroll
    for (int t = 0; t < 4; ++t) {
      v2u o;
      o[0] = nomark(pk2(pre[t][0] + lo2(oth[t])));
      o[1] = nomark(pk2(pre[t][1] + hi2(oth[t])));
      st64<SC1>(o, rws, xoff + t * 512, WS_X1T + (d0 + cch) * 4096 + eh * 2048);
    }
  }
  STAMP(1, 3);
  SB();
  v4u wf[3][8];
  const unsigned rot = uh * 4;
#define LOAD_WP(half, k, i) ld128<SC1>(rws, l16 + ((i) & 3) * 1024, WS_WP + (((half) * 44 + ub_own + (k)) * 16 + kh * 8 + (((i) & 4) ^ rot)) * 1024 + ((k) == 2 ? oob3 : 0u))
#define LOAD_XS(i) ld128<SC1>(rws, wsp_v, WS_X1T + ((i) * 32 + w * 8) * 4096 + mt * 128)
  unsigned char* wslot = smem + LDS_W + w * 16384;
  const int wr_off = (lane >> 3) * 128 + (((lane & 7) ^ ((lane >> 4) & 1)) * 16);
#define PARK1(ck, slot, i) *(v4u*)((slot) + (i) * 1024 + (wr_off ^ (((i) & 3) * 32))) = wr[ck][i]
#define PARK_WSP(ck, slot) _Pragma("unroll") for (int i_ = 0; i_ < 8; ++i_) PARK1(ck, slot, i_)
  PARK_WSP(0, wslot);
  PARK_WSP(1, wslot + 8192);
  v4u xs[16];
  const unsigned char* tb[2][4];
  unsigned char* xdst;
  {
    const int row = 8 * q + ((lane >> 2) & 3);
    const int sg = ((row >> 1) & 1) | (((row >> 3) & 1) << 1);
#pragma unroll
    for (int tu = 0; tu < 4; ++tu) {
      const unsigned char* t0 = smem + LDS_A + kh * 32768 + row * 128 + ((tu ^ (2 * kh) ^ sg) * 32) + (lane & 3) * 8;
      tb[0][tu] = t0 + uh * 16384;
      tb[1][tu] = t0 + (uh ^ 1) * 16384;
    }
    const int rl = w * 8 + (lane >> 3), chn = lane & 7;
    const int sg2 = ((rl >> 1) & 1) | (((rl >> 3) & 1) << 1);
    xdst = smem + LDS_A + rl * 128 + (((chn >> 1) ^ sg2) * 32) + (chn & 1) * 16;
  }
  SB();
  f32x4 cz[2][3][2];
  {
    STAMP(1, 20);
    hint_wait(rws, (lane & 7) * 128, WS_CNT + xcd * 1024, 32u);
    STAMP(1, 21);
#pragma unroll
    for (int i = 0; i < WF_EARLY; ++i)
#pragma unroll
      for (int k = 0; k < 3; ++k) wf[k][i] = LOAD_WP(1, k, i);
#pragma unroll
    for (int i = 0; i < 16; ++i) xs[i] = LOAD_XS(i);
    SB();
    STAMP(1, 44);
    unsigned m = 0;
#pragma unroll
    for (int i = 0; i < 16; ++i) {
      m = macc(m, xs[i]); *(v4u*)(xdst + i * 4096) = xs[i];
      if (WF_EARLY * 3 + i < 24) wf[(WF_EARLY * 3 + i) % 3][(WF_EARLY * 3 + i) / 3] = LOAD_WP(1, (WF_EARLY * 3 + i) % 3, (WF_EARLY * 3 + i) / 3);
      SB();
    }
    PIN(m);
    STAMP(1, 45);
    RETRY_BEGIN(m)
#pragma unroll
      for (int i = 0; i < 16; ++i) xs[i] = LOAD_XS(i);
      SB();
#pragma unroll
      for (int i = 0; i < 16; ++i) { m = macc(m, xs[i]); *(v4u*)(xdst + i * 4096) = xs[i]; }
    RETRY_END(m)
  }
  STAMP(1, 4);
  bar();
  STAMP(1, 5);
#define AHALF(tu, i, h) trread(tb[(i) >> 2][tu] + ((i) & 3) * 4096 + (h) * 512)
  f32x4 acc[3][4], s1[4], s2[4];
#pragma unroll
  for (int tu = 0; tu < 4; ++tu) {
    s1[tu] = f32x4{0.f, 0.f, 0.f, 0.f}; s2[tu] = s1[tu];
#pragma unroll
    for (int k = 0; k < 3; ++k) acc[k][tu] = s1[tu];
  }
  v4u wg[3][8];
  v2u ah[2][4][2];
#pragma unroll
  for (int tu = 0; tu < 4; ++tu) { ah[0][tu][0] = AHALF(tu, 0, 0); ah[0][tu][1] = AHALF(tu, 0, 1); }
  float* st = (float*)(smem + LDS_S);
  const v4u ones4 = {ONES, ONES, ONES, ONES};
  f32x2 csb[2][3];
  SB();
#pragma unroll
  for (int i = 0; i < 8; ++i) {
#pragma unroll
    for (int j = 0; j < 12; ++j) {
      const int tu = j / 3, k = j % 3;
      const v4u a = {ah[i & 1][tu][0][0], ah[i & 1][tu][0][1], ah[i & 1][tu][1][0], ah[i & 1][tu][1][1]};
      acc[k][tu] = mfma(a, wf[k][i], acc[k][tu]);
      if (j < 8) ah[(i + 1) & 1][j >> 1][j & 1] = AHALF(j >> 1, (i + 1) & 7, j & 1);
      if (i < 4 && k == 0) s1[tu] = mfma(ones4, a, s1[tu]);
      if (i < 4 && k == 1) s2[tu] = mfma(a, a, s2[tu]);
      if ((i == 5 || i == 6) && (j == 8 || j == 10)) {
        const int t0 = (i - 5) * 2 + ((j - 8) >> 1);
        const float dg = (c & 2) ? ((c & 1) ? s2[t0][3] : s2[t0][2]) : ((c & 1) ? s2[t0][1] : s2[t0][0]);
        float* dst = st + ((c >> 2) == q ? w * 128 + (t0 ^ (2 * kh)) * 16 + c : 1280);
        dst[0] = s1[t0][0]; dst[64] = dg;
      }
      if (i < 2 && (j & 1)) {
        const int kc = (j >> 1) % 3, ic = (j >> 1) / 3;
        cz[1 - i][kc][ic] = asf(ld128<SC1>(rws, c * 32 + ic * 16, WS_CSB + ((1 - i) * 44 + ub_own + kc) * 512 + (kc == 2 ? oob3 : 0u)));
      }
      if ((i == 2 || i == 3) && (j & 3) == 2) LOAD_WSPN(WSP_P0 + 28 + (i - 2) * 3 + (j >> 2));
      if (i == 4 && j >= 6) {
        const int hf = (j - 6) / 3;
        const f32x4 t4 = cz[hf][k][0] + cz[hf][k][1];
        csb[hf][k] = lo2(t4) + hi2(t4);
        PIN(csb[hf][k]);
      }
      if (i == 5 && (j & 3) == 1) st128<SC1>(mk, rws, l16, WS_Z2 + ((ub_own + (j >> 2)) * 64 + mt * 2 + kh) * 1024 + ((j >> 2) == 2 ? oob3 : 0u));
      if (i >= 6 && (j & 3) == 3) wg[j >> 2][i - 6] = LOAD_WP(0, j >> 2, i - 6);
      if (i == 0 && j == 11) STAMP(1, 46);
      if (i == 3 && j == 11) STAMP(1, 47);
      SB();
    }
  }
  STAMP(1, 6);
  f32x4* xch = (f32x4*)(smem + LDS_X);
#pragma unroll
  for (int k = 0; k < 3; ++k)
#pragma unroll
    for (int t = 0; t < 2; ++t) xch[(w * 6 + k * 2 + t) * 64 + lane] = acc[k][2 + t];
#pragma unroll
  for (int k = 0; k < 3; ++k) { PIN(acc[k][0]); PIN(acc[k][1]); }
  STAMP(1, 48);
  bar();
  STAMP(1, 49);
  float* fin = st + 512 + w * 64;
  f32x4 z2v[3][2];
  {
    const int row = kh * 32 + (lane & 31);
    float pa[4], pb[4];
#pragma unroll
    for (int k = 0; k < 4; ++k) { pa[k] = st[k * 128 + row]; pb[k] = st[k * 128 + 64 + row]; }
    f32x4 oth[3][2];
#pragma unroll
    for (int k = 0; k < 3; ++k)
#pragma unroll
      for (int t = 0; t < 2; ++t) oth[k][t] = xch[((w ^ 1) * 6 + k * 2 + t) * 64 + lane];
    SB();
    const float m = ((pa[0] + pa[1]) + (pa[2] + pa[3])) * (1.0f / D);
    const float r = __builtin_amdgcn_rsqf(fmaxf(((pb[0] + pb[1]) + (pb[2] + pb[3])) * (1.0f / D) - m * m, 0.f) + EPS);
    fin[lane] = lane < 32 ? r : -r * m;
    f32x4 r4[2], m4[2];
#pragma unroll
    for (int t = 0; t < 2; ++t) { r4[t] = *(const f32x4*)(fin + t * 16 + q * 4); m4[t] = *(const f32x4*)(fin + 32 + t * 16 + q * 4); }
    SB();
#pragma unroll
    for (int k = 0; k < 3; ++k)
#pragma unroll
      for (int t = 0; t < 2; ++t) { oth[k][t] += acc[k][t]; PIN(oth[k][t]); }
    SB();
    f32x4 rs[2], rq[2];
#pragma unroll
    for (int t = 0; t < 2; ++t) {
#pragma unroll
      for (int k = 0; k < 3; ++k) {
        const f32x4 a = oth[k][t];
        const f32x2 zl = gelu2(fma2(lo2(a), lo2(r4[t]), fma2(lo2(m4[t]), bc2(csb[1][k][0]), bc2(csb[1][k][1]))));
        const f32x2 zh = gelu2(fma2(hi2(a), hi2(r4[t]), fma2(hi2(m4[t]), bc2(csb[1][k][0]), bc2(csb[1][k][1]))));
        z2v[k][t] = f32x4{zl[0], zl[1], zh[0], zh[1]};
        if (k == 0) { rs[t] = z2v[k][t]; rq[t] = z2v[k][t] * z2v[k][t]; }
        else { rs[t] += z2v[k][t]; rq[t] += z2v[k][t] * z2v[k][t]; }
        PIN(rs[t]); PIN(rq[t]);
#pragma unroll
        for (int kk = 0; kk < 3; ++kk) wg[kk][2 + t * 3 + k] = LOAD_WP(0, kk, 2 + t * 3 + k);
        SB();
      }
    }
    STAMP(1, 50);
    float v8[8], v4[4], v2[2];
    const bool b3 = c & 8, b2 = c & 4, b1 = c & 2, b0 = c & 1;
#pragma unroll
    for (int r_ = 0; r_ < 4; ++r_) {
      const float a0 = rs[0][r_] + dpp<0x128>(rs[0][r_]), a1 = rs[1][r_] + dpp<0x128>(rs[1][r_]);
      const float q0 = rq[0][r_] + dpp<0x128>(rq[0][r_]), q1 = rq[1][r_] + dpp<0x128>(rq[1][r_]);
      v8[2 * r_] = b3 ? a1 : a0; v8[2 * r_ + 1] = b3 ? q1 : q0;
    }
#pragma unroll
    for (int j = 0; j < 4; ++j) {
      const float x0 = v8[j] + dpp<0x141>(v8[j]), x1 = v8[4 + j] + dpp<0x141>(v8[4 + j]);
      v4[j] = b2 ? x1 : x0;
    }
#pragma unroll
    for (int j = 0; j < 2; ++j) {
      const float x0 = v4[j] + dpp<0x4E>(v4[j]), x1 = v4[2 + j] + dpp<0x4E>(v4[2 + j]);
      v2[j] = b1 ? x1 : x0;
    }
    const float x0 = v2[0] + dpp<0xB1>(v2[0]), x1 = v2[1] + dpp<0xB1>(v2[1]);
    const float outv = b0 ? x1 : x0;
    __builtin_amdgcn_raw_buffer_store_b32(nomark(__float_as_uint(outv)), rws, lane * 4, WS_ST2 + ((mt * 2 + kh) * 16 + nt * 2 + uh) * 256, SC1);
  }
  STAMP(1, 7);
  SB();
#pragma unroll
  for (int tu = 0; tu < 4; ++tu)
#pragma unroll
    for (int k = 0; k < 3; ++k) acc[k][tu] = f32x4{0.f, 0.f, 0.f, 0.f};
  const int th = xcd & 1, dq = idx >> 3;
  v2u x1h[2][2];
  int x1o;
  {
    const int row = 8 * q + ((lane >> 2) & 3);
    const int sg = ((row >> 1) & 1) | (((row >> 3) & 1) << 1);
    x1o = LDS_A + (dq * 4 + w) * 4096 + row * 128 + (((2 * th) ^ sg) * 32) + (lane & 3) * 8;
  }
  f32x4 pt[4];
  v2u gq[4];
  unsigned bspq[2];
#define LOAD_PT(i) asf(ld128<SC1>(rws, l16 + (i) * 1024, WS_ST2 + (mt * 2 + kh) * 4096))
  SB();
#pragma unroll
  for (int i = 0; i < 8; ++i) {
#pragma unroll
    for (int j = 0; j < 12; ++j) {
      const int tu = j / 3, k = j % 3;
      const v4u a = {ah[i & 1][tu][0][0], ah[i & 1][tu][0][1], ah[i & 1][tu][1][0], ah[i & 1][tu][1][1]};
      acc[k][tu] = mfma(wg[k][i], a, acc[k][tu]);
      if (j < 8 && i < 7) ah[(i + 1) & 1][j >> 1][j & 1] = AHALF(j >> 1, i + 1, j & 1);
      if (j < 4 && i == 7) x1h[j >> 1][j & 1] = trread(smem + (x1o ^ ((j >> 1) * 32)) + (j & 1) * 512);
      if (i < 6 && (j == 1 || j == 5 || j == 9)) LOAD_WSPN(WSP_P0 + 34 + i * 3 + (j >> 2));
      if (j >= 8 && i == 6) pt[j - 8] = LOAD_PT(j - 8);
      if (OPT_D && i == 0 && j >= 6 && j < 10) gq[j - 6] = ld64<SC1>(rws, (lane & 31) * 8, WS_MODG + ((j - 6) * 3072 + 5 * 512 + (dq * 4 + w) * 32) * 8);
      if (OPT_D && i == 0 && j >= 10) bspq[j - 10] = p.bsp[mt * 64 + perm(2 * kh + j - 10, c)];
      if (OPT_D && i == 4 && j == 10) st[1408 + w * 32 + (lane & 31)] = rbf((__uint_as_float(gq[0][0]) + __uint_as_float(gq[1][0])) + (__uint_as_float(gq[2][0]) + __uint_as_float(gq[3][0])));
      SB();
    }
  }
  STAMP(1, 8);
  {
    unsigned m = 0;
#pragma unroll
    for (int i = 0; i < 4; ++i) m = macc(m, __builtin_bit_cast(v4u, pt[i]));
    RETRY_BEGIN(m)
#pragma unroll
      for (int i = 0; i < 4; ++i) { pt[i] = LOAD_PT(i); m = macc(m, __builtin_bit_cast(v4u, pt[i])); }
    RETRY_END(m)
    bar();
    if (tid < 4) hint_add(p.ws, 64 + (nt * 4 + (mt >> 3)) * 4 + tid);
    const f32x4 sx = (pt[0] + pt[1]) + (pt[2] + pt[3]);
    const float tv = xrow_sum4(sx[0], sx[2], sx[1], sx[3]);
    auto sw = __builtin_amdgcn_permlane32_swap(__float_as_uint(tv), __float_as_uint(tv), false, false);
    const float mm = __uint_as_float(sw[0]) * (1.0f / FH);
    const float r = __builtin_amdgcn_rsqf(fmaxf(__uint_as_float(sw[1]) * (1.0f / FH) - mm * mm, 0.f) + EPS);
    float* f2 = st + 768 + w * 64;
    f2[(q >> 1) * 32 + 2 * c + (q & 1)] = (q >> 1) ? -r * mm : r;
    f32x4 r4[2], m4[2];
#pragma unroll
    for (int t = 0; t < 2; ++t) { r4[t] = *(const f32x4*)(f2 + q * 8 + t * 4); m4[t] = *(const f32x4*)(f2 + 32 + q * 8 + t * 4); }
#pragma unroll
    for (int k = 0; k < 3; ++k) {
      v4u o;
#pragma unroll
      for (int t = 0; t < 2; ++t) {
        o[2 * t] = nomark(pk2(fma2(lo2(z2v[k][t]), lo2(r4[t]), lo2(m4[t]))));
        o[2 * t + 1] = nomark(pk2(fma2(hi2(z2v[k][t]), hi2(r4[t]), hi2(m4[t]))));
      }
      st128<SC1>(o, rws, l16, WS_Z2 + ((ub_own + k) * 64 + mt * 2 + kh) * 1024 + (k == 2 ? oob3 : 0u));
    }
  }
  STAMP(1, 9);
  SB();
  unsigned z1p[3][2][2];
  unsigned char* wslot2 = smem + LDS_A + w * 18432;
  v4u wo[2][22];
#define LOAD_WO(f) wo[(f) & 1][(f) >> 1] = ld128<SC1>(rws, l16 + (((f) >> 1) & 3) * 1024, WS_WOT + (((dq * 4 + w) * 2 + ((f) & 1)) * 22 + (((f) >> 1) & ~3)) * 1024)
  f32x4 z1a[3][2];
  {
#pragma unroll
    for (int i = WSP_P0 + 52; i < 64; ++i) if (!OPT_D) LOAD_WSPN(i);
#pragma unroll
    for (int i = 0; i < 4; ++i) if (!OPT_D) gq[i] = ld64<SC1>(rws, (lane & 31) * 8, WS_MODG + (i * 3072 + 5 * 512 + (dq * 4 + w) * 32) * 8);
#pragma unroll
    for (int t = 0; t < 2; ++t) if (!OPT_D) bspq[t] = p.bsp[mt * 64 + perm(2 * kh + t, c)];
    float* c1s = st + 1024 + w * 96;
#pragma unroll
    for (int k = 0; k < 3; ++k) c1s[k * 32 + (q & 1) * 16 + c] = (q & 1) ? csb[0][k][1] : csb[0][k][0];
#pragma unroll
    for (int k = 0; k < 3; ++k)
#pragma unroll
      for (int t = 0; t < 2; ++t) xch[(w * 6 + k * 2 + t) * 64 + lane] = acc[k][2 + t];
    SB();
    bar();
    STAMP(1, 34);
    f32x4 oth[3][2], cs1[3], bs1[3];
    float r1[2], m1[2];
#pragma unroll
    for (int t = 0; t < 2; ++t) { r1[t] = fin[t * 16 + c]; m1[t] = fin[32 + t * 16 + c]; }
#pragma unroll
    for (int k = 0; k < 3; ++k) {
      cs1[k] = *(const f32x4*)(c1s + k * 32 + q * 4); bs1[k] = *(const f32x4*)(c1s + k * 32 + 16 + q * 4);
#pragma unroll
      for (int t = 0; t < 2; ++t) oth[k][t] = xch[((w ^ 1) * 6 + k * 2 + t) * 64 + lane];
    }
    SB();
    PARK_WSP(2, wslot2);
#pragma unroll
    for (int k = 0; k < 3; ++k)
#pragma unroll
      for (int t = 0; t < 2; ++t) {
        const f32x4 a = acc[k][t] + oth[k][t];
        const f32x2 lo = fma2(lo2(a), bc2(r1[t]), fma2(lo2(cs1[k]), bc2(m1[t]), lo2(bs1[k])));
        const f32x2 hi = fma2(hi2(a), bc2(r1[t]), fma2(hi2(cs1[k]), bc2(m1[t]), hi2(bs1[k])));
        z1a[k][t] = f32x4{lo[0], lo[1], hi[0], hi[1]};
        PIN(z1a[k][t]);
      }
  }
  STAMP(1, 10);
  SB();
  f32x4 ac2[6][4];
#pragma unroll
  for (int i = 0; i < 6; ++i)
#pragma unroll
    for (int j = 0; j < 4; ++j) { const float b0 = (i < 3 && j < 2) ? bf2f(bspq[j & 1]) : 0.f; ac2[i][j] = f32x4{b0, b0, b0, b0}; }
  int rd_off[4];
#pragma unroll
  for (int tu = 0; tu < 4; ++tu) rd_off[tu] = perm(tu ^ (2 * kh), c) * 128 + ((q ^ (c >> 1)) * 16);
  constexpr int ZD = 3;
  v4u zf[ZD][6];
  v4u wfr[2][2][4];
#define LOAD_Z(ks, i) ld128<SC1>(rws, l16 + ((((ks) >> 1) + kr) & 7) * 2048 + ((ks) & 1) * 1024, WS_Z2 + ((((i) < 3 ? ub_own : ub_oth) + (i) % 3) * 64 + w * 16) * 1024 + ((i) % 3 == 2 ? oob3 : 0u))
#pragma unroll
  for (int k2 = 0; k2 < 2; ++k2)
#pragma unroll
    for (int tu = 0; tu < 4; ++tu) wfr[0][k2][tu] = *(const v4u*)(wslot + (rd_off[tu] ^ (k2 * 64)));
  PARK_WSP(3, wslot);
#define WSLOT(ck) ((ck) % 3 == 2 ? wslot2 : wslot + ((ck) % 3) * 8192)
  hint_wait(rws, 0u, WS_CNT + (64 + (nt * 4 + w) * 4 + (xcd >> 1)) * 128, 8u);
  STAMP(1, 22);
#pragma unroll
  for (int s_ = 0; s_ < ZD; ++s_)
#pragma unroll
    for (int i = 0; i < 6; ++i) zf[s_][i] = LOAD_Z(s_, i);
  SB();
  STAMP(1, 11);
  unsigned zm = 0;
  sfor<0, 16 * 24>([&](auto G) __attribute__((always_inline)) {
    constexpr int ks = decltype(G)::value / 24, j = decltype(G)::value % 24, ck = ks >> 1, k2 = ks & 1, i = j >> 2, tu = j & 3;
    ac2[i][tu] = mfma(zf[ks % ZD][i], wfr[ck & 1][k2][tu], ac2[i][tu]);
    if constexpr (j == 0 && (ks == 1 || ks == 4 || ks == 8 || ks == 12)) STAMP(1, 56 + (ks >> 2));
    if constexpr (tu == 1) { zm = macc(zm, zf[ks % ZD][i]); PIN(zm); }
    if constexpr (ks < 12 && j == 19) {
      constexpr int k = ks >> 2, t = (ks >> 1) & 1;
      z1p[k][t][ks & 1] = pk2(gelu2((ks & 1) ? hi2(z1a[k][t]) : lo2(z1a[k][t])));
      PIN(z1p[k][t][ks & 1]);
    }
    if constexpr (OPT_D && ks < 4 && (j == 9 || j == 21)) LOAD_WSPN(WSP_P0 + 52 + ks * 2 + (j == 21));
    if constexpr (!OPT_D && ks == 12 && j == 19)
      st[1408 + w * 32 + (lane & 31)] = rbf((__uint_as_float(gq[0][0]) + __uint_as_float(gq[1][0])) + (__uint_as_float(gq[2][0]) + __uint_as_float(gq[3][0])));
    if constexpr (ks >= 5 && ks < 5 + WO0 / 2 && (j == 9 || j == 21)) LOAD_WO((ks - 5) * 2 + (j == 21));
    if constexpr (j == 17 && ks >= 2 && ks < 5) st128<SC1>(mk, rws, l16, WS_V + ((mt * 4 + w) * 22 + pr0 + ks - 2) * 1024 + (ks == 4 ? oob3 : 0u));
    if constexpr (!OPT_C && j == 1 && ks == HINT4_KS) {
      asm volatile("s_waitcnt vmcnt(20)" ::: "memory");
      __builtin_amdgcn_s_barrier();
      if (tid < 2) hint_add(p.ws, 192 + mt * 2 + tid);
    }
    if constexpr (tu == 3) {
      if constexpr (ks + ZD < 16) zf[ks % ZD][i] = LOAD_Z(ks + ZD, i);
      else if constexpr (ks > 16 - ZD) LOAD_WO(WO0 + (ks + ZD - 17) * 6 + i);
    }
    if constexpr ((j & 1) == 0 && j < 16 && ck < 7) {
      constexpr int jj = j >> 1;
      if constexpr (k2 == 0) { if constexpr (ck >= 1 && ck + 3 < 8) PARK1(ck + 3, WSLOT(ck), jj); }
      else wfr[(ck + 1) & 1][jj >> 2][jj & 3] = *(const v4u*)(WSLOT(ck + 1) + (rd_off[jj & 3] ^ ((jj >> 2) * 64)));
    }
    SB();
  });
  if (__builtin_expect(any_mark(zm), 0)) {
    int l2 = lane;
    PIN(l2);
    const int c2 = l2 & 15, q2 = l2 >> 4;
#pragma unroll
    for (int i = 0; i < 6; ++i)
#pragma unroll
      for (int j = 0; j < 4; ++j) { const float b0 = (i < 3 && j < 2) ? bf2f(p.bsp[mt * 64 + perm(2 * kh + (j & 1), c2)]) : 0.f; ac2[i][j] = f32x4{b0, b0, b0, b0}; }
#pragma unroll 1
    for (int ks = 0; ks < 16; ++ks) {
      v4u zz[6], ww[4];
      unsigned sp = 0;
      do {
        MEMFENCE();
        zm = 0;
#pragma unroll
        for (int i = 0; i < 6; ++i) {
          zz[i] = ld128<SC1>(rws, l16 + ks * 1024, WS_Z2 + (((i < 3 ? ub_own : ub_oth) + i % 3) * 64 + w * 16) * 1024 + (i % 3 == 2 ? oob3 : 0u));
          zm = macc(zm, zz[i]);
        }
      } while (any_mark(zm) & (++sp <= SPIN_CAP));
#pragma unroll
      for (int tu = 0; tu < 4; ++tu) ww[tu] = ld128<0>(rwsp, perm(tu ^ (2 * kh), c2) * 4096 + q2 * 16 + ks * 64, wsp_s);
#pragma unroll
      for (int i = 0; i < 6; ++i)
#pragma unroll
        for (int tu = 0; tu < 4; ++tu) ac2[i][tu] = mfma(zz[i], ww[tu], ac2[i][tu]);
    }
  }
  STAMP(1, 12);
  constexpr int WO1 = WO0 + (ZD - 1) * 6;
  {
    f32x4* red = (f32x4*)(smem + LDS_A);
#pragma unroll
    for (int i = 0; i < 6; ++i)
#pragma unroll
      for (int tu = 0; tu < 4; ++tu) {
        const int rel = (tu >> 1) | ((i / 3) << 1);
        if (rel) red[((w * 3 + rel - 1) * 6 + (i % 3) * 2 + (tu & 1)) * 64 + lane] = ac2[i][tu];
      }
#pragma unroll
    for (int f = WO1; f < WO1 + 10 && f < 44; ++f) LOAD_WO(f);
    f32x4 own[3][2];
#pragma unroll
    for (int k = 0; k < 3; ++k)
#pragma unroll
      for (int t = 0; t < 2; ++t) { own[k][t] = ac2[k][t]; PIN(own[k][t]); }
    STAMP(1, 51);
    if (OPT_C) asm volatile("s_waitcnt vmcnt(40)" ::: "memory");
    bar();
    if (OPT_C && tid < 2) hint_add(p.ws, 192 + mt * 2 + tid);
    STAMP(1, 52);
    f32x4 o3[3][2][3];
#pragma unroll
    for (int k = 0; k < 3; ++k)
#pragma unroll
      for (int t = 0; t < 2; ++t)
#pragma unroll
        for (int rel = 1; rel < 4; ++rel) o3[k][t][rel - 1] = red[(((w ^ rel) * 3 + rel - 1) * 6 + k * 2 + t) * 64 + lane];
    SB();
#pragma unroll
    for (int k = 0; k < 3; ++k) {
#pragma unroll
      for (int t = 0; t < 2; ++t) {
        const f32x4 g = (own[k][t] + o3[k][t][0]) + (o3[k][t][1] + o3[k][t][2]);
        v2u o;
        o[0] = nomark(pk2(unpk(z1p[k][t][0]) * lo2(g)));
        o[1] = nomark(pk2(unpk(z1p[k][t][1]) * hi2(g)));
        const int uu = ub_own + k;
        st64<SC1>(o, rws, l16, WS_V + ((mt * 4 + 2 * kh + t) * 22 + (uu >> 1)) * 1024 + (uu & 1) * 8 + (k == 2 ? oob3 : 0u));
      }
    }
  }
  STAMP(1, 13);
  SB();
#pragma unroll
  for (int f = WO1 + 10; f < 44; ++f) LOAD_WO(f);
  SB();
  STAMP(1, 53);
  hint_wait(rws, 0u, WS_CNT + (192 + mt * 2 + th) * 128, 8u);
  STAMP(1, 23);
  {
    v4u vs[11];
    unsigned spins = 0;
    for (;;) {
      MEMFENCE();
#pragma unroll
      for (int i = 0; i < 11; ++i) vs[i] = ld128<SC1>(rws, l16 + (i & 3) * 1024, WS_V + ((mt * 4 + 2 * th) * 22 + w * 11 + (i & ~3)) * 1024);
      SB();
      unsigned m = 0;
#pragma unroll
      for (int i = 0; i < 11; ++i) { m = macc(m, vs[i]); *(v4u*)(smem + LDS_W + (w * 11 + i) * 1024 + l16) = vs[i]; }
      if (!(any_mark(m) & (++spins <= SPIN_CAP))) break;
    }
  }
  STAMP(1, 14);
  bar();
  f32x4 a3[2][2];
#pragma unroll
  for (int e = 0; e < 2; ++e)
#pragma unroll
    for (int t = 0; t < 2; ++t) a3[e][t] = f32x4{0.f, 0.f, 0.f, 0.f};
  STAMP(1, 54);
  constexpr int VD = 8;
  v4u vf[VD];
#define VFRAG(f) (*(const v4u*)(smem + LDS_W + (((f) & 1) * 22 + ((f) >> 1)) * 1024 + l16))
#pragma unroll
  for (int f = 0; f < VD; ++f) vf[f] = VFRAG(f);
  const f32x4 gbl = *(const f32x4*)(st + 1408 + w * 32 + q * 8), gbh = *(const f32x4*)(st + 1408 + w * 32 + q * 8 + 4);
  f32x2 x1f[2][4];
  SB();
#pragma unroll
  for (int f = 0; f < 44; ++f) {
    const v4u cur = vf[f % VD];
    a3[0][f & 1] = mfma(wo[0][f >> 1], cur, a3[0][f & 1]);
    a3[1][f & 1] = mfma(wo[1][f >> 1], cur, a3[1][f & 1]);
    if (f + VD < 44) vf[f % VD] = VFRAG(f + VD);
    if (f >= 36) { x1f[(f - 36) >> 2][f & 3] = unpk(x1h[(f - 36) >> 2][(f >> 1) & 1][f & 1]); PIN(x1f[(f - 36) >> 2][f & 3]); }
    SB();
  }
  STAMP(1, 55);
  const rsrc_t rout = mkrs(p.out, L * D * 2);
#pragma unroll
  for (int t = 0; t < 2; ++t) {
    v4u o;
    o[0] = pk2(fma2(lo2(a3[0][t]), lo2(gbl), x1f[t][0]));
    o[1] = pk2(fma2(hi2(a3[0][t]), hi2(gbl), x1f[t][1]));
    o[2] = pk2(fma2(lo2(a3[1][t]), lo2(gbh), x1f[t][2]));
    o[3] = pk2(fma2(hi2(a3[1][t]), hi2(gbh), x1f[t][3]));
    st128<0>(o, rout, perm(t, c) * 1024 + q * 16, (mt * 64 + th * 32) * 1024 + (dq * 4 + w) * 64);
  }
  STAMP(1, 15);
  STAMP_FLUSH(1);
  asm volatile("adaln_code_end:");
}

extern "C" void kernel_launch(void* const* d_in, const int* in_sizes, int n_in, void* d_out, int out_size, void* d_ws,
                              size_t ws_size, hipStream_t stream) {
  Params p;
  p.u = (const u16*)d_in[0];
  p.lar = (const u16*)d_in[1];
  p.aim = (const u16*)d_in[2];
  p.cre = (const u16*)d_in[3];
  p.cim = (const u16*)d_in[4];
  p.ldt = (const u16*)d_in[5];
  p.dsk = (const u16*)d_in[6];
  p.win = (const u16*)d_in[8];
  p.wsp = (const u16*)d_in[9];
  p.bsp = (const u16*)d_in[10];
  p.wout = (const u16*)d_in[11];
  p.c = (const u16*)d_in[12];
  p.wmod = (const u16*)d_in[13];
  p.out = (u16*)d_out;
  p.ws = (unsigned char*)d_ws;
  (void)hipFuncSetAttribute((const void*)adaln_mega, hipFuncAttributeMaxDynamicSharedMemorySize, MEGA_LDS);
  adaln_mega<<<256, 256, MEGA_LDS, stream>>>(p);
}
```

```cpp
#define XI_MID 0
#define WSP_P0 4
#include <hip/hip_runtime.h>
#include <hip/hip_bf16.h>
#include <stdint.h>

#define LAS __attribute__((address_space(3)))
typedef unsigned v4u __attribute__((ext_vector_type(4)));
typedef unsigned v2u __attribute__((ext_vector_type(2)));
typedef float f32x4 __attribute__((ext_vector_type(4)));
typedef float f32x2 __attribute__((ext_vector_type(2)));
typedef short bf16x8 __attribute__((ext_vector_type(8)));
typedef _Float16 f16x8 __attribute__((ext_vector_type(8)));
typedef _Float16 f16x2 __attribute__((ext_vector_type(2)));
typedef __bf16 bf16x2_t __attribute__((ext_vector_type(2)));
typedef unsigned short u16;
typedef __amdgpu_buffer_rsrc_t rsrc_t;

#define STAMP_DECL do { } while (0)
#define STAMP(k, i) do { } while (0)
#define STAMP_FLUSH(k) do { } while (0)

#define OPT_A 1
#define OPT_B 1
#define OPT_C 1
#define OPT_D 0
#define OPT_E 2
#define OPT_F 1
constexpr int L = 2048, D = 512, NM = 128, FH = 688, FF = 1376;
constexpr float EPS = 1e-5f;
constexpr int SC1 = 16;
constexpr unsigned OOB = 0x40000000u;
constexpr unsigned SPIN_CAP = 1u << 14;

constexpr unsigned WS_EPOCH = 0;
constexpr unsigned WS_CNT = 4096;
constexpr unsigned WS_SR = WS_CNT + 256 * 128;
constexpr unsigned WS_SB = WS_SR + 2048 * 8;
constexpr unsigned WS_MODG = WS_SB + 2048 * 8;
constexpr unsigned WS_CSB = WS_MODG + 3072 * 32;
constexpr unsigned WS_ST2 = WS_CSB + 88 * 16 * 32;
constexpr unsigned WS_UG = 1u << 20;
static_assert(WS_ST2 + 32 * 2 * 16 * 32 * 8 <= WS_UG, "small buffers");
constexpr unsigned WS_X1T = WS_UG + 256 * 2048 * 8;
constexpr unsigned WS_WP = WS_X1T + 512 * 2048 * 2;
constexpr unsigned WS_WOT = WS_WP + 88 * 16 * 1024;
constexpr unsigned WS_Z2 = WS_WOT + 32 * 22 * 1024;
constexpr unsigned WS_V = WS_Z2 + 44 * 64 * 1024;
constexpr unsigned WS_END = WS_V + 128 * 22 * 1024;

struct Params {
  const u16 *u, *lar, *aim, *cre, *cim, *ldt, *dsk, *win, *wsp, *bsp, *wout, *c, *wmod;
  u16* out;
  unsigned char* ws;
};

#define DI __device__ __forceinline__
template <int V> struct IC { static constexpr int value = V; };
template <int I, int N, class F> DI void sfor(F&& f) {
  if constexpr (I < N) { f(IC<I>{}); sfor<I + 1, N>(f); }
}
DI float bf2f(unsigned b) { return __builtin_bit_cast(float, b << 16); }
DI float bflo(unsigned w) { return __builtin_bit_cast(float, w << 16); }
DI float bfhi(unsigned w) { return __builtin_bit_cast(float, w & 0xFFFF0000u); }
DI f32x2 unpk(unsigned w) { return f32x2{bflo(w), bfhi(w)}; }
DI unsigned pk2(f32x2 v) { return __builtin_bit_cast(unsigned, __builtin_convertvector(v, bf16x2_t)); }
DI unsigned pk2(float lo, float hi) { return pk2(f32x2{lo, hi}); }
DI unsigned nomark(unsigned x) { return x < 0xFFFFFFFEu ? x : 0xFFFFFFFEu; }
DI float rbf(float x) { return bflo(pk2(x, 0.f)); }
DI f32x2 fma2(f32x2 a, f32x2 b, f32x2 c) { return __builtin_elementwise_fma(a, b, c); }
DI f32x2 lo2(f32x4 v) { return f32x2{v[0], v[1]}; }
DI f32x2 hi2(f32x4 v) { return f32x2{v[2], v[3]}; }
DI f32x2 bc2(float x) { return f32x2{x, x}; }
DI f32x4 mfma(v4u a, v4u b, f32x4 c) {
  return __builtin_amdgcn_mfma_f32_16x16x32_bf16(__builtin_bit_cast(bf16x8, a), __builtin_bit_cast(bf16x8, b), c, 0, 0, 0);
}
DI f32x4 mfma_h(v4u a, v4u b, f32x4 c) {
  return __builtin_amdgcn_mfma_f32_16x16x32_f16(__builtin_bit_cast(f16x8, a), __builtin_bit_cast(f16x8, b), c, 0, 0, 0);
}
DI float dot2(unsigned a, unsigned b, float acc) {
  return __builtin_amdgcn_fdot2_f32_bf16(__builtin_bit_cast(bf16x2_t, a), __builtin_bit_cast(bf16x2_t, b), acc, false);
}
constexpr unsigned ONES = 0x3F803F80u;
DI int perm(int u, int c) { return 32 * (u >> 1) + 8 * (c >> 2) + 4 * (u & 1) + (c & 3); }
template <int CTRL> DI float dpp(float x) {
  return __builtin_bit_cast(float, __builtin_amdgcn_mov_dpp(__builtin_bit_cast(int, x), CTRL, 0xf, 0xf, true));
}
DI float row16_sum(float x) {
  x += dpp<0xB1>(x); x += dpp<0x4E>(x); x += dpp<0x141>(x); x += dpp<0x128>(x);
  return x;
}
DI float row16_max(float x) {
  x = fmaxf(x, dpp<0xB1>(x)); x = fmaxf(x, dpp<0x4E>(x)); x = fmaxf(x, dpp<0x141>(x)); x = fmaxf(x, dpp<0x128>(x));
  return x;
}
DI float xrow_sum(float x) {
  auto s = __builtin_amdgcn_permlane16_swap(__float_as_uint(x), __float_as_uint(x), false, false);
  x = __uint_as_float(s[0]) + __uint_as_float(s[1]);
  auto t = __builtin_amdgcn_permlane32_swap(__float_as_uint(x), __float_as_uint(x), false, false);
  return __uint_as_float(t[0]) + __uint_as_float(t[1]);
}
DI float xrow_sum4(float a, float b, float c, float d) {
  auto s = __builtin_amdgcn_permlane16_swap(__float_as_uint(a), __float_as_uint(b), false, false);
  auto t = __builtin_amdgcn_permlane16_swap(__float_as_uint(c), __float_as_uint(d), false, false);
  const float x = __uint_as_float(s[0]) + __uint_as_float(s[1]), y = __uint_as_float(t[0]) + __uint_as_float(t[1]);
  auto z = __builtin_amdgcn_permlane32_swap(__float_as_uint(x), __float_as_uint(y), false, false);
  return __uint_as_float(z[0]) + __uint_as_float(z[1]);
}
DI float xrow_max(float x) {
  auto s = __builtin_amdgcn_permlane16_swap(__float_as_uint(x), __float_as_uint(x), false, false);
  x = fmaxf(__uint_as_float(s[0]), __uint_as_float(s[1]));
  auto t = __builtin_amdgcn_permlane32_swap(__float_as_uint(x), __float_as_uint(x), false, false);
  return fmaxf(__uint_as_float(t[0]), __uint_as_float(t[1]));
}
DI f32x2 gelu2(f32x2 x) {
  const float k0 = -2.0f * 0.7978845608028654f * 1.4426950408889634f, k1 = k0 * 0.044715f;
  const f32x2 a = x * fma2(x * x, bc2(k1), bc2(k0));
  const f32x2 e = f32x2{__builtin_amdgcn_exp2f(a[0]), __builtin_amdgcn_exp2f(a[1])} + bc2(1.0f);
  return x * f32x2{__builtin_amdgcn_rcpf(e[0]), __builtin_amdgcn_rcpf(e[1])};
}
DI rsrc_t mkrs(const void* ptr, unsigned bytes) { return __builtin_amdgcn_make_buffer_rsrc((void*)ptr, 0, (int)bytes, 0x00020000); }
template <int AUX> DI v4u ld128(rsrc_t rs, unsigned v, unsigned s) { return __builtin_amdgcn_raw_buffer_load_b128(rs, (int)v, (int)s, AUX); }
template <int AUX> DI v2u ld64(rsrc_t rs, unsigned v, unsigned s) { return __builtin_amdgcn_raw_buffer_load_b64(rs, (int)v, (int)s, AUX); }
template <int AUX> DI void st128(v4u x, rsrc_t rs, unsigned v, unsigned s) { __builtin_amdgcn_raw_buffer_store_b128(x, rs, (int)(v + s), 0, AUX); }
template <int AUX> DI void st64(v2u x, rsrc_t rs, unsigned v, unsigned s) { __builtin_amdgcn_raw_buffer_store_b64(x, rs, (int)v, (int)s, AUX); }
DI f32x4 asf(v4u x) { return __builtin_bit_cast(f32x4, x); }
DI float sum4(f32x4 x) { return (x[0] + x[1]) + (x[2] + x[3]); }
DI unsigned umax3(unsigned a, unsigned b, unsigned c) { unsigned m = a > b ? a : b; return m > c ? m : c; }
DI unsigned macc(unsigned m, v4u x) { return umax3(umax3(m, x[0], x[1]), x[2], x[3]); }
DI bool any_mark(unsigned m) { return __builtin_amdgcn_ballot_w64(m == 0xFFFFFFFFu) != 0ull; }
#define SB() __builtin_amdgcn_sched_barrier(0)
#define PIN(x) asm volatile("" : "+v"(x))
#define RETRY_BEGIN(m) if (__builtin_expect(any_mark(m), 0)) { unsigned sp_ = 0; do { MEMFENCE(); m = 0;
#define RETRY_END(m) } while (any_mark(m) & (++sp_ <= SPIN_CAP)); }
#define MEMFENCE() asm volatile("" ::: "memory")
DI void bar() { asm volatile("s_waitcnt lgkmcnt(0)" ::: "memory"); __builtin_amdgcn_s_barrier(); asm volatile("" ::: "memory"); }

DI f32x2 cmul(f32x2 a, f32x2 b) { return fma2(bc2(a[1]), f32x2{-b[1], b[0]}, bc2(a[0]) * b); }
DI f32x2 cexp_p(float are2, float rev_hi, float rev_lo, float p) {
  const float mag = __builtin_amdgcn_exp2f(are2 * p);
  const float hi = rev_hi * p;
  const float fr = (hi - __builtin_rintf(hi)) + rev_lo * p;
  return f32x2{mag * __builtin_amdgcn_cosf(fr), mag * __builtin_amdgcn_sinf(fr)};
}
DI float exp_acc(float x) {
  const float hi = x * 1.4426950408889634f;
  const float lo = __builtin_fmaf(x, 1.4426950408889634f, -hi) + x * 1.925963033500011e-8f;
  const float e = __builtin_amdgcn_exp2f(hi);
  return __builtin_fmaf(e, lo * 0.6931471805599453f, e);
}
DI unsigned pkh(float a, float b) {
  f16x2 v = {(_Float16)a, (_Float16)b};
  return __builtin_bit_cast(unsigned, v);
}

constexpr int CV_RK = 4160;
constexpr int CV_H = 2 * CV_RK;
constexpr int CV_CH = CV_H + 4608;
constexpr int LDS_A = 0;
constexpr int LDS_U = 2 * CV_CH;
constexpr int LDS_GS = LDS_U + 8192;
constexpr int LDS_TAB = 36864;
constexpr int KP_CH = 13840;
constexpr int LDS_KP = LDS_TAB + 2 * 64 * 528 + 16;
constexpr int LDS_LN = 0, LDS_MR = 8448;
constexpr int LDS_WI = 145472, LDS_WO = LDS_WI + 10240;
constexpr int LDS_X = 65536;
constexpr int LDS_S = LDS_X + 24576;
constexpr int LDS_W = LDS_S + 6144;
constexpr int MEGA_LDS = LDS_W + 65536;
static_assert(LDS_WO + 4608 <= MEGA_LDS && LDS_KP + 2 * KP_CH <= LDS_WI && LDS_S >= 73728, "lds");
static_assert(LDS_GS + 1280 <= LDS_TAB && LDS_KP + 2 * KP_CH <= MEGA_LDS && MEGA_LDS <= 160 * 1024, "lds");
#define FLAG_AT 110
static_assert(FLAG_AT > 57 && FLAG_AT < 144 && FLAG_AT % 6 != 3, "flag position");
#define WF_EARLY 3
#define WO0 16
#define HINT4_KS 15
#define IN_AUX 0
#define WSP_AUX 0

DI v2u trread(const unsigned char* ptr) {
  typedef short v4i16_t __attribute__((ext_vector_type(4)));
  return __builtin_bit_cast(v2u, __builtin_amdgcn_ds_read_tr16_b64_v4i16((LAS v4i16_t*)ptr));
}
struct CvOrder { int qq[36], pp[36]; };
constexpr CvOrder cv_order() {
  CvOrder o{};
  int n = 0;
  for (int m = 0; m < 8; ++m) {
    for (int j = 0; j < m; ++j) if (m + j < 8) { o.qq[n] = m; o.pp[n] = j; ++n; }
    for (int j = 0; j <= m; ++j) if (m + j < 8) { o.qq[n] = j; o.pp[n] = m; ++n; }
  }
  return o;
}
constexpr CvOrder CVO = cv_order();

typedef __attribute__((address_space(1))) unsigned gu32;
DI unsigned tacc(unsigned m, v4u x, unsigned tag) { return m | (x[1] ^ tag) | (x[3] ^ tag); }
DI bool any_nz(unsigned m) { return __builtin_amdgcn_ballot_w64(m != 0u) != 0ull; }
DI void hint_add(unsigned char* ws, int line) { (void)__hip_atomic_fetch_add((gu32*)(ws + WS_CNT + line * 128), 1u, __ATOMIC_RELAXED, __HIP_MEMORY_SCOPE_AGENT); }
DI void hint_wait(rsrc_t rws, unsigned voff, unsigned soff, unsigned want) {
  unsigned sp = 0;
  for (;;) {
    MEMFENCE();
    const unsigned v = __builtin_amdgcn_raw_buffer_load_b32(rws, (int)voff, (int)soff, SC1);
    if (!((__builtin_amdgcn_ballot_w64(v < want) != 0ull) & (++sp <= SPIN_CAP))) break;
    __builtin_amdgcn_s_sleep(OPT_E);
  }
  MEMFENCE();
}

extern "C" __global__ void __launch_bounds__(256, 1) adaln_mega(Params p) {
  extern __shared__ __attribute__((aligned(16))) unsigned char smem[];
  const int tid = threadIdx.x, lane = tid & 63, w = __builtin_amdgcn_readfirstlane(tid >> 6);
  const int b = blockIdx.x, xcd = b & 7, idx = b >> 3;
  const int pb = xcd * 32 + idx;
  const unsigned l16 = lane * 16;
  STAMP_DECL;
  STAMP(1, 0);
  v2u codepf;
  {
    unsigned long long pc;
    int rel_m, rel_e;
    asm volatile("s_getpc_b64 %0\n\ts_mov_b32 %1, adaln_mega@rel32@lo+4\n\ts_mov_b32 %2, adaln_code_end@rel32@lo+12" : "=s"(pc), "=s"(rel_m), "=s"(rel_e));
    const rsrc_t rcode = mkrs((const void*)(pc + (long long)rel_m), rel_e > rel_m ? (unsigned)(rel_e - rel_m) & ~15u : 0u);
    codepf = ld64<0>(rcode, tid * 8, idx * 2048);
  }
  SB();
  const int cch = w >> 1, eh = w & 1, ns = tid & 127;
  const unsigned q_cre = p.cre[pb * 256 + tid], q_cim = p.cim[pb * 256 + tid], q_lar = p.lar[pb * 256 + tid], q_aim = p.aim[pb * 256 + tid];
  const unsigned q_ldt = p.ldt[pb * 2 + cch], q_dsk = p.dsk[pb * 2 + cch];
  SB();
  const rsrc_t ru = mkrs(p.u, L * D * 2);
  v4u xr[2];
  xr[0] = ld128<IN_AUX>(ru, l16, (pb * 8 + w * 2) * 1024);
  xr[1] = ld128<IN_AUX>(ru, l16 + 1024, (pb * 8 + w * 2) * 1024);
  const int mkg = b & 3, mcg = b >> 2;
  const unsigned cq = p.c[mkg * 128 + (tid & 3) * 32 + (tid >> 3)];
  const unsigned epoch = *(const unsigned*)(p.ws + WS_EPOCH);
  const rsrc_t rws = mkrs(p.ws, WS_END);
  __builtin_amdgcn_raw_buffer_store_b32(0u, rws, tid == 0 ? 0u : OOB, WS_CNT + b * 128, SC1);
  SB();
  const rsrc_t rmod = mkrs(p.wmod, D * 3072 * 2);
  v4u xm[4];
#pragma unroll
  for (int i = 0; i < 4; ++i) xm[i] = ld128<IN_AUX>(rmod, (tid >> 3) * 6144 + (tid & 7) * 16, (mkg * 128 + i * 32) * 6144 + mcg * 128 + (b < 192 ? 0u : OOB));
  SB();
  STAMP(1, 24);
  const int c = lane & 15, q = lane >> 4;
  int pos4[9];
  {
    const float f4 = (float)(8188 - tid * 32);
#pragma unroll
    for (int j = 0; j < 10; j += 2) {
      const unsigned pp = pk2(bc2(f4) - f32x2{4.f * j, 4.f * j + 4.f});
      pos4[j] = (int)bflo(pp);
      if (OPT_A) pos4[j] += (pos4[j] >> 5) << 2;
      PIN(pos4[j]);
      if (j + 1 < 9) { pos4[j + 1] = (int)bfhi(pp); if (OPT_A) pos4[j + 1] += (pos4[j + 1] >> 5) << 2; PIN(pos4[j + 1]); }
    }
  }
  int cob[2], cos_[2];
  cob[0] = (eh ? 1024 + 512 * q : 128 * (q >> 1) + 4 * (q & 1)) * 4 + 32 * c + LDS_KP + cch * KP_CH;
  cob[1] = (q < 2 ? 256 + 128 * q : 504 + 4 * q) * 4 + 32 * c + LDS_KP + cch * KP_CH;
  cos_[0] = eh ? 512 : 4;
  cos_[1] = q < 2 ? 8 : 512;
  if (OPT_A) {
    cob[0] = (eh ? 1152 + 576 * q : 144 * (q >> 1) + 4 * (q & 1)) * 4 + 36 * c + LDS_KP + cch * KP_CH;
    cob[1] = (q < 2 ? 288 + 144 * q : 568 + 4 * q) * 4 + 36 * c + LDS_KP + cch * KP_CH;
    cos_[0] = eh ? 576 : 4;
    cos_[1] = q < 2 ? 8 : 576;
  }
  PIN(cob[0]); PIN(cob[1]); PIN(cos_[1]);
  SB();
  STAMP(1, 26);
  const unsigned wp_oob = b < 176 ? 0u : OOB;
  const int wpr = b >> 2, wkg = b & 3;
  v4u xi[2], xo;
  const int d0 = pb * 2, dk = d0 + cch;
  const int mt = (xcd >> 1) * 8 + (idx & 7), nt = (xcd & 1) * 4 + (idx >> 3);
  const int big = (nt + 2) >> 3;
  const int np = 3 - big, pr0 = 3 * nt - big * (nt - 6);
  const int kh = w & 1, uh = w >> 1;
  const int ub_own = pr0 * 2 + uh * np, ub_oth = pr0 * 2 + (uh ^ 1) * np;
  const unsigned oob3 = (unsigned)big << 30;
  const int kr = 2 * (idx >> 3) + (xcd & 1) + (OPT_B == 0 ? (idx & 7) : OPT_B == 1 ? 4 * (idx & 1) : OPT_B == 2 ? 2 * (idx & 3) : 0);
  const rsrc_t rwsp = mkrs(p.wsp, L * L * 2);
  const unsigned wp_fr = (wpr * 2 * 16 + wkg * 4 + w) * 1024 + wp_oob;
  v4u wr[8][8];
  const unsigned wsp_v = (lane >> 3) * 4096 + (lane & 7) * 16, wsp_s = mt * 64 * 4096 + w * 1024;
#define LOAD_WSP1(ck, i) wr[ck][i] = ld128<WSP_AUX>(rwsp, wsp_v + (((ck) + kr) & 7) * 128, wsp_s + (i) * 8 * 4096)
#define LOAD_WSPN(n) LOAD_WSP1((n) >> 3, (n) & 7)
  const v4u mk = {~0u, ~0u, ~0u, ~0u};
  {
    const float dt = rbf(exp_acc(bf2f(q_ldt)));
    const float cre = bf2f(q_cre) * dt, cim = bf2f(q_cim) * dt;
    const float are2 = -rbf(exp_acc(bf2f(q_lar))) * dt * 1.4426950408889634f;
    const float aim = bf2f(q_aim) * dt;
    const float rev_hi = aim * 0.15915494309189535f;
    const float rev_lo = __builtin_fmaf(aim, 0.15915494309189535f, -rev_hi) + aim * 6.4206383e-9f;
    int ex = __builtin_amdgcn_frexp_expf(xrow_max(row16_max(fmaxf(fabsf(cre), fabsf(cim))))) + 1;
    ex = ex < -100 ? -100 : ex;
    const float scale = __builtin_amdgcn_ldexpf(1.f, -ex);
    ((float*)(smem + LDS_GS + 1024))[w] = __builtin_amdgcn_ldexpf(2.f, ex);
    const f32x2 c0 = {cre * scale, cim * scale};
    const f32x2 e1 = cexp_p(are2, rev_hi, rev_lo, 1.f), e8 = cexp_p(are2, rev_hi, rev_lo, 8.f), e64 = cexp_p(are2, rev_hi, rev_lo, 64.f);
    unsigned char* tn = smem + LDS_TAB + cch * (64 * 528) + ns * 4;
#define TROW(r, z) *(unsigned*)(tn + (r) * 528) = pkh((z)[0], -(z)[1])
    SB();
    const f32x2 e128 = cmul(e64, e64), e2 = cmul(e1, e1), e256 = cmul(e128, e128), e4 = cmul(e2, e2), e512 = cmul(e256, e256);
    f32x2 f0 = {1.f, 0.f}, f1 = e64;
    f32x2 ca = cmul(c0, e512), cb = cmul(ca, e512);
#pragma unroll
    for (int j = 0; j < 8; ++j) {
      *(unsigned*)(tn + (48 + j) * 528) = pkh(f0[0], f0[1]);
      *(unsigned*)(tn + (56 + j) * 528) = pkh(f1[0], f1[1]);
      TROW(32 + j, cb);
      f0 = j == 0 ? e8 : cmul(f0, e8); f1 = cmul(f1, e8); cb = cmul(cb, e128);
      if (j < 4) {
        const f32x2 z = cmul(ca, e4);
        TROW(24 + j, ca); TROW(28 + j, z);
        ca = cmul(ca, e128);
      }
      if (j == 3 || j == 7) {
        PIN(f0); PIN(f1); PIN(cb);
        if (WSP_P0 >= 4) { LOAD_WSPN(j >> 2 << 1); LOAD_WSPN((j >> 2 << 1) + 1); }
        SB();
      }
    }
    TROW(40, cb);
    f32x2 t1[8];
    t1[1] = e1; t1[2] = e2; t1[4] = e4;
    t1[3] = cmul(e2, e1); t1[5] = cmul(e4, e1); t1[6] = cmul(e2, e4); t1[7] = cmul(t1[6], e1);
    const f32x2 c1 = cmul(c0, e128), c2 = cmul(c0, e256), c3 = cmul(c2, e128);
    TROW(0, c0); TROW(8, c1); TROW(16, c2); TROW(20, c3);
#pragma unroll
    for (int v = 1; v < 8; ++v) {
      const f32x2 z0 = cmul(t1[v], c0), z1 = cmul(t1[v], c1);
      TROW(v, z0); TROW(8 + v, z1);
      if ((v & 1) == 0) {
        const f32x2 z2 = cmul(t1[v], c2), z3 = cmul(t1[v], c3);
        TROW(16 + v / 2, z2); TROW(20 + v / 2, z3);
        if (v < 6 && WSP_P0 >= 8) { LOAD_WSPN(v + 2); LOAD_WSPN(v + 3); }
        SB();
      }
    }
  }
  STAMP(1, 29);
  {
    *(v4u*)(smem + LDS_LN + (w * 2) * 1040 + l16) = xr[0];
    *(v4u*)(smem + LDS_LN + (w * 2 + 1) * 1040 + l16) = xr[1];
    float sm[2], sq[2];
#pragma unroll
    for (int i = 0; i < 2; ++i) {
      sm[i] = dot2(xr[i][0], ONES, dot2(xr[i][1], ONES, dot2(xr[i][2], ONES, dot2(xr[i][3], ONES, 0.f))));
      sq[i] = dot2(xr[i][0], xr[i][0], dot2(xr[i][1], xr[i][1], dot2(xr[i][2], xr[i][2], dot2(xr[i][3], xr[i][3], 0.f))));
    }
    const float tv = row16_sum(xrow_sum4(sm[0], sm[1], sq[0], sq[1]));
    auto sw = __builtin_amdgcn_permlane32_swap(__float_as_uint(tv), __float_as_uint(tv), false, false);
    const float m = __uint_as_float(sw[0]) * (1.0f / D);
    const float r = __builtin_amdgcn_rsqf(fmaxf(__uint_as_float(sw[1]) * (1.0f / D) - m * m, 0.f) + EPS);
    ((float*)(smem + LDS_GS + 1088))[(q >> 1) * 8 + w * 2 + (q & 1)] = (q >> 1) ? -m * r : r;
    const float cv = bf2f(cq);
    const float sl = rbf(cv * __builtin_amdgcn_rcpf(1.0f + __builtin_amdgcn_exp2f(cv * -1.4426950408889634f)));
    const float sc[4] = {dpp<0x00>(sl), dpp<0x55>(sl), dpp<0xAA>(sl), dpp<0xFF>(sl)};
    f32x2 a[4];
#pragma unroll
    for (int i = 0; i < 4; ++i)
#pragma unroll
      for (int j = 0; j < 4; ++j) a[j] = i == 0 ? unpk(xm[i][j]) * bc2(sc[i]) : fma2(unpk(xm[i][j]), bc2(sc[i]), a[j]);
    float* red = (float*)(smem + LDS_MR) + (tid >> 3) * 68 + (tid & 7) * 8;
    *(f32x4*)red = f32x4{a[0][0], a[0][1], a[1][0], a[1][1]};
    *(f32x4*)(red + 4) = f32x4{a[2][0], a[2][1], a[3][0], a[3][1]};
  }
  const unsigned tag = epoch * 2654435761u + 0x9E3779B9u;
  STAMP(1, 28);
  asm volatile("s_waitcnt vmcnt(%0)" ::"i"(WSP_P0 + 3 * XI_MID) : "memory");
  bar();
  STAMP(1, 16);
  {
    unsigned ua[4][2];
#pragma unroll
    for (int i = 0; i < 4; ++i) {
      const unsigned char* src = smem + LDS_LN + (tid & 3) * 2080 + (i * 64 + (tid >> 2)) * 4;
      ua[i][0] = *(const unsigned*)src; ua[i][1] = *(const unsigned*)(src + 1040);
    }
    float ms8[8];
#pragma unroll
    for (int i = 0; i < 8; ++i) ms8[i] = ((const float*)(smem + LDS_MR))[(q * 8 + i) * 68 + w * 16 + c];
    const unsigned lnv = ((const unsigned*)(smem + LDS_GS + 1088))[c];
    SB();
#pragma unroll
    for (int i = 0; i < 4; ++i) st128<SC1>(v4u{ua[i][0], tag, ua[i][1], tag}, rws, (tid >> 2) * 16384 + (tid & 3) * 16, WS_UG + i * 64 * 16384 + pb * 64);
    const float s = xrow_sum(((ms8[0] + ms8[1]) + (ms8[2] + ms8[3])) + ((ms8[4] + ms8[5]) + (ms8[6] + ms8[7])));
    st64<SC1>(v2u{__float_as_uint(s), tag}, rws, q == 0 ? c * 8 : OOB, WS_MODG + (mkg * 3072 + mcg * 64 + w * 16) * 8 + (b < 192 ? 0u : OOB));
    st64<SC1>(v2u{lnv, tag}, rws, tid < 16 ? (c >> 3) * 16384 + (c & 7) * 8 : OOB, WS_SR + pb * 64);
  }
  STAMP(1, 17);
  SB();
  {
    const rsrc_t rwin = mkrs(p.win, D * FF * 2), rwout = mkrs(p.wout, FH * D * 2);
    const int hf = wpr >= 22, pp = wpr - 22 * hf;
    const unsigned v = ((tid >> 2) * (FF * 2) + (tid & 3) * 16) | ((pp == 21 ? tid & 2 : 0) << 29);
#pragma unroll
    for (int i = 0; i < 2; ++i) xi[i] = ld128<IN_AUX>(rwin, v, (wkg * 128 + i * 64) * (FF * 2) + (hf * FH + pp * 32) * 2 + wp_oob);
    xo = ld128<IN_AUX>(rwout, (tid >> 3) * 1024 + (tid & 7) * 16, idx * 32 * 1024 + xcd * 128);
  }
  SB();
  v4u gr[4], gb_[4];
  v2u gm[4], gc[4];
  const unsigned gm_off = ((cch ? 3 * 512 : 4 * 512) + wkg * 128 + ns) * 8;
  const unsigned gc_off = (((lane & 7) > 5 ? 0 : ((lane & 7) >> 1)) * 512 + d0 + (lane & 1)) * 8;
#define LOAD_GS() _Pragma("unroll") for (int i_ = 0; i_ < 4; ++i_) { gr[i_] = ld128<SC1>(rws, tid * 16, WS_SR + i_ * 4096); gb_[i_] = ld128<SC1>(rws, tid * 16, WS_SB + i_ * 4096); }
#define LOAD_GM() _Pragma("unroll") for (int i_ = 0; i_ < 4; ++i_) { gm[i_] = ld64<SC1>(rws, gm_off, WS_MODG + i_ * 24576); gc[i_] = ld64<SC1>(rws, gc_off, WS_MODG + i_ * 24576); }
  {
    const unsigned char* tab = smem + LDS_TAB + cch * (64 * 528) + c * 528 + q * 16;
    v4u fa[2][8], fb[8];
#pragma unroll
    for (int ks = 0; ks < 4; ++ks) {
      fb[ks] = *(const v4u*)(tab + 48 * 528 + ks * 64);
      fa[0][ks] = *(const v4u*)(tab + eh * (32 * 528) + ks * 64);
      fa[1][ks] = *(const v4u*)(tab + 16 * 528 + ks * 64);
    }
    const f32x2 us = *(const f32x2*)(smem + LDS_GS + 1024 + cch * 8);
    SB();
    f32x4 kk[2][2];
#pragma unroll
    for (int i = 0; i < 4; ++i) kk[i >> 1][i & 1] = f32x4{0.f, 0.f, 0.f, 0.f};
#pragma unroll
    for (int ks = 0; ks < 8; ++ks) {
      if (ks < 4) {
        fb[4 + ks] = *(const v4u*)(tab + 48 * 528 + (4 + ks) * 64);
        fa[0][4 + ks] = *(const v4u*)(tab + eh * (32 * 528) + (4 + ks) * 64);
        fa[1][4 + ks] = *(const v4u*)(tab + 16 * 528 + (4 + ks) * 64);
      }
      kk[0][ks >> 2] = mfma_h(fa[0][ks], fb[ks], kk[0][ks >> 2]); kk[1][ks >> 2] = mfma_h(fa[1][ks], fb[ks], kk[1][ks >> 2]);
      SB();
    }
    STAMP(1, 37);
    *(unsigned*)(smem + LDS_KP - 16 + cch * KP_CH + (lane & 3) * 4) = 0u;
#pragma unroll
    for (int i = 0; i < 2; ++i) *(v4u*)(smem + LDS_WI + (i * 64 + (tid >> 2)) * 80 + (tid & 3) * 16) = xi[i];
    *(v4u*)(smem + LDS_WO + (tid >> 3) * 144 + (tid & 7) * 16) = xo;
    f32x4 k0 = kk[0][0] * us[0] + kk[0][1] * us[1];
    const f32x4 k1 = kk[1][0] * us[0] + kk[1][1] * us[1];
    k0[0] += ns == 0 ? bf2f(q_dsk) : 0.f;
#pragma unroll
    for (int r = 0; r < 4; ++r) { *(float*)(smem + cob[0] + r * cos_[0]) = k0[r]; *(float*)(smem + cob[1] + r * cos_[1]) = k1[r]; }
  }
  v4u ug[4];
#define LOAD_UG() _Pragma("unroll") for (int i_ = 0; i_ < 4; ++i_) ug[i_] = ld128<SC1>(rws, tid * 16, WS_UG + pb * 16384 + i_ * 4096)
  STAMP(1, 31);
  LOAD_GM();
  LOAD_UG();
  bar();
  STAMP(1, 19);
  v4u wt[2];
  {
    float v[2][9];
#pragma unroll
    for (int j = 0; j < 9; ++j) v[0][j] = *(const float*)(smem + LDS_KP + pos4[j]);
    LOAD_GS();
#pragma unroll
    for (int j = 0; j < 9; ++j) v[1][j] = *(const float*)(smem + LDS_KP + KP_CH + pos4[j]);
    SB();
    STAMP(1, 36);
    *(v4u*)(smem + cch * CV_CH + (lane < 32 ? CV_H + l16 : ((lane >> 1) & 1) * CV_RK + 4096 + (lane & 1) * 16)) = v4u{0u, 0u, 0u, 0u};
#pragma unroll
    for (int ch = 0; ch < 2; ++ch) {
      unsigned char* rk = smem + ch * CV_CH + tid * 16;
#pragma unroll
      for (int sft = 0; sft < 2; ++sft)
        *(v4u*)(rk + sft * CV_RK) = v4u{pk2(v[ch][sft], v[ch][sft + 1]), pk2(v[ch][sft + 2], v[ch][sft + 3]), pk2(v[ch][sft + 4], v[ch][sft + 5]), pk2(v[ch][sft + 6], v[ch][sft + 7])};
    }
    const unsigned char* a0 = smem + LDS_WI + (w * 32 + 8 * q + ((lane >> 2) & 3)) * 80 + (lane & 3) * 16;
#pragma unroll
    for (int e = 0; e < 2; ++e) {
      const v2u lo = trread(a0 + e * 8), hi = trread(a0 + e * 8 + 320);
      wt[e] = v4u{lo[0], lo[1], hi[0], hi[1]};
    }
    const unsigned char* o0 = smem + LDS_WO + (8 * q + ((lane >> 2) & 3)) * 144 + (w >> 1) * 64 + (lane & 3) * 16 + (w & 1) * 8;
    const v2u olo = trread(o0), ohi = trread(o0 + 576);
    st128<SC1>(v4u{olo[0], olo[1], ohi[0], ohi[1]}, rws, l16, WS_WOT + ((xcd * 4 + w) * 22 + idx) * 1024 + wp_oob);
    SB();
    STAMP(1, 32);
    unsigned m = 0;
#pragma unroll
    for (int i = 0; i < 4; ++i) m = m | (gm[i][1] ^ tag) | (gc[i][1] ^ tag);
    if (__builtin_expect(any_nz(m), 0)) {
      unsigned sp = 0;
      do {
        MEMFENCE(); LOAD_GM(); m = 0;
#pragma unroll
        for (int i = 0; i < 4; ++i) m = m | (gm[i][1] ^ tag) | (gc[i][1] ^ tag);
      } while (any_nz(m) & (++sp <= SPIN_CAP));
    }
    const float mv = rbf((__uint_as_float(gm[0][0]) + __uint_as_float(gm[1][0])) + (__uint_as_float(gm[2][0]) + __uint_as_float(gm[3][0])));
    ((float*)(smem + LDS_GS))[tid] = cch ? mv : rbf(1.0f + mv);
    const float cv = rbf((__uint_as_float(gc[0][0]) + __uint_as_float(gc[1][0])) + (__uint_as_float(gc[2][0]) + __uint_as_float(gc[3][0])));
    const float gav = dpp<0x104>(cv), gav2 = dpp<0x102>(cv);
    const float hbv = cv * gav, hsv = rbf(1.0f + cv) * gav2;
    const f32x2 hb = {__builtin_bit_cast(float, __builtin_amdgcn_readlane(__builtin_bit_cast(int, hbv), 0)), __builtin_bit_cast(float, __builtin_amdgcn_readlane(__builtin_bit_cast(int, hbv), 1))};
    const f32x2 hs = {__builtin_bit_cast(float, __builtin_amdgcn_readlane(__builtin_bit_cast(int, hsv), 2)), __builtin_bit_cast(float, __builtin_amdgcn_readlane(__builtin_bit_cast(int, hsv), 3))};
    SB();
    STAMP(1, 33);
    m = 0;
#pragma unroll
    for (int i = 0; i < 4; ++i) { m = tacc(m, ug[i], tag); m = tacc(m, gr[i], tag); m = tacc(m, gb_[i], tag); }
    if (__builtin_expect(any_nz(m), 0)) {
      unsigned sp = 0;
      do {
        MEMFENCE(); LOAD_UG(); LOAD_GS(); m = 0;
#pragma unroll
        for (int i = 0; i < 4; ++i) { m = tacc(m, ug[i], tag); m = tacc(m, gr[i], tag); m = tacc(m, gb_[i], tag); }
      } while (any_nz(m) & (++sp <= SPIN_CAP));
    }
#pragma unroll
    for (int i = WSP_P0; i < WSP_P0 + 4; ++i) LOAD_WSPN(i);
    STAMP(1, 38);
#pragma unroll
    for (int i = 0; i < 4; ++i) {
      const f32x2 h0 = fma2(fma2(unpk(ug[i][0]), bc2(__uint_as_float(gr[i][0])), bc2(__uint_as_float(gb_[i][0]))), hs, hb);
      const f32x2 h1 = fma2(fma2(unpk(ug[i][2]), bc2(__uint_as_float(gr[i][2])), bc2(__uint_as_float(gb_[i][2]))), hs, hb);
      *(unsigned*)(smem + CV_H + 512 + i * 1024 + tid * 4) = pk2(h0[0], h1[0]);
      *(unsigned*)(smem + CV_CH + CV_H + 512 + i * 1024 + tid * 4) = pk2(h0[1], h1[1]);
      *(unsigned*)(smem + LDS_U + i * 1024 + tid * 4) = __builtin_amdgcn_perm(ug[i][2], ug[i][0], 0x05040100u);
      *(unsigned*)(smem + LDS_U + 4096 + i * 1024 + tid * 4) = __builtin_amdgcn_perm(ug[i][2], ug[i][0], 0x07060302u);
    }
  }
  asm volatile("" ::"v"(codepf));
  const unsigned char* abase = smem + cch * CV_CH + (1 - (c & 1)) * CV_RK + 2 * (2030 - 2 * (c >> 1) + 8 * q) - eh * 256 - 192 - 3584;
  const unsigned char* bbase = smem + cch * CV_CH + CV_H + 2 * (256 - 16 + 16 * c + 8 * q) - eh * 256 - 192;
  STAMP(1, 39);
  bar();
  STAMP(1, 1);
#define CV_A(ee, k) ({ const unsigned* p_ = (const unsigned*)(abase + 192 - 64 * (ee) + 3584 - 512 * (k)); v4u{p_[0], p_[1], p_[2], p_[3]}; })
#define CV_B(ee, k) (*(const v4u*)(bbase + 192 - 64 * (ee) + 512 * (k)))
  v4u cA[2][8], cB[2][8];
  f32x4 gs[4];
#pragma unroll
  for (int k = 0; k < 2; ++k) { cA[0][k] = CV_A(0, k); cB[0][k] = CV_B(0, k); }
  {
    const float* gp = (const float*)(smem + LDS_GS) + w * 32 + q * 8;
    gs[0] = *(const f32x4*)gp; gs[1] = *(const f32x4*)(gp + 4); gs[2] = *(const f32x4*)(gp + 128); gs[3] = *(const f32x4*)(gp + 132);
  }
  SB();
  f32x4 cacc[8];
  v2u ue[4];
  {
    f32x2 cs[2], bs[2];
    v4u wo_;
    f32x2 cp[4];
#pragma unroll
    for (int t = 0; t < 8; ++t) cacc[t] = f32x4{0.f, 0.f, 0.f, 0.f};
    sfor<0, 144>([&](auto G) __attribute__((always_inline)) {
      constexpr int g = decltype(G)::value, ee = g / 36, j = g % 36;
      if constexpr (ee == 0 && j < 6) { cA[0][2 + j] = CV_A(0, 2 + j); cB[0][2 + j] = CV_B(0, 2 + j); }
      cacc[CVO.pp[j] + CVO.qq[j]] = mfma(cA[ee & 1][CVO.qq[j]], cB[ee & 1][CVO.pp[j]], cacc[CVO.pp[j] + CVO.qq[j]]);
      constexpr int jn = ee == 0 ? j - 20 : (j & 1) ? j >> 1 : -1;
      if constexpr (ee < 3 && jn >= 0 && jn < 16) {
        if constexpr (jn & 1) cB[(ee + 1) & 1][jn >> 1] = CV_B(ee + 1, jn >> 1);
        else cA[(ee + 1) & 1][jn >> 1] = CV_A(ee + 1, jn >> 1);
      }
      if constexpr (ee == 3 && j < 4) ue[j] = *(const v2u*)(smem + LDS_U + cch * 4096 + (eh * 4 + j) * 512 + (c * 16 + q * 4) * 2);
      if constexpr (g >= 4 && g < 36 && (g & 3) == 0) {
        constexpr int i = (g - 4) >> 4, jj = ((g - 4) >> 2) & 3;
        const f32x2 x = unpk(wt[i][jj]);
        const f32x2 g2 = (jj & 1) ? hi2(gs[jj >> 1]) : lo2(gs[jj >> 1]);
        const f32x2 s2 = (jj & 1) ? hi2(gs[2 + (jj >> 1)]) : lo2(gs[2 + (jj >> 1)]);
        wo_[jj] = pk2(x * g2);
        if constexpr (jj == 0) { cs[i] = unpk(wo_[jj]); bs[i] = x * s2; }
        else { cs[i] += unpk(wo_[jj]); bs[i] = fma2(x, s2, bs[i]); }
        PIN(cs[i]); PIN(bs[i]);
        if constexpr (jj == 3) st128<SC1>(wo_, rws, l16 + i * 16384, WS_WP + wp_fr);
      }
      if constexpr (g == 38) {
        const float t2 = xrow_sum4(cs[0][0] + cs[0][1], bs[0][0] + bs[0][1], cs[1][0] + cs[1][1], bs[1][0] + bs[1][1]);
        ((float*)(smem + LDS_S + 4096))[(w * 16 + c) * 4 + q] = t2;
      }
      if constexpr (g == 46) bar();
      if constexpr (g == 47) {
#pragma unroll
        for (int k = 0; k < 4; ++k) cp[k] = *(const f32x2*)(smem + LDS_S + 4096 + (k * 16 + c) * 16 + (q & 1) * 8);
      }
      if constexpr (g == 54) {
        const f32x2 sx = (cp[0] + cp[1]) + (cp[2] + cp[3]);
        st64<SC1>(v2u{__float_as_uint(sx[0]), __float_as_uint(sx[1])}, rws, tid < 32 ? tid * 32 : OOB, WS_CSB + wpr * 1024 + wkg * 8 + wp_oob);
      }
      if constexpr (g == 1 || g == 5) st128<SC1>(mk, rws, l16 + (g == 5) * 1024, WS_X1T + dk * 4096 + eh * 2048);
      if constexpr (g == 9) __builtin_amdgcn_raw_buffer_store_b32(~0u, rws, lane * 4, WS_ST2 + ((mt * 2 + kh) * 16 + nt * 2 + uh) * 256, SC1);
      if constexpr (g == 42) { if (b == 0 && tid == 0) *(unsigned*)(p.ws + WS_EPOCH) = tag; }
      if constexpr (g == 36) STAMP(1, 40);
      if constexpr (g == 72) STAMP(1, 41);
      if constexpr (g == FLAG_AT) {
        STAMP(1, 42);
        asm volatile("s_waitcnt vmcnt(%0)" ::"i"((FLAG_AT - 57 + 5) / 6) : "memory");
        __builtin_amdgcn_s_barrier();
        if (tid < 8) hint_add(p.ws, tid * 8 + (idx & 7));
        STAMP(1, 43);
      }
      if constexpr (g % 6 == 3) LOAD_WSPN(WSP_P0 + 4 + g / 6);
      SB();
    });
  }
  STAMP(1, 2);
  {
    f32x4* red = (f32x4*)(smem + LDS_X) + w * 256 + lane;
    f32x4 keep[4];
    if (eh) {
#pragma unroll
      for (int t = 0; t < 4; ++t) { red[t * 64] = cacc[t]; keep[t] = cacc[4 + t]; PIN(keep[t]); }
    } else {
#pragma unroll
      for (int t = 0; t < 4; ++t) { red[t * 64] = cacc[4 + t]; keep[t] = cacc[t]; PIN(keep[t]); }
    }
    f32x2 pre[4][2];
#pragma unroll
    for (int t = 0; t < 4; ++t) {
      pre[t][0] = unpk(ue[t][0]) + lo2(keep[t]);
      pre[t][1] = unpk(ue[t][1]) + hi2(keep[t]);
      PIN(pre[t][0]); PIN(pre[t][1]);
    }
    const unsigned xoff = ((c >> 1) * 32 + ((q & 1) * 4 + 2 * (c & 1) + (q >> 1)) * 4) * 2;
    bar();
    const f32x4* rd = (const f32x4*)(smem + LDS_X) + (w ^ 1) * 256 + lane;
    f32x4 oth[4];
#pragma unroll
    for (int t = 0; t < 4; ++t) oth[t] = rd[t * 64];
#pragma unroll
    for (int t = 0; t < 4; ++t) {
      v2u o;
      o[0] = nomark(pk2(pre[t][0] + lo2(oth[t])));
      o[1] = nomark(pk2(pre[t][1] + hi2(oth[t])));
      st64<SC1>(o, rws, xoff + t * 512, WS_X1T + (d0 + cch) * 4096 + eh * 2048);
    }
  }
  STAMP(1, 3);
  SB();
  v4u wf[3][8];
  const unsigned rot = uh * 4;
#define LOAD_WP(half, k, i) ld128<SC1>(rws, l16 + ((i) & 3) * 1024, WS_WP + (((half) * 44 + ub_own + (k)) * 16 + kh * 8 + (((i) & 4) ^ rot)) * 1024 + ((k) == 2 ? oob3 : 0u))
#define LOAD_XS(i) ld128<SC1>(rws, wsp_v, WS_X1T + ((i) * 32 + w * 8) * 4096 + mt * 128)
  unsigned char* wslot = smem + LDS_W + w * 16384;
  const int wr_off = (lane >> 3) * 128 + (((lane & 7) ^ ((lane >> 4) & 1)) * 16);
#define PARK1(ck, slot, i) *(v4u*)((slot) + (i) * 1024 + (wr_off ^ (((i) & 3) * 32))) = wr[ck][i]
#define PARK_WSP(ck, slot) _Pragma("unroll") for (int i_ = 0; i_ < 8; ++i_) PARK1(ck, slot, i_)
  PARK_WSP(0, wslot);
  PARK_WSP(1, wslot + 8192);
  v4u xs[16];
  const unsigned char* tb[2][4];
  unsigned char* xdst;
  {
    const int row = 8 * q + ((lane >> 2) & 3);
    const int sg = ((row >> 1) & 1) | (((row >> 3) & 1) << 1);
#pragma unroll
    for (int tu = 0; tu < 4; ++tu) {
      const unsigned char* t0 = smem + LDS_A + kh * 32768 + row * 128 + ((tu ^ (2 * kh) ^ sg) * 32) + (lane & 3) * 8;
      tb[0][tu] = t0 + uh * 16384;
      tb[1][tu] = t0 + (uh ^ 1) * 16384;
    }
    const int rl = w * 8 + (lane >> 3), chn = lane & 7;
    const int sg2 = ((rl >> 1) & 1) | (((rl >> 3) & 1) << 1);
    xdst = smem + LDS_A + rl * 128 + (((chn >> 1) ^ sg2) * 32) + (chn & 1) * 16;
  }
  SB();
  f32x4 cz[2][3][2];
  {
    STAMP(1, 20);
    hint_wait(rws, (lane & 7) * 128, WS_CNT + xcd * 1024, 32u);
    STAMP(1, 21);
#pragma unroll
    for (int i = 0; i < WF_EARLY; ++i)
#pragma unroll
      for (int k = 0; k < 3; ++k) wf[k][i] = LOAD_WP(1, k, i);
#pragma unroll
    for (int i = 0; i < 16; ++i) xs[i] = LOAD_XS(i);
    SB();
    STAMP(1, 44);
    unsigned m = 0;
#pragma unroll
    for (int i = 0; i < 16; ++i) {
      m = macc(m, xs[i]); *(v4u*)(xdst + i * 4096) = xs[i];
      if (WF_EARLY * 3 + i < 24) wf[(WF_EARLY * 3 + i) % 3][(WF_EARLY * 3 + i) / 3] = LOAD_WP(1, (WF_EARLY * 3 + i) % 3, (WF_EARLY * 3 + i) / 3);
      SB();
    }
    PIN(m);
    STAMP(1, 45);
    RETRY_BEGIN(m)
#pragma unroll
      for (int i = 0; i < 16; ++i) xs[i] = LOAD_XS(i);
      SB();
#pragma unroll
      for (int i = 0; i < 16; ++i) { m = macc(m, xs[i]); *(v4u*)(xdst + i * 4096) = xs[i]; }
    RETRY_END(m)
  }
  STAMP(1, 4);
  bar();
  STAMP(1, 5);
#define AHALF(tu, i, h) trread(tb[(i) >> 2][tu] + ((i) & 3) * 4096 + (h) * 512)
  f32x4 acc[3][4], s1[4], s2[4];
#pragma unroll
  for (int tu = 0; tu < 4; ++tu) {
    s1[tu] = f32x4{0.f, 0.f, 0.f, 0.f}; s2[tu] = s1[tu];
#pragma unroll
    for (int k = 0; k < 3; ++k) acc[k][tu] = s1[tu];
  }
  v4u wg[3][8];
  v2u ah[2][4][2];
#pragma unroll
  for (int tu = 0; tu < 4; ++tu) { ah[0][tu][0] = AHALF(tu, 0, 0); ah[0][tu][1] = AHALF(tu, 0, 1); }
  float* st = (float*)(smem + LDS_S);
  const v4u ones4 = {ONES, ONES, ONES, ONES};
  f32x2 csb[2][3];
  SB();
#pragma unroll
  for (int i = 0; i < 8; ++i) {
#pragma unroll
    for (int j = 0; j < 12; ++j) {
      const int tu = j / 3, k = j % 3;
      const v4u a = {ah[i & 1][tu][0][0], ah[i & 1][tu][0][1], ah[i & 1][tu][1][0], ah[i & 1][tu][1][1]};
      acc[k][tu] = mfma(a, wf[k][i], acc[k][tu]);
      if (j < 8) ah[(i + 1) & 1][j >> 1][j & 1] = AHALF(j >> 1, (i + 1) & 7, j & 1);
      if (i < 4 && k == 0) s1[tu] = mfma(ones4, a, s1[tu]);
      if (i < 4 && k == 1) s2[tu] = mfma(a, a, s2[tu]);
      if ((i == 5 || i == 6) && (j == 8 || j == 10)) {
        const int t0 = (i - 5) * 2 + ((j - 8) >> 1);
        const float dg = (c & 2) ? ((c & 1) ? s2[t0][3] : s2[t0][2]) : ((c & 1) ? s2[t0][1] : s2[t0][0]);
        float* dst = st + ((c >> 2) == q ? w * 128 + (t0 ^ (2 * kh)) * 16 + c : 1280);
        dst[0] = s1[t0][0]; dst[64] = dg;
      }
      if (i < 2 && (j & 1)) {
        const int kc = (j >> 1) % 3, ic = (j >> 1) / 3;
        cz[1 - i][kc][ic] = asf(ld128<SC1>(rws, c * 32 + ic * 16, WS_CSB + ((1 - i) * 44 + ub_own + kc) * 512 + (kc == 2 ? oob3 : 0u)));
      }
      if ((i == 2 || i == 3) && (j & 3) == 2) LOAD_WSPN(WSP_P0 + 28 + (i - 2) * 3 + (j >> 2));
      if (i == 4 && j >= 6) {
        const int hf = (j - 6) / 3;
        const f32x4 t4 = cz[hf][k][0] + cz[hf][k][1];
        csb[hf][k] = lo2(t4) + hi2(t4);
        PIN(csb[hf][k]);
      }
      if (i == 5 && (j & 3) == 1) st128<SC1>(mk, rws, l16, WS_Z2 + ((ub_own + (j >> 2)) * 64 + mt * 2 + kh) * 1024 + ((j >> 2) == 2 ? oob3 : 0u));
      if (i >= 6 && (j & 3) == 3) wg[j >> 2][i - 6] = LOAD_WP(0, j >> 2, i - 6);
      if (i == 0 && j == 11) STAMP(1, 46);
      if (i == 3 && j == 11) STAMP(1, 47);
      SB();
    }
  }
  STAMP(1, 6);
  f32x4* xch = (f32x4*)(smem + LDS_X);
#pragma unroll
  for (int k = 0; k < 3; ++k)
#pragma unroll
    for (int t = 0; t < 2; ++t) xch[(w * 6 + k * 2 + t) * 64 + lane] = acc[k][2 + t];
#pragma unroll
  for (int k = 0; k < 3; ++k) { PIN(acc[k][0]); PIN(acc[k][1]); }
  STAMP(1, 48);
  bar();
  STAMP(1, 49);
  float* fin = st + 512 + w * 64;
  f32x4 z2v[3][2];
  {
    const int row = kh * 32 + (lane & 31);
    float pa[4], pb[4];
#pragma unroll
    for (int k = 0; k < 4; ++k) { pa[k] = st[k * 128 + row]; pb[k] = st[k * 128 + 64 + row]; }
    f32x4 oth[3][2];
#pragma unroll
    for (int k = 0; k < 3; ++k)
#pragma unroll
      for (int t = 0; t < 2; ++t) oth[k][t] = xch[((w ^ 1) * 6 + k * 2 + t) * 64 + lane];
    SB();
    const float m = ((pa[0] + pa[1]) + (pa[2] + pa[3])) * (1.0f / D);
    const float r = __builtin_amdgcn_rsqf(fmaxf(((pb[0] + pb[1]) + (pb[2] + pb[3])) * (1.0f / D) - m * m, 0.f) + EPS);
    fin[lane] = lane < 32 ? r : -r * m;
    f32x4 r4[2], m4[2];
#pragma unroll
    for (int t = 0; t < 2; ++t) { r4[t] = *(const f32x4*)(fin + t * 16 + q * 4); m4[t] = *(const f32x4*)(fin + 32 + t * 16 + q * 4); }
    SB();
#pragma unroll
    for (int k = 0; k < 3; ++k)
#pragma unroll
      for (int t = 0; t < 2; ++t) { oth[k][t] += acc[k][t]; PIN(oth[k][t]); }
    SB();
    f32x4 rs[2], rq[2];
#pragma unroll
    for (int t = 0; t < 2; ++t) {
#pragma unroll
      for (int k = 0; k < 3; ++k) {
        const f32x4 a = oth[k][t];
        const f32x2 zl = gelu2(fma2(lo2(a), lo2(r4[t]), fma2(lo2(m4[t]), bc2(csb[1][k][0]), bc2(csb[1][k][1]))));
        const f32x2 zh = gelu2(fma2(hi2(a), hi2(r4[t]), fma2(hi2(m4[t]), bc2(csb[1][k][0]), bc2(csb[1][k][1]))));
        z2v[k][t] = f32x4{zl[0], zl[1], zh[0], zh[1]};
        if (k == 0) { rs[t] = z2v[k][t]; rq[t] = z2v[k][t] * z2v[k][t]; }
        else { rs[t] += z2v[k][t]; rq[t] += z2v[k][t] * z2v[k][t]; }
        PIN(rs[t]); PIN(rq[t]);
#pragma unroll
        for (int kk = 0; kk < 3; ++kk) wg[kk][2 + t * 3 + k] = LOAD_WP(0, kk, 2 + t * 3 + k);
        SB();
      }
    }
    STAMP(1, 50);
    float v8[8], v4[4], v2[2];
    const bool b3 = c & 8, b2 = c & 4, b1 = c & 2, b0 = c & 1;
#pragma unroll
    for (int r_ = 0; r_ < 4; ++r_) {
      const float a0 = rs[0][r_] + dpp<0x128>(rs[0][r_]), a1 = rs[1][r_] + dpp<0x128>(rs[1][r_]);
      const float q0 = rq[0][r_] + dpp<0x128>(rq[0][r_]), q1 = rq[1][r_] + dpp<0x128>(rq[1][r_]);
      v8[2 * r_] = b3 ? a1 : a0; v8[2 * r_ + 1] = b3 ? q1 : q0;
    }
#pragma unroll
    for (int j = 0; j < 4; ++j) {
      const float x0 = v8[j] + dpp<0x141>(v8[j]), x1 = v8[4 + j] + dpp<0x141>(v8[4 + j]);
      v4[j] = b2 ? x1 : x0;
    }
#pragma unroll
    for (int j = 0; j < 2; ++j) {
      const float x0 = v4[j] + dpp<0x4E>(v4[j]), x1 = v4[2 + j] + dpp<0x4E>(v4[2 + j]);
      v2[j] = b1 ? x1 : x0;
    }
    const float x0 = v2[0] + dpp<0xB1>(v2[0]), x1 = v2[1] + dpp<0xB1>(v2[1]);
    const float outv = b0 ? x1 : x0;
    __builtin_amdgcn_raw_buffer_store_b32(nomark(__float_as_uint(outv)), rws, lane * 4, WS_ST2 + ((mt * 2 + kh) * 16 + nt * 2 + uh) * 256, SC1);
  }
  STAMP(1, 7);
  SB();
#pragma unroll
  for (int tu = 0; tu < 4; ++tu)
#pragma unroll
    for (int k = 0; k < 3; ++k) acc[k][tu] = f32x4{0.f, 0.f, 0.f, 0.f};
  const int th = xcd & 1, dq = idx >> 3;
  v2u x1h[2][2];
  int x1o;
  {
    const int row = 8 * q + ((lane >> 2) & 3);
    const int sg = ((row >> 1) & 1) | (((row >> 3) & 1) << 1);
    x1o = LDS_A + (dq * 4 + w) * 4096 + row * 128 + (((2 * th) ^ sg) * 32) + (lane & 3) * 8;
  }
  f32x4 pt[4];
  v2u gq[4];
  unsigned bspq[2];
#define LOAD_PT(i) asf(ld128<SC1>(rws, l16 + (i) * 1024, WS_ST2 + (mt * 2 + kh) * 4096))
  SB();
#pragma unroll
  for (int i = 0; i < 8; ++i) {
#pragma unroll
    for (int j = 0; j < 12; ++j) {
      const int tu = j / 3, k = j % 3;
      const v4u a = {ah[i & 1][tu][0][0], ah[i & 1][tu][0][1], ah[i & 1][tu][1][0], ah[i & 1][tu][1][1]};
      acc[k][tu] = mfma(wg[k][i], a, acc[k][tu]);
      if (j < 8 && i < 7) ah[(i + 1) & 1][j >> 1][j & 1] = AHALF(j >> 1, i + 1, j & 1);
      if (j < 4 && i == 7) x1h[j >> 1][j & 1] = trread(smem + (x1o ^ ((j >> 1) * 32)) + (j & 1) * 512);
      if (i < 6 && (j == 1 || j == 5 || j == 9)) LOAD_WSPN(WSP_P0 + 34 + i * 3 + (j >> 2));
      if (j >= 8 && i == 6) pt[j - 8] = LOAD_PT(j - 8);
      if (OPT_D && i == 0 && j >= 6 && j < 10) gq[j - 6] = ld64<SC1>(rws, (lane & 31) * 8, WS_MODG + ((j - 6) * 3072 + 5 * 512 + (dq * 4 + w) * 32) * 8);
      if (OPT_D && i == 0 && j >= 10) bspq[j - 10] = p.bsp[mt * 64 + perm(2 * kh + j - 10, c)];
      if (OPT_D && i == 4 && j == 10) st[1408 + w * 32 + (lane & 31)] = rbf((__uint_as_float(gq[0][0]) + __uint_as_float(gq[1][0])) + (__uint_as_float(gq[2][0]) + __uint_as_float(gq[3][0])));
      SB();
    }
  }
  STAMP(1, 8);
  {
    unsigned m = 0;
#pragma unroll
    for (int i = 0; i < 4; ++i) m = macc(m, __builtin_bit_cast(v4u, pt[i]));
    RETRY_BEGIN(m)
#pragma unroll
      for (int i = 0; i < 4; ++i) { pt[i] = LOAD_PT(i); m = macc(m, __builtin_bit_cast(v4u, pt[i])); }
    RETRY_END(m)
    bar();
    if (tid < 4) hint_add(p.ws, 64 + (nt * 4 + (mt >> 3)) * 4 + tid);
    const f32x4 sx = (pt[0] + pt[1]) + (pt[2] + pt[3]);
    const float tv = xrow_sum4(sx[0], sx[2], sx[1], sx[3]);
    auto sw = __builtin_amdgcn_permlane32_swap(__float_as_uint(tv), __float_as_uint(tv), false, false);
    const float mm = __uint_as_float(sw[0]) * (1.0f / FH);
    const float r = __builtin_amdgcn_rsqf(fmaxf(__uint_as_float(sw[1]) * (1.0f / FH) - mm * mm, 0.f) + EPS);
    float* f2 = st + 768 + w * 64;
    f2[(q >> 1) * 32 + 2 * c + (q & 1)] = (q >> 1) ? -r * mm : r;
    f32x4 r4[2], m4[2];
#pragma unroll
    for (int t = 0; t < 2; ++t) { r4[t] = *(const f32x4*)(f2 + q * 8 + t * 4); m4[t] = *(const f32x4*)(f2 + 32 + q * 8 + t * 4); }
#pragma unroll
    for (int k = 0; k < 3; ++k) {
      v4u o;
#pragma unroll
      for (int t = 0; t < 2; ++t) {
        o[2 * t] = nomark(pk2(fma2(lo2(z2v[k][t]), lo2(r4[t]), lo2(m4[t]))));
        o[2 * t + 1] = nomark(pk2(fma2(hi2(z2v[k][t]), hi2(r4[t]), hi2(m4[t]))));
      }
      st128<SC1>(o, rws, l16, WS_Z2 + ((ub_own + k) * 64 + mt * 2 + kh) * 1024 + (k == 2 ? oob3 : 0u));
    }
  }
  STAMP(1, 9);
  SB();
  unsigned z1p[3][2][2];
  unsigned char* wslot2 = smem + LDS_A + w * 18432;
  v4u wo[2][22];
#define LOAD_WO(f) wo[(f) & 1][(f) >> 1] = ld128<SC1>(rws, l16 + (((f) >> 1) & 3) * 1024, WS_WOT + (((dq * 4 + w) * 2 + ((f) & 1)) * 22 + (((f) >> 1) & ~3)) * 1024)
  f32x4 z1a[3][2];
  {
#pragma unroll
    for (int i = WSP_P0 + 52; i < 64; ++i) if (!OPT_D) LOAD_WSPN(i);
#pragma unroll
    for (int i = 0; i < 4; ++i) if (!OPT_D) gq[i] = ld64<SC1>(rws, (lane & 31) * 8, WS_MODG + (i * 3072 + 5 * 512 + (dq * 4 + w) * 32) * 8);
#pragma unroll
    for (int t = 0; t < 2; ++t) if (!OPT_D) bspq[t] = p.bsp[mt * 64 + perm(2 * kh + t, c)];
    float* c1s = st + 1024 + w * 96;
#pragma unroll
    for (int k = 0; k < 3; ++k) c1s[k * 32 + (q & 1) * 16 + c] = (q & 1) ? csb[0][k][1] : csb[0][k][0];
#pragma unroll
    for (int k = 0; k < 3; ++k)
#pragma unroll
      for (int t = 0; t < 2; ++t) xch[(w * 6 + k * 2 + t) * 64 + lane] = acc[k][2 + t];
    SB();
    bar();
    STAMP(1, 34);
    f32x4 oth[3][2], cs1[3], bs1[3];
    float r1[2], m1[2];
#pragma unroll
    for (int t = 0; t < 2; ++t) { r1[t] = fin[t * 16 + c]; m1[t] = fin[32 + t * 16 + c]; }
#pragma unroll
    for (int k = 0; k < 3; ++k) {
      cs1[k] = *(const f32x4*)(c1s + k * 32 + q * 4); bs1[k] = *(const f32x4*)(c1s + k * 32 + 16 + q * 4);
#pragma unroll
      for (int t = 0; t < 2; ++t) oth[k][t] = xch[((w ^ 1) * 6 + k * 2 + t) * 64 + lane];
    }
    SB();
    PARK_WSP(2, wslot2);
#pragma unroll
    for (int k = 0; k < 3; ++k)
#pragma unroll
      for (int t = 0; t < 2; ++t) {
        const f32x4 a = acc[k][t] + oth[k][t];
        const f32x2 lo = fma2(lo2(a), bc2(r1[t]), fma2(lo2(cs1[k]), bc2(m1[t]), lo2(bs1[k])));
        const f32x2 hi = fma2(hi2(a), bc2(r1[t]), fma2(hi2(cs1[k]), bc2(m1[t]), hi2(bs1[k])));
        z1a[k][t] = f32x4{lo[0], lo[1], hi[0], hi[1]};
        PIN(z1a[k][t]);
      }
  }
  STAMP(1, 10);
  SB();
  f32x4 ac2[6][4];
#pragma unroll
  for (int i = 0; i < 6; ++i)
#pragma unroll
    for (int j = 0; j < 4; ++j) { const float b0 = (i < 3 && j < 2) ? bf2f(bspq[j & 1]) : 0.f; ac2[i][j] = f32x4{b0, b0, b0, b0}; }
  int rd_off[4];
#pragma unroll
  for (int tu = 0; tu < 4; ++tu) rd_off[tu] = perm(tu ^ (2 * kh), c) * 128 + ((q ^ (c >> 1)) * 16);
  constexpr int ZD = 3;
  v4u zf[ZD][6];
  v4u wfr[2][2][4];
#define LOAD_Z(ks, i) ld128<SC1>(rws, l16 + ((((ks) >> 1) + kr) & 7) * 2048 + ((ks) & 1) * 1024, WS_Z2 + ((((i) < 3 ? ub_own : ub_oth) + (i) % 3) * 64 + w * 16) * 1024 + ((i) % 3 == 2 ? oob3 : 0u))
#pragma unroll
  for (int k2 = 0; k2 < 2; ++k2)
#pragma unroll
    for (int tu = 0; tu < 4; ++tu) wfr[0][k2][tu] = *(const v4u*)(wslot + (rd_off[tu] ^ (k2 * 64)));
  PARK_WSP(3, wslot);
#define WSLOT(ck) ((ck) % 3 == 2 ? wslot2 : wslot + ((ck) % 3) * 8192)
  hint_wait(rws, 0u, WS_CNT + (64 + (nt * 4 + w) * 4 + (xcd >> 1)) * 128, 8u);
  STAMP(1, 22);
#pragma unroll
  for (int s_ = 0; s_ < ZD; ++s_)
#pragma unroll
    for (int i = 0; i < 6; ++i) zf[s_][i] = LOAD_Z(s_, i);
  SB();
  STAMP(1, 11);
  unsigned zm = 0;
  sfor<0, 16 * 24>([&](auto G) __attribute__((always_inline)) {
    constexpr int ks = decltype(G)::value / 24, j = decltype(G)::value % 24, ck = ks >> 1, k2 = ks & 1, i = j >> 2, tu = j & 3;
    ac2[i][tu] = mfma(zf[ks % ZD][i], wfr[ck & 1][k2][tu], ac2[i][tu]);
    if constexpr (j == 0 && (ks == 1 || ks == 4 || ks == 8 || ks == 12)) STAMP(1, 56 + (ks >> 2));
    if constexpr (tu == 1) { zm = macc(zm, zf[ks % ZD][i]); PIN(zm); }
    if constexpr (ks < 12 && j == 19) {
      constexpr int k = ks >> 2, t = (ks >> 1) & 1;
      z1p[k][t][ks & 1] = pk2(gelu2((ks & 1) ? hi2(z1a[k][t]) : lo2(z1a[k][t])));
      PIN(z1p[k][t][ks & 1]);
    }
    if constexpr (OPT_D && ks < 4 && (j == 9 || j == 21)) LOAD_WSPN(WSP_P0 + 52 + ks * 2 + (j == 21));
    if constexpr (!OPT_D && ks == 12 && j == 19)
      st[1408 + w * 32 + (lane & 31)] = rbf((__uint_as_float(gq[0][0]) + __uint_as_float(gq[1][0])) + (__uint_as_float(gq[2][0]) + __uint_as_float(gq[3][0])));
    if constexpr (ks >= 5 && ks < 5 + WO0 / 2 && (j == 9 || j == 21)) LOAD_WO((ks - 5) * 2 + (j == 21));
    if constexpr (j == 17 && ks >= 2 && ks < 5) st128<SC1>(mk, rws, l16, WS_V + ((mt * 4 + w) * 22 + pr0 + ks - 2) * 1024 + (ks == 4 ? oob3 : 0u));
    if constexpr (!OPT_C && j == 1 && ks == HINT4_KS) {
      asm volatile("s_waitcnt vmcnt(20)" ::: "memory");
      __builtin_amdgcn_s_barrier();
      if (tid < 2) hint_add(p.ws, 192 + mt * 2 + tid);
    }
    if constexpr (tu == 3) {
      if constexpr (ks + ZD < 16) zf[ks % ZD][i] = LOAD_Z(ks + ZD, i);
      else if constexpr (ks > 16 - ZD) LOAD_WO(WO0 + (ks + ZD - 17) * 6 + i);
    }
    if constexpr ((j & 1) == 0 && j < 16 && ck < 7) {
      constexpr int jj = j >> 1;
      if constexpr (k2 == 0) { if constexpr (ck >= 1 && ck + 3 < 8) PARK1(ck + 3, WSLOT(ck), jj); }
      else wfr[(ck + 1) & 1][jj >> 2][jj & 3] = *(const v4u*)(WSLOT(ck + 1) + (rd_off[jj & 3] ^ ((jj >> 2) * 64)));
    }
    SB();
  });
  if (__builtin_expect(any_mark(zm), 0)) {
    int l2 = lane;
    PIN(l2);
    const int c2 = l2 & 15, q2 = l2 >> 4;
#pragma unroll
    for (int i = 0; i < 6; ++i)
#pragma unroll
      for (int j = 0; j < 4; ++j) { const float b0 = (i < 3 && j < 2) ? bf2f(p.bsp[mt * 64 + perm(2 * kh + (j & 1), c2)]) : 0.f; ac2[i][j] = f32x4{b0, b0, b0, b0}; }
#pragma unroll 1
    for (int ks = 0; ks < 16; ++ks) {
      v4u zz[6], ww[4];
      unsigned sp = 0;
      do {
        MEMFENCE();
        zm = 0;
#pragma unroll
        for (int i = 0; i < 6; ++i) {
          zz[i] = ld128<SC1>(rws, l16 + ks * 1024, WS_Z2 + (((i < 3 ? ub_own : ub_oth) + i % 3) * 64 + w * 16) * 1024 + (i % 3 == 2 ? oob3 : 0u));
          zm = macc(zm, zz[i]);
        }
      } while (any_mark(zm) & (++sp <= SPIN_CAP));
#pragma unroll
      for (int tu = 0; tu < 4; ++tu) ww[tu] = ld128<0>(rwsp, perm(tu ^ (2 * kh), c2) * 4096 + q2 * 16 + ks * 64, wsp_s);
#pragma unroll
      for (int i = 0; i < 6; ++i)
#pragma unroll
        for (int tu = 0; tu < 4; ++tu) ac2[i][tu] = mfma(zz[i], ww[tu], ac2[i][tu]);
    }
  }
  STAMP(1, 12);
  constexpr int WO1 = WO0 + (ZD - 1) * 6;
  {
    f32x4* red = (f32x4*)(smem + LDS_A);
#pragma unroll
    for (int i = 0; i < 6; ++i)
#pragma unroll
      for (int tu = 0; tu < 4; ++tu) {
        const int rel = (tu >> 1) | ((i / 3) << 1);
        if (rel) red[((w * 3 + rel - 1) * 6 + (i % 3) * 2 + (tu & 1)) * 64 + lane] = ac2[i][tu];
      }
#pragma unroll
    for (int f = WO1; f < WO1 + 10 && f < 44; ++f) LOAD_WO(f);
    f32x4 own[3][2];
#pragma unroll
    for (int k = 0; k < 3; ++k)
#pragma unroll
      for (int t = 0; t < 2; ++t) { own[k][t] = ac2[k][t]; PIN(own[k][t]); }
    STAMP(1, 51);
    if (OPT_C) asm volatile("s_waitcnt vmcnt(40)" ::: "memory");
    bar();
    if (OPT_C && tid < 2) hint_add(p.ws, 192 + mt * 2 + tid);
    STAMP(1, 52);
    f32x4 o3[3][2][3];
#pragma unroll
    for (int k = 0; k < 3; ++k)
#pragma unroll
      for (int t = 0; t < 2; ++t)
#pragma unroll
        for (int rel = 1; rel < 4; ++rel) o3[k][t][rel - 1] = red[(((w ^ rel) * 3 + rel - 1) * 6 + k * 2 + t) * 64 + lane];
    SB();
#pragma unroll
    for (int k = 0; k < 3; ++k) {
#pragma unroll
      for (int t = 0; t < 2; ++t) {
        const f32x4 g = (own[k][t] + o3[k][t][0]) + (o3[k][t][1] + o3[k][t][2]);
        v2u o;
        o[0] = nomark(pk2(unpk(z1p[k][t][0]) * lo2(g)));
        o[1] = nomark(pk2(unpk(z1p[k][t][1]) * hi2(g)));
        const int uu = ub_own + k;
        st64<SC1>(o, rws, l16, WS_V + ((mt * 4 + 2 * kh + t) * 22 + (uu >> 1)) * 1024 + (uu & 1) * 8 + (k == 2 ? oob3 : 0u));
      }
    }
  }
  STAMP(1, 13);
  SB();
#pragma unroll
  for (int f = WO1 + 10; f < 44; ++f) LOAD_WO(f);
  SB();
  STAMP(1, 53);
  hint_wait(rws, 0u, WS_CNT + (192 + mt * 2 + th) * 128, 8u);
  STAMP(1, 23);
  {
    v4u vs[11];
    unsigned spins = 0;
    for (;;) {
      MEMFENCE();
#pragma unroll
      for (int i = 0; i < 11; ++i) vs[i] = ld128<SC1>(rws, l16 + (i & 3) * 1024, WS_V + ((mt * 4 + 2 * th) * 22 + w * 11 + (i & ~3)) * 1024);
      SB();
      unsigned m = 0;
#pragma unroll
      for (int i = 0; i < 11; ++i) { m = macc(m, vs[i]); *(v4u*)(smem + LDS_W + (w * 11 + i) * 1024 + l16) = vs[i]; }
      if (!(any_mark(m) & (++spins <= SPIN_CAP))) break;
    }
  }
  STAMP(1, 14);
  bar();
  f32x4 a3[2][2];
#pragma unroll
  for (int e = 0; e < 2; ++e)
#pragma unroll
    for (int t = 0; t < 2; ++t) a3[e][t] = f32x4{0.f, 0.f, 0.f, 0.f};
  STAMP(1, 54);
  constexpr int VD = 8;
  v4u vf[VD];
#define VFRAG(f) (*(const v4u*)(smem + LDS_W + (((f) & 1) * 22 + ((f) >> 1)) * 1024 + l16))
#pragma unroll
  for (int f = 0; f < VD; ++f) vf[f] = VFRAG(f);
  const f32x4 gbl = *(const f32x4*)(st + 1408 + w * 32 + q * 8), gbh = *(const f32x4*)(st + 1408 + w * 32 + q * 8 + 4);
  f32x2 x1f[2][4];
  SB();
#pragma unroll
  for (int f = 0; f < 44; ++f) {
    const v4u cur = vf[f % VD];
    a3[0][f & 1] = mfma(wo[0][f >> 1], cur, a3[0][f & 1]);
    a3[1][f & 1] = mfma(wo[1][f >> 1], cur, a3[1][f & 1]);
    if (f + VD < 44) vf[f % VD] = VFRAG(f + VD);
    if (f >= 36) { x1f[(f - 36) >> 2][f & 3] = unpk(x1h[(f - 36) >> 2][(f >> 1) & 1][f & 1]); PIN(x1f[(f - 36) >> 2][f & 3]); }
    SB();
  }
  STAMP(1, 55);
  const rsrc_t rout = mkrs(p.out, L * D * 2);
#pragma unroll
  for (int t = 0; t < 2; ++t) {
    v4u o;
    o[0] = pk2(fma2(lo2(a3[0][t]), lo2(gbl), x1f[t][0]));
    o[1] = pk2(fma2(hi2(a3[0][t]), hi2(gbl), x1f[t][1]));
    o[2] = pk2(fma2(lo2(a3[1][t]), lo2(gbh), x1f[t][2]));
    o[3] = pk2(fma2(hi2(a3[1][t]), hi2(gbh), x1f[t][3]));
    st128<0>(o, rout, perm(t, c) * 1024 + q * 16, (mt * 64 + th * 32) * 1024 + (dq * 4 + w) * 64);
  }
  STAMP(1, 15);
  STAMP_FLUSH(1);
  asm volatile("adaln_code_end:");
}

extern "C" void kernel_launch(void* const* d_in, const int* in_sizes, int n_in, void* d_out, int out_size, void* d_ws,
                              size_t ws_size, hipStream_t stream) {
  Params p;
  p.u = (const u16*)d_in[0];
  p.lar = (const u16*)d_in[1];
  p.aim = (const u16*)d_in[2];
  p.cre = (const u16*)d_in[3];
  p.cim = (const u16*)d_in[4];
  p.ldt = (const u16*)d_in[5];
  p.dsk = (const u16*)d_in[6];
  p.win = (const u16*)d_in[8];
  p.wsp = (const u16*)d_in[9];
  p.bsp = (const u16*)d_in[10];
  p.wout = (const u16*)d_in[11];
  p.c = (const u16*)d_in[12];
  p.wmod = (const u16*)d_in[13];
  p.out = (u16*)d_out;
  p.ws = (unsigned char*)d_ws;
  (void)hipFuncSetAttribute((const void*)adaln_mega, hipFuncAttributeMaxDynamicSharedMemorySize, MEGA_LDS);
  adaln_mega<<<256, 256, MEGA_LDS, stream>>>(p);
}
```

```cpp
#include <hip/hip_runtime.h>
#include <hip/hip_bf16.h>
#include <stdint.h>
#include <utility>

#define LAS __attribute__((address_space(3)))
typedef unsigned v4u __attribute__((ext_vector_type(4)));
typedef unsigned v2u __attribute__((ext_vector_type(2)));
typedef float f32x4 __attribute__((ext_vector_type(4)));
typedef float f32x2 __attribute__((ext_vector_type(2)));
typedef short bf16x8 __attribute__((ext_vector_type(8)));
typedef _Float16 f16x8 __attribute__((ext_vector_type(8)));
typedef _Float16 f16x2 __attribute__((ext_vector_type(2)));
typedef __bf16 bf16x2_t __attribute__((ext_vector_type(2)));
typedef unsigned short u16;
typedef __amdgpu_buffer_rsrc_t rsrc_t;

#define STAMP_DECL do { } while (0)
#define STAMP(k, i) do { } while (0)
#define STAMP_FLUSH(k) do { } while (0)

#define OPT_A 1
#define OPT_B 1
#define OPT_C 2
#define OPT_D 1
#define OPT_E 2
#define OPT_F 0
constexpr int L = 2048, D = 512, NM = 128, FH = 688, FF = 1376;
constexpr float EPS = 1e-5f;
constexpr int SC1 = 16;
constexpr unsigned OOB = 0x40000000u;
constexpr unsigned SPIN_CAP = 1u << 14;

constexpr unsigned WS_EPOCH = 0;
constexpr unsigned WS_CNT = 4096;
constexpr unsigned WS_SR = WS_CNT + 256 * 128;
constexpr unsigned WS_SB = WS_SR + 2048 * 8;
constexpr unsigned WS_MODG = WS_SB + 2048 * 8;
constexpr unsigned WS_CSB = WS_MODG + 3072 * 32;
constexpr unsigned WS_ST2 = WS_CSB + 88 * 16 * 32;
constexpr unsigned WS_UG = 1u << 20;
static_assert(WS_ST2 + 32 * 2 * 16 * 32 * 8 <= WS_UG, "small buffers");
constexpr unsigned WS_X1T = WS_UG + 256 * 2048 * 8;
constexpr unsigned WS_WP = WS_X1T + 512 * 2048 * 2;
constexpr unsigned WS_WOT = WS_WP + 88 * 16 * 1024;
constexpr unsigned WS_Z2 = WS_WOT + 32 * 22 * 1024;
constexpr unsigned WS_V = WS_Z2 + 44 * 64 * 1024;
constexpr unsigned WS_END = WS_V + 128 * 22 * 1024;

struct Params {
  const u16 *u, *lar, *aim, *cre, *cim, *ldt, *dsk, *win, *wsp, *bsp, *wout, *c, *wmod;
  u16* out;
  unsigned char* ws;
};

#define DI __device__ __forceinline__
template <int V> struct IC { static constexpr int value = V; };
template <class F, int... I> DI void sfor_(F&& f, std::integer_sequence<int, I...>) { int d[] = {(f(IC<I>{}), 0)...}; (void)d; }
template <int N, class F> DI void sfor(F&& f) { sfor_(f, std::make_integer_sequence<int, N>{}); }
DI float bf2f(unsigned b) { return __builtin_bit_cast(float, b << 16); }
DI float bflo(unsigned w) { return __builtin_bit_cast(float, w << 16); }
DI float bfhi(unsigned w) { return __builtin_bit_cast(float, w & 0xFFFF0000u); }
DI f32x2 unpk(unsigned w) { return f32x2{bflo(w), bfhi(w)}; }
DI unsigned pk2(f32x2 v) { return __builtin_bit_cast(unsigned, __builtin_convertvector(v, bf16x2_t)); }
DI unsigned pk2(float lo, float hi) { return pk2(f32x2{lo, hi}); }
DI unsigned nomark(unsigned x) { return x < 0xFFFFFFFEu ? x : 0xFFFFFFFEu; }
DI float rbf(float x) { return bflo(pk2(x, 0.f)); }
DI f32x2 fma2(f32x2 a, f32x2 b, f32x2 c) { return __builtin_elementwise_fma(a, b, c); }
DI f32x2 lo2(f32x4 v) { return f32x2{v[0], v[1]}; }
DI f32x2 hi2(f32x4 v) { return f32x2{v[2], v[3]}; }
DI f32x2 bc2(float x) { return f32x2{x, x}; }
DI f32x4 mfma(v4u a, v4u b, f32x4 c) {
  return __builtin_amdgcn_mfma_f32_16x16x32_bf16(__builtin_bit_cast(bf16x8, a), __builtin_bit_cast(bf16x8, b), c, 0, 0, 0);
}
DI f32x4 mfma_h(v4u a, v4u b, f32x4 c) {
  return __builtin_amdgcn_mfma_f32_16x16x32_f16(__builtin_bit_cast(f16x8, a), __builtin_bit_cast(f16x8, b), c, 0, 0, 0);
}
DI float dot2(unsigned a, unsigned b, float acc) {
  return __builtin_amdgcn_fdot2_f32_bf16(__builtin_bit_cast(bf16x2_t, a), __builtin_bit_cast(bf16x2_t, b), acc, false);
}
constexpr unsigned ONES = 0x3F803F80u;
DI int perm(int u, int c) { return 32 * (u >> 1) + 8 * (c >> 2) + 4 * (u & 1) + (c & 3); }
template <int CTRL> DI float dpp(float x) {
  return __builtin_bit_cast(float, __builtin_amdgcn_mov_dpp(__builtin_bit_cast(int, x), CTRL, 0xf, 0xf, true));
}
DI float row16_sum(float x) {
  x += dpp<0xB1>(x); x += dpp<0x4E>(x); x += dpp<0x141>(x); x += dpp<0x128>(x);
  return x;
}
DI float row16_max(float x) {
  x = fmaxf(x, dpp<0xB1>(x)); x = fmaxf(x, dpp<0x4E>(x)); x = fmaxf(x, dpp<0x141>(x)); x = fmaxf(x, dpp<0x128>(x));
  return x;
}
DI float xrow_sum(float x) {
  auto s = __builtin_amdgcn_permlane16_swap(__float_as_uint(x), __float_as_uint(x), false, false);
  x = __uint_as_float(s[0]) + __uint_as_float(s[1]);
  auto t = __builtin_amdgcn_permlane32_swap(__float_as_uint(x), __float_as_uint(x), false, false);
  return __uint_as_float(t[0]) + __uint_as_float(t[1]);
}
DI float xrow_sum4(float a, float b, float c, float d) {
  auto s = __builtin_amdgcn_permlane16_swap(__float_as_uint(a), __float_as_uint(b), false, false);
  auto t = __builtin_amdgcn_permlane16_swap(__float_as_uint(c), __float_as_uint(d), false, false);
  const float x = __uint_as_float(s[0]) + __uint_as_float(s[1]), y = __uint_as_float(t[0]) + __uint_as_float(t[1]);
  auto z = __builtin_amdgcn_permlane32_swap(__float_as_uint(x), __float_as_uint(y), false, false);
  return __uint_as_float(z[0]) + __uint_as_float(z[1]);
}
DI float xrow_max(float x) {
  auto s = __builtin_amdgcn_permlane16_swap(__float_as_uint(x), __float_as_uint(x), false, false);
  x = fmaxf(__uint_as_float(s[0]), __uint_as_float(s[1]));
  auto t = __builtin_amdgcn_permlane32_swap(__float_as_uint(x), __float_as_uint(x), false, false);
  return fmaxf(__uint_as_float(t[0]), __uint_as_float(t[1]));
}
DI f32x2 gelu2(f32x2 x) {
  const float k0 = -2.0f * 0.7978845608028654f * 1.4426950408889634f, k1 = k0 * 0.044715f;
  const f32x2 a = x * fma2(x * x, bc2(k1), bc2(k0));
  const f32x2 e = f32x2{__builtin_amdgcn_exp2f(a[0]), __builtin_amdgcn_exp2f(a[1])} + bc2(1.0f);
  return x * f32x2{__builtin_amdgcn_rcpf(e[0]), __builtin_amdgcn_rcpf(e[1])};
}
DI rsrc_t mkrs(const void* ptr, unsigned bytes) { return __builtin_amdgcn_make_buffer_rsrc((void*)ptr, 0, (int)bytes, 0x00020000); }
template <int AUX> DI v4u ld128(rsrc_t rs, unsigned v, unsigned s) { return __builtin_amdgcn_raw_buffer_load_b128(rs, (int)v, (int)s, AUX); }
template <int AUX> DI v2u ld64(rsrc_t rs, unsigned v, unsigned s) { return __builtin_amdgcn_raw_buffer_load_b64(rs, (int)v, (int)s, AUX); }
template <int AUX> DI void st128(v4u x, rsrc_t rs, unsigned v, unsigned s) { __builtin_amdgcn_raw_buffer_store_b128(x, rs, (int)(v + s), 0, AUX); }
template <int AUX> DI void st64(v2u x, rsrc_t rs, unsigned v, unsigned s) { __builtin_amdgcn_raw_buffer_store_b64(x, rs, (int)v, (int)s, AUX); }
DI f32x4 asf(v4u x) { return __builtin_bit_cast(f32x4, x); }
DI float sum4(f32x4 x) { return (x[0] + x[1]) + (x[2] + x[3]); }
DI unsigned umax3(unsigned a, unsigned b, unsigned c) { unsigned m = a > b ? a : b; return m > c ? m : c; }
DI unsigned macc4(unsigned m, v4u x) { return umax3(umax3(m, x[0], x[1]), x[2], x[3]); }
DI unsigned macc(unsigned m, v4u x) { return umax3(m, x[0], x[2]); }
DI bool any_mark(unsigned m) { return __builtin_amdgcn_ballot_w64(m == 0xFFFFFFFFu) != 0ull; }
#define SB() __builtin_amdgcn_sched_barrier(0)
#define PIN(x) asm volatile("" : "+v"(x))
#define RETRY_BEGIN(m) if (__builtin_expect(any_mark(m), 0)) { unsigned sp_ = 0; do { MEMFENCE(); m = 0;
#define RETRY_END(m) } while (any_mark(m) & (++sp_ <= SPIN_CAP)); }
#define MEMFENCE() asm volatile("" ::: "memory")
DI void bar() { asm volatile("s_waitcnt lgkmcnt(0)" ::: "memory"); __builtin_amdgcn_s_barrier(); asm volatile("" ::: "memory"); }

DI f32x2 cmul(f32x2 a, f32x2 b) { return fma2(bc2(a[1]), f32x2{-b[1], b[0]}, bc2(a[0]) * b); }
DI f32x2 cexp_p(float are2, float rev_hi, float rev_lo, float p) {
  const float mag = __builtin_amdgcn_exp2f(are2 * p);
  const float hi = rev_hi * p;
  const float fr = (hi - __builtin_rintf(hi)) + rev_lo * p;
  return f32x2{mag * __builtin_amdgcn_cosf(fr), mag * __builtin_amdgcn_sinf(fr)};
}
DI float exp_acc(float x) {
  const float hi = x * 1.4426950408889634f;
  const float lo = __builtin_fmaf(x, 1.4426950408889634f, -hi) + x * 1.925963033500011e-8f;
  const float e = __builtin_amdgcn_exp2f(hi);
  return __builtin_fmaf(e, lo * 0.6931471805599453f, e);
}
DI unsigned pkh(float a, float b) {
  f16x2 v = {(_Float16)a, (_Float16)b};
  return __builtin_bit_cast(unsigned, v);
}

constexpr int CV_RK = 4160;
constexpr int CV_H = 2 * CV_RK;
constexpr int CV_CH = CV_H + 4608;
constexpr int LDS_A = 0;
constexpr int LDS_U = 2 * CV_CH;
constexpr int LDS_GS = LDS_U + 8192;
constexpr int LDS_TAB = 36864;
constexpr int KP_CH = 13840;
constexpr int LDS_KP = LDS_TAB + 2 * 64 * 528 + 16;
constexpr int LDS_LN = 0, LDS_MR = 8448;
constexpr int LDS_WI = 145472, LDS_WO = LDS_WI + 10240;
constexpr int LDS_X = 65536;
constexpr int LDS_S = LDS_X + 24576;
constexpr int LDS_W = LDS_S + 6144;
constexpr int MEGA_LDS = LDS_W + 65536;
static_assert(LDS_WO + 4608 <= MEGA_LDS && LDS_KP + 2 * KP_CH <= LDS_WI && LDS_S >= 73728, "lds");
static_assert(LDS_GS + 1280 <= LDS_TAB && LDS_KP + 2 * KP_CH <= MEGA_LDS && MEGA_LDS <= 160 * 1024, "lds");
#define FLAG_AT 110
static_assert(FLAG_AT > 57 && FLAG_AT < 144 && FLAG_AT % 6 != 3, "flag position");
#define WF_EARLY 3
#define WO0 12
#define WSP_P0 4
#define HINT4_KS 15
#define ZDEPTH 3
#define GELU_IN 0
#define EPI_W 0
#define IN_AUX 0
#define WSP_AUX 0

DI v2u trread(const unsigned char* ptr) {
  typedef short v4i16_t __attribute__((ext_vector_type(4)));
  return __builtin_bit_cast(v2u, __builtin_amdgcn_ds_read_tr16_b64_v4i16((LAS v4i16_t*)ptr));
}
struct CvOrder { int qq[36], pp[36]; };
constexpr CvOrder cv_order() {
  CvOrder o{};
  int n = 0;
  for (int m = 0; m < 8; ++m) {
    for (int j = 0; j < m; ++j) if (m + j < 8) { o.qq[n] = m; o.pp[n] = j; ++n; }
    for (int j = 0; j <= m; ++j) if (m + j < 8) { o.qq[n] = j; o.pp[n] = m; ++n; }
  }
  return o;
}
constexpr CvOrder CVO = cv_order();

typedef __attribute__((address_space(1))) unsigned gu32;
DI unsigned tacc(unsigned m, v4u x, unsigned tag) { return m | (x[1] ^ tag) | (x[3] ^ tag); }
DI bool any_nz(unsigned m) { return __builtin_amdgcn_ballot_w64(m != 0u) != 0ull; }
DI void hint_add(unsigned char* ws, int line) { (void)__hip_atomic_fetch_add((gu32*)(ws + WS_CNT + line * 128), 1u, __ATOMIC_RELAXED, __HIP_MEMORY_SCOPE_AGENT); }
DI void hint_wait(rsrc_t rws, unsigned voff, unsigned soff, unsigned want) {
  unsigned sp = 0;
  for (;;) {
    MEMFENCE();
    const unsigned v = __builtin_amdgcn_raw_buffer_load_b32(rws, (int)voff, (int)soff, SC1);
    if (!((__builtin_amdgcn_ballot_w64(v < want) != 0ull) & (++sp <= SPIN_CAP))) break;
    __builtin_amdgcn_s_sleep(OPT_E);
  }
  MEMFENCE();
}

extern "C" __global__ void __launch_bounds__(256, 1) adaln_mega(Params p) {
  extern __shared__ __attribute__((aligned(16))) unsigned char smem[];
  const int tid = threadIdx.x, lane = tid & 63, w = __builtin_amdgcn_readfirstlane(tid >> 6);
  const int b = blockIdx.x, xcd = b & 7, idx = b >> 3;
  const int pb = xcd * 32 + idx;
  const unsigned l16 = lane * 16;
  STAMP_DECL;
  STAMP(1, 0);
  v2u codepf;
  const rsrc_t ru = mkrs(p.u, L * D * 2);
  v4u xr[2];
  xr[0] = ld128<IN_AUX>(ru, l16, (pb * 8 + w * 2) * 1024);
  xr[1] = ld128<IN_AUX>(ru, l16 + 1024, (pb * 8 + w * 2) * 1024);
  const int mkg = b & 3, mcg = b >> 2;
  const rsrc_t rmod = mkrs(p.wmod, D * 3072 * 2);
  v4u xm[4];
#pragma unroll
  for (int i = 0; i < 4; ++i) xm[i] = ld128<IN_AUX>(rmod, (tid >> 3) * 6144 + (tid & 7) * 16, (mkg * 128 + i * 32) * 6144 + mcg * 128 + (b < 192 ? 0u : OOB));
  const unsigned cq = p.c[mkg * 128 + (tid & 3) * 32 + (tid >> 3)];
  const unsigned epoch = *(const unsigned*)(p.ws + WS_EPOCH);
  const rsrc_t rws = mkrs(p.ws, WS_END);
  __builtin_amdgcn_raw_buffer_store_b32(0u, rws, tid == 0 ? 0u : OOB, WS_CNT + b * 128, SC1);
  SB();
  const int cch = w >> 1, eh = w & 1, ns = tid & 127;
  const unsigned q_cre = p.cre[pb * 256 + tid], q_cim = p.cim[pb * 256 + tid], q_lar = p.lar[pb * 256 + tid], q_aim = p.aim[pb * 256 + tid];
  const unsigned q_ldt = p.ldt[pb * 2 + cch], q_dsk = p.dsk[pb * 2 + cch];
  {
    unsigned long long pc;
    int rel_m, rel_e;
    asm volatile("s_getpc_b64 %0\n\ts_mov_b32 %1, adaln_mega@rel32@lo+4\n\ts_mov_b32 %2, adaln_code_end@rel32@lo+12" : "=s"(pc), "=s"(rel_m), "=s"(rel_e));
    const rsrc_t rcode = mkrs((const void*)(pc + (long long)rel_m), rel_e > rel_m ? (unsigned)(rel_e - rel_m) & ~15u : 0u);
    codepf = ld64<0>(rcode, tid * 8, idx * 2048);
  }
  SB();
  STAMP(1, 24);
  const int c = lane & 15, q = lane >> 4;
  int pos4[9];
  {
    const float f4 = (float)(8188 - tid * 32);
#pragma unroll
    for (int j = 0; j < 10; j += 2) {
      const unsigned pp = pk2(bc2(f4) - f32x2{4.f * j, 4.f * j + 4.f});
      pos4[j] = (int)bflo(pp);
      if (OPT_A) pos4[j] += (pos4[j] >> 5) << 2;
      PIN(pos4[j]);
      if (j + 1 < 9) { pos4[j + 1] = (int)bfhi(pp); if (OPT_A) pos4[j + 1] += (pos4[j + 1] >> 5) << 2; PIN(pos4[j + 1]); }
    }
  }
  int cob[2], cos_[2];
  cob[0] = (eh ? 1024 + 512 * q : 128 * (q >> 1) + 4 * (q & 1)) * 4 + 32 * c + LDS_KP + cch * KP_CH;
  cob[1] = (q < 2 ? 256 + 128 * q : 504 + 4 * q) * 4 + 32 * c + LDS_KP + cch * KP_CH;
  cos_[0] = eh ? 512 : 4;
  cos_[1] = q < 2 ? 8 : 512;
  if (OPT_A) {
    cob[0] = (eh ? 1152 + 576 * q : 144 * (q >> 1) + 4 * (q & 1)) * 4 + 36 * c + LDS_KP + cch * KP_CH;
    cob[1] = (q < 2 ? 288 + 144 * q : 568 + 4 * q) * 4 + 36 * c + LDS_KP + cch * KP_CH;
    cos_[0] = eh ? 576 : 4;
    cos_[1] = q < 2 ? 8 : 576;
  }
  PIN(cob[0]); PIN(cob[1]); PIN(cos_[1]);
  SB();
  STAMP(1, 26);
  {
    *(v4u*)(smem + LDS_LN + (w * 2) * 1040 + l16) = xr[0];
    *(v4u*)(smem + LDS_LN + (w * 2 + 1) * 1040 + l16) = xr[1];
    float sm[2], sq[2];
#pragma unroll
    for (int i = 0; i < 2; ++i) {
      sm[i] = dot2(xr[i][0], ONES, dot2(xr[i][1], ONES, dot2(xr[i][2], ONES, dot2(xr[i][3], ONES, 0.f))));
      sq[i] = dot2(xr[i][0], xr[i][0], dot2(xr[i][1], xr[i][1], dot2(xr[i][2], xr[i][2], dot2(xr[i][3], xr[i][3], 0.f))));
    }
    const float tv = row16_sum(xrow_sum4(sm[0], sm[1], sq[0], sq[1]));
    auto sw = __builtin_amdgcn_permlane32_swap(__float_as_uint(tv), __float_as_uint(tv), false, false);
    const float m = __uint_as_float(sw[0]) * (1.0f / D);
    const float r = __builtin_amdgcn_rsqf(fmaxf(__uint_as_float(sw[1]) * (1.0f / D) - m * m, 0.f) + EPS);
    ((float*)(smem + LDS_GS + 1088))[(q >> 1) * 8 + w * 2 + (q & 1)] = (q >> 1) ? -m * r : r;
    const float cv = bf2f(cq);
    const float sl = rbf(cv * __builtin_amdgcn_rcpf(1.0f + __builtin_amdgcn_exp2f(cv * -1.4426950408889634f)));
    const float sc[4] = {dpp<0x00>(sl), dpp<0x55>(sl), dpp<0xAA>(sl), dpp<0xFF>(sl)};
    f32x2 a[4];
#pragma unroll
    for (int i = 0; i < 4; ++i)
#pragma unroll
      for (int j = 0; j < 4; ++j) a[j] = i == 0 ? unpk(xm[i][j]) * bc2(sc[i]) : fma2(unpk(xm[i][j]), bc2(sc[i]), a[j]);
    float* red = (float*)(smem + LDS_MR) + (tid >> 3) * 68 + (tid & 7) * 8;
    *(f32x4*)red = f32x4{a[0][0], a[0][1], a[1][0], a[1][1]};
    *(f32x4*)(red + 4) = f32x4{a[2][0], a[2][1], a[3][0], a[3][1]};
  }
  const unsigned tag = epoch * 2654435761u + 0x9E3779B9u;
  STAMP(1, 28);
  asm volatile("s_waitcnt vmcnt(3)" ::: "memory");
  bar();
  STAMP(1, 16);
  {
    unsigned ua[4][2];
#pragma unroll
    for (int i = 0; i < 4; ++i) {
      const unsigned char* src = smem + LDS_LN + (tid & 3) * 2080 + (i * 64 + (tid >> 2)) * 4;
      ua[i][0] = *(const unsigned*)src; ua[i][1] = *(const unsigned*)(src + 1040);
    }
    float ms8[8];
#pragma unroll
    for (int i = 0; i < 8; ++i) ms8[i] = ((const float*)(smem + LDS_MR))[(q * 8 + i) * 68 + w * 16 + c];
    const unsigned lnv = ((const unsigned*)(smem + LDS_GS + 1088))[c];
    SB();
#pragma unroll
    for (int i = 0; i < 4; ++i) st128<SC1>(v4u{ua[i][0], tag, ua[i][1], tag}, rws, (tid >> 2) * 16384 + (tid & 3) * 16, WS_UG + i * 64 * 16384 + pb * 64);
    const float s = xrow_sum(((ms8[0] + ms8[1]) + (ms8[2] + ms8[3])) + ((ms8[4] + ms8[5]) + (ms8[6] + ms8[7])));
    st64<SC1>(v2u{__float_as_uint(s), tag}, rws, q == 0 ? c * 8 : OOB, WS_MODG + (mkg * 3072 + mcg * 64 + w * 16) * 8 + (b < 192 ? 0u : OOB));
    st64<SC1>(v2u{lnv, tag}, rws, tid < 16 ? (c >> 3) * 16384 + (c & 7) * 8 : OOB, WS_SR + pb * 64);
  }
  STAMP(1, 17);
  SB();
  const unsigned wp_oob = b < 176 ? 0u : OOB;
  const int wpr = b >> 2, wkg = b & 3;
  v4u xi[2], xo;
  {
    const rsrc_t rwin = mkrs(p.win, D * FF * 2), rwout = mkrs(p.wout, FH * D * 2);
    const int hf = wpr >= 22, pp = wpr - 22 * hf;
    const unsigned v = ((tid >> 2) * (FF * 2) + (tid & 3) * 16) | ((pp == 21 ? tid & 2 : 0) << 29);
#pragma unroll
    for (int i = 0; i < 2; ++i) xi[i] = ld128<IN_AUX>(rwin, v, (wkg * 128 + i * 64) * (FF * 2) + (hf * FH + pp * 32) * 2 + wp_oob);
    xo = ld128<IN_AUX>(rwout, (tid >> 3) * 1024 + (tid & 7) * 16, idx * 32 * 1024 + xcd * 128);
  }
  SB();
  const int d0 = pb * 2, dk = d0 + cch;
  const int mt = (xcd >> 1) * 8 + (idx & 7), nt = (xcd & 1) * 4 + (idx >> 3);
  const int big = (nt + 2) >> 3;
  const int np = 3 - big, pr0 = 3 * nt - big * (nt - 6);
  const int kh = w & 1, uh = w >> 1;
  const int ub_own = pr0 * 2 + uh * np, ub_oth = pr0 * 2 + (uh ^ 1) * np;
  const unsigned oob3 = (unsigned)big << 30;
  const int kr = 2 * (idx >> 3) + (xcd & 1) + (OPT_B == 0 ? (idx & 7) : OPT_B == 1 ? 4 * (idx & 1) : OPT_B == 2 ? 2 * (idx & 3) : 0);
  const rsrc_t rwsp = mkrs(p.wsp, L * L * 2);
  const unsigned wp_fr = (wpr * 2 * 16 + wkg * 4 + w) * 1024 + wp_oob;
  v4u wr[8][8];
  const unsigned wsp_v = (lane >> 3) * 4096 + (lane & 7) * 16, wsp_s = mt * 64 * 4096 + w * 1024;
#define LOAD_WSP1(ck, i) wr[ck][i] = ld128<WSP_AUX>(rwsp, wsp_v + (((ck) + kr) & 7) * 128, wsp_s + (i) * 8 * 4096)
#define LOAD_WSPN(n) LOAD_WSP1((n) >> 3, (n) & 7)
  const v4u mk = {~0u, ~0u, ~0u, ~0u};
  {
    const float dt = rbf(exp_acc(bf2f(q_ldt)));
    const float cre = bf2f(q_cre) * dt, cim = bf2f(q_cim) * dt;
    const float are2 = -rbf(exp_acc(bf2f(q_lar))) * dt * 1.4426950408889634f;
    const float aim = bf2f(q_aim) * dt;
    const float rev_hi = aim * 0.15915494309189535f;
    const float rev_lo = __builtin_fmaf(aim, 0.15915494309189535f, -rev_hi) + aim * 6.4206383e-9f;
    int ex = __builtin_amdgcn_frexp_expf(xrow_max(row16_max(fmaxf(fabsf(cre), fabsf(cim))))) + 1;
    ex = ex < -100 ? -100 : ex;
    const float scale = __builtin_amdgcn_ldexpf(1.f, -ex);
    ((float*)(smem + LDS_GS + 1024))[w] = __builtin_amdgcn_ldexpf(2.f, ex);
    const f32x2 c0 = {cre * scale, cim * scale};
    const f32x2 e1 = cexp_p(are2, rev_hi, rev_lo, 1.f), e8 = cexp_p(are2, rev_hi, rev_lo, 8.f), e64 = cexp_p(are2, rev_hi, rev_lo, 64.f);
    unsigned char* tn = smem + LDS_TAB + cch * (64 * 528) + ns * 4;
#define TROW(r, z) *(unsigned*)(tn + (r) * 528) = pkh((z)[0], -(z)[1])
    SB();
    const f32x2 e128 = cmul(e64, e64), e2 = cmul(e1, e1), e256 = cmul(e128, e128), e4 = cmul(e2, e2), e512 = cmul(e256, e256);
    f32x2 f0 = {1.f, 0.f}, f1 = e64;
    f32x2 ca = cmul(c0, e512), cb = cmul(ca, e512);
#pragma unroll
    for (int j = 0; j < 8; ++j) {
      *(unsigned*)(tn + (48 + j) * 528) = pkh(f0[0], f0[1]);
      *(unsigned*)(tn + (56 + j) * 528) = pkh(f1[0], f1[1]);
      TROW(32 + j, cb);
      f0 = j == 0 ? e8 : cmul(f0, e8); f1 = cmul(f1, e8); cb = cmul(cb, e128);
      if (j < 4) {
        const f32x2 z = cmul(ca, e4);
        TROW(24 + j, ca); TROW(28 + j, z);
        ca = cmul(ca, e128);
      }
      if (j == 3 || j == 7) {
        PIN(f0); PIN(f1); PIN(cb);
        if (WSP_P0 >= 4) { LOAD_WSPN(j >> 2 << 1); LOAD_WSPN((j >> 2 << 1) + 1); }
        SB();
      }
    }
    TROW(40, cb);
    f32x2 t1[8];
    t1[1] = e1; t1[2] = e2; t1[4] = e4;
    t1[3] = cmul(e2, e1); t1[5] = cmul(e4, e1); t1[6] = cmul(e2, e4); t1[7] = cmul(t1[6], e1);
    const f32x2 c1 = cmul(c0, e128), c2 = cmul(c0, e256), c3 = cmul(c2, e128);
    TROW(0, c0); TROW(8, c1); TROW(16, c2); TROW(20, c3);
#pragma unroll
    for (int v = 1; v < 8; ++v) {
      const f32x2 z0 = cmul(t1[v], c0), z1 = cmul(t1[v], c1);
      TROW(v, z0); TROW(8 + v, z1);
      if ((v & 1) == 0) {
        const f32x2 z2 = cmul(t1[v], c2), z3 = cmul(t1[v], c3);
        TROW(16 + v / 2, z2); TROW(20 + v / 2, z3);
        if (v < 6 && WSP_P0 >= 8) { LOAD_WSPN(v + 2); LOAD_WSPN(v + 3); }
        SB();
      }
    }
  }
  STAMP(1, 29);
  bar();
  STAMP(1, 18);
  v4u gr[4], gb_[4];
  v2u gm[4], gc[4];
  const unsigned gm_off = ((cch ? 3 * 512 : 4 * 512) + wkg * 128 + ns) * 8;
  const unsigned gc_off = (((lane & 7) > 5 ? 0 : ((lane & 7) >> 1)) * 512 + d0 + (lane & 1)) * 8;
#define LOAD_GS() _Pragma("unroll") for (int i_ = 0; i_ < 4; ++i_) { gr[i_] = ld128<SC1>(rws, tid * 16, WS_SR + i_ * 4096); gb_[i_] = ld128<SC1>(rws, tid * 16, WS_SB + i_ * 4096); }
#define LOAD_GM() _Pragma("unroll") for (int i_ = 0; i_ < 4; ++i_) { gm[i_] = ld64<SC1>(rws, gm_off, WS_MODG + i_ * 24576); gc[i_] = ld64<SC1>(rws, gc_off, WS_MODG + i_ * 24576); }
  {
    const unsigned char* tab = smem + LDS_TAB + cch * (64 * 528) + c * 528 + q * 16;
    v4u fa[2][8], fb[8];
#pragma unroll
    for (int ks = 0; ks < 4; ++ks) {
      fb[ks] = *(const v4u*)(tab + 48 * 528 + ks * 64);
      fa[0][ks] = *(const v4u*)(tab + eh * (32 * 528) + ks * 64);
      fa[1][ks] = *(const v4u*)(tab + 16 * 528 + ks * 64);
    }
    const f32x2 us = *(const f32x2*)(smem + LDS_GS + 1024 + cch * 8);
    SB();
    f32x4 kk[2][2];
#pragma unroll
    for (int i = 0; i < 4; ++i) kk[i >> 1][i & 1] = f32x4{0.f, 0.f, 0.f, 0.f};
#pragma unroll
    for (int ks = 0; ks < 8; ++ks) {
      if (ks < 4) {
        fb[4 + ks] = *(const v4u*)(tab + 48 * 528 + (4 + ks) * 64);
        fa[0][4 + ks] = *(const v4u*)(tab + eh * (32 * 528) + (4 + ks) * 64);
        fa[1][4 + ks] = *(const v4u*)(tab + 16 * 528 + (4 + ks) * 64);
      }
      kk[0][ks >> 2] = mfma_h(fa[0][ks], fb[ks], kk[0][ks >> 2]); kk[1][ks >> 2] = mfma_h(fa[1][ks], fb[ks], kk[1][ks >> 2]);
      SB();
    }
    STAMP(1, 37);
    *(v4u*)(smem + (lane == 63 ? LDS_KP - 16 + cch * KP_CH : cch * CV_CH + (lane < 32 ? CV_H + l16 : ((lane >> 1) & 1) * CV_RK + 4096 + (lane & 1) * 16))) = v4u{0u, 0u, 0u, 0u};
#pragma unroll
    for (int i = 0; i < 2; ++i) *(v4u*)(smem + LDS_WI + (i * 64 + (tid >> 2)) * 80 + (tid & 3) * 16) = xi[i];
    *(v4u*)(smem + LDS_WO + (tid >> 3) * 144 + (tid & 7) * 16) = xo;
    f32x4 k0 = kk[0][0] * us[0] + kk[0][1] * us[1];
    const f32x4 k1 = kk[1][0] * us[0] + kk[1][1] * us[1];
    k0[0] += ns == 0 ? bf2f(q_dsk) : 0.f;
#pragma unroll
    for (int r = 0; r < 4; ++r) { *(float*)(smem + cob[0] + r * cos_[0]) = k0[r]; *(float*)(smem + cob[1] + r * cos_[1]) = k1[r]; }
  }
  v4u ug[4];
#define LOAD_UG() _Pragma("unroll") for (int i_ = 0; i_ < 4; ++i_) ug[i_] = ld128<SC1>(rws, tid * 16, WS_UG + pb * 16384 + i_ * 4096)
  STAMP(1, 31);
  LOAD_GM();
  LOAD_UG();
  bar();
  STAMP(1, 19);
  v4u wt[2];
  {
    float v[2][9];
#pragma unroll
    for (int j = 0; j < 9; ++j) v[0][j] = *(const float*)(smem + LDS_KP + pos4[j]);
    LOAD_GS();
#pragma unroll
    for (int j = 0; j < 9; ++j) v[1][j] = *(const float*)(smem + LDS_KP + KP_CH + pos4[j]);
    SB();
    STAMP(1, 36);
#pragma unroll
    for (int ch = 0; ch < 2; ++ch) {
      unsigned char* rk = smem + ch * CV_CH + tid * 16;
#pragma unroll
      for (int sft = 0; sft < 2; ++sft)
        *(v4u*)(rk + sft * CV_RK) = v4u{pk2(v[ch][sft], v[ch][sft + 1]), pk2(v[ch][sft + 2], v[ch][sft + 3]), pk2(v[ch][sft + 4], v[ch][sft + 5]), pk2(v[ch][sft + 6], v[ch][sft + 7])};
    }
    const unsigned char* a0 = smem + LDS_WI + (w * 32 + 8 * q + ((lane >> 2) & 3)) * 80 + (lane & 3) * 16;
#pragma unroll
    for (int e = 0; e < 2; ++e) {
      const v2u lo = trread(a0 + e * 8), hi = trread(a0 + e * 8 + 320);
      wt[e] = v4u{lo[0], lo[1], hi[0], hi[1]};
    }
    const unsigned char* o0 = smem + LDS_WO + (8 * q + ((lane >> 2) & 3)) * 144 + (w >> 1) * 64 + (lane & 3) * 16 + (w & 1) * 8;
    const v2u olo = trread(o0), ohi = trread(o0 + 576);
    st128<SC1>(v4u{olo[0], olo[1], ohi[0], ohi[1]}, rws, l16, WS_WOT + ((xcd * 4 + w) * 22 + idx) * 1024 + wp_oob);
    SB();
    STAMP(1, 32);
    unsigned m = 0;
#pragma unroll
    for (int i = 0; i < 4; ++i) m = m | (gm[i][1] ^ tag) | (gc[i][1] ^ tag);
    if (__builtin_expect(any_nz(m), 0)) {
      unsigned sp = 0;
      do {
        MEMFENCE(); LOAD_GM(); m = 0;
#pragma unroll
        for (int i = 0; i < 4; ++i) m = m | (gm[i][1] ^ tag) | (gc[i][1] ^ tag);
      } while (any_nz(m) & (++sp <= SPIN_CAP));
    }
    const float mv = rbf((__uint_as_float(gm[0][0]) + __uint_as_float(gm[1][0])) + (__uint_as_float(gm[2][0]) + __uint_as_float(gm[3][0])));
    if (cch) *(u16*)(smem + LDS_GS + 512 + ns * 2) = (u16)(__float_as_uint(mv) >> 16);
    else *(float*)(smem + LDS_GS + ns * 4) = rbf(1.0f + mv);
    const float cv = rbf((__uint_as_float(gc[0][0]) + __uint_as_float(gc[1][0])) + (__uint_as_float(gc[2][0]) + __uint_as_float(gc[3][0])));
    const float gav = dpp<0x104>(cv), gav2 = dpp<0x102>(cv);
    const float hbv = cv * gav, hsv = rbf(1.0f + cv) * gav2;
    const f32x2 hb = {__builtin_bit_cast(float, __builtin_amdgcn_readlane(__builtin_bit_cast(int, hbv), 0)), __builtin_bit_cast(float, __builtin_amdgcn_readlane(__builtin_bit_cast(int, hbv), 1))};
    const f32x2 hs = {__builtin_bit_cast(float, __builtin_amdgcn_readlane(__builtin_bit_cast(int, hsv), 2)), __builtin_bit_cast(float, __builtin_amdgcn_readlane(__builtin_bit_cast(int, hsv), 3))};
    SB();
    STAMP(1, 33);
    m = 0;
#pragma unroll
    for (int i = 0; i < 4; ++i) { m = tacc(m, ug[i], tag); m = tacc(m, gr[i], tag); m = tacc(m, gb_[i], tag); }
    if (__builtin_expect(any_nz(m), 0)) {
      unsigned sp = 0;
      do {
        MEMFENCE(); LOAD_UG(); LOAD_GS(); m = 0;
#pragma unroll
        for (int i = 0; i < 4; ++i) { m = tacc(m, ug[i], tag); m = tacc(m, gr[i], tag); m = tacc(m, gb_[i], tag); }
      } while (any_nz(m) & (++sp <= SPIN_CAP));
    }
#pragma unroll
    for (int i = WSP_P0; i < WSP_P0 + 4; ++i) LOAD_WSPN(i);
    STAMP(1, 38);
#pragma unroll
    for (int i = 0; i < 4; ++i) {
      const f32x2 h0 = fma2(fma2(unpk(ug[i][0]), bc2(__uint_as_float(gr[i][0])), bc2(__uint_as_float(gb_[i][0]))), hs, hb);
      const f32x2 h1 = fma2(fma2(unpk(ug[i][2]), bc2(__uint_as_float(gr[i][2])), bc2(__uint_as_float(gb_[i][2]))), hs, hb);
      *(unsigned*)(smem + CV_H + 512 + i * 1024 + tid * 4) = pk2(h0[0], h1[0]);
      *(unsigned*)(smem + CV_CH + CV_H + 512 + i * 1024 + tid * 4) = pk2(h0[1], h1[1]);
      *(unsigned*)(smem + LDS_U + i * 1024 + tid * 4) = __builtin_amdgcn_perm(ug[i][2], ug[i][0], 0x05040100u);
      *(unsigned*)(smem + LDS_U + 4096 + i * 1024 + tid * 4) = __builtin_amdgcn_perm(ug[i][2], ug[i][0], 0x07060302u);
    }
  }
  asm volatile("" ::"v"(codepf));
  const unsigned char* abase = smem + cch * CV_CH + (1 - (c & 1)) * CV_RK + 2 * (2030 - 2 * (c >> 1) + 8 * q) - eh * 256 - 192 - 3584;
  const unsigned char* bbase = smem + cch * CV_CH + CV_H + 2 * (256 - 16 + 16 * c + 8 * q) - eh * 256 - 192;
  STAMP(1, 39);
  bar();
  STAMP(1, 1);
#define CV_A(ee, k) ({ const unsigned* p_ = (const unsigned*)(abase + 192 - 64 * (ee) + 3584 - 512 * (k)); v4u{p_[0], p_[1], p_[2], p_[3]}; })
#define CV_B(ee, k) (*(const v4u*)(bbase + 192 - 64 * (ee) + 512 * (k)))
  v4u cA[2][8], cB[2][8];
  f32x4 gs[2];
  v4u sbf;
  const v4u ones4 = {ONES, ONES, ONES, ONES};
#pragma unroll
  for (int k = 0; k < 2; ++k) { cA[0][k] = CV_A(0, k); cB[0][k] = CV_B(0, k); }
  {
    const float* gp = (const float*)(smem + LDS_GS) + w * 32 + q * 8;
    gs[0] = *(const f32x4*)gp; gs[1] = *(const f32x4*)(gp + 4);
    sbf = *(const v4u*)(smem + LDS_GS + 512 + w * 64 + q * 16);
  }
  SB();
  f32x4 cacc[8];
  v2u ue[4];
  {
    f32x4 cb4[4];
    v4u wo_[2];
    float wl, wh;
    f32x2 cp[4];
#pragma unroll
    for (int t = 0; t < 8; ++t) cacc[t] = f32x4{0.f, 0.f, 0.f, 0.f};
    sfor<144>([&](auto G) __attribute__((always_inline)) {
      constexpr int g = decltype(G)::value, ee = g / 36, j = g % 36;
      if constexpr (ee == 0 && j < 6) { cA[0][2 + j] = CV_A(0, 2 + j); cB[0][2 + j] = CV_B(0, 2 + j); }
      cacc[CVO.pp[j] + CVO.qq[j]] = mfma(cA[ee & 1][CVO.qq[j]], cB[ee & 1][CVO.pp[j]], cacc[CVO.pp[j] + CVO.qq[j]]);
      constexpr int jn = ee == 0 ? j - 20 : (j & 1) ? j >> 1 : -1;
      if constexpr (ee < 3 && jn >= 0 && jn < 16) {
        if constexpr (jn & 1) cB[(ee + 1) & 1][jn >> 1] = CV_B(ee + 1, jn >> 1);
        else cA[(ee + 1) & 1][jn >> 1] = CV_A(ee + 1, jn >> 1);
      }
      if constexpr (ee == 3 && j < 4) ue[j] = *(const v2u*)(smem + LDS_U + cch * 4096 + (eh * 4 + j) * 512 + (c * 16 + q * 4) * 2);
      if constexpr (g >= 6 && g < 30) {
        constexpr int i = (g - 6) / 12, jj = ((g - 6) / 3) & 3, ph = (g - 6) % 3;
        if constexpr (ph == 0) { wl = bflo(wt[i][jj]) * gs[jj >> 1][(jj & 1) * 2]; PIN(wl); }
        if constexpr (ph == 1) { wh = bfhi(wt[i][jj]) * gs[jj >> 1][(jj & 1) * 2 + 1]; PIN(wh); }
        if constexpr (ph == 2) {
          wo_[i][jj] = pk2(wl, wh);
          if constexpr (jj == 3) st128<SC1>(wo_[i], rws, l16 + i * 16384, WS_WP + wp_fr);
          else PIN(wo_[i][jj]);
        }
      }
      if constexpr (g == 4 || g == 5) cb4[2 * (g - 4) + 1] = mfma(sbf, wt[g - 4], f32x4{0.f, 0.f, 0.f, 0.f});
      if constexpr (g == 19 || g == 31) cb4[g == 19 ? 0 : 2] = mfma(ones4, wo_[g == 31], f32x4{0.f, 0.f, 0.f, 0.f});
      if constexpr (g == 38) *(f32x4*)(smem + LDS_S + 4096 + (w * 16 + c) * 16) = f32x4{cb4[0][0], cb4[1][0], cb4[2][0], cb4[3][0]};
      if constexpr (g == 46) bar();
      if constexpr (g == 47) {
#pragma unroll
        for (int k = 0; k < 4; ++k) cp[k] = *(const f32x2*)(smem + LDS_S + 4096 + (k * 16 + c) * 16 + (q & 1) * 8);
      }
      if constexpr (g == 54) {
        float s0 = cp[0][0] + cp[1][0], s1 = cp[2][0] + cp[3][0];
        PIN(s0); PIN(s1);
        s0 += s1;
        PIN(s0);
        float s2 = cp[0][1] + cp[1][1], s3 = cp[2][1] + cp[3][1];
        PIN(s2); PIN(s3);
        st64<SC1>(v2u{__float_as_uint(s0), __float_as_uint(s2 + s3)}, rws, tid < 32 ? tid * 32 : OOB, WS_CSB + wpr * 1024 + wkg * 8 + wp_oob);
      }
      if constexpr (g == 1 || g == 5) st128<SC1>(mk, rws, l16 + (g == 5) * 1024, WS_X1T + dk * 4096 + eh * 2048);
      if constexpr (g == 9) __builtin_amdgcn_raw_buffer_store_b32(~0u, rws, lane * 4, WS_ST2 + ((mt * 2 + kh) * 16 + nt * 2 + uh) * 256, SC1);
      if constexpr (g == 42) { if (b == 0 && tid == 0) *(unsigned*)(p.ws + WS_EPOCH) = tag; }
      if constexpr (g == 36) STAMP(1, 40);
      if constexpr (g == 72) STAMP(1, 41);
      if constexpr (g == FLAG_AT) {
        STAMP(1, 42);
        asm volatile("s_waitcnt vmcnt(%0)" ::"i"((FLAG_AT - 57 + 5) / 6) : "memory");
        __builtin_amdgcn_s_barrier();
        if (tid < 8) hint_add(p.ws, tid * 8 + (idx & 7));
        STAMP(1, 43);
      }
      if constexpr (g % 6 == 3) LOAD_WSPN(WSP_P0 + 4 + g / 6);
      SB();
    });
  }
  STAMP(1, 2);
  {
    f32x4* red = (f32x4*)(smem + LDS_X) + w * 256 + lane;
    f32x4 keep[4];
    if (eh) {
#pragma unroll
      for (int t = 0; t < 4; ++t) { red[t * 64] = cacc[t]; keep[t] = cacc[4 + t]; PIN(keep[t]); }
    } else {
#pragma unroll
      for (int t = 0; t < 4; ++t) { red[t * 64] = cacc[4 + t]; keep[t] = cacc[t]; PIN(keep[t]); }
    }
    f32x2 pre[4][2];
#pragma unroll
    for (int t = 0; t < 4; ++t) {
      pre[t][0] = unpk(ue[t][0]) + lo2(keep[t]);
      pre[t][1] = unpk(ue[t][1]) + hi2(keep[t]);
      PIN(pre[t][0]); PIN(pre[t][1]);
    }
    const unsigned xoff = ((c >> 1) * 32 + ((q & 1) * 4 + 2 * (c & 1) + (q >> 1)) * 4) * 2;
    bar();
    const f32x4* rd = (const f32x4*)(smem + LDS_X) + (w ^ 1) * 256 + lane;
    f32x4 oth[4];
#pragma unroll
    for (int t = 0; t < 4; ++t) oth[t] = rd[t * 64];
#pragma unroll
    for (int t = 0; t < 4; ++t) {
      v2u o;
      o[0] = nomark(pk2(pre[t][0] + lo2(oth[t])));
      o[1] = pk2(pre[t][1] + hi2(oth[t]));
      st64<SC1>(o, rws, xoff + t * 512, WS_X1T + (d0 + cch) * 4096 + eh * 2048);
    }
  }
  STAMP(1, 3);
  SB();
  v4u wf[3][8];
  const unsigned rot = uh * 4;
#define LOAD_WP(half, k, i) ld128<SC1>(rws, l16 + ((i) & 3) * 1024, WS_WP + (((half) * 44 + ub_own + (k)) * 16 + kh * 8 + (((i) & 4) ^ rot)) * 1024 + ((k) == 2 ? oob3 : 0u))
#define LOAD_XS(i) ld128<SC1>(rws, wsp_v, WS_X1T + ((i) * 32 + w * 8) * 4096 + mt * 128)
  unsigned char* wslot = smem + LDS_W + w * 16384;
  const int wr_off = (lane >> 3) * 128 + (((lane & 7) ^ ((lane >> 4) & 1)) * 16);
#define PARK1(ck, slot, i) *(v4u*)((slot) + (i) * 1024 + (wr_off ^ (((i) & 3) * 32))) = wr[ck][i]
#define PARK_WSP(ck, slot) _Pragma("unroll") for (int i_ = 0; i_ < 8; ++i_) PARK1(ck, slot, i_)
  PARK_WSP(0, wslot);
  PARK_WSP(1, wslot + 8192);
  v4u xs[16];
  const unsigned char* tb[2][4];
  unsigned char* xdst;
  {
    const int row = 8 * q + ((lane >> 2) & 3);
    const int sg = ((row >> 1) & 1) | (((row >> 3) & 1) << 1);
#pragma unroll
    for (int tu = 0; tu < 4; ++tu) {
      const unsigned char* t0 = smem + LDS_A + kh * 32768 + row * 128 + ((tu ^ (2 * kh) ^ sg) * 32) + (lane & 3) * 8;
      tb[0][tu] = t0 + uh * 16384;
      tb[1][tu] = t0 + (uh ^ 1) * 16384;
    }
    const int rl = w * 8 + (lane >> 3), chn = lane & 7;
    const int sg2 = ((rl >> 1) & 1) | (((rl >> 3) & 1) << 1);
    xdst = smem + LDS_A + rl * 128 + (((chn >> 1) ^ sg2) * 32) + (chn & 1) * 16;
  }
  SB();
  f32x4 cz[2][3][2];
#define LOAD_CZ(hf, kc, ic) asf(ld128<SC1>(rws, c * 32 + (ic) * 16, WS_CSB + ((hf) * 44 + ub_own + (kc)) * 512 + ((kc) == 2 ? oob3 : 0u)))
  {
    STAMP(1, 20);
    hint_wait(rws, (lane & 7) * 128, WS_CNT + xcd * 1024, 32u);
    STAMP(1, 21);
#pragma unroll
    for (int i = 0; i < WF_EARLY; ++i)
#pragma unroll
      for (int k = 0; k < 3; ++k) wf[k][i] = LOAD_WP(1, k, i);
#pragma unroll
    for (int i = 0; i < 16; ++i) xs[i] = LOAD_XS(i);
    SB();
    STAMP(1, 44);
    unsigned m = 0;
#pragma unroll
    for (int i = 0; i < 16; ++i) {
      m = macc(m, xs[i]); *(v4u*)(xdst + i * 4096) = xs[i];
      if (WF_EARLY * 3 + i < 24) wf[(WF_EARLY * 3 + i) % 3][(WF_EARLY * 3 + i) / 3] = LOAD_WP(1, (WF_EARLY * 3 + i) % 3, (WF_EARLY * 3 + i) / 3);
      SB();
    }
    PIN(m);
    STAMP(1, 45);
    RETRY_BEGIN(m)
#pragma unroll
      for (int i = 0; i < 16; ++i) xs[i] = LOAD_XS(i);
      SB();
#pragma unroll
      for (int i = 0; i < 16; ++i) { m = macc(m, xs[i]); *(v4u*)(xdst + i * 4096) = xs[i]; }
    RETRY_END(m)
  }
  STAMP(1, 4);
  bar();
  STAMP(1, 5);
#define AHALF(tu, i, h) trread(tb[(i) >> 2][tu] + ((i) & 3) * 4096 + (h) * 512)
  f32x4 acc[3][4], s1[4], s2[4];
#pragma unroll
  for (int tu = 0; tu < 4; ++tu) {
    s1[tu] = f32x4{0.f, 0.f, 0.f, 0.f}; s2[tu] = s1[tu];
#pragma unroll
    for (int k = 0; k < 3; ++k) acc[k][tu] = s1[tu];
  }
  v4u wg[3][8];
  v2u ah[2][4][2];
#pragma unroll
  for (int tu = 0; tu < 4; ++tu) { ah[0][tu][0] = AHALF(tu, 0, 0); ah[0][tu][1] = AHALF(tu, 0, 1); }
  float* st = (float*)(smem + LDS_S);
  f32x2 csb[2][3];
  SB();
#pragma unroll
  for (int i = 0; i < 8; ++i) {
#pragma unroll
    for (int j = 0; j < 12; ++j) {
      const int tu = j / 3, k = j % 3;
      const v4u a = {ah[i & 1][tu][0][0], ah[i & 1][tu][0][1], ah[i & 1][tu][1][0], ah[i & 1][tu][1][1]};
      acc[k][tu] = mfma(a, wf[k][i], acc[k][tu]);
      if (j < 8) ah[(i + 1) & 1][j >> 1][j & 1] = AHALF(j >> 1, (i + 1) & 7, j & 1);
      if (i < 4 && k == 0) s1[tu] = mfma(ones4, a, s1[tu]);
      if (i < 4 && k == 1) s2[tu] = mfma(a, a, s2[tu]);
      if ((i == 5 || i == 6) && (j == 8 || j == 10)) {
        const int t0 = (i - 5) * 2 + ((j - 8) >> 1);
        const float dg = (c & 2) ? ((c & 1) ? s2[t0][3] : s2[t0][2]) : ((c & 1) ? s2[t0][1] : s2[t0][0]);
        float* dst = st + ((c >> 2) == q ? w * 128 + (t0 ^ (2 * kh)) * 16 + c : 1280);
        dst[0] = s1[t0][0]; dst[64] = dg;
      }
      if (i == 4 && (j & 1) == 0) cz[1][(j >> 1) % 3][(j >> 1) / 3] = LOAD_CZ(1, (j >> 1) % 3, (j >> 1) / 3);
      if (i < 4 && j % 3 == 2) LOAD_WSPN(32 + i * 4 + j / 3);
      if (i == 5 && (j & 3) == 1) st128<SC1>(mk, rws, l16, WS_Z2 + ((ub_own + (j >> 2)) * 64 + mt * 2 + kh) * 1024 + ((j >> 2) == 2 ? oob3 : 0u));
      if (i >= 6 && (j & 3) == 3) wg[j >> 2][i - 6] = LOAD_WP(0, j >> 2, i - 6);
      if (i == 0 && j == 11) STAMP(1, 46);
      if (i == 3 && j == 11) STAMP(1, 47);
      SB();
    }
  }
  STAMP(1, 6);
  f32x4* xch = (f32x4*)(smem + LDS_X);
#pragma unroll
  for (int k = 0; k < 3; ++k)
#pragma unroll
    for (int t = 0; t < 2; ++t) xch[(w * 6 + k * 2 + t) * 64 + lane] = acc[k][2 + t];
#pragma unroll
  for (int k = 3; k < 6; ++k) {
    const f32x4 t4 = cz[k / 3][k % 3][0] + cz[k / 3][k % 3][1];
    csb[k / 3][k % 3] = lo2(t4) + hi2(t4);
    PIN(csb[k / 3][k % 3]);
  }
#pragma unroll
  for (int k = 0; k < 3; ++k) { PIN(acc[k][0]); PIN(acc[k][1]); }
  STAMP(1, 48);
  bar();
  STAMP(1, 49);
  float* fin = st + 512 + w * 64;
  f32x4 z2v[3][2];
  {
    const int row = kh * 32 + (lane & 31);
    float pa[4], pb[4];
#pragma unroll
    for (int k = 0; k < 4; ++k) { pa[k] = st[k * 128 + row]; pb[k] = st[k * 128 + 64 + row]; }
    f32x4 oth[3][2];
#pragma unroll
    for (int k = 0; k < 3; ++k)
#pragma unroll
      for (int t = 0; t < 2; ++t) oth[k][t] = xch[((w ^ 1) * 6 + k * 2 + t) * 64 + lane];
    SB();
    const float m = ((pa[0] + pa[1]) + (pa[2] + pa[3])) * (1.0f / D);
    const float r = __builtin_amdgcn_rsqf(fmaxf(((pb[0] + pb[1]) + (pb[2] + pb[3])) * (1.0f / D) - m * m, 0.f) + EPS);
    fin[lane] = lane < 32 ? r : -r * m;
    f32x4 r4[2], m4[2];
#pragma unroll
    for (int t = 0; t < 2; ++t) { r4[t] = *(const f32x4*)(fin + t * 16 + q * 4); m4[t] = *(const f32x4*)(fin + 32 + t * 16 + q * 4); }
    SB();
#pragma unroll
    for (int k = 0; k < 3; ++k)
#pragma unroll
      for (int t = 0; t < 2; ++t) { oth[k][t] += acc[k][t]; PIN(oth[k][t]); }
    SB();
    f32x4 rs[2], rq[2];
#pragma unroll
    for (int t = 0; t < 2; ++t) {
#pragma unroll
      for (int k = 0; k < 3; ++k) {
        const f32x4 a = oth[k][t];
        const f32x2 zl = gelu2(fma2(lo2(a), lo2(r4[t]), fma2(lo2(m4[t]), bc2(csb[1][k][0]), bc2(csb[1][k][1]))));
        const f32x2 zh = gelu2(fma2(hi2(a), hi2(r4[t]), fma2(hi2(m4[t]), bc2(csb[1][k][0]), bc2(csb[1][k][1]))));
        z2v[k][t] = f32x4{zl[0], zl[1], zh[0], zh[1]};
        if (k == 0) { rs[t] = z2v[k][t]; rq[t] = z2v[k][t] * z2v[k][t]; }
        else { rs[t] += z2v[k][t]; rq[t] += z2v[k][t] * z2v[k][t]; }
        PIN(rs[t]); PIN(rq[t]);
#pragma unroll
        for (int kk = 0; kk < 3; ++kk) wg[kk][2 + t * 3 + k] = LOAD_WP(0, kk, 2 + t * 3 + k);
        if (EPI_W) { LOAD_WSPN(48 + (t * 3 + k) * 2); LOAD_WSPN(49 + (t * 3 + k) * 2); }
        SB();
      }
    }
    STAMP(1, 50);
    float v8[8], v4[4], v2[2];
    const bool b3 = c & 8, b2 = c & 4, b1 = c & 2, b0 = c & 1;
#pragma unroll
    for (int r_ = 0; r_ < 4; ++r_) {
      const float a0 = rs[0][r_] + dpp<0x128>(rs[0][r_]), a1 = rs[1][r_] + dpp<0x128>(rs[1][r_]);
      const float q0 = rq[0][r_] + dpp<0x128>(rq[0][r_]), q1 = rq[1][r_] + dpp<0x128>(rq[1][r_]);
      v8[2 * r_] = b3 ? a1 : a0; v8[2 * r_ + 1] = b3 ? q1 : q0;
    }
#pragma unroll
    for (int j = 0; j < 4; ++j) {
      const float x0 = v8[j] + dpp<0x141>(v8[j]), x1 = v8[4 + j] + dpp<0x141>(v8[4 + j]);
      v4[j] = b2 ? x1 : x0;
    }
#pragma unroll
    for (int j = 0; j < 2; ++j) {
      const float x0 = v4[j] + dpp<0x4E>(v4[j]), x1 = v4[2 + j] + dpp<0x4E>(v4[2 + j]);
      v2[j] = b1 ? x1 : x0;
    }
    const float x0 = v2[0] + dpp<0xB1>(v2[0]), x1 = v2[1] + dpp<0xB1>(v2[1]);
    const float outv = b0 ? x1 : x0;
    __builtin_amdgcn_raw_buffer_store_b32(nomark(__float_as_uint(outv)), rws, lane * 4, WS_ST2 + ((mt * 2 + kh) * 16 + nt * 2 + uh) * 256, SC1);
  }
  STAMP(1, 7);
  SB();
#pragma unroll
  for (int tu = 0; tu < 4; ++tu)
#pragma unroll
    for (int k = 0; k < 3; ++k) acc[k][tu] = f32x4{0.f, 0.f, 0.f, 0.f};
  const int k30 = w * 6 - (w >> 1) * (w & 1);
  const unsigned oob6 = (unsigned)(w >> 1) << 30;
  v2u x1h[2][2];
  int x1o;
  {
    const int row = 8 * q + ((lane >> 2) & 3);
    const int sg = ((row >> 1) & 1) | (((row >> 3) & 1) << 1);
    x1o = LDS_A + nt * 8192 + row * 128 + ((w ^ sg) * 32) + (lane & 3) * 8;
  }
  v4u wo[6][4];
#define LOAD_WO(f) wo[(f) >> 2][(f) & 3] = ld128<SC1>(rws, l16 + (((f) >> 2) & 3) * 1024, WS_WOT + ((nt * 4 + ((f) & 3)) * 22 + k30 + (((f) >> 2) & 4)) * 1024 + ((f) >= 20 ? oob6 : 0u))
  f32x4 pt[4];
  v2u gq[4];
  unsigned bspq[2];
#define LOAD_PT(i) asf(ld128<SC1>(rws, l16 + (i) * 1024, WS_ST2 + (mt * 2 + kh) * 4096))
  SB();
#pragma unroll
  for (int i = 0; i < 8; ++i) {
#pragma unroll
    for (int j = 0; j < 12; ++j) {
      const int tu = j / 3, k = j % 3;
      const v4u a = {ah[i & 1][tu][0][0], ah[i & 1][tu][0][1], ah[i & 1][tu][1][0], ah[i & 1][tu][1][1]};
      acc[k][tu] = mfma(wg[k][i], a, acc[k][tu]);
      if (j < 8 && i < 7) ah[(i + 1) & 1][j >> 1][j & 1] = AHALF(j >> 1, i + 1, j & 1);
      if (j < 4 && i == 7) x1h[j >> 1][j & 1] = trread(smem + x1o + (j >> 1) * 4096 + (j & 1) * 512);
      if (i == 0 && j < 4) LOAD_WSPN(60 + j);
      if (EPI_W && i >= 1 && i < 6 && (j == 1 || j == 5 || j == 9) && (i - 1) * 3 + (j >> 2) < WO0) LOAD_WO((i - 1) * 3 + (j >> 2));
      if (!EPI_W && i >= 1 && i < 5 && (j == 1 || j == 5 || j == 9)) LOAD_WSPN(48 + (i - 1) * 3 + (j >> 2));
      if (j >= 8 && i == 6) pt[j - 8] = LOAD_PT(j - 8);
      if (i == 7 && j >= 8) LOAD_WO(j - 8);
      if (i == 5 && j == 0) {
        asm volatile("s_waitcnt vmcnt(30)" ::: "memory");
        __builtin_amdgcn_s_barrier();
      }
      if (i == 5 && j >= 2 && j < 8) cz[0][(j - 2) % 3][(j - 2) / 3] = LOAD_CZ(0, (j - 2) % 3, (j - 2) / 3);
      if (OPT_D && i == 0 && j >= 6 && j < 10) gq[j - 6] = ld64<SC1>(rws, lane * 8, WS_MODG + ((j - 6) * 3072 + 5 * 512 + nt * 64) * 8);
      if (OPT_D && i == 0 && j >= 10) bspq[j - 10] = p.bsp[mt * 64 + perm(2 * kh + j - 10, c)];
      if (OPT_D && i == 4 && j == 10) st[1408 + lane] = rbf((__uint_as_float(gq[0][0]) + __uint_as_float(gq[1][0])) + (__uint_as_float(gq[2][0]) + __uint_as_float(gq[3][0])));
      SB();
    }
  }
  STAMP(1, 8);
  float* c1s = st + 1024 + w * 96;
#pragma unroll
  for (int k = 0; k < 3; ++k) {
    const f32x4 t4 = cz[0][k][0] + cz[0][k][1];
    const f32x2 t2 = lo2(t4) + hi2(t4);
    c1s[k * 32 + (q & 1) * 16 + c] = (q & 1) ? t2[1] : t2[0];
  }
#pragma unroll
  for (int k = 0; k < 3; ++k)
#pragma unroll
    for (int t = 0; t < 2; ++t) xch[(w * 6 + k * 2 + t) * 64 + lane] = acc[k][2 + t];
  SB();
  {
    unsigned m = 0;
#pragma unroll
    for (int i = 0; i < 4; ++i) m = macc4(m, __builtin_bit_cast(v4u, pt[i]));
    RETRY_BEGIN(m)
#pragma unroll
      for (int i = 0; i < 4; ++i) { pt[i] = LOAD_PT(i); m = macc4(m, __builtin_bit_cast(v4u, pt[i])); }
    RETRY_END(m)
    if (tid < 4) hint_add(p.ws, 64 + (nt * 4 + (mt >> 3)) * 4 + tid);
    const f32x4 sx = (pt[0] + pt[1]) + (pt[2] + pt[3]);
    const float tv = xrow_sum4(sx[0], sx[2], sx[1], sx[3]);
    auto sw = __builtin_amdgcn_permlane32_swap(__float_as_uint(tv), __float_as_uint(tv), false, false);
    const float mm = __uint_as_float(sw[0]) * (1.0f / FH);
    const float r = __builtin_amdgcn_rsqf(fmaxf(__uint_as_float(sw[1]) * (1.0f / FH) - mm * mm, 0.f) + EPS);
    float* f2 = st + 768 + w * 64;
    f2[(q >> 1) * 32 + 2 * c + (q & 1)] = (q >> 1) ? -r * mm : r;
    f32x4 r4[2], m4[2];
#pragma unroll
    for (int t = 0; t < 2; ++t) { r4[t] = *(const f32x4*)(f2 + q * 8 + t * 4); m4[t] = *(const f32x4*)(f2 + 32 + q * 8 + t * 4); }
#pragma unroll
    for (int k = 0; k < 3; ++k) {
      v4u o;
#pragma unroll
      for (int t = 0; t < 2; ++t) {
        o[2 * t] = nomark(pk2(fma2(lo2(z2v[k][t]), lo2(r4[t]), lo2(m4[t]))));
        o[2 * t + 1] = pk2(fma2(hi2(z2v[k][t]), hi2(r4[t]), hi2(m4[t])));
      }
      st128<SC1>(o, rws, l16, WS_Z2 + ((ub_own + k) * 64 + mt * 2 + kh) * 1024 + (k == 2 ? oob3 : 0u));
    }
  }
  STAMP(1, 9);
  SB();
  unsigned z1p[3][2][2];
  unsigned char* wslot2 = smem + LDS_A + w * 18432;
  int rd_off[4];
#pragma unroll
  for (int tu = 0; tu < 4; ++tu) rd_off[tu] = perm(tu ^ (2 * kh), c) * 128 + ((q ^ (c >> 1)) * 16);
  constexpr int ZD = ZDEPTH;
  v4u zf[ZD][6];
  v4u wfr[2][4];
#define LOAD_Z(ks, i) ld128<SC1>(rws, l16 + ((((ks) >> 1) + kr) & 7) * 2048 + ((ks) & 1) * 1024, WS_Z2 + ((((i) < 3 ? ub_own : ub_oth) + (i) % 3) * 64 + w * 16) * 1024 + ((i) % 3 == 2 ? oob3 : 0u))
#define WSLOT(ck) ((ck) % 3 == 2 ? wslot2 : wslot + ((ck) % 3) * 8192)
  {
#pragma unroll
    for (int k2 = 0; k2 < 2; ++k2)
#pragma unroll
      for (int tu = 0; tu < 4; ++tu) wfr[k2][tu] = *(const v4u*)(wslot + (rd_off[tu] ^ (k2 * 64)));
    bar();
    STAMP(1, 34);
    f32x4 oth[3][2], cs1[3], bs1[3];
    float r1[2], m1[2];
#pragma unroll
    for (int t = 0; t < 2; ++t) { r1[t] = fin[t * 16 + c]; m1[t] = fin[32 + t * 16 + c]; }
#pragma unroll
    for (int k = 0; k < 3; ++k) {
      cs1[k] = *(const f32x4*)(c1s + k * 32 + q * 4); bs1[k] = *(const f32x4*)(c1s + k * 32 + 16 + q * 4);
#pragma unroll
      for (int t = 0; t < 2; ++t) oth[k][t] = xch[((w ^ 1) * 6 + k * 2 + t) * 64 + lane];
    }
    SB();
    f32x2 z1a[6][2];
#pragma unroll
    for (int i = 0; i < 8; ++i) {
      PARK1(2, wslot2, i);
      PARK1(3, wslot, i);
      if (i < WO0 - 4) LOAD_WO(4 + i);
      if (i >= 2) {
        const int k = (i - 2) >> 1, t = i & 1;
        const f32x4 a = acc[k][t] + oth[k][t];
        z1a[i - 2][0] = fma2(lo2(a), bc2(r1[t]), fma2(lo2(cs1[k]), bc2(m1[t]), lo2(bs1[k])));
        z1a[i - 2][1] = fma2(hi2(a), bc2(r1[t]), fma2(hi2(cs1[k]), bc2(m1[t]), hi2(bs1[k])));
        PIN(z1a[i - 2][0]); PIN(z1a[i - 2][1]);
      }
      SB();
    }
    STAMP(1, 10);
    hint_wait(rws, 0u, WS_CNT + (64 + (nt * 4 + w) * 4 + (xcd >> 1)) * 128, 8u);
    STAMP(1, 22);
#pragma unroll
    for (int s_ = 0; s_ < 2; ++s_)
#pragma unroll
      for (int i = 0; i < 6; ++i) zf[s_][i] = LOAD_Z(s_, i);
    SB();
#pragma unroll
    for (int i = 0; i < 6; ++i) {
      z1p[i >> 1][i & 1][0] = pk2(gelu2(z1a[i][0])); z1p[i >> 1][i & 1][1] = pk2(gelu2(z1a[i][1]));
      PIN(z1p[i >> 1][i & 1][0]); PIN(z1p[i >> 1][i & 1][1]);
      if (ZD > 2 && i >= 4) { zf[ZD - 1][(i - 4) * 3] = LOAD_Z(2, (i - 4) * 3); zf[ZD - 1][(i - 4) * 3 + 1] = LOAD_Z(2, (i - 4) * 3 + 1); zf[ZD - 1][(i - 4) * 3 + 2] = LOAD_Z(2, (i - 4) * 3 + 2); }
      SB();
    }
  }
  f32x4 ac2[6][4];
#pragma unroll
  for (int i = 0; i < 6; ++i)
#pragma unroll
    for (int j = 0; j < 4; ++j) { const float b0 = (i < 3 && j < 2) ? bf2f(bspq[j & 1]) : 0.f; ac2[i][j] = f32x4{b0, b0, b0, b0}; }
  SB();
  STAMP(1, 11);
  unsigned zm = 0;
  sfor<16 * 24>([&](auto G) __attribute__((always_inline)) {
    constexpr int ks = decltype(G)::value / 24, j = decltype(G)::value % 24, ck = ks >> 1, k2 = ks & 1, i = j >> 2, tu = j & 3;
    ac2[i][tu] = mfma(zf[ks % ZD][i], wfr[k2][tu], ac2[i][tu]);
    if constexpr (j == 0 && (ks == 1 || ks == 4 || ks == 8 || ks == 12)) STAMP(1, 56 + (ks >> 2));
    if constexpr (tu == 1) { zm = macc(zm, zf[ks % ZD][i]); PIN(zm); }
    if constexpr (j == 17 && ks >= 2 && ks < 5) st128<SC1>(mk, rws, l16, WS_V + ((mt * 4 + w) * 22 + pr0 + ks - 2) * 1024 + (ks == 4 ? oob3 : 0u));
    if constexpr (OPT_C == 2 && j == 1 && ks == 12) { asm volatile("s_waitcnt vmcnt(20)" ::: "memory"); __builtin_amdgcn_s_barrier(); }
    if constexpr (OPT_C == 2 && j == 23 && ks == 15) { if (tid < 2) hint_add(p.ws, 192 + mt * 2 + tid); }
    if constexpr (!OPT_C && j == 1 && ks == HINT4_KS) {
      asm volatile("s_waitcnt vmcnt(20)" ::: "memory");
      __builtin_amdgcn_s_barrier();
      if (tid < 2) hint_add(p.ws, 192 + mt * 2 + tid);
    }
    if constexpr (tu == 3) {
      if constexpr (ks + ZD < 16) zf[ks % ZD][i] = LOAD_Z(ks + ZD, i);
      else if constexpr (ks >= 14 && WO0 + (ks - 14) * 6 + i < 24) LOAD_WO(WO0 + (ks - 14) * 6 + i);
    }
    if constexpr (k2 == 1 && j % 3 == 2 && ck >= 1 && ck + 3 < 8) PARK1(ck + 3, WSLOT(ck), j / 3);
    if constexpr (j % 4 == 0 && j < 16 && ks >= 1 && ks < 15) wfr[k2 ^ 1][j / 4] = *(const v4u*)(WSLOT((ks + 1) >> 1) + (rd_off[j / 4] ^ ((k2 ^ 1) * 64)));
    SB();
  });
  STAMP(1, 12);
  {
    f32x4* red = (f32x4*)(smem + LDS_A);
#pragma unroll
    for (int i = 0; i < 6; ++i)
#pragma unroll
      for (int tu = 0; tu < 4; ++tu) {
        const int rel = (tu >> 1) | ((i / 3) << 1);
        if (rel) red[((w * 3 + rel - 1) * 6 + (i % 3) * 2 + (tu & 1)) * 64 + lane] = ac2[i][tu];
      }
    f32x4 own[3][2];
#pragma unroll
    for (int k = 0; k < 3; ++k)
#pragma unroll
      for (int t = 0; t < 2; ++t) { own[k][t] = ac2[k][t]; PIN(own[k][t]); }
  if (__builtin_expect(any_mark(zm), 0)) {
    f32x4 ar[6][4];
    int l2 = lane;
    PIN(l2);
    const int c2 = l2 & 15, q2 = l2 >> 4;
#pragma unroll
    for (int i = 0; i < 6; ++i)
#pragma unroll
      for (int j = 0; j < 4; ++j) { const float b0 = (i < 3 && j < 2) ? bf2f(p.bsp[mt * 64 + perm(2 * kh + (j & 1), c2)]) : 0.f; ar[i][j] = f32x4{b0, b0, b0, b0}; }
#pragma unroll 1
    for (int ks = 0; ks < 16; ++ks) {
      v4u zz[6], ww[4];
      unsigned sp = 0;
      do {
        MEMFENCE();
        zm = 0;
#pragma unroll
        for (int i = 0; i < 6; ++i) {
          zz[i] = ld128<SC1>(rws, l16 + ks * 1024, WS_Z2 + (((i < 3 ? ub_own : ub_oth) + i % 3) * 64 + w * 16) * 1024 + (i % 3 == 2 ? oob3 : 0u));
          zm = macc(zm, zz[i]);
        }
      } while (any_mark(zm) & (++sp <= SPIN_CAP));
#pragma unroll
      for (int tu = 0; tu < 4; ++tu) ww[tu] = ld128<0>(rwsp, perm(tu ^ (2 * kh), c2) * 4096 + q2 * 16 + ks * 64, wsp_s);
#pragma unroll
      for (int i = 0; i < 6; ++i)
#pragma unroll
        for (int tu = 0; tu < 4; ++tu) ar[i][tu] = mfma(zz[i], ww[tu], ar[i][tu]);
    }
#pragma unroll
    for (int i = 0; i < 6; ++i)
#pragma unroll
      for (int tu = 0; tu < 4; ++tu) {
        const int rel = (tu >> 1) | ((i / 3) << 1);
        if (rel) red[((w * 3 + rel - 1) * 6 + (i % 3) * 2 + (tu & 1)) * 64 + l2] = ar[i][tu];
        else own[i][tu] = ar[i][tu];
      }
  }
    STAMP(1, 51);
    if (OPT_C == 1) asm volatile("s_waitcnt vmcnt(40)" ::: "memory");
    bar();
    if (OPT_C == 1 && tid < 2) hint_add(p.ws, 192 + mt * 2 + tid);
    STAMP(1, 52);
    f32x4 o3[3][2][3];
#pragma unroll
    for (int k = 0; k < 3; ++k)
#pragma unroll
      for (int t = 0; t < 2; ++t)
#pragma unroll
        for (int rel = 1; rel < 4; ++rel) o3[k][t][rel - 1] = red[(((w ^ rel) * 3 + rel - 1) * 6 + k * 2 + t) * 64 + lane];
    SB();
#pragma unroll
    for (int k = 0; k < 3; ++k) {
#pragma unroll
      for (int t = 0; t < 2; ++t) {
        const f32x4 g = (own[k][t] + o3[k][t][0]) + (o3[k][t][1] + o3[k][t][2]);
        v2u o;
        o[0] = nomark(pk2(unpk(z1p[k][t][0]) * lo2(g)));
        o[1] = pk2(unpk(z1p[k][t][1]) * hi2(g));
        const int uu = ub_own + k;
        st64<SC1>(o, rws, l16, WS_V + ((mt * 4 + 2 * kh + t) * 22 + (uu >> 1)) * 1024 + (uu & 1) * 8 + (k == 2 ? oob3 : 0u));
      }
    }
  }
  STAMP(1, 13);
  SB();
  f32x4 gb4[4];
  f32x2 x1f[2][4];
#pragma unroll
  for (int i = 0; i < 4; ++i) gb4[i] = *(const f32x4*)(st + 1408 + (i >> 1) * 32 + q * 8 + (i & 1) * 4);
#pragma unroll
  for (int i = 0; i < 8; ++i) { x1f[i >> 2][i & 3] = unpk(x1h[i >> 2][(i >> 1) & 1][i & 1]); PIN(x1f[i >> 2][i & 3]); }
  STAMP(1, 53);
  hint_wait(rws, 0u, WS_CNT + (192 + mt * 2 + (xcd & 1)) * 128, 8u);
  STAMP(1, 23);
  f32x4 a3[4][4];
  {
    unsigned spins = 0;
    for (;;) {
      MEMFENCE();
      v4u vs[6][4];
#define LOAD_V(f) vs[(f) >> 2][(f) & 3] = ld128<SC1>(rws, l16 + (((f) >> 2) & 3) * 1024, WS_V + ((mt * 4 + (((f) & 3) ^ w)) * 22 + k30 + (((f) >> 2) & 4)) * 1024 + ((f) >= 20 ? oob6 : 0u))
#pragma unroll
      for (int f = 0; f < 12; ++f) LOAD_V(f);
      SB();
      unsigned m = 0;
      sfor<96>([&](auto G) __attribute__((always_inline)) {
        constexpr int g = decltype(G)::value, ks = g >> 4, tu = (g >> 2) & 3, e = g & 3;
        a3[e][tu] = mfma(wo[ks][e], vs[ks][tu], ks == 0 ? f32x4{0.f, 0.f, 0.f, 0.f} : a3[e][tu]);
        if constexpr (e == 0 && 12 + (g >> 2) < 24) LOAD_V(12 + (g >> 2));
        if constexpr (e == 1) { m = macc(m, vs[ks][tu]); PIN(m); }
        SB();
      });
      if (!(any_mark(m) & (++spins <= SPIN_CAP))) break;
    }
  }
  STAMP(1, 14);
  {
    f32x4* red = (f32x4*)(smem + LDS_W);
#pragma unroll
    for (int tu = 1; tu < 4; ++tu)
#pragma unroll
      for (int e = 0; e < 4; ++e) red[((w * 3 + tu - 1) * 4 + e) * 64 + lane] = a3[e][tu];
#pragma unroll
    for (int e = 0; e < 4; ++e) PIN(a3[e][0]);
    bar();
    STAMP(1, 54);
    f32x4 o3[4][3];
#pragma unroll
    for (int e = 0; e < 4; ++e)
#pragma unroll
      for (int rel = 1; rel < 4; ++rel) o3[e][rel - 1] = red[(((w ^ rel) * 3 + rel - 1) * 4 + e) * 64 + lane];
    const rsrc_t rout = mkrs(p.out, L * D * 2);
    SB();
#pragma unroll
    for (int pp = 0; pp < 2; ++pp) {
      v4u o;
#pragma unroll
      for (int h = 0; h < 2; ++h) {
        const int e = pp * 2 + h;
        const f32x4 y = (a3[e][0] + o3[e][0]) + (o3[e][1] + o3[e][2]);
        o[2 * h] = pk2(fma2(lo2(y), lo2(gb4[e]), x1f[pp][2 * h]));
        o[2 * h + 1] = pk2(fma2(hi2(y), hi2(gb4[e]), x1f[pp][2 * h + 1]));
      }
      st128<0>(o, rout, perm(w, c) * 1024 + q * 16 + pp * 64, mt * 64 * 1024 + nt * 128);
    }
  }
  STAMP(1, 15);
  STAMP_FLUSH(1);
  asm volatile("adaln_code_end:");
}

extern "C" void kernel_launch(void* const* d_in, const int* in_sizes, int n_in, void* d_out, int out_size, void* d_ws,
                              size_t ws_size, hipStream_t stream) {
  Params p;
  p.u = (const u16*)d_in[0];
  p.lar = (const u16*)d_in[1];
  p.aim = (const u16*)d_in[2];
  p.cre = (const u16*)d_in[3];
  p.cim = (const u16*)d_in[4];
  p.ldt = (const u16*)d_in[5];
  p.dsk = (const u16*)d_in[6];
  p.win = (const u16*)d_in[8];
  p.wsp = (const u16*)d_in[9];
  p.bsp = (const u16*)d_in[10];
  p.wout = (const u16*)d_in[11];
  p.c = (const u16*)d_in[12];
  p.wmod = (const u16*)d_in[13];
  p.out = (u16*)d_out;
  p.ws = (unsigned char*)d_ws;
  (void)hipFuncSetAttribute((const void*)adaln_mega, hipFuncAttributeMaxDynamicSharedMemorySize, MEGA_LDS);
  adaln_mega<<<256, 256, MEGA_LDS, stream>>>(p);
}
```

```cpp
#include <hip/hip_runtime.h>
#include <hip/hip_bf16.h>
#include <stdint.h>
#include <utility>

#define LAS __attribute__((address_space(3)))
typedef unsigned v4u __attribute__((ext_vector_type(4)));
typedef unsigned v2u __attribute__((ext_vector_type(2)));
typedef float f32x4 __attribute__((ext_vector_type(4)));
typedef float f32x2 __attribute__((ext_vector_type(2)));
typedef short bf16x8 __attribute__((ext_vector_type(8)));
typedef _Float16 f16x8 __attribute__((ext_vector_type(8)));
typedef _Float16 f16x2 __attribute__((ext_vector_type(2)));
typedef __bf16 bf16x2_t __attribute__((ext_vector_type(2)));
typedef unsigned short u16;
typedef __amdgpu_buffer_rsrc_t rsrc_t;

#define STAMP_DECL do { } while (0)
#define STAMP(k, i) do { } while (0)
#define STAMP_FLUSH(k) do { } while (0)

#define OPT_A 1
#define OPT_B 1
#define OPT_C 2
#define OPT_D 1
#define OPT_E 2
#define OPT_F 1
constexpr int L = 2048, D = 512, NM = 128, FH = 688, FF = 1376;
constexpr float EPS = 1e-5f;
constexpr int SC1 = 16;
constexpr unsigned OOB = 0x40000000u;
constexpr unsigned SPIN_CAP = 1u << 14;

constexpr unsigned WS_EPOCH = 0;
constexpr unsigned WS_CNT = 4096;
constexpr unsigned WS_SR = WS_CNT + 512 * 128;
constexpr unsigned WS_SB = WS_SR + 2048 * 8;
constexpr unsigned WS_MODG = WS_SB + 2048 * 8;
constexpr unsigned WS_CSB = WS_MODG + 3072 * 32;
constexpr unsigned WS_ST2 = WS_CSB + 88 * 16 * 32;
constexpr unsigned WS_UG = 1u << 20;
static_assert(WS_ST2 + 32 * 2 * 16 * 32 * 8 <= WS_UG, "small buffers");
constexpr unsigned WS_X1T = WS_UG + 256 * 2048 * 8;
constexpr unsigned WS_WP = WS_X1T + 512 * 2048 * 2;
constexpr unsigned WS_WOT = WS_WP + 88 * 16 * 1024;
constexpr unsigned WS_Z2 = WS_WOT + 32 * 22 * 1024;
constexpr unsigned WS_V = WS_Z2 + 44 * 64 * 1024;
constexpr unsigned WS_END = WS_V + 128 * 22 * 1024;

struct Params {
  const u16 *u, *lar, *aim, *cre, *cim, *ldt, *dsk, *win, *wsp, *bsp, *wout, *c, *wmod;
  u16* out;
  unsigned char* ws;
};

#define DI __device__ __forceinline__
template <int V> struct IC { static constexpr int value = V; };
template <class F, int... I> DI void sfor_(F&& f, std::integer_sequence<int, I...>) { int d[] = {(f(IC<I>{}), 0)...}; (void)d; }
template <int N, class F> DI void sfor(F&& f) { sfor_(f, std::make_integer_sequence<int, N>{}); }
DI float bf2f(unsigned b) { return __builtin_bit_cast(float, b << 16); }
DI float bflo(unsigned w) { return __builtin_bit_cast(float, w << 16); }
DI float bfhi(unsigned w) { return __builtin_bit_cast(float, w & 0xFFFF0000u); }
DI f32x2 unpk(unsigned w) { return f32x2{bflo(w), bfhi(w)}; }
DI unsigned pk2(f32x2 v) { return __builtin_bit_cast(unsigned, __builtin_convertvector(v, bf16x2_t)); }
DI unsigned pk2(float lo, float hi) { return pk2(f32x2{lo, hi}); }
DI unsigned nomark(unsigned x) { return x < 0xFFFFFFFEu ? x : 0xFFFFFFFEu; }
DI float rbf(float x) { return bflo(pk2(x, 0.f)); }
DI f32x2 fma2(f32x2 a, f32x2 b, f32x2 c) { return __builtin_elementwise_fma(a, b, c); }
DI f32x2 lo2(f32x4 v) { return f32x2{v[0], v[1]}; }
DI f32x2 hi2(f32x4 v) { return f32x2{v[2], v[3]}; }
DI f32x2 bc2(float x) { return f32x2{x, x}; }
DI f32x4 mfma(v4u a, v4u b, f32x4 c) {
  return __builtin_amdgcn_mfma_f32_16x16x32_bf16(__builtin_bit_cast(bf16x8, a), __builtin_bit_cast(bf16x8, b), c, 0, 0, 0);
}
DI f32x4 mfma_h(v4u a, v4u b, f32x4 c) {
  return __builtin_amdgcn_mfma_f32_16x16x32_f16(__builtin_bit_cast(f16x8, a), __builtin_bit_cast(f16x8, b), c, 0, 0, 0);
}
DI float dot2(unsigned a, unsigned b, float acc) {
  return __builtin_amdgcn_fdot2_f32_bf16(__builtin_bit_cast(bf16x2_t, a), __builtin_bit_cast(bf16x2_t, b), acc, false);
}
constexpr unsigned ONES = 0x3F803F80u;
DI int perm(int u, int c) { return 32 * (u >> 1) + 8 * (c >> 2) + 4 * (u & 1) + (c & 3); }
template <int CTRL> DI float dpp(float x) {
  return __builtin_bit_cast(float, __builtin_amdgcn_mov_dpp(__builtin_bit_cast(int, x), CTRL, 0xf, 0xf, true));
}
DI float row16_sum(float x) {
  x += dpp<0xB1>(x); x += dpp<0x4E>(x); x += dpp<0x141>(x); x += dpp<0x128>(x);
  return x;
}
DI float row16_max(float x) {
  x = fmaxf(x, dpp<0xB1>(x)); x = fmaxf(x, dpp<0x4E>(x)); x = fmaxf(x, dpp<0x141>(x)); x = fmaxf(x, dpp<0x128>(x));
  return x;
}
DI float xrow_sum(float x) {
  auto s = __builtin_amdgcn_permlane16_swap(__float_as_uint(x), __float_as_uint(x), false, false);
  x = __uint_as_float(s[0]) + __uint_as_float(s[1]);
  auto t = __builtin_amdgcn_permlane32_swap(__float_as_uint(x), __float_as_uint(x), false, false);
  return __uint_as_float(t[0]) + __uint_as_float(t[1]);
}
DI float xrow_sum4(float a, float b, float c, float d) {
  auto s = __builtin_amdgcn_permlane16_swap(__float_as_uint(a), __float_as_uint(b), false, false);
  auto t = __builtin_amdgcn_permlane16_swap(__float_as_uint(c), __float_as_uint(d), false, false);
  const float x = __uint_as_float(s[0]) + __uint_as_float(s[1]), y = __uint_as_float(t[0]) + __uint_as_float(t[1]);
  auto z = __builtin_amdgcn_permlane32_swap(__float_as_uint(x), __float_as_uint(y), false, false);
  return __uint_as_float(z[0]) + __uint_as_float(z[1]);
}
DI float xrow_max(float x) {
  auto s = __builtin_amdgcn_permlane16_swap(__float_as_uint(x), __float_as_uint(x), false, false);
  x = fmaxf(__uint_as_float(s[0]), __uint_as_float(s[1]));
  auto t = __builtin_amdgcn_permlane32_swap(__float_as_uint(x), __float_as_uint(x), false, false);
  return fmaxf(__uint_as_float(t[0]), __uint_as_float(t[1]));
}
DI f32x2 gelu2(f32x2 x) {
  const float k0 = -2.0f * 0.7978845608028654f * 1.4426950408889634f, k1 = k0 * 0.044715f;
  const f32x2 a = x * fma2(x * x, bc2(k1), bc2(k0));
  const f32x2 e = f32x2{__builtin_amdgcn_exp2f(a[0]), __builtin_amdgcn_exp2f(a[1])} + bc2(1.0f);
  return x * f32x2{__builtin_amdgcn_rcpf(e[0]), __builtin_amdgcn_rcpf(e[1])};
}
DI rsrc_t mkrs(const void* ptr, unsigned bytes) { return __builtin_amdgcn_make_buffer_rsrc((void*)ptr, 0, (int)bytes, 0x00020000); }
template <int AUX> DI v4u ld128(rsrc_t rs, unsigned v, unsigned s) { return __builtin_amdgcn_raw_buffer_load_b128(rs, (int)v, (int)s, AUX); }
template <int AUX> DI v2u ld64(rsrc_t rs, unsigned v, unsigned s) { return __builtin_amdgcn_raw_buffer_load_b64(rs, (int)v, (int)s, AUX); }
template <int AUX> DI void st128(v4u x, rsrc_t rs, unsigned v, unsigned s) { __builtin_amdgcn_raw_buffer_store_b128(x, rs, (int)(v + s), 0, AUX); }
template <int AUX> DI void st64(v2u x, rsrc_t rs, unsigned v, unsigned s) { __builtin_amdgcn_raw_buffer_store_b64(x, rs, (int)v, (int)s, AUX); }
DI f32x4 asf(v4u x) { return __builtin_bit_cast(f32x4, x); }
DI float sum4(f32x4 x) { return (x[0] + x[1]) + (x[2] + x[3]); }
DI unsigned umax3(unsigned a, unsigned b, unsigned c) { unsigned m = a > b ? a : b; return m > c ? m : c; }
DI unsigned macc4(unsigned m, v4u x) { return umax3(umax3(m, x[0], x[1]), x[2], x[3]); }
DI unsigned macc(unsigned m, v4u x) { return umax3(m, x[0], x[2]); }
DI bool any_mark(unsigned m) { return __builtin_amdgcn_ballot_w64(m == 0xFFFFFFFFu) != 0ull; }
#define SB() __builtin_amdgcn_sched_barrier(0)
#define PIN(x) asm volatile("" : "+v"(x))
#define RETRY_BEGIN(m) if (__builtin_expect(any_mark(m), 0)) { unsigned sp_ = 0; do { MEMFENCE(); m = 0;
#define RETRY_END(m) } while (any_mark(m) & (++sp_ <= SPIN_CAP)); }
#define MEMFENCE() asm volatile("" ::: "memory")
DI void bar() { asm volatile("s_waitcnt lgkmcnt(0)" ::: "memory"); __builtin_amdgcn_s_barrier(); asm volatile("" ::: "memory"); }

DI f32x2 cmul(f32x2 a, f32x2 b) { return fma2(bc2(a[1]), f32x2{-b[1], b[0]}, bc2(a[0]) * b); }
DI f32x2 cexp_p(float are2, float rev_hi, float rev_lo, float p) {
  const float mag = __builtin_amdgcn_exp2f(are2 * p);
  const float hi = rev_hi * p;
  const float fr = (hi - __builtin_rintf(hi)) + rev_lo * p;
  return f32x2{mag * __builtin_amdgcn_cosf(fr), mag * __builtin_amdgcn_sinf(fr)};
}
DI float exp_acc(float x) {
  const float hi = x * 1.4426950408889634f;
  const float lo = __builtin_fmaf(x, 1.4426950408889634f, -hi) + x * 1.925963033500011e-8f;
  const float e = __builtin_amdgcn_exp2f(hi);
  return __builtin_fmaf(e, lo * 0.6931471805599453f, e);
}
DI unsigned pkh(float a, float b) {
  f16x2 v = {(_Float16)a, (_Float16)b};
  return __builtin_bit_cast(unsigned, v);
}

constexpr int CV_RK = 4160;
constexpr int CV_H = 2 * CV_RK;
constexpr int CV_CH = CV_H + 4608;
constexpr int LDS_A = 0;
constexpr int LDS_U = 2 * CV_CH;
constexpr int LDS_GS = LDS_U + 8192;
constexpr int LDS_TAB = 36864;
constexpr int KP_CH = 13840;
constexpr int LDS_KP = LDS_TAB + 2 * 64 * 528 + 16;
constexpr int LDS_LN = 0, LDS_MR = 8448;
constexpr int LDS_WI = 145472, LDS_WO = LDS_WI + 10240;
constexpr int LDS_X = 65536;
constexpr int LDS_S = LDS_X + 24576;
constexpr int LDS_W = LDS_S + 6144;
constexpr int MEGA_LDS = LDS_W + 65536;
static_assert(LDS_WO + 4608 <= MEGA_LDS && LDS_KP + 2 * KP_CH <= LDS_WI && LDS_S >= 73728, "lds");
static_assert(LDS_GS + 1280 <= LDS_TAB && LDS_KP + 2 * KP_CH <= MEGA_LDS && MEGA_LDS <= 160 * 1024, "lds");
#define FLAG_AT 110
static_assert(FLAG_AT > 57 && FLAG_AT < 144 && FLAG_AT % 6 != 3, "flag position");
#define WF_EARLY 3
#define WO0 12
#define WSP_P0 4
#define HINT4_KS 15
#define ZDEPTH 3
#define GELU_IN 0
#define EPI_W 0
#define MOD_POS 0
#define POLL3 0
#define IN_AUX 0
#define WSP_AUX 0

DI v2u trread(const unsigned char* ptr) {
  typedef short v4i16_t __attribute__((ext_vector_type(4)));
  return __builtin_bit_cast(v2u, __builtin_amdgcn_ds_read_tr16_b64_v4i16((LAS v4i16_t*)ptr));
}
struct CvOrder { int qq[36], pp[36]; };
constexpr CvOrder cv_order() {
  CvOrder o{};
  int n = 0;
  for (int m = 0; m < 8; ++m) {
    for (int j = 0; j < m; ++j) if (m + j < 8) { o.qq[n] = m; o.pp[n] = j; ++n; }
    for (int j = 0; j <= m; ++j) if (m + j < 8) { o.qq[n] = j; o.pp[n] = m; ++n; }
  }
  return o;
}
constexpr CvOrder CVO = cv_order();

typedef __attribute__((address_space(1))) unsigned gu32;
DI unsigned tacc(unsigned m, v4u x, unsigned tag) { return m | (x[1] ^ tag) | (x[3] ^ tag); }
DI bool any_nz(unsigned m) { return __builtin_amdgcn_ballot_w64(m != 0u) != 0ull; }
DI void hint_add(unsigned char* ws, int line) { (void)__hip_atomic_fetch_add((gu32*)(ws + WS_CNT + line * 128), 1u, __ATOMIC_RELAXED, __HIP_MEMORY_SCOPE_AGENT); }
DI void hint_wait(rsrc_t rws, unsigned voff, unsigned soff, unsigned want) {
  unsigned sp = 0;
  for (;;) {
    MEMFENCE();
    const unsigned v = __builtin_amdgcn_raw_buffer_load_b32(rws, (int)voff, (int)soff, SC1);
    if (!((__builtin_amdgcn_ballot_w64(v < want) != 0ull) & (++sp <= SPIN_CAP))) break;
    __builtin_amdgcn_s_sleep(OPT_E);
  }
  MEMFENCE();
}

extern "C" __global__ void __launch_bounds__(256, 1) adaln_mega(Params p) {
  extern __shared__ __attribute__((aligned(16))) unsigned char smem[];
  const int tid = threadIdx.x, lane = tid & 63, w = __builtin_amdgcn_readfirstlane(tid >> 6);
  const int b = blockIdx.x, xcd = b & 7, idx = b >> 3;
  const int pb = xcd * 32 + idx;
  const unsigned l16 = lane * 16;
  STAMP_DECL;
  STAMP(1, 0);
  v2u codepf;
  {
    unsigned long long pc;
    int rel_m, rel_e;
    asm volatile("s_getpc_b64 %0\n\ts_mov_b32 %1, adaln_mega@rel32@lo+4\n\ts_mov_b32 %2, adaln_code_end@rel32@lo+12" : "=s"(pc), "=s"(rel_m), "=s"(rel_e));
    const rsrc_t rcode = mkrs((const void*)(pc + (long long)rel_m), rel_e > rel_m ? (unsigned)(rel_e - rel_m) & ~15u : 0u);
    codepf = ld64<0>(rcode, tid * 8, idx * 2048);
  }
  SB();
  const rsrc_t ru = mkrs(p.u, L * D * 2);
  v4u xr[2];
  xr[0] = ld128<IN_AUX>(ru, l16, (pb * 8 + w * 2) * 1024);
  xr[1] = ld128<IN_AUX>(ru, l16 + 1024, (pb * 8 + w * 2) * 1024);
  const int mkg = b & 3, mcg = b >> 2;
  const unsigned cq = p.c[mkg * 128 + (tid & 3) * 32 + (tid >> 3)];
  const unsigned epoch = *(const unsigned*)(p.ws + WS_EPOCH);
  const rsrc_t rws = mkrs(p.ws, WS_END);
  __builtin_amdgcn_raw_buffer_store_b32(0u, rws, tid < 2 ? tid * 32768u : OOB, WS_CNT + b * 128, SC1);
  SB();
  const int cch = w >> 1, eh = w & 1, ns = tid & 127;
  const unsigned q_cre = p.cre[pb * 256 + tid], q_cim = p.cim[pb * 256 + tid], q_lar = p.lar[pb * 256 + tid], q_aim = p.aim[pb * 256 + tid];
  const unsigned q_ldt = p.ldt[pb * 2 + cch], q_dsk = p.dsk[pb * 2 + cch];
  v4u xm[4];
#define LOAD_XM() { \
  const rsrc_t rmod = mkrs(p.wmod, D * 3072 * 2); \
_Pragma("unroll") \
  for (int i = 0; i < 4; ++i) xm[i] = ld128<IN_AUX>(rmod, (tid >> 3) * 6144 + (tid & 7) * 16, (mkg * 128 + i * 32) * 6144 + mcg * 128 + (b < 192 ? 0u : OOB)); }
  SB();
  LOAD_XM();
  SB();
  STAMP(1, 24);
  const int c = lane & 15, q = lane >> 4;
  int pos4[9];
  {
    const float f4 = (float)(8188 - tid * 32);
#pragma unroll
    for (int j = 0; j < 10; j += 2) {
      const unsigned pp = pk2(bc2(f4) - f32x2{4.f * j, 4.f * j + 4.f});
      pos4[j] = (int)bflo(pp);
      if (OPT_A) pos4[j] += (pos4[j] >> 5) << 2;
      PIN(pos4[j]);
      if (j + 1 < 9) { pos4[j + 1] = (int)bfhi(pp); if (OPT_A) pos4[j + 1] += (pos4[j + 1] >> 5) << 2; PIN(pos4[j + 1]); }
    }
  }
  int cob[2], cos_[2];
  cob[0] = (eh ? 1024 + 512 * q : 128 * (q >> 1) + 4 * (q & 1)) * 4 + 32 * c + LDS_KP + cch * KP_CH;
  cob[1] = (q < 2 ? 256 + 128 * q : 504 + 4 * q) * 4 + 32 * c + LDS_KP + cch * KP_CH;
  cos_[0] = eh ? 512 : 4;
  cos_[1] = q < 2 ? 8 : 512;
  if (OPT_A) {
    cob[0] = (eh ? 1152 + 576 * q : 144 * (q >> 1) + 4 * (q & 1)) * 4 + 36 * c + LDS_KP + cch * KP_CH;
    cob[1] = (q < 2 ? 288 + 144 * q : 568 + 4 * q) * 4 + 36 * c + LDS_KP + cch * KP_CH;
    cos_[0] = eh ? 576 : 4;
    cos_[1] = q < 2 ? 8 : 576;
  }
  PIN(cob[0]); PIN(cob[1]); PIN(cos_[1]);
  SB();
  STAMP(1, 26);
  {
    *(v4u*)(smem + LDS_LN + (w * 2) * 1040 + l16) = xr[0];
    *(v4u*)(smem + LDS_LN + (w * 2 + 1) * 1040 + l16) = xr[1];
    float sm[2], sq[2];
#pragma unroll
    for (int i = 0; i < 2; ++i) {
      sm[i] = dot2(xr[i][0], ONES, dot2(xr[i][1], ONES, dot2(xr[i][2], ONES, dot2(xr[i][3], ONES, 0.f))));
      sq[i] = dot2(xr[i][0], xr[i][0], dot2(xr[i][1], xr[i][1], dot2(xr[i][2], xr[i][2], dot2(xr[i][3], xr[i][3], 0.f))));
    }
    const float tv = row16_sum(xrow_sum4(sm[0], sm[1], sq[0], sq[1]));
    auto sw = __builtin_amdgcn_permlane32_swap(__float_as_uint(tv), __float_as_uint(tv), false, false);
    const float m = __uint_as_float(sw[0]) * (1.0f / D);
    const float r = __builtin_amdgcn_rsqf(fmaxf(__uint_as_float(sw[1]) * (1.0f / D) - m * m, 0.f) + EPS);
    ((float*)(smem + LDS_GS + 1088))[(q >> 1) * 8 + w * 2 + (q & 1)] = (q >> 1) ? -m * r : r;
  }
  const unsigned tag = epoch * 2654435761u + 0x9E3779B9u;
  STAMP(1, 28);
  asm volatile("s_waitcnt vmcnt(%0)" ::"i"(MOD_POS == 0 ? 8 : 4) : "memory");
  bar();
  STAMP(1, 16);
  {
    unsigned ua[4][2];
#pragma unroll
    for (int i = 0; i < 4; ++i) {
      const unsigned char* src = smem + LDS_LN + (tid & 3) * 2080 + (i * 64 + (tid >> 2)) * 4;
      ua[i][0] = *(const unsigned*)src; ua[i][1] = *(const unsigned*)(src + 1040);
    }
    const unsigned lnv = ((const unsigned*)(smem + LDS_GS + 1088))[c];
    SB();
#pragma unroll
    for (int i = 0; i < 4; ++i) st128<SC1>(v4u{ua[i][0], tag, ua[i][1], tag}, rws, (tid >> 2) * 16384 + (tid & 3) * 16, WS_UG + i * 64 * 16384 + pb * 64);
    st64<SC1>(v2u{lnv, tag}, rws, tid < 16 ? (c >> 3) * 16384 + (c & 7) * 8 : OOB, WS_SR + pb * 64);
  }
  STAMP(1, 17);
  SB();
  const unsigned wp_oob = b < 176 ? 0u : OOB;
  const int wpr = b >> 2, wkg = b & 3;
  v4u xi[2], xo;
  {
    const rsrc_t rwin = mkrs(p.win, D * FF * 2), rwout = mkrs(p.wout, FH * D * 2);
    const int hf = wpr >= 22, pp = wpr - 22 * hf;
    const unsigned v = ((tid >> 2) * (FF * 2) + (tid & 3) * 16) | ((pp == 21 ? tid & 2 : 0) << 29);
#pragma unroll
    for (int i = 0; i < 2; ++i) xi[i] = ld128<IN_AUX>(rwin, v, (wkg * 128 + i * 64) * (FF * 2) + (hf * FH + pp * 32) * 2 + wp_oob);
    xo = ld128<IN_AUX>(rwout, (tid >> 3) * 1024 + (tid & 7) * 16, idx * 32 * 1024 + xcd * 128);
  }
  SB();
  const int d0 = pb * 2, dk = d0 + cch;
  const int mt = (xcd >> 1) * 8 + (idx & 7), nt = (xcd & 1) * 4 + (idx >> 3);
  const int big = (nt + 2) >> 3;
  const int np = 3 - big, pr0 = 3 * nt - big * (nt - 6);
  const int kh = w & 1, uh = w >> 1;
  const int ub_own = pr0 * 2 + uh * np, ub_oth = pr0 * 2 + (uh ^ 1) * np;
  const unsigned oob3 = (unsigned)big << 30;
  const int kr = 2 * (idx >> 3) + (xcd & 1) + (OPT_B == 0 ? (idx & 7) : OPT_B == 1 ? 4 * (idx & 1) : OPT_B == 2 ? 2 * (idx & 3) : 0);
  const rsrc_t rwsp = mkrs(p.wsp, L * L * 2);
  const unsigned wp_fr = (wpr * 2 * 16 + wkg * 4 + w) * 1024 + wp_oob;
  v4u wr[8][8];
  const unsigned wsp_v = (lane >> 3) * 4096 + (lane & 7) * 16, wsp_s = mt * 64 * 4096 + w * 1024;
#define LOAD_WSP1(ck, i) wr[ck][i] = ld128<WSP_AUX>(rwsp, wsp_v + (((ck) + kr) & 7) * 128, wsp_s + (i) * 8 * 4096)
#define LOAD_WSPN(n) LOAD_WSP1((n) >> 3, (n) & 7)
  const v4u mk = {~0u, ~0u, ~0u, ~0u};
  {
    const float dt = rbf(exp_acc(bf2f(q_ldt)));
    const float cre = bf2f(q_cre) * dt, cim = bf2f(q_cim) * dt;
    const float are2 = -rbf(exp_acc(bf2f(q_lar))) * dt * 1.4426950408889634f;
    const float aim = bf2f(q_aim) * dt;
    const float rev_hi = aim * 0.15915494309189535f;
    const float rev_lo = __builtin_fmaf(aim, 0.15915494309189535f, -rev_hi) + aim * 6.4206383e-9f;
    int ex = __builtin_amdgcn_frexp_expf(xrow_max(row16_max(fmaxf(fabsf(cre), fabsf(cim))))) + 1;
    ex = ex < -100 ? -100 : ex;
    const float scale = __builtin_amdgcn_ldexpf(1.f, -ex);
    ((float*)(smem + LDS_GS + 1024))[w] = __builtin_amdgcn_ldexpf(2.f, ex);
    const f32x2 c0 = {cre * scale, cim * scale};
    const f32x2 e1 = cexp_p(are2, rev_hi, rev_lo, 1.f), e8 = cexp_p(are2, rev_hi, rev_lo, 8.f), e64 = cexp_p(are2, rev_hi, rev_lo, 64.f);
    unsigned char* tn = smem + LDS_TAB + cch * (64 * 528) + ns * 4;
#define TROW(r, z) *(unsigned*)(tn + (r) * 528) = pkh((z)[0], -(z)[1])
    SB();
    const f32x2 e128 = cmul(e64, e64), e2 = cmul(e1, e1), e256 = cmul(e128, e128), e4 = cmul(e2, e2), e512 = cmul(e256, e256);
    f32x2 f0 = {1.f, 0.f}, f1 = e64;
    f32x2 ca = cmul(c0, e512), cb = cmul(ca, e512);
#pragma unroll
    for (int j = 0; j < 8; ++j) {
      *(unsigned*)(tn + (48 + j) * 528) = pkh(f0[0], f0[1]);
      *(unsigned*)(tn + (56 + j) * 528) = pkh(f1[0], f1[1]);
      TROW(32 + j, cb);
      f0 = j == 0 ? e8 : cmul(f0, e8); f1 = cmul(f1, e8); cb = cmul(cb, e128);
      if (j < 4) {
        const f32x2 z = cmul(ca, e4);
        TROW(24 + j, ca); TROW(28 + j, z);
        ca = cmul(ca, e128);
      }
      if (j == 3 || j == 7) {
        PIN(f0); PIN(f1); PIN(cb);
        if (WSP_P0 >= 4) { LOAD_WSPN(j >> 2 << 1); LOAD_WSPN((j >> 2 << 1) + 1); }
        SB();
      }
    }
    TROW(40, cb);
    f32x2 t1[8];
    t1[1] = e1; t1[2] = e2; t1[4] = e4;
    t1[3] = cmul(e2, e1); t1[5] = cmul(e4, e1); t1[6] = cmul(e2, e4); t1[7] = cmul(t1[6], e1);
    const f32x2 c1 = cmul(c0, e128), c2 = cmul(c0, e256), c3 = cmul(c2, e128);
    TROW(0, c0); TROW(8, c1); TROW(16, c2); TROW(20, c3);
#pragma unroll
    for (int v = 1; v < 8; ++v) {
      const f32x2 z0 = cmul(t1[v], c0), z1 = cmul(t1[v], c1);
      TROW(v, z0); TROW(8 + v, z1);
      if ((v & 1) == 0) {
        const f32x2 z2 = cmul(t1[v], c2), z3 = cmul(t1[v], c3);
        TROW(16 + v / 2, z2); TROW(20 + v / 2, z3);
        if (v < 6 && WSP_P0 >= 8) { LOAD_WSPN(v + 2); LOAD_WSPN(v + 3); }
        SB();
      }
    }
  }
  STAMP(1, 27);
  {
    const float cv = bf2f(cq);
    const float sl = rbf(cv * __builtin_amdgcn_rcpf(1.0f + __builtin_amdgcn_exp2f(cv * -1.4426950408889634f)));
    const float sc[4] = {dpp<0x00>(sl), dpp<0x55>(sl), dpp<0xAA>(sl), dpp<0xFF>(sl)};
    f32x2 a[4];
#pragma unroll
    for (int i = 0; i < 4; ++i)
#pragma unroll
      for (int j = 0; j < 4; ++j) a[j] = i == 0 ? unpk(xm[i][j]) * bc2(sc[i]) : fma2(unpk(xm[i][j]), bc2(sc[i]), a[j]);
    float* red = (float*)(smem + LDS_MR) + (tid >> 3) * 68 + (tid & 7) * 8;
    *(f32x4*)red = f32x4{a[0][0], a[0][1], a[1][0], a[1][1]};
    *(f32x4*)(red + 4) = f32x4{a[2][0], a[2][1], a[3][0], a[3][1]};
  }
  STAMP(1, 29);
  bar();
  STAMP(1, 18);
  {
    float ms8[8];
#pragma unroll
    for (int i = 0; i < 8; ++i) ms8[i] = ((const float*)(smem + LDS_MR))[(q * 8 + i) * 68 + w * 16 + c];
    const float s = xrow_sum(((ms8[0] + ms8[1]) + (ms8[2] + ms8[3])) + ((ms8[4] + ms8[5]) + (ms8[6] + ms8[7])));
    st64<SC1>(v2u{__float_as_uint(s), tag}, rws, q == 0 ? c * 8 : OOB, WS_MODG + (mkg * 3072 + mcg * 64 + w * 16) * 8 + (b < 192 ? 0u : OOB));
  }
  SB();
  v4u gr[4], gb_[4];
  v2u gm[4], gc[4];
  const unsigned gm_off = ((cch ? 3 * 512 : 4 * 512) + wkg * 128 + ns) * 8;
  const unsigned gc_off = (((lane & 7) > 5 ? 0 : ((lane & 7) >> 1)) * 512 + d0 + (lane & 1)) * 8;
#define LOAD_GS() _Pragma("unroll") for (int i_ = 0; i_ < 4; ++i_) { gr[i_] = ld128<SC1>(rws, tid * 16, WS_SR + i_ * 4096); gb_[i_] = ld128<SC1>(rws, tid * 16, WS_SB + i_ * 4096); }
#define LOAD_GM() _Pragma("unroll") for (int i_ = 0; i_ < 4; ++i_) { gm[i_] = ld64<SC1>(rws, gm_off, WS_MODG + i_ * 24576); gc[i_] = ld64<SC1>(rws, gc_off, WS_MODG + i_ * 24576); }
  {
    const unsigned char* tab = smem + LDS_TAB + cch * (64 * 528) + c * 528 + q * 16;
    v4u fa[2][8], fb[8];
#pragma unroll
    for (int ks = 0; ks < 4; ++ks) {
      fb[ks] = *(const v4u*)(tab + 48 * 528 + ks * 64);
      fa[0][ks] = *(const v4u*)(tab + eh * (32 * 528) + ks * 64);
      fa[1][ks] = *(const v4u*)(tab + 16 * 528 + ks * 64);
    }
    const f32x2 us = *(const f32x2*)(smem + LDS_GS + 1024 + cch * 8);
    SB();
    f32x4 kk[2][2];
#pragma unroll
    for (int i = 0; i < 4; ++i) kk[i >> 1][i & 1] = f32x4{0.f, 0.f, 0.f, 0.f};
#pragma unroll
    for (int ks = 0; ks < 8; ++ks) {
      if (ks < 4) {
        fb[4 + ks] = *(const v4u*)(tab + 48 * 528 + (4 + ks) * 64);
        fa[0][4 + ks] = *(const v4u*)(tab + eh * (32 * 528) + (4 + ks) * 64);
        fa[1][4 + ks] = *(const v4u*)(tab + 16 * 528 + (4 + ks) * 64);
      }
      kk[0][ks >> 2] = mfma_h(fa[0][ks], fb[ks], kk[0][ks >> 2]); kk[1][ks >> 2] = mfma_h(fa[1][ks], fb[ks], kk[1][ks >> 2]);
      SB();
    }
    STAMP(1, 37);
    *(unsigned*)(smem + LDS_KP - 16 + cch * KP_CH + (lane & 3) * 4) = 0u;
#pragma unroll
    for (int i = 0; i < 2; ++i) *(v4u*)(smem + LDS_WI + (i * 64 + (tid >> 2)) * 80 + (tid & 3) * 16) = xi[i];
    *(v4u*)(smem + LDS_WO + (tid >> 3) * 144 + (tid & 7) * 16) = xo;
    f32x4 k0 = kk[0][0] * us[0] + kk[0][1] * us[1];
    const f32x4 k1 = kk[1][0] * us[0] + kk[1][1] * us[1];
    k0[0] += ns == 0 ? bf2f(q_dsk) : 0.f;
#pragma unroll
    for (int r = 0; r < 4; ++r) { *(float*)(smem + cob[0] + r * cos_[0]) = k0[r]; *(float*)(smem + cob[1] + r * cos_[1]) = k1[r]; }
  }
  v4u ug[4];
#define LOAD_UG() _Pragma("unroll") for (int i_ = 0; i_ < 4; ++i_) ug[i_] = ld128<SC1>(rws, tid * 16, WS_UG + pb * 16384 + i_ * 4096)
  STAMP(1, 31);
  LOAD_UG();
  bar();
  STAMP(1, 19);
  v4u wt[2];
  {
    float v[2][9];
#pragma unroll
    for (int j = 0; j < 9; ++j) v[0][j] = *(const float*)(smem + LDS_KP + pos4[j]);
    LOAD_GS();
#pragma unroll
    for (int j = 0; j < 9; ++j) v[1][j] = *(const float*)(smem + LDS_KP + KP_CH + pos4[j]);
    SB();
    STAMP(1, 36);
    *(v4u*)(smem + cch * CV_CH + (lane < 32 ? CV_H + l16 : ((lane >> 1) & 1) * CV_RK + 4096 + (lane & 1) * 16)) = v4u{0u, 0u, 0u, 0u};
#pragma unroll
    for (int ch = 0; ch < 2; ++ch) {
      unsigned char* rk = smem + ch * CV_CH + tid * 16;
#pragma unroll
      for (int sft = 0; sft < 2; ++sft)
        *(v4u*)(rk + sft * CV_RK) = v4u{pk2(v[ch][sft], v[ch][sft + 1]), pk2(v[ch][sft + 2], v[ch][sft + 3]), pk2(v[ch][sft + 4], v[ch][sft + 5]), pk2(v[ch][sft + 6], v[ch][sft + 7])};
    }
    const unsigned char* a0 = smem + LDS_WI + (w * 32 + 8 * q + ((lane >> 2) & 3)) * 80 + (lane & 3) * 16;
#pragma unroll
    for (int e = 0; e < 2; ++e) {
      const v2u lo = trread(a0 + e * 8), hi = trread(a0 + e * 8 + 320);
      wt[e] = v4u{lo[0], lo[1], hi[0], hi[1]};
    }
    const unsigned char* o0 = smem + LDS_WO + (8 * q + ((lane >> 2) & 3)) * 144 + (w >> 1) * 64 + (lane & 3) * 16 + (w & 1) * 8;
    const v2u olo = trread(o0), ohi = trread(o0 + 576);
    st128<SC1>(v4u{olo[0], olo[1], ohi[0], ohi[1]}, rws, l16, WS_WOT + ((xcd * 4 + w) * 22 + idx) * 1024 + wp_oob);
    SB();
    LOAD_GM();
    SB();
    STAMP(1, 32);
    unsigned m = 0;
#pragma unroll
    for (int i = 0; i < 4; ++i) { m = tacc(m, ug[i], tag); m = tacc(m, gr[i], tag); m = tacc(m, gb_[i], tag); }
    if (__builtin_expect(any_nz(m), 0)) {
      unsigned sp = 0;
      do {
        MEMFENCE(); LOAD_UG(); LOAD_GS(); m = 0;
#pragma unroll
        for (int i = 0; i < 4; ++i) { m = tacc(m, ug[i], tag); m = tacc(m, gr[i], tag); m = tacc(m, gb_[i], tag); }
      } while (any_nz(m) & (++sp <= SPIN_CAP));
    }
    f32x2 nn[4][2];
#pragma unroll
    for (int i = 0; i < 4; ++i) {
      nn[i][0] = fma2(unpk(ug[i][0]), bc2(__uint_as_float(gr[i][0])), bc2(__uint_as_float(gb_[i][0])));
      nn[i][1] = fma2(unpk(ug[i][2]), bc2(__uint_as_float(gr[i][2])), bc2(__uint_as_float(gb_[i][2])));
      PIN(nn[i][0]); PIN(nn[i][1]);
      *(unsigned*)(smem + LDS_U + i * 1024 + tid * 4) = __builtin_amdgcn_perm(ug[i][2], ug[i][0], 0x05040100u);
      *(unsigned*)(smem + LDS_U + 4096 + i * 1024 + tid * 4) = __builtin_amdgcn_perm(ug[i][2], ug[i][0], 0x07060302u);
    }
    SB();
    STAMP(1, 33);
    m = 0;
#pragma unroll
    for (int i = 0; i < 4; ++i) m = m | (gm[i][1] ^ tag) | (gc[i][1] ^ tag);
    if (__builtin_expect(any_nz(m), 0)) {
      unsigned sp = 0;
      do {
        __builtin_amdgcn_s_sleep(3);
        MEMFENCE(); LOAD_GM(); m = 0;
#pragma unroll
        for (int i = 0; i < 4; ++i) m = m | (gm[i][1] ^ tag) | (gc[i][1] ^ tag);
      } while (any_nz(m) & (++sp <= SPIN_CAP));
    }
    const float mv = rbf((__uint_as_float(gm[0][0]) + __uint_as_float(gm[1][0])) + (__uint_as_float(gm[2][0]) + __uint_as_float(gm[3][0])));
    if (cch) *(u16*)(smem + LDS_GS + 512 + ns * 2) = (u16)(__float_as_uint(mv) >> 16);
    else *(float*)(smem + LDS_GS + ns * 4) = rbf(1.0f + mv);
    const float cv = rbf((__uint_as_float(gc[0][0]) + __uint_as_float(gc[1][0])) + (__uint_as_float(gc[2][0]) + __uint_as_float(gc[3][0])));
    const float gav = dpp<0x104>(cv), gav2 = dpp<0x102>(cv);
    const float hbv = cv * gav, hsv = rbf(1.0f + cv) * gav2;
    const f32x2 hb = {__builtin_bit_cast(float, __builtin_amdgcn_readlane(__builtin_bit_cast(int, hbv), 0)), __builtin_bit_cast(float, __builtin_amdgcn_readlane(__builtin_bit_cast(int, hbv), 1))};
    const f32x2 hs = {__builtin_bit_cast(float, __builtin_amdgcn_readlane(__builtin_bit_cast(int, hsv), 2)), __builtin_bit_cast(float, __builtin_amdgcn_readlane(__builtin_bit_cast(int, hsv), 3))};
    SB();
#pragma unroll
    for (int i = WSP_P0; i < WSP_P0 + 4; ++i) LOAD_WSPN(i);
    STAMP(1, 38);
#pragma unroll
    for (int i = 0; i < 4; ++i) {
      const f32x2 h0 = fma2(nn[i][0], hs, hb), h1 = fma2(nn[i][1], hs, hb);
      *(unsigned*)(smem + CV_H + 512 + i * 1024 + tid * 4) = pk2(h0[0], h1[0]);
      *(unsigned*)(smem + CV_CH + CV_H + 512 + i * 1024 + tid * 4) = pk2(h0[1], h1[1]);
    }
  }
  asm volatile("" ::"v"(codepf));
  const unsigned char* abase = smem + cch * CV_CH + (1 - (c & 1)) * CV_RK + 2 * (2030 - 2 * (c >> 1) + 8 * q) - eh * 256 - 192 - 3584;
  const unsigned char* bbase = smem + cch * CV_CH + CV_H + 2 * (256 - 16 + 16 * c + 8 * q) - eh * 256 - 192;
  STAMP(1, 39);
  bar();
  STAMP(1, 1);
#define CV_A(ee, k) ({ const unsigned* p_ = (const unsigned*)(abase + 192 - 64 * (ee) + 3584 - 512 * (k)); v4u{p_[0], p_[1], p_[2], p_[3]}; })
#define CV_B(ee, k) (*(const v4u*)(bbase + 192 - 64 * (ee) + 512 * (k)))
  v4u cA[2][8], cB[2][8];
  f32x4 gs[2];
  v4u sbf;
  const v4u ones4 = {ONES, ONES, ONES, ONES};
#pragma unroll
  for (int k = 0; k < 2; ++k) { cA[0][k] = CV_A(0, k); cB[0][k] = CV_B(0, k); }
  {
    const float* gp = (const float*)(smem + LDS_GS) + w * 32 + q * 8;
    gs[0] = *(const f32x4*)gp; gs[1] = *(const f32x4*)(gp + 4);
    sbf = *(const v4u*)(smem + LDS_GS + 512 + w * 64 + q * 16);
  }
  SB();
  f32x4 cacc[8];
  v2u ue[4];
  {
    f32x4 cb4[4];
    v4u wo_[2];
    float wl, wh;
    f32x2 cp[4];
#pragma unroll
    for (int t = 0; t < 8; ++t) cacc[t] = f32x4{0.f, 0.f, 0.f, 0.f};
    sfor<144>([&](auto G) __attribute__((always_inline)) {
      constexpr int g = decltype(G)::value, ee = g / 36, j = g % 36;
      if constexpr (ee == 0 && j < 6) { cA[0][2 + j] = CV_A(0, 2 + j); cB[0][2 + j] = CV_B(0, 2 + j); }
      cacc[CVO.pp[j] + CVO.qq[j]] = mfma(cA[ee & 1][CVO.qq[j]], cB[ee & 1][CVO.pp[j]], cacc[CVO.pp[j] + CVO.qq[j]]);
      constexpr int jn = ee == 0 ? j - 20 : (j & 1) ? j >> 1 : -1;
      if constexpr (ee < 3 && jn >= 0 && jn < 16) {
        if constexpr (jn & 1) cB[(ee + 1) & 1][jn >> 1] = CV_B(ee + 1, jn >> 1);
        else cA[(ee + 1) & 1][jn >> 1] = CV_A(ee + 1, jn >> 1);
      }
      if constexpr (ee == 3 && j < 4) ue[j] = *(const v2u*)(smem + LDS_U + cch * 4096 + (eh * 4 + j) * 512 + (c * 16 + q * 4) * 2);
      if constexpr (g >= 6 && g < 30) {
        constexpr int i = (g - 6) / 12, jj = ((g - 6) / 3) & 3, ph = (g - 6) % 3;
        if constexpr (ph == 0) { wl = bflo(wt[i][jj]) * gs[jj >> 1][(jj & 1) * 2]; PIN(wl); }
        if constexpr (ph == 1) { wh = bfhi(wt[i][jj]) * gs[jj >> 1][(jj & 1) * 2 + 1]; PIN(wh); }
        if constexpr (ph == 2) {
          wo_[i][jj] = pk2(wl, wh);
          if constexpr (jj == 3) st128<SC1>(wo_[i], rws, l16 + i * 16384, WS_WP + wp_fr);
          else PIN(wo_[i][jj]);
        }
      }
      if constexpr (g == 4 || g == 5) cb4[2 * (g - 4) + 1] = mfma(sbf, wt[g - 4], f32x4{0.f, 0.f, 0.f, 0.f});
      if constexpr (g == 19 || g == 31) cb4[g == 19 ? 0 : 2] = mfma(ones4, wo_[g == 31], f32x4{0.f, 0.f, 0.f, 0.f});
      if constexpr (g == 38) *(f32x4*)(smem + LDS_S + 4096 + (w * 16 + c) * 16) = f32x4{cb4[0][0], cb4[1][0], cb4[2][0], cb4[3][0]};
      if constexpr (g == 46) bar();
      if constexpr (g == 47) {
#pragma unroll
        for (int k = 0; k < 4; ++k) cp[k] = *(const f32x2*)(smem + LDS_S + 4096 + (k * 16 + c) * 16 + (q & 1) * 8);
      }
      if constexpr (g == 54) {
        float s0 = cp[0][0] + cp[1][0], s1 = cp[2][0] + cp[3][0];
        PIN(s0); PIN(s1);
        s0 += s1;
        PIN(s0);
        float s2 = cp[0][1] + cp[1][1], s3 = cp[2][1] + cp[3][1];
        PIN(s2); PIN(s3);
        st64<SC1>(v2u{__float_as_uint(s0), __float_as_uint(s2 + s3)}, rws, tid < 32 ? tid * 32 : OOB, WS_CSB + wpr * 1024 + wkg * 8 + wp_oob);
      }
      if constexpr (g == 1 || g == 5) st128<SC1>(mk, rws, l16 + (g == 5) * 1024, WS_X1T + dk * 4096 + eh * 2048);
      if constexpr (g == 9) __builtin_amdgcn_raw_buffer_store_b32(~0u, rws, lane * 4, WS_ST2 + ((mt * 2 + kh) * 16 + nt * 2 + uh) * 256, SC1);
      if constexpr (g == 42) { if (b == 0 && tid == 0) *(unsigned*)(p.ws + WS_EPOCH) = tag; }
      if constexpr (g == 36) STAMP(1, 40);
      if constexpr (g == 72) STAMP(1, 41);
      if constexpr (g == FLAG_AT) {
        STAMP(1, 42);
        asm volatile("s_waitcnt vmcnt(%0)" ::"i"((FLAG_AT - 57 + 5) / 6) : "memory");
        __builtin_amdgcn_s_barrier();
        if (tid < 8) hint_add(p.ws, 256 + tid * 16 + (idx & 15));
        STAMP(1, 43);
      }
      if constexpr (g % 6 == 3) LOAD_WSPN(WSP_P0 + 4 + g / 6);
      SB();
    });
  }
  STAMP(1, 2);
  {
    f32x4* red = (f32x4*)(smem + LDS_X) + w * 256 + lane;
    f32x4 keep[4];
    if (eh) {
#pragma unroll
      for (int t = 0; t < 4; ++t) { red[t * 64] = cacc[t]; keep[t] = cacc[4 + t]; PIN(keep[t]); }
    } else {
#pragma unroll
      for (int t = 0; t < 4; ++t) { red[t * 64] = cacc[4 + t]; keep[t] = cacc[t]; PIN(keep[t]); }
    }
    f32x2 pre[4][2];
#pragma unroll
    for (int t = 0; t < 4; ++t) {
      pre[t][0] = unpk(ue[t][0]) + lo2(keep[t]);
      pre[t][1] = unpk(ue[t][1]) + hi2(keep[t]);
      PIN(pre[t][0]); PIN(pre[t][1]);
    }
    const unsigned xoff = ((c >> 1) * 32 + ((q & 1) * 4 + 2 * (c & 1) + (q >> 1)) * 4) * 2;
    bar();
    const f32x4* rd = (const f32x4*)(smem + LDS_X) + (w ^ 1) * 256 + lane;
    f32x4 oth[4];
#pragma unroll
    for (int t = 0; t < 4; ++t) oth[t] = rd[t * 64];
#pragma unroll
    for (int t = 0; t < 4; ++t) {
      v2u o;
      o[0] = nomark(pk2(pre[t][0] + lo2(oth[t])));
      o[1] = pk2(pre[t][1] + hi2(oth[t]));
      st64<SC1>(o, rws, xoff + t * 512, WS_X1T + (d0 + cch) * 4096 + eh * 2048);
    }
  }
  STAMP(1, 3);
  SB();
  v4u wf[3][8];
  const unsigned rot = uh * 4;
#define LOAD_WP(half, k, i) ld128<SC1>(rws, l16 + ((i) & 3) * 1024, WS_WP + (((half) * 44 + ub_own + (k)) * 16 + kh * 8 + (((i) & 4) ^ rot)) * 1024 + ((k) == 2 ? oob3 : 0u))
#define LOAD_XS(i) ld128<SC1>(rws, wsp_v, WS_X1T + ((i) * 32 + w * 8) * 4096 + mt * 128)
  unsigned char* wslot = smem + LDS_W + w * 16384;
  const int wr_off = (lane >> 3) * 128 + (((lane & 7) ^ ((lane >> 4) & 1)) * 16);
#define PARK1(ck, slot, i) *(v4u*)((slot) + (i) * 1024 + (wr_off ^ (((i) & 3) * 32))) = wr[ck][i]
#define PARK_WSP(ck, slot) _Pragma("unroll") for (int i_ = 0; i_ < 8; ++i_) PARK1(ck, slot, i_)
  PARK_WSP(0, wslot);
  PARK_WSP(1, wslot + 8192);
  v4u xs[16];
  const unsigned char* tb[2][4];
  unsigned char* xdst;
  {
    const int row = 8 * q + ((lane >> 2) & 3);
    const int sg = ((row >> 1) & 1) | (((row >> 3) & 1) << 1);
#pragma unroll
    for (int tu = 0; tu < 4; ++tu) {
      const unsigned char* t0 = smem + LDS_A + kh * 32768 + row * 128 + ((tu ^ (2 * kh) ^ sg) * 32) + (lane & 3) * 8;
      tb[0][tu] = t0 + uh * 16384;
      tb[1][tu] = t0 + (uh ^ 1) * 16384;
    }
    const int rl = w * 8 + (lane >> 3), chn = lane & 7;
    const int sg2 = ((rl >> 1) & 1) | (((rl >> 3) & 1) << 1);
    xdst = smem + LDS_A + rl * 128 + (((chn >> 1) ^ sg2) * 32) + (chn & 1) * 16;
  }
  SB();
  f32x4 cz[2][3][2];
#define LOAD_CZ(hf, kc, ic) asf(ld128<SC1>(rws, c * 32 + (ic) * 16, WS_CSB + ((hf) * 44 + ub_own + (kc)) * 512 + ((kc) == 2 ? oob3 : 0u)))
  {
    STAMP(1, 20);
    if (w == 0) hint_wait(rws, c * 128, WS_CNT + (256 + xcd * 16) * 128, 16u);
    __builtin_amdgcn_s_barrier();
    MEMFENCE();
    STAMP(1, 21);
#pragma unroll
    for (int i = 0; i < WF_EARLY; ++i)
#pragma unroll
      for (int k = 0; k < 3; ++k) wf[k][i] = LOAD_WP(1, k, i);
#pragma unroll
    for (int i = 0; i < 16; ++i) xs[i] = LOAD_XS(i);
    SB();
    STAMP(1, 44);
    unsigned m = 0;
#pragma unroll
    for (int i = 0; i < 16; ++i) {
      m = macc(m, xs[i]); *(v4u*)(xdst + i * 4096) = xs[i];
      if (WF_EARLY * 3 + i < 24) wf[(WF_EARLY * 3 + i) % 3][(WF_EARLY * 3 + i) / 3] = LOAD_WP(1, (WF_EARLY * 3 + i) % 3, (WF_EARLY * 3 + i) / 3);
      SB();
    }
    PIN(m);
    STAMP(1, 45);
    RETRY_BEGIN(m)
#pragma unroll
      for (int i = 0; i < 16; ++i) xs[i] = LOAD_XS(i);
      SB();
#pragma unroll
      for (int i = 0; i < 16; ++i) { m = macc(m, xs[i]); *(v4u*)(xdst + i * 4096) = xs[i]; }
    RETRY_END(m)
  }
  STAMP(1, 4);
  bar();
  STAMP(1, 5);
#define AHALF(tu, i, h) trread(tb[(i) >> 2][tu] + ((i) & 3) * 4096 + (h) * 512)
  f32x4 acc[3][4], s1[4], s2[4];
#pragma unroll
  for (int tu = 0; tu < 4; ++tu) {
    s1[tu] = f32x4{0.f, 0.f, 0.f, 0.f}; s2[tu] = s1[tu];
#pragma unroll
    for (int k = 0; k < 3; ++k) acc[k][tu] = s1[tu];
  }
  v4u wg[3][8];
  v2u ah[2][4][2];
#pragma unroll
  for (int tu = 0; tu < 4; ++tu) { ah[0][tu][0] = AHALF(tu, 0, 0); ah[0][tu][1] = AHALF(tu, 0, 1); }
  float* st = (float*)(smem + LDS_S);
  f32x2 csb[2][3];
  SB();
#pragma unroll
  for (int i = 0; i < 8; ++i) {
#pragma unroll
    for (int j = 0; j < 12; ++j) {
      const int tu = j / 3, k = j % 3;
      const v4u a = {ah[i & 1][tu][0][0], ah[i & 1][tu][0][1], ah[i & 1][tu][1][0], ah[i & 1][tu][1][1]};
      acc[k][tu] = mfma(a, wf[k][i], acc[k][tu]);
      if (j < 8) ah[(i + 1) & 1][j >> 1][j & 1] = AHALF(j >> 1, (i + 1) & 7, j & 1);
      if (i < 4 && k == 0) s1[tu] = mfma(ones4, a, s1[tu]);
      if (i < 4 && k == 1) s2[tu] = mfma(a, a, s2[tu]);
      if ((i == 5 || i == 6) && (j == 8 || j == 10)) {
        const int t0 = (i - 5) * 2 + ((j - 8) >> 1);
        const float dg = (c & 2) ? ((c & 1) ? s2[t0][3] : s2[t0][2]) : ((c & 1) ? s2[t0][1] : s2[t0][0]);
        float* dst = st + ((c >> 2) == q ? w * 128 + (t0 ^ (2 * kh)) * 16 + c : 1280);
        dst[0] = s1[t0][0]; dst[64] = dg;
      }
      if (i == 4 && (j & 1) == 0) cz[1][(j >> 1) % 3][(j >> 1) / 3] = LOAD_CZ(1, (j >> 1) % 3, (j >> 1) / 3);
      if (i < 4 && j % 3 == 2) LOAD_WSPN(32 + i * 4 + j / 3);
      if (i == 5 && (j & 3) == 1) st128<SC1>(mk, rws, l16, WS_Z2 + ((ub_own + (j >> 2)) * 64 + mt * 2 + kh) * 1024 + ((j >> 2) == 2 ? oob3 : 0u));
      if (i >= 6 && (j & 3) == 3) wg[j >> 2][i - 6] = LOAD_WP(0, j >> 2, i - 6);
      if (i == 0 && j == 11) STAMP(1, 46);
      if (i == 3 && j == 11) STAMP(1, 47);
      SB();
    }
  }
  STAMP(1, 6);
  f32x4* xch = (f32x4*)(smem + LDS_X);
#pragma unroll
  for (int k = 0; k < 3; ++k)
#pragma unroll
    for (int t = 0; t < 2; ++t) xch[(w * 6 + k * 2 + t) * 64 + lane] = acc[k][2 + t];
#pragma unroll
  for (int k = 3; k < 6; ++k) {
    const f32x4 t4 = cz[k / 3][k % 3][0] + cz[k / 3][k % 3][1];
    csb[k / 3][k % 3] = lo2(t4) + hi2(t4);
    PIN(csb[k / 3][k % 3]);
  }
#pragma unroll
  for (int k = 0; k < 3; ++k) { PIN(acc[k][0]); PIN(acc[k][1]); }
  STAMP(1, 48);
  bar();
  STAMP(1, 49);
  float* fin = st + 512 + w * 64;
  f32x4 z2v[3][2];
  {
    const int row = kh * 32 + (lane & 31);
    float pa[4], pb[4];
#pragma unroll
    for (int k = 0; k < 4; ++k) { pa[k] = st[k * 128 + row]; pb[k] = st[k * 128 + 64 + row]; }
    f32x4 oth[3][2];
#pragma unroll
    for (int k = 0; k < 3; ++k)
#pragma unroll
      for (int t = 0; t < 2; ++t) oth[k][t] = xch[((w ^ 1) * 6 + k * 2 + t) * 64 + lane];
    SB();
    const float m = ((pa[0] + pa[1]) + (pa[2] + pa[3])) * (1.0f / D);
    const float r = __builtin_amdgcn_rsqf(fmaxf(((pb[0] + pb[1]) + (pb[2] + pb[3])) * (1.0f / D) - m * m, 0.f) + EPS);
    fin[lane] = lane < 32 ? r : -r * m;
    f32x4 r4[2], m4[2];
#pragma unroll
    for (int t = 0; t < 2; ++t) { r4[t] = *(const f32x4*)(fin + t * 16 + q * 4); m4[t] = *(const f32x4*)(fin + 32 + t * 16 + q * 4); }
    SB();
#pragma unroll
    for (int k = 0; k < 3; ++k)
#pragma unroll
      for (int t = 0; t < 2; ++t) { oth[k][t] += acc[k][t]; PIN(oth[k][t]); }
    SB();
    f32x4 rs[2], rq[2];
#pragma unroll
    for (int t = 0; t < 2; ++t) {
#pragma unroll
      for (int k = 0; k < 3; ++k) {
        const f32x4 a = oth[k][t];
        const f32x2 zl = gelu2(fma2(lo2(a), lo2(r4[t]), fma2(lo2(m4[t]), bc2(csb[1][k][0]), bc2(csb[1][k][1]))));
        const f32x2 zh = gelu2(fma2(hi2(a), hi2(r4[t]), fma2(hi2(m4[t]), bc2(csb[1][k][0]), bc2(csb[1][k][1]))));
        z2v[k][t] = f32x4{zl[0], zl[1], zh[0], zh[1]};
        if (k == 0) { rs[t] = z2v[k][t]; rq[t] = z2v[k][t] * z2v[k][t]; }
        else { rs[t] += z2v[k][t]; rq[t] += z2v[k][t] * z2v[k][t]; }
        PIN(rs[t]); PIN(rq[t]);
#pragma unroll
        for (int kk = 0; kk < 3; ++kk) wg[kk][2 + t * 3 + k] = LOAD_WP(0, kk, 2 + t * 3 + k);
        if (EPI_W) { LOAD_WSPN(48 + (t * 3 + k) * 2); LOAD_WSPN(49 + (t * 3 + k) * 2); }
        SB();
      }
    }
    STAMP(1, 50);
    float v8[8], v4[4], v2[2];
    const bool b3 = c & 8, b2 = c & 4, b1 = c & 2, b0 = c & 1;
#pragma unroll
    for (int r_ = 0; r_ < 4; ++r_) {
      const float a0 = rs[0][r_] + dpp<0x128>(rs[0][r_]), a1 = rs[1][r_] + dpp<0x128>(rs[1][r_]);
      const float q0 = rq[0][r_] + dpp<0x128>(rq[0][r_]), q1 = rq[1][r_] + dpp<0x128>(rq[1][r_]);
      v8[2 * r_] = b3 ? a1 : a0; v8[2 * r_ + 1] = b3 ? q1 : q0;
    }
#pragma unroll
    for (int j = 0; j < 4; ++j) {
      const float x0 = v8[j] + dpp<0x141>(v8[j]), x1 = v8[4 + j] + dpp<0x141>(v8[4 + j]);
      v4[j] = b2 ? x1 : x0;
    }
#pragma unroll
    for (int j = 0; j < 2; ++j) {
      const float x0 = v4[j] + dpp<0x4E>(v4[j]), x1 = v4[2 + j] + dpp<0x4E>(v4[2 + j]);
      v2[j] = b1 ? x1 : x0;
    }
    const float x0 = v2[0] + dpp<0xB1>(v2[0]), x1 = v2[1] + dpp<0xB1>(v2[1]);
    const float outv = b0 ? x1 : x0;
    __builtin_amdgcn_raw_buffer_store_b32(nomark(__float_as_uint(outv)), rws, lane * 4, WS_ST2 + ((mt * 2 + kh) * 16 + nt * 2 + uh) * 256, SC1);
  }
  STAMP(1, 7);
  SB();
#pragma unroll
  for (int tu = 0; tu < 4; ++tu)
#pragma unroll
    for (int k = 0; k < 3; ++k) acc[k][tu] = f32x4{0.f, 0.f, 0.f, 0.f};
  const int k30 = w * 6 - (w >> 1) * (w & 1);
  const unsigned oob6 = (unsigned)(w >> 1) << 30;
  v2u x1h[2][2];
  int x1o;
  {
    const int row = 8 * q + ((lane >> 2) & 3);
    const int sg = ((row >> 1) & 1) | (((row >> 3) & 1) << 1);
    x1o = LDS_A + nt * 8192 + row * 128 + ((w ^ sg) * 32) + (lane & 3) * 8;
  }
  v4u wo[6][4];
#define LOAD_WO(f) wo[(f) >> 2][(f) & 3] = ld128<SC1>(rws, l16 + (((f) >> 2) & 3) * 1024, WS_WOT + ((nt * 4 + ((f) & 3)) * 22 + k30 + (((f) >> 2) & 4)) * 1024 + ((f) >= 20 ? oob6 : 0u))
  f32x4 pt[4];
  v2u gq[4];
  unsigned bspq[2];
#define LOAD_PT(i) asf(ld128<SC1>(rws, l16 + (i) * 1024, WS_ST2 + (mt * 2 + kh) * 4096))
  SB();
#pragma unroll
  for (int i = 0; i < 8; ++i) {
#pragma unroll
    for (int j = 0; j < 12; ++j) {
      const int tu = j / 3, k = j % 3;
      const v4u a = {ah[i & 1][tu][0][0], ah[i & 1][tu][0][1], ah[i & 1][tu][1][0], ah[i & 1][tu][1][1]};
      acc[k][tu] = mfma(wg[k][i], a, acc[k][tu]);
      if (j < 8 && i < 7) ah[(i + 1) & 1][j >> 1][j & 1] = AHALF(j >> 1, i + 1, j & 1);
      if (j < 4 && i == 7) x1h[j >> 1][j & 1] = trread(smem + x1o + (j >> 1) * 4096 + (j & 1) * 512);
      if (i == 0 && j < 4) LOAD_WSPN(60 + j);
      if (EPI_W && i >= 1 && i < 6 && (j == 1 || j == 5 || j == 9) && (i - 1) * 3 + (j >> 2) < WO0) LOAD_WO((i - 1) * 3 + (j >> 2));
      if (!EPI_W && i >= 1 && i < 5 && (j == 1 || j == 5 || j == 9)) LOAD_WSPN(48 + (i - 1) * 3 + (j >> 2));
      if (j >= 8 && i == 6) pt[j - 8] = LOAD_PT(j - 8);
      if (i == 7 && j >= 8) LOAD_WO(j - 8);
      if (i == 5 && j == 0) {
        asm volatile("s_waitcnt vmcnt(30)" ::: "memory");
        __builtin_amdgcn_s_barrier();
      }
      if (i == 5 && j >= 2 && j < 8) cz[0][(j - 2) % 3][(j - 2) / 3] = LOAD_CZ(0, (j - 2) % 3, (j - 2) / 3);
      if (OPT_D && i == 0 && j >= 6 && j < 10) gq[j - 6] = ld64<SC1>(rws, lane * 8, WS_MODG + ((j - 6) * 3072 + 5 * 512 + nt * 64) * 8);
      if (OPT_D && i == 0 && j >= 10) bspq[j - 10] = p.bsp[mt * 64 + perm(2 * kh + j - 10, c)];
      if (OPT_D && i == 4 && j == 10) st[1408 + lane] = rbf((__uint_as_float(gq[0][0]) + __uint_as_float(gq[1][0])) + (__uint_as_float(gq[2][0]) + __uint_as_float(gq[3][0])));
      SB();
    }
  }
  STAMP(1, 8);
  float* c1s = st + 1024 + w * 96;
#pragma unroll
  for (int k = 0; k < 3; ++k) {
    const f32x4 t4 = cz[0][k][0] + cz[0][k][1];
    const f32x2 t2 = lo2(t4) + hi2(t4);
    c1s[k * 32 + (q & 1) * 16 + c] = (q & 1) ? t2[1] : t2[0];
  }
#pragma unroll
  for (int k = 0; k < 3; ++k)
#pragma unroll
    for (int t = 0; t < 2; ++t) xch[(w * 6 + k * 2 + t) * 64 + lane] = acc[k][2 + t];
  SB();
  {
    unsigned m = 0;
#pragma unroll
    for (int i = 0; i < 4; ++i) m = macc4(m, __builtin_bit_cast(v4u, pt[i]));
    RETRY_BEGIN(m)
#pragma unroll
      for (int i = 0; i < 4; ++i) { pt[i] = LOAD_PT(i); m = macc4(m, __builtin_bit_cast(v4u, pt[i])); }
    RETRY_END(m)
    if (tid < 4) hint_add(p.ws, 64 + (nt * 4 + (mt >> 3)) * 4 + tid);
    const f32x4 sx = (pt[0] + pt[1]) + (pt[2] + pt[3]);
    const float tv = xrow_sum4(sx[0], sx[2], sx[1], sx[3]);
    auto sw = __builtin_amdgcn_permlane32_swap(__float_as_uint(tv), __float_as_uint(tv), false, false);
    const float mm = __uint_as_float(sw[0]) * (1.0f / FH);
    const float r = __builtin_amdgcn_rsqf(fmaxf(__uint_as_float(sw[1]) * (1.0f / FH) - mm * mm, 0.f) + EPS);
    float* f2 = st + 768 + w * 64;
    f2[(q >> 1) * 32 + 2 * c + (q & 1)] = (q >> 1) ? -r * mm : r;
    f32x4 r4[2], m4[2];
#pragma unroll
    for (int t = 0; t < 2; ++t) { r4[t] = *(const f32x4*)(f2 + q * 8 + t * 4); m4[t] = *(const f32x4*)(f2 + 32 + q * 8 + t * 4); }
#pragma unroll
    for (int k = 0; k < 3; ++k) {
      v4u o;
#pragma unroll
      for (int t = 0; t < 2; ++t) {
        o[2 * t] = nomark(pk2(fma2(lo2(z2v[k][t]), lo2(r4[t]), lo2(m4[t]))));
        o[2 * t + 1] = pk2(fma2(hi2(z2v[k][t]), hi2(r4[t]), hi2(m4[t])));
      }
      st128<SC1>(o, rws, l16, WS_Z2 + ((ub_own + k) * 64 + mt * 2 + kh) * 1024 + (k == 2 ? oob3 : 0u));
    }
  }
  STAMP(1, 9);
  SB();
  unsigned z1p[3][2][2];
  unsigned char* wslot2 = smem + LDS_A + w * 18432;
  int rd_off[4];
#pragma unroll
  for (int tu = 0; tu < 4; ++tu) rd_off[tu] = perm(tu ^ (2 * kh), c) * 128 + ((q ^ (c >> 1)) * 16);
  constexpr int ZD = ZDEPTH;
  v4u zf[ZD][6];
  v4u wfr[2][4];
#define LOAD_Z(ks, i) ld128<SC1>(rws, l16 + ((((ks) >> 1) + kr) & 7) * 2048 + ((ks) & 1) * 1024, WS_Z2 + ((((i) < 3 ? ub_own : ub_oth) + (i) % 3) * 64 + w * 16) * 1024 + ((i) % 3 == 2 ? oob3 : 0u))
#define WSLOT(ck) ((ck) % 3 == 2 ? wslot2 : wslot + ((ck) % 3) * 8192)
  {
#pragma unroll
    for (int k2 = 0; k2 < 2; ++k2)
#pragma unroll
      for (int tu = 0; tu < 4; ++tu) wfr[k2][tu] = *(const v4u*)(wslot + (rd_off[tu] ^ (k2 * 64)));
    bar();
    STAMP(1, 34);
    f32x4 oth[3][2], cs1[3], bs1[3];
    float r1[2], m1[2];
#pragma unroll
    for (int t = 0; t < 2; ++t) { r1[t] = fin[t * 16 + c]; m1[t] = fin[32 + t * 16 + c]; }
#pragma unroll
    for (int k = 0; k < 3; ++k) {
      cs1[k] = *(const f32x4*)(c1s + k * 32 + q * 4); bs1[k] = *(const f32x4*)(c1s + k * 32 + 16 + q * 4);
#pragma unroll
      for (int t = 0; t < 2; ++t) oth[k][t] = xch[((w ^ 1) * 6 + k * 2 + t) * 64 + lane];
    }
    SB();
    f32x2 z1a[6][2];
#pragma unroll
    for (int i = 0; i < 8; ++i) {
      PARK1(2, wslot2, i);
      PARK1(3, wslot, i);
      if (i < WO0 - 4) LOAD_WO(4 + i);
      if (i >= 2) {
        const int k = (i - 2) >> 1, t = i & 1;
        const f32x4 a = acc[k][t] + oth[k][t];
        z1a[i - 2][0] = fma2(lo2(a), bc2(r1[t]), fma2(lo2(cs1[k]), bc2(m1[t]), lo2(bs1[k])));
        z1a[i - 2][1] = fma2(hi2(a), bc2(r1[t]), fma2(hi2(cs1[k]), bc2(m1[t]), hi2(bs1[k])));
        PIN(z1a[i - 2][0]); PIN(z1a[i - 2][1]);
      }
      SB();
    }
    STAMP(1, 10);
    hint_wait(rws, 0u, WS_CNT + (64 + (nt * 4 + w) * 4 + (xcd >> 1)) * 128, 8u);
    STAMP(1, 22);
#pragma unroll
    for (int s_ = 0; s_ < 2; ++s_)
#pragma unroll
      for (int i = 0; i < 6; ++i) zf[s_][i] = LOAD_Z(s_, i);
    SB();
#pragma unroll
    for (int i = 0; i < 6; ++i) {
      z1p[i >> 1][i & 1][0] = pk2(gelu2(z1a[i][0])); z1p[i >> 1][i & 1][1] = pk2(gelu2(z1a[i][1]));
      PIN(z1p[i >> 1][i & 1][0]); PIN(z1p[i >> 1][i & 1][1]);
      if (ZD > 2 && i >= 4) { zf[ZD - 1][(i - 4) * 3] = LOAD_Z(2, (i - 4) * 3); zf[ZD - 1][(i - 4) * 3 + 1] = LOAD_Z(2, (i - 4) * 3 + 1); zf[ZD - 1][(i - 4) * 3 + 2] = LOAD_Z(2, (i - 4) * 3 + 2); }
      SB();
    }
  }
  f32x4 ac2[6][4];
#pragma unroll
  for (int i = 0; i < 6; ++i)
#pragma unroll
    for (int j = 0; j < 4; ++j) { const float b0 = (i < 3 && j < 2) ? bf2f(bspq[j & 1]) : 0.f; ac2[i][j] = f32x4{b0, b0, b0, b0}; }
  SB();
  STAMP(1, 11);
  unsigned zm = 0;
  sfor<16 * 24>([&](auto G) __attribute__((always_inline)) {
    constexpr int ks = decltype(G)::value / 24, j = decltype(G)::value % 24, ck = ks >> 1, k2 = ks & 1, i = j >> 2, tu = j & 3;
    ac2[i][tu] = mfma(zf[ks % ZD][i], wfr[k2][tu], ac2[i][tu]);
    if constexpr (j == 0 && (ks == 1 || ks == 4 || ks == 8 || ks == 12)) STAMP(1, 56 + (ks >> 2));
    if constexpr (tu == 1) { zm = macc(zm, zf[ks % ZD][i]); PIN(zm); }
    if constexpr (j == 17 && ks >= 2 && ks < 5) st128<SC1>(mk, rws, l16, WS_V + ((mt * 4 + w) * 22 + pr0 + ks - 2) * 1024 + (ks == 4 ? oob3 : 0u));
    if constexpr (OPT_C == 2 && j == 1 && ks == 12) { asm volatile("s_waitcnt vmcnt(20)" ::: "memory"); __builtin_amdgcn_s_barrier(); }
    if constexpr (OPT_C == 2 && j == 23 && ks == 15) { if (tid < 2) hint_add(p.ws, 192 + mt * 2 + tid); }
    if constexpr (!OPT_C && j == 1 && ks == HINT4_KS) {
      asm volatile("s_waitcnt vmcnt(20)" ::: "memory");
      __builtin_amdgcn_s_barrier();
      if (tid < 2) hint_add(p.ws, 192 + mt * 2 + tid);
    }
    if constexpr (tu == 3) {
      if constexpr (ks + ZD < 16) zf[ks % ZD][i] = LOAD_Z(ks + ZD, i);
      else if constexpr (ks >= 14 && WO0 + (ks - 14) * 6 + i < 24) LOAD_WO(WO0 + (ks - 14) * 6 + i);
    }
    if constexpr (k2 == 1 && j % 3 == 2 && ck >= 1 && ck + 3 < 8) PARK1(ck + 3, WSLOT(ck), j / 3);
    if constexpr (j % 4 == 0 && j < 16 && ks >= 1 && ks < 15) wfr[k2 ^ 1][j / 4] = *(const v4u*)(WSLOT((ks + 1) >> 1) + (rd_off[j / 4] ^ ((k2 ^ 1) * 64)));
    SB();
  });
  STAMP(1, 12);
  {
    f32x4* red = (f32x4*)(smem + LDS_A);
#pragma unroll
    for (int i = 0; i < 6; ++i)
#pragma unroll
      for (int tu = 0; tu < 4; ++tu) {
        const int rel = (tu >> 1) | ((i / 3) << 1);
        if (rel) red[((w * 3 + rel - 1) * 6 + (i % 3) * 2 + (tu & 1)) * 64 + lane] = ac2[i][tu];
      }
    f32x4 own[3][2];
#pragma unroll
    for (int k = 0; k < 3; ++k)
#pragma unroll
      for (int t = 0; t < 2; ++t) { own[k][t] = ac2[k][t]; PIN(own[k][t]); }
  if (__builtin_expect(any_mark(zm), 0)) {
    f32x4 ar[6][4];
    int l2 = lane;
    PIN(l2);
    const int c2 = l2 & 15, q2 = l2 >> 4;
#pragma unroll
    for (int i = 0; i < 6; ++i)
#pragma unroll
      for (int j = 0; j < 4; ++j) { const float b0 = (i < 3 && j < 2) ? bf2f(p.bsp[mt * 64 + perm(2 * kh + (j & 1), c2)]) : 0.f; ar[i][j] = f32x4{b0, b0, b0, b0}; }
#pragma unroll 1
    for (int ks = 0; ks < 16; ++ks) {
      v4u zz[6], ww[4];
      unsigned sp = 0;
      do {
        MEMFENCE();
        zm = 0;
#pragma unroll
        for (int i = 0; i < 6; ++i) {
          zz[i] = ld128<SC1>(rws, l16 + ks * 1024, WS_Z2 + (((i < 3 ? ub_own : ub_oth) + i % 3) * 64 + w * 16) * 1024 + (i % 3 == 2 ? oob3 : 0u));
          zm = macc(zm, zz[i]);
        }
      } while (any_mark(zm) & (++sp <= SPIN_CAP));
#pragma unroll
      for (int tu = 0; tu < 4; ++tu) ww[tu] = ld128<0>(rwsp, perm(tu ^ (2 * kh), c2) * 4096 + q2 * 16 + ks * 64, wsp_s);
#pragma unroll
      for (int i = 0; i < 6; ++i)
#pragma unroll
        for (int tu = 0; tu < 4; ++tu) ar[i][tu] = mfma(zz[i], ww[tu], ar[i][tu]);
    }
#pragma unroll
    for (int i = 0; i < 6; ++i)
#pragma unroll
      for (int tu = 0; tu < 4; ++tu) {
        const int rel = (tu >> 1) | ((i / 3) << 1);
        if (rel) red[((w * 3 + rel - 1) * 6 + (i % 3) * 2 + (tu & 1)) * 64 + l2] = ar[i][tu];
        else own[i][tu] = ar[i][tu];
      }
  }
    STAMP(1, 51);
    if (OPT_C == 1) asm volatile("s_waitcnt vmcnt(40)" ::: "memory");
    bar();
    if (OPT_C == 1 && tid < 2) hint_add(p.ws, 192 + mt * 2 + tid);
    STAMP(1, 52);
    f32x4 o3[3][2][3];
#pragma unroll
    for (int k = 0; k < 3; ++k)
#pragma unroll
      for (int t = 0; t < 2; ++t)
#pragma unroll
        for (int rel = 1; rel < 4; ++rel) o3[k][t][rel - 1] = red[(((w ^ rel) * 3 + rel - 1) * 6 + k * 2 + t) * 64 + lane];
    SB();
#pragma unroll
    for (int k = 0; k < 3; ++k) {
#pragma unroll
      for (int t = 0; t < 2; ++t) {
        const f32x4 g = (own[k][t] + o3[k][t][0]) + (o3[k][t][1] + o3[k][t][2]);
        v2u o;
        o[0] = nomark(pk2(unpk(z1p[k][t][0]) * lo2(g)));
        o[1] = pk2(unpk(z1p[k][t][1]) * hi2(g));
        const int uu = ub_own + k;
        st64<SC1>(o, rws, l16, WS_V + ((mt * 4 + 2 * kh + t) * 22 + (uu >> 1)) * 1024 + (uu & 1) * 8 + (k == 2 ? oob3 : 0u));
      }
    }
  }
  STAMP(1, 13);
  SB();
  f32x4 gb4[4];
  f32x2 x1f[2][4];
#pragma unroll
  for (int i = 0; i < 4; ++i) gb4[i] = *(const f32x4*)(st + 1408 + (i >> 1) * 32 + q * 8 + (i & 1) * 4);
#pragma unroll
  for (int i = 0; i < 8; ++i) { x1f[i >> 2][i & 3] = unpk(x1h[i >> 2][(i >> 1) & 1][i & 1]); PIN(x1f[i >> 2][i & 3]); }
  STAMP(1, 53);
  if (w == 0) hint_wait(rws, 0u, WS_CNT + (192 + mt * 2 + (xcd & 1)) * 128, 8u);
  __builtin_amdgcn_s_barrier();
  MEMFENCE();
  STAMP(1, 23);
  f32x4 a3[4][4];
  {
    unsigned spins = 0;
    for (;;) {
      MEMFENCE();
      v4u vs[6][4];
#define LOAD_V(f) vs[(f) >> 2][(f) & 3] = ld128<SC1>(rws, l16 + (((f) >> 2) & 3) * 1024, WS_V + ((mt * 4 + (((f) & 3) ^ w)) * 22 + k30 + (((f) >> 2) & 4)) * 1024 + ((f) >= 20 ? oob6 : 0u))
#pragma unroll
      for (int f = 0; f < 12; ++f) LOAD_V(f);
      SB();
      unsigned m = 0;
      sfor<96>([&](auto G) __attribute__((always_inline)) {
        constexpr int g = decltype(G)::value, ks = g >> 4, tu = (g >> 2) & 3, e = g & 3;
        a3[e][tu] = mfma(wo[ks][e], vs[ks][tu], ks == 0 ? f32x4{0.f, 0.f, 0.f, 0.f} : a3[e][tu]);
        if constexpr (e == 0 && 12 + (g >> 2) < 24) LOAD_V(12 + (g >> 2));
        if constexpr (e == 1) { m = macc(m, vs[ks][tu]); PIN(m); }
        SB();
      });
      if (!(any_mark(m) & (++spins <= SPIN_CAP))) break;
    }
  }
  STAMP(1, 14);
  {
    f32x4* red = (f32x4*)(smem + LDS_W);
#pragma unroll
    for (int tu = 1; tu < 4; ++tu)
#pragma unroll
      for (int e = 0; e < 4; ++e) red[((w * 3 + tu - 1) * 4 + e) * 64 + lane] = a3[e][tu];
#pragma unroll
    for (int e = 0; e < 4; ++e) PIN(a3[e][0]);
    bar();
    STAMP(1, 54);
    f32x4 o3[4][3];
#pragma unroll
    for (int e = 0; e < 4; ++e)
#pragma unroll
      for (int rel = 1; rel < 4; ++rel) o3[e][rel - 1] = red[(((w ^ rel) * 3 + rel - 1) * 4 + e) * 64 + lane];
    const rsrc_t rout = mkrs(p.out, L * D * 2);
    SB();
#pragma unroll
    for (int pp = 0; pp < 2; ++pp) {
      v4u o;
#pragma unroll
      for (int h = 0; h < 2; ++h) {
        const int e = pp * 2 + h;
        const f32x4 y = (a3[e][0] + o3[e][0]) + (o3[e][1] + o3[e][2]);
        o[2 * h] = pk2(fma2(lo2(y), lo2(gb4[e]), x1f[pp][2 * h]));
        o[2 * h + 1] = pk2(fma2(hi2(y), hi2(gb4[e]), x1f[pp][2 * h + 1]));
      }
      st128<0>(o, rout, perm(w, c) * 1024 + q * 16 + pp * 64, mt * 64 * 1024 + nt * 128);
    }
  }
  STAMP(1, 15);
  STAMP_FLUSH(1);
  asm volatile("adaln_code_end:");
}

extern "C" void kernel_launch(void* const* d_in, const int* in_sizes, int n_in, void* d_out, int out_size, void* d_ws,
                              size_t ws_size, hipStream_t stream) {
  Params p;
  p.u = (const u16*)d_in[0];
  p.lar = (const u16*)d_in[1];
  p.aim = (const u16*)d_in[2];
  p.cre = (const u16*)d_in[3];
  p.cim = (const u16*)d_in[4];
  p.ldt = (const u16*)d_in[5];
  p.dsk = (const u16*)d_in[6];
  p.win = (const u16*)d_in[8];
  p.wsp = (const u16*)d_in[9];
  p.bsp = (const u16*)d_in[10];
  p.wout = (const u16*)d_in[11];
  p.c = (const u16*)d_in[12];
  p.wmod = (const u16*)d_in[13];
  p.out = (u16*)d_out;
  p.ws = (unsigned char*)d_ws;
  (void)hipFuncSetAttribute((const void*)adaln_mega, hipFuncAttributeMaxDynamicSharedMemorySize, MEGA_LDS);
  adaln_mega<<<256, 256, MEGA_LDS, stream>>>(p);
}
```

```cpp
#define GELU_PRE 4
#include <hip/hip_runtime.h>
#include <hip/hip_bf16.h>
#include <stdint.h>
#include <utility>

#define LAS __attribute__((address_space(3)))
typedef unsigned v4u __attribute__((ext_vector_type(4)));
typedef unsigned v2u __attribute__((ext_vector_type(2)));
typedef float f32x4 __attribute__((ext_vector_type(4)));
typedef float f32x2 __attribute__((ext_vector_type(2)));
typedef short bf16x8 __attribute__((ext_vector_type(8)));
typedef _Float16 f16x8 __attribute__((ext_vector_type(8)));
typedef _Float16 f16x2 __attribute__((ext_vector_type(2)));
typedef __bf16 bf16x2_t __attribute__((ext_vector_type(2)));
typedef unsigned short u16;
typedef __amdgpu_buffer_rsrc_t rsrc_t;

#define STAMP_DECL do { } while (0)
#define STAMP(k, i) do { } while (0)
#define STAMP_FLUSH(k) do { } while (0)

#define OPT_A 1
#define OPT_B 1
#define OPT_C 2
#define OPT_D 1
#define OPT_E 2
#define OPT_F 1
constexpr int L = 2048, D = 512, NM = 128, FH = 688, FF = 1376;
constexpr float EPS = 1e-5f;
constexpr int SC1 = 16;
constexpr unsigned OOB = 0x40000000u;
constexpr unsigned SPIN_CAP = 1u << 14;

constexpr unsigned WS_EPOCH = 0;
constexpr unsigned WS_CNT = 4096;
constexpr unsigned WS_SR = WS_CNT + 512 * 128;
constexpr unsigned WS_SB = WS_SR + 2048 * 8;
constexpr unsigned WS_MODG = WS_SB + 2048 * 8;
constexpr unsigned WS_CSB = WS_MODG + 3072 * 32;
constexpr unsigned WS_ST2 = WS_CSB + 88 * 16 * 32;
constexpr unsigned WS_UG = 1u << 20;
static_assert(WS_ST2 + 32 * 2 * 16 * 32 * 8 <= WS_UG, "small buffers");
constexpr unsigned WS_X1T = WS_UG + 256 * 2048 * 8;
constexpr unsigned WS_WP = WS_X1T + 512 * 2048 * 2;
constexpr unsigned WS_WOT = WS_WP + 88 * 16 * 1024;
constexpr unsigned WS_Z2 = WS_WOT + 32 * 22 * 1024;
constexpr unsigned WS_V = WS_Z2 + 44 * 64 * 1024;
constexpr unsigned WS_END = WS_V + 128 * 22 * 1024;

struct Params {
  const u16 *u, *lar, *aim, *cre, *cim, *ldt, *dsk, *win, *wsp, *bsp, *wout, *c, *wmod;
  u16* out;
  unsigned char* ws;
};

#define DI __device__ __forceinline__
template <int V> struct IC { static constexpr int value = V; };
template <class F, int... I> DI void sfor_(F&& f, std::integer_sequence<int, I...>) { int d[] = {(f(IC<I>{}), 0)...}; (void)d; }
template <int N, class F> DI void sfor(F&& f) { sfor_(f, std::make_integer_sequence<int, N>{}); }
DI float bf2f(unsigned b) { return __builtin_bit_cast(float, b << 16); }
DI float bflo(unsigned w) { return __builtin_bit_cast(float, w << 16); }
DI float bfhi(unsigned w) { return __builtin_bit_cast(float, w & 0xFFFF0000u); }
DI f32x2 unpk(unsigned w) { return f32x2{bflo(w), bfhi(w)}; }
DI unsigned pk2(f32x2 v) { return __builtin_bit_cast(unsigned, __builtin_convertvector(v, bf16x2_t)); }
DI unsigned pk2(float lo, float hi) { return pk2(f32x2{lo, hi}); }
DI unsigned nomark(unsigned x) { return x < 0xFFFFFFFEu ? x : 0xFFFFFFFEu; }
DI float rbf(float x) { return bflo(pk2(x, 0.f)); }
DI f32x2 fma2(f32x2 a, f32x2 b, f32x2 c) { return __builtin_elementwise_fma(a, b, c); }
DI f32x2 lo2(f32x4 v) { return f32x2{v[0], v[1]}; }
DI f32x2 hi2(f32x4 v) { return f32x2{v[2], v[3]}; }
DI f32x2 bc2(float x) { return f32x2{x, x}; }
DI f32x4 mfma(v4u a, v4u b, f32x4 c) {
  return __builtin_amdgcn_mfma_f32_16x16x32_bf16(__builtin_bit_cast(bf16x8, a), __builtin_bit_cast(bf16x8, b), c, 0, 0, 0);
}
DI f32x4 mfma_h(v4u a, v4u b, f32x4 c) {
  return __builtin_amdgcn_mfma_f32_16x16x32_f16(__builtin_bit_cast(f16x8, a), __builtin_bit_cast(f16x8, b), c, 0, 0, 0);
}
DI float dot2(unsigned a, unsigned b, float acc) {
  return __builtin_amdgcn_fdot2_f32_bf16(__builtin_bit_cast(bf16x2_t, a), __builtin_bit_cast(bf16x2_t, b), acc, false);
}
constexpr unsigned ONES = 0x3F803F80u;
DI int perm(int u, int c) { return 32 * (u >> 1) + 8 * (c >> 2) + 4 * (u & 1) + (c & 3); }
template <int CTRL> DI float dpp(float x) {
  return __builtin_bit_cast(float, __builtin_amdgcn_mov_dpp(__builtin_bit_cast(int, x), CTRL, 0xf, 0xf, true));
}
DI float row16_sum(float x) {
  x += dpp<0xB1>(x); x += dpp<0x4E>(x); x += dpp<0x141>(x); x += dpp<0x128>(x);
  return x;
}
DI float row16_max(float x) {
  x = fmaxf(x, dpp<0xB1>(x)); x = fmaxf(x, dpp<0x4E>(x)); x = fmaxf(x, dpp<0x141>(x)); x = fmaxf(x, dpp<0x128>(x));
  return x;
}
DI float xrow_sum(float x) {
  auto s = __builtin_amdgcn_permlane16_swap(__float_as_uint(x), __float_as_uint(x), false, false);
  x = __uint_as_float(s[0]) + __uint_as_float(s[1]);
  auto t = __builtin_amdgcn_permlane32_swap(__float_as_uint(x), __float_as_uint(x), false, false);
  return __uint_as_float(t[0]) + __uint_as_float(t[1]);
}
DI float xrow_sum4(float a, float b, float c, float d) {
  auto s = __builtin_amdgcn_permlane16_swap(__float_as_uint(a), __float_as_uint(b), false, false);
  auto t = __builtin_amdgcn_permlane16_swap(__float_as_uint(c), __float_as_uint(d), false, false);
  const float x = __uint_as_float(s[0]) + __uint_as_float(s[1]), y = __uint_as_float(t[0]) + __uint_as_float(t[1]);
  auto z = __builtin_amdgcn_permlane32_swap(__float_as_uint(x), __float_as_uint(y), false, false);
  return __uint_as_float(z[0]) + __uint_as_float(z[1]);
}
DI float xrow_max(float x) {
  auto s = __builtin_amdgcn_permlane16_swap(__float_as_uint(x), __float_as_uint(x), false, false);
  x = fmaxf(__uint_as_float(s[0]), __uint_as_float(s[1]));
  auto t = __builtin_amdgcn_permlane32_swap(__float_as_uint(x), __float_as_uint(x), false, false);
  return fmaxf(__uint_as_float(t[0]), __uint_as_float(t[1]));
}
DI f32x2 gelu2(f32x2 x) {
  const float k0 = -2.0f * 0.7978845608028654f * 1.4426950408889634f, k1 = k0 * 0.044715f;
  const f32x2 a = x * fma2(x * x, bc2(k1), bc2(k0));
  const f32x2 e = f32x2{__builtin_amdgcn_exp2f(a[0]), __builtin_amdgcn_exp2f(a[1])} + bc2(1.0f);
  return x * f32x2{__builtin_amdgcn_rcpf(e[0]), __builtin_amdgcn_rcpf(e[1])};
}
DI rsrc_t mkrs(const void* ptr, unsigned bytes) { return __builtin_amdgcn_make_buffer_rsrc((void*)ptr, 0, (int)bytes, 0x00020000); }
template <int AUX> DI v4u ld128(rsrc_t rs, unsigned v, unsigned s) { return __builtin_amdgcn_raw_buffer_load_b128(rs, (int)v, (int)s, AUX); }
template <int AUX> DI v2u ld64(rsrc_t rs, unsigned v, unsigned s) { return __builtin_amdgcn_raw_buffer_load_b64(rs, (int)v, (int)s, AUX); }
template <int AUX> DI void st128(v4u x, rsrc_t rs, unsigned v, unsigned s) { __builtin_amdgcn_raw_buffer_store_b128(x, rs, (int)(v + s), 0, AUX); }
template <int AUX> DI void st64(v2u x, rsrc_t rs, unsigned v, unsigned s) { __builtin_amdgcn_raw_buffer_store_b64(x, rs, (int)v, (int)s, AUX); }
DI f32x4 asf(v4u x) { return __builtin_bit_cast(f32x4, x); }
DI float sum4(f32x4 x) { return (x[0] + x[1]) + (x[2] + x[3]); }
DI unsigned umax3(unsigned a, unsigned b, unsigned c) { unsigned m = a > b ? a : b; return m > c ? m : c; }
DI unsigned macc4(unsigned m, v4u x) { return umax3(umax3(m, x[0], x[1]), x[2], x[3]); }
DI unsigned macc(unsigned m, v4u x) { return umax3(m, x[0], x[2]); }
DI bool any_mark(unsigned m) { return __builtin_amdgcn_ballot_w64(m == 0xFFFFFFFFu) != 0ull; }
#define SB() __builtin_amdgcn_sched_barrier(0)
#define PIN(x) asm volatile("" : "+v"(x))
#define PINA(x) asm volatile("" : "+a"(x))
#define USE(x) asm volatile("" ::"v"(x))
#define RETRY_BEGIN(m) if (__builtin_expect(any_mark(m), 0)) { unsigned sp_ = 0; do { MEMFENCE(); m = 0;
#define RETRY_END(m) } while (any_mark(m) & (++sp_ <= SPIN_CAP)); }
#define MEMFENCE() asm volatile("" ::: "memory")
DI void bar() { asm volatile("s_waitcnt lgkmcnt(0)" ::: "memory"); __builtin_amdgcn_s_barrier(); asm volatile("" ::: "memory"); }

DI f32x2 cmul(f32x2 a, f32x2 b) { return fma2(bc2(a[1]), f32x2{-b[1], b[0]}, bc2(a[0]) * b); }
DI f32x2 cexp_p(float are2, float rev_hi, float rev_lo, float p) {
  const float mag = __builtin_amdgcn_exp2f(are2 * p);
  const float hi = rev_hi * p;
  const float fr = (hi - __builtin_rintf(hi)) + rev_lo * p;
  return f32x2{mag * __builtin_amdgcn_cosf(fr), mag * __builtin_amdgcn_sinf(fr)};
}
DI float exp_acc(float x) {
  const float hi = x * 1.4426950408889634f;
  const float lo = __builtin_fmaf(x, 1.4426950408889634f, -hi) + x * 1.925963033500011e-8f;
  const float e = __builtin_amdgcn_exp2f(hi);
  return __builtin_fmaf(e, lo * 0.6931471805599453f, e);
}
DI unsigned pkh(float a, float b) {
  f16x2 v = {(_Float16)a, (_Float16)b};
  return __builtin_bit_cast(unsigned, v);
}

constexpr int CV_RK = 4160;
constexpr int CV_H = 2 * CV_RK;
constexpr int CV_CH = CV_H + 4608;
constexpr int LDS_A = 0;
constexpr int LDS_U = 2 * CV_CH;
constexpr int LDS_GS = LDS_U + 8192;
constexpr int LDS_TAB = 36864;
constexpr int KP_CH = 13840;
constexpr int LDS_KP = LDS_TAB + 2 * 64 * 528 + 16;
constexpr int LDS_LN = 0, LDS_MR = 8448;
constexpr int LDS_WI = 145472, LDS_WO = LDS_WI + 10240;
constexpr int LDS_X = 65536;
constexpr int LDS_S = LDS_X + 24576;
constexpr int LDS_W = LDS_S + 6144;
constexpr int MEGA_LDS = LDS_W + 65536;
static_assert(LDS_WO + 4608 <= MEGA_LDS && LDS_KP + 2 * KP_CH <= LDS_WI && LDS_S >= 73728, "lds");
static_assert(LDS_GS + 1280 <= LDS_TAB && LDS_KP + 2 * KP_CH <= MEGA_LDS && MEGA_LDS <= 160 * 1024, "lds");
#define FLAG_AT 110
static_assert(FLAG_AT > 57 && FLAG_AT < 144 && FLAG_AT % 6 != 3, "flag position");
#define WF_EARLY 3
#define WO0 12
#define WSP_P0 4
#define HINT4_KS 15
#define ZDEPTH 3
#define GELU_IN 0
#define EPI_W 0
#define MOD_POS 0
#define POLL3 0
#define HINT4_G 50
#define IN_AUX 0
#define WSP_AUX 0

DI v2u trread(const unsigned char* ptr) {
  typedef short v4i16_t __attribute__((ext_vector_type(4)));
  return __builtin_bit_cast(v2u, __builtin_amdgcn_ds_read_tr16_b64_v4i16((LAS v4i16_t*)ptr));
}
struct CvOrder { int qq[36], pp[36]; };
constexpr CvOrder cv_order() {
  CvOrder o{};
  int n = 0;
  for (int m = 0; m < 8; ++m) {
    for (int j = 0; j < m; ++j) if (m + j < 8) { o.qq[n] = m; o.pp[n] = j; ++n; }
    for (int j = 0; j <= m; ++j) if (m + j < 8) { o.qq[n] = j; o.pp[n] = m; ++n; }
  }
  return o;
}
constexpr CvOrder CVO = cv_order();

typedef __attribute__((address_space(1))) unsigned gu32;
DI unsigned tacc(unsigned m, v4u x, unsigned tag) { return m | (x[1] ^ tag) | (x[3] ^ tag); }
DI bool any_nz(unsigned m) { return __builtin_amdgcn_ballot_w64(m != 0u) != 0ull; }
DI void hint_add(unsigned char* ws, int line) { (void)__hip_atomic_fetch_add((gu32*)(ws + WS_CNT + line * 128), 1u, __ATOMIC_RELAXED, __HIP_MEMORY_SCOPE_AGENT); }
DI void hint_wait(rsrc_t rws, unsigned voff, unsigned soff, unsigned want) {
  unsigned sp = 0;
  for (;;) {
    MEMFENCE();
    const unsigned v = __builtin_amdgcn_raw_buffer_load_b32(rws, (int)voff, (int)soff, SC1);
    if (!((__builtin_amdgcn_ballot_w64(v < want) != 0ull) & (++sp <= SPIN_CAP))) break;
    __builtin_amdgcn_s_sleep(OPT_E);
  }
  MEMFENCE();
}

extern "C" __global__ void __launch_bounds__(256, 1) adaln_mega(Params p) {
  extern __shared__ __attribute__((aligned(16))) unsigned char smem[];
  const int tid = threadIdx.x, lane = tid & 63, w = __builtin_amdgcn_readfirstlane(tid >> 6);
  const int b = blockIdx.x, xcd = b & 7, idx = b >> 3;
  const int pb = xcd * 32 + idx;
  const unsigned l16 = lane * 16;
  STAMP_DECL;
  STAMP(1, 0);
  v2u codepf;
  {
    unsigned long long pc;
    int rel_m, rel_e;
    asm volatile("s_getpc_b64 %0\n\ts_mov_b32 %1, adaln_mega@rel32@lo+4\n\ts_mov_b32 %2, adaln_code_end@rel32@lo+12" : "=s"(pc), "=s"(rel_m), "=s"(rel_e));
    const rsrc_t rcode = mkrs((const void*)(pc + (long long)rel_m), rel_e > rel_m ? (unsigned)(rel_e - rel_m) & ~15u : 0u);
    codepf = ld64<0>(rcode, tid * 8, idx * 2048);
  }
  SB();
  const rsrc_t ru = mkrs(p.u, L * D * 2);
  v4u xr[2];
  xr[0] = ld128<IN_AUX>(ru, l16, (pb * 8 + w * 2) * 1024);
  xr[1] = ld128<IN_AUX>(ru, l16 + 1024, (pb * 8 + w * 2) * 1024);
  const int mkg = b & 3, mcg = b >> 2;
  const unsigned cq = p.c[mkg * 128 + (tid & 3) * 32 + (tid >> 3)];
  const unsigned epoch = *(const unsigned*)(p.ws + WS_EPOCH);
  const rsrc_t rws = mkrs(p.ws, WS_END);
  __builtin_amdgcn_raw_buffer_store_b32(0u, rws, tid < 2 ? tid * 32768u : OOB, WS_CNT + b * 128, SC1);
  SB();
  const int cch = w >> 1, eh = w & 1, ns = tid & 127;
  const unsigned q_cre = p.cre[pb * 256 + tid], q_cim = p.cim[pb * 256 + tid], q_lar = p.lar[pb * 256 + tid], q_aim = p.aim[pb * 256 + tid];
  const unsigned q_ldt = p.ldt[pb * 2 + cch], q_dsk = p.dsk[pb * 2 + cch];
  v4u xm[4];
#define LOAD_XM() { \
  const rsrc_t rmod = mkrs(p.wmod, D * 3072 * 2); \
_Pragma("unroll") \
  for (int i = 0; i < 4; ++i) xm[i] = ld128<IN_AUX>(rmod, (tid >> 3) * 6144 + (tid & 7) * 16, (mkg * 128 + i * 32) * 6144 + mcg * 128 + (b < 192 ? 0u : OOB)); }
  SB();
  LOAD_XM();
  SB();
  STAMP(1, 24);
  const int c = lane & 15, q = lane >> 4;
  int pos4[9];
  {
    const float f4 = (float)(8188 - tid * 32);
#pragma unroll
    for (int j = 0; j < 10; j += 2) {
      const unsigned pp = pk2(bc2(f4) - f32x2{4.f * j, 4.f * j + 4.f});
      pos4[j] = (int)bflo(pp);
      if (OPT_A) pos4[j] += (pos4[j] >> 5) << 2;
      PIN(pos4[j]);
      if (j + 1 < 9) { pos4[j + 1] = (int)bfhi(pp); if (OPT_A) pos4[j + 1] += (pos4[j + 1] >> 5) << 2; PIN(pos4[j + 1]); }
    }
  }
  int cob[2], cos_[2];
  cob[0] = (eh ? 1024 + 512 * q : 128 * (q >> 1) + 4 * (q & 1)) * 4 + 32 * c + LDS_KP + cch * KP_CH;
  cob[1] = (q < 2 ? 256 + 128 * q : 504 + 4 * q) * 4 + 32 * c + LDS_KP + cch * KP_CH;
  cos_[0] = eh ? 512 : 4;
  cos_[1] = q < 2 ? 8 : 512;
  if (OPT_A) {
    cob[0] = (eh ? 1152 + 576 * q : 144 * (q >> 1) + 4 * (q & 1)) * 4 + 36 * c + LDS_KP + cch * KP_CH;
    cob[1] = (q < 2 ? 288 + 144 * q : 568 + 4 * q) * 4 + 36 * c + LDS_KP + cch * KP_CH;
    cos_[0] = eh ? 576 : 4;
    cos_[1] = q < 2 ? 8 : 576;
  }
  PIN(cob[0]); PIN(cob[1]); PIN(cos_[1]);
  SB();
  STAMP(1, 26);
  {
    *(v4u*)(smem + LDS_LN + (w * 2) * 1040 + l16) = xr[0];
    *(v4u*)(smem + LDS_LN + (w * 2 + 1) * 1040 + l16) = xr[1];
    float sm[2], sq[2];
#pragma unroll
    for (int i = 0; i < 2; ++i) {
      sm[i] = dot2(xr[i][0], ONES, dot2(xr[i][1], ONES, dot2(xr[i][2], ONES, dot2(xr[i][3], ONES, 0.f))));
      sq[i] = dot2(xr[i][0], xr[i][0], dot2(xr[i][1], xr[i][1], dot2(xr[i][2], xr[i][2], dot2(xr[i][3], xr[i][3], 0.f))));
    }
    const float tv = row16_sum(xrow_sum4(sm[0], sm[1], sq[0], sq[1]));
    auto sw = __builtin_amdgcn_permlane32_swap(__float_as_uint(tv), __float_as_uint(tv), false, false);
    const float m = __uint_as_float(sw[0]) * (1.0f / D);
    const float r = __builtin_amdgcn_rsqf(fmaxf(__uint_as_float(sw[1]) * (1.0f / D) - m * m, 0.f) + EPS);
    ((float*)(smem + LDS_GS + 1088))[(q >> 1) * 8 + w * 2 + (q & 1)] = (q >> 1) ? -m * r : r;
  }
  const unsigned tag = epoch * 2654435761u + 0x9E3779B9u;
  STAMP(1, 28);
  asm volatile("s_waitcnt vmcnt(%0)" ::"i"(MOD_POS == 0 ? 8 : 4) : "memory");
  bar();
  STAMP(1, 16);
  {
    unsigned ua[4][2];
#pragma unroll
    for (int i = 0; i < 4; ++i) {
      const unsigned char* src = smem + LDS_LN + (tid & 3) * 2080 + (i * 64 + (tid >> 2)) * 4;
      ua[i][0] = *(const unsigned*)src; ua[i][1] = *(const unsigned*)(src + 1040);
    }
    const unsigned lnv = ((const unsigned*)(smem + LDS_GS + 1088))[c];
    SB();
#pragma unroll
    for (int i = 0; i < 4; ++i) st128<SC1>(v4u{ua[i][0], tag, ua[i][1], tag}, rws, (tid >> 2) * 16384 + (tid & 3) * 16, WS_UG + i * 64 * 16384 + pb * 64);
    st64<SC1>(v2u{lnv, tag}, rws, tid < 16 ? (c >> 3) * 16384 + (c & 7) * 8 : OOB, WS_SR + pb * 64);
  }
  STAMP(1, 17);
  SB();
  const unsigned wp_oob = b < 176 ? 0u : OOB;
  const int wpr = b >> 2, wkg = b & 3;
  v4u xi[2], xo;
  {
    const rsrc_t rwin = mkrs(p.win, D * FF * 2), rwout = mkrs(p.wout, FH * D * 2);
    const int hf = wpr >= 22, pp = wpr - 22 * hf;
    const unsigned v = ((tid >> 2) * (FF * 2) + (tid & 3) * 16) | ((pp == 21 ? tid & 2 : 0) << 29);
#pragma unroll
    for (int i = 0; i < 2; ++i) xi[i] = ld128<IN_AUX>(rwin, v, (wkg * 128 + i * 64) * (FF * 2) + (hf * FH + pp * 32) * 2 + wp_oob);
    xo = ld128<IN_AUX>(rwout, (tid >> 3) * 1024 + (tid & 7) * 16, idx * 32 * 1024 + xcd * 128);
  }
  SB();
  const int d0 = pb * 2, dk = d0 + cch;
  const int mt = (xcd >> 1) * 8 + (idx & 7), nt = (xcd & 1) * 4 + (idx >> 3);
  const int big = (nt + 2) >> 3;
  const int np = 3 - big, pr0 = 3 * nt - big * (nt - 6);
  const int kh = w & 1, uh = w >> 1;
  const int ub_own = pr0 * 2 + uh * np, ub_oth = pr0 * 2 + (uh ^ 1) * np;
  const unsigned oob3 = (unsigned)big << 30;
  const int kr = 2 * (idx >> 3) + (xcd & 1) + (OPT_B == 0 ? (idx & 7) : OPT_B == 1 ? 4 * (idx & 1) : OPT_B == 2 ? 2 * (idx & 3) : 0);
  const rsrc_t rwsp = mkrs(p.wsp, L * L * 2);
  const unsigned wp_fr = (wpr * 2 * 16 + wkg * 4 + w) * 1024 + wp_oob;
  v4u wr[1][8];
  const unsigned wsp_v = (lane >> 3) * 4096 + (lane & 7) * 16, wsp_s = mt * 64 * 4096 + w * 1024;
#define LOAD_WSP1(ck, i) wr[ck][i] = ld128<WSP_AUX>(rwsp, wsp_v + (((ck) + kr) & 7) * 128, wsp_s + (i) * 8 * 4096)
#define LOAD_WSPN(n) LOAD_WSP1((n) >> 3, (n) & 7)
  const v4u mk = {~0u, ~0u, ~0u, ~0u};
  {
    const float dt = rbf(exp_acc(bf2f(q_ldt)));
    const float cre = bf2f(q_cre) * dt, cim = bf2f(q_cim) * dt;
    const float are2 = -rbf(exp_acc(bf2f(q_lar))) * dt * 1.4426950408889634f;
    const float aim = bf2f(q_aim) * dt;
    const float rev_hi = aim * 0.15915494309189535f;
    const float rev_lo = __builtin_fmaf(aim, 0.15915494309189535f, -rev_hi) + aim * 6.4206383e-9f;
    int ex = __builtin_amdgcn_frexp_expf(xrow_max(row16_max(fmaxf(fabsf(cre), fabsf(cim))))) + 1;
    ex = ex < -100 ? -100 : ex;
    const float scale = __builtin_amdgcn_ldexpf(1.f, -ex);
    ((float*)(smem + LDS_GS + 1024))[w] = __builtin_amdgcn_ldexpf(2.f, ex);
    const f32x2 c0 = {cre * scale, cim * scale};
    const f32x2 e1 = cexp_p(are2, rev_hi, rev_lo, 1.f), e8 = cexp_p(are2, rev_hi, rev_lo, 8.f), e64 = cexp_p(are2, rev_hi, rev_lo, 64.f);
    unsigned char* tn = smem + LDS_TAB + cch * (64 * 528) + ns * 4;
#define TROW(r, z) *(unsigned*)(tn + (r) * 528) = pkh((z)[0], -(z)[1])
    SB();
    const f32x2 e128 = cmul(e64, e64), e2 = cmul(e1, e1), e256 = cmul(e128, e128), e4 = cmul(e2, e2), e512 = cmul(e256, e256);
    f32x2 f0 = {1.f, 0.f}, f1 = e64;
    f32x2 ca = cmul(c0, e512), cb = cmul(ca, e512);
#pragma unroll
    for (int j = 0; j < 8; ++j) {
      *(unsigned*)(tn + (48 + j) * 528) = pkh(f0[0], f0[1]);
      *(unsigned*)(tn + (56 + j) * 528) = pkh(f1[0], f1[1]);
      TROW(32 + j, cb);
      f0 = j == 0 ? e8 : cmul(f0, e8); f1 = cmul(f1, e8); cb = cmul(cb, e128);
      if (j < 4) {
        const f32x2 z = cmul(ca, e4);
        TROW(24 + j, ca); TROW(28 + j, z);
        ca = cmul(ca, e128);
      }
      if (j == 3 || j == 7) {
        PIN(f0); PIN(f1); PIN(cb);
        if (WSP_P0 >= 4) { LOAD_WSPN(j >> 2 << 1); LOAD_WSPN((j >> 2 << 1) + 1); }
        SB();
      }
    }
    TROW(40, cb);
    f32x2 t1[8];
    t1[1] = e1; t1[2] = e2; t1[4] = e4;
    t1[3] = cmul(e2, e1); t1[5] = cmul(e4, e1); t1[6] = cmul(e2, e4); t1[7] = cmul(t1[6], e1);
    const f32x2 c1 = cmul(c0, e128), c2 = cmul(c0, e256), c3 = cmul(c2, e128);
    TROW(0, c0); TROW(8, c1); TROW(16, c2); TROW(20, c3);
#pragma unroll
    for (int v = 1; v < 8; ++v) {
      const f32x2 z0 = cmul(t1[v], c0), z1 = cmul(t1[v], c1);
      TROW(v, z0); TROW(8 + v, z1);
      if ((v & 1) == 0) {
        const f32x2 z2 = cmul(t1[v], c2), z3 = cmul(t1[v], c3);
        TROW(16 + v / 2, z2); TROW(20 + v / 2, z3);
        if (v < 6 && WSP_P0 >= 8) { LOAD_WSPN(v + 2); LOAD_WSPN(v + 3); }
        SB();
      }
    }
  }
  STAMP(1, 27);
  {
    const float cv = bf2f(cq);
    const float sl = rbf(cv * __builtin_amdgcn_rcpf(1.0f + __builtin_amdgcn_exp2f(cv * -1.4426950408889634f)));
    const float sc[4] = {dpp<0x00>(sl), dpp<0x55>(sl), dpp<0xAA>(sl), dpp<0xFF>(sl)};
    f32x2 a[4];
#pragma unroll
    for (int i = 0; i < 4; ++i)
#pragma unroll
      for (int j = 0; j < 4; ++j) a[j] = i == 0 ? unpk(xm[i][j]) * bc2(sc[i]) : fma2(unpk(xm[i][j]), bc2(sc[i]), a[j]);
    float* red = (float*)(smem + LDS_MR) + (tid >> 3) * 68 + (tid & 7) * 8;
    *(f32x4*)red = f32x4{a[0][0], a[0][1], a[1][0], a[1][1]};
    *(f32x4*)(red + 4) = f32x4{a[2][0], a[2][1], a[3][0], a[3][1]};
  }
  STAMP(1, 29);
  bar();
  STAMP(1, 18);
  {
    float ms8[8];
#pragma unroll
    for (int i = 0; i < 8; ++i) ms8[i] = ((const float*)(smem + LDS_MR))[(q * 8 + i) * 68 + w * 16 + c];
    const float s = xrow_sum(((ms8[0] + ms8[1]) + (ms8[2] + ms8[3])) + ((ms8[4] + ms8[5]) + (ms8[6] + ms8[7])));
    st64<SC1>(v2u{__float_as_uint(s), tag}, rws, q == 0 ? c * 8 : OOB, WS_MODG + (mkg * 3072 + mcg * 64 + w * 16) * 8 + (b < 192 ? 0u : OOB));
  }
  SB();
  v4u gr[4], gb_[4];
  v2u gm[4], gc[4];
  const unsigned gm_off = ((cch ? 3 * 512 : 4 * 512) + wkg * 128 + ns) * 8;
  const unsigned gc_off = (((lane & 7) > 5 ? 0 : ((lane & 7) >> 1)) * 512 + d0 + (lane & 1)) * 8;
#define LOAD_GS() _Pragma("unroll") for (int i_ = 0; i_ < 4; ++i_) { gr[i_] = ld128<SC1>(rws, tid * 16, WS_SR + i_ * 4096); gb_[i_] = ld128<SC1>(rws, tid * 16, WS_SB + i_ * 4096); }
#define LOAD_GM() _Pragma("unroll") for (int i_ = 0; i_ < 4; ++i_) { gm[i_] = ld64<SC1>(rws, gm_off, WS_MODG + i_ * 24576); gc[i_] = ld64<SC1>(rws, gc_off, WS_MODG + i_ * 24576); }
  {
    const unsigned char* tab = smem + LDS_TAB + cch * (64 * 528) + c * 528 + q * 16;
    v4u fa[2][8], fb[8];
#pragma unroll
    for (int ks = 0; ks < 4; ++ks) {
      fb[ks] = *(const v4u*)(tab + 48 * 528 + ks * 64);
      fa[0][ks] = *(const v4u*)(tab + eh * (32 * 528) + ks * 64);
      fa[1][ks] = *(const v4u*)(tab + 16 * 528 + ks * 64);
    }
    const f32x2 us = *(const f32x2*)(smem + LDS_GS + 1024 + cch * 8);
    SB();
    f32x4 kk[2][2];
#pragma unroll
    for (int i = 0; i < 4; ++i) kk[i >> 1][i & 1] = f32x4{0.f, 0.f, 0.f, 0.f};
#pragma unroll
    for (int ks = 0; ks < 8; ++ks) {
      if (ks < 4) {
        fb[4 + ks] = *(const v4u*)(tab + 48 * 528 + (4 + ks) * 64);
        fa[0][4 + ks] = *(const v4u*)(tab + eh * (32 * 528) + (4 + ks) * 64);
        fa[1][4 + ks] = *(const v4u*)(tab + 16 * 528 + (4 + ks) * 64);
      }
      kk[0][ks >> 2] = mfma_h(fa[0][ks], fb[ks], kk[0][ks >> 2]); kk[1][ks >> 2] = mfma_h(fa[1][ks], fb[ks], kk[1][ks >> 2]);
      SB();
    }
    STAMP(1, 37);
    *(unsigned*)(smem + LDS_KP - 16 + cch * KP_CH + (lane & 3) * 4) = 0u;
#pragma unroll
    for (int i = 0; i < 2; ++i) *(v4u*)(smem + LDS_WI + (i * 64 + (tid >> 2)) * 80 + (tid & 3) * 16) = xi[i];
    *(v4u*)(smem + LDS_WO + (tid >> 3) * 144 + (tid & 7) * 16) = xo;
    f32x4 k0 = kk[0][0] * us[0] + kk[0][1] * us[1];
    const f32x4 k1 = kk[1][0] * us[0] + kk[1][1] * us[1];
    k0[0] += ns == 0 ? bf2f(q_dsk) : 0.f;
#pragma unroll
    for (int r = 0; r < 4; ++r) { *(float*)(smem + cob[0] + r * cos_[0]) = k0[r]; *(float*)(smem + cob[1] + r * cos_[1]) = k1[r]; }
  }
  v4u ug[4];
#define LOAD_UG() _Pragma("unroll") for (int i_ = 0; i_ < 4; ++i_) ug[i_] = ld128<SC1>(rws, tid * 16, WS_UG + pb * 16384 + i_ * 4096)
  STAMP(1, 31);
  LOAD_UG();
  bar();
  STAMP(1, 19);
  v4u wt[2];
  {
    float v[2][9];
#pragma unroll
    for (int j = 0; j < 9; ++j) v[0][j] = *(const float*)(smem + LDS_KP + pos4[j]);
    LOAD_GS();
#pragma unroll
    for (int j = 0; j < 9; ++j) v[1][j] = *(const float*)(smem + LDS_KP + KP_CH + pos4[j]);
    SB();
    STAMP(1, 36);
    *(v4u*)(smem + cch * CV_CH + (lane < 32 ? CV_H + l16 : ((lane >> 1) & 1) * CV_RK + 4096 + (lane & 1) * 16)) = v4u{0u, 0u, 0u, 0u};
#pragma unroll
    for (int ch = 0; ch < 2; ++ch) {
      unsigned char* rk = smem + ch * CV_CH + tid * 16;
#pragma unroll
      for (int sft = 0; sft < 2; ++sft)
        *(v4u*)(rk + sft * CV_RK) = v4u{pk2(v[ch][sft], v[ch][sft + 1]), pk2(v[ch][sft + 2], v[ch][sft + 3]), pk2(v[ch][sft + 4], v[ch][sft + 5]), pk2(v[ch][sft + 6], v[ch][sft + 7])};
    }
    const unsigned char* a0 = smem + LDS_WI + (w * 32 + 8 * q + ((lane >> 2) & 3)) * 80 + (lane & 3) * 16;
#pragma unroll
    for (int e = 0; e < 2; ++e) {
      const v2u lo = trread(a0 + e * 8), hi = trread(a0 + e * 8 + 320);
      wt[e] = v4u{lo[0], lo[1], hi[0], hi[1]};
    }
    const unsigned char* o0 = smem + LDS_WO + (8 * q + ((lane >> 2) & 3)) * 144 + (w >> 1) * 64 + (lane & 3) * 16 + (w & 1) * 8;
    const v2u olo = trread(o0), ohi = trread(o0 + 576);
    st128<SC1>(v4u{olo[0], olo[1], ohi[0], ohi[1]}, rws, l16, WS_WOT + ((xcd * 4 + w) * 22 + idx) * 1024 + wp_oob);
    SB();
    LOAD_GM();
    SB();
    STAMP(1, 32);
    unsigned m = 0;
#pragma unroll
    for (int i = 0; i < 4; ++i) { m = tacc(m, ug[i], tag); m = tacc(m, gr[i], tag); m = tacc(m, gb_[i], tag); }
    if (__builtin_expect(any_nz(m), 0)) {
      unsigned sp = 0;
      do {
        MEMFENCE(); LOAD_UG(); LOAD_GS(); m = 0;
#pragma unroll
        for (int i = 0; i < 4; ++i) { m = tacc(m, ug[i], tag); m = tacc(m, gr[i], tag); m = tacc(m, gb_[i], tag); }
      } while (any_nz(m) & (++sp <= SPIN_CAP));
    }
    f32x2 nn[4][2];
#pragma unroll
    for (int i = 0; i < 4; ++i) {
      nn[i][0] = fma2(unpk(ug[i][0]), bc2(__uint_as_float(gr[i][0])), bc2(__uint_as_float(gb_[i][0])));
      nn[i][1] = fma2(unpk(ug[i][2]), bc2(__uint_as_float(gr[i][2])), bc2(__uint_as_float(gb_[i][2])));
      PIN(nn[i][0]); PIN(nn[i][1]);
      *(unsigned*)(smem + LDS_U + i * 1024 + tid * 4) = __builtin_amdgcn_perm(ug[i][2], ug[i][0], 0x05040100u);
      *(unsigned*)(smem + LDS_U + 4096 + i * 1024 + tid * 4) = __builtin_amdgcn_perm(ug[i][2], ug[i][0], 0x07060302u);
    }
    SB();
    STAMP(1, 33);
    m = 0;
#pragma unroll
    for (int i = 0; i < 4; ++i) m = m | (gm[i][1] ^ tag) | (gc[i][1] ^ tag);
    if (__builtin_expect(any_nz(m), 0)) {
      unsigned sp = 0;
      do {
        __builtin_amdgcn_s_sleep(3);
        MEMFENCE(); LOAD_GM(); m = 0;
#pragma unroll
        for (int i = 0; i < 4; ++i) m = m | (gm[i][1] ^ tag) | (gc[i][1] ^ tag);
      } while (any_nz(m) & (++sp <= SPIN_CAP));
    }
    const float mv = rbf((__uint_as_float(gm[0][0]) + __uint_as_float(gm[1][0])) + (__uint_as_float(gm[2][0]) + __uint_as_float(gm[3][0])));
    if (cch) *(u16*)(smem + LDS_GS + 512 + ns * 2) = (u16)(__float_as_uint(mv) >> 16);
    else *(float*)(smem + LDS_GS + ns * 4) = rbf(1.0f + mv);
    const float cv = rbf((__uint_as_float(gc[0][0]) + __uint_as_float(gc[1][0])) + (__uint_as_float(gc[2][0]) + __uint_as_float(gc[3][0])));
    const float gav = dpp<0x104>(cv), gav2 = dpp<0x102>(cv);
    const float hbv = cv * gav, hsv = rbf(1.0f + cv) * gav2;
    const f32x2 hb = {__builtin_bit_cast(float, __builtin_amdgcn_readlane(__builtin_bit_cast(int, hbv), 0)), __builtin_bit_cast(float, __builtin_amdgcn_readlane(__builtin_bit_cast(int, hbv), 1))};
    const f32x2 hs = {__builtin_bit_cast(float, __builtin_amdgcn_readlane(__builtin_bit_cast(int, hsv), 2)), __builtin_bit_cast(float, __builtin_amdgcn_readlane(__builtin_bit_cast(int, hsv), 3))};
    SB();
#pragma unroll
    for (int i = WSP_P0; i < WSP_P0 + 4; ++i) LOAD_WSPN(i);
    STAMP(1, 38);
#pragma unroll
    for (int i = 0; i < 4; ++i) {
      const f32x2 h0 = fma2(nn[i][0], hs, hb), h1 = fma2(nn[i][1], hs, hb);
      *(unsigned*)(smem + CV_H + 512 + i * 1024 + tid * 4) = pk2(h0[0], h1[0]);
      *(unsigned*)(smem + CV_CH + CV_H + 512 + i * 1024 + tid * 4) = pk2(h0[1], h1[1]);
    }
  }
  asm volatile("" ::"v"(codepf));
  const unsigned char* abase = smem + cch * CV_CH + (1 - (c & 1)) * CV_RK + 2 * (2030 - 2 * (c >> 1) + 8 * q) - eh * 256 - 192 - 3584;
  const unsigned char* bbase = smem + cch * CV_CH + CV_H + 2 * (256 - 16 + 16 * c + 8 * q) - eh * 256 - 192;
  STAMP(1, 39);
  bar();
  STAMP(1, 1);
#define CV_A(ee, k) ({ const unsigned* p_ = (const unsigned*)(abase + 192 - 64 * (ee) + 3584 - 512 * (k)); v4u{p_[0], p_[1], p_[2], p_[3]}; })
#define CV_B(ee, k) (*(const v4u*)(bbase + 192 - 64 * (ee) + 512 * (k)))
  unsigned dv[4];
#pragma unroll
  for (int i = 0; i < 4; ++i) dv[i] = (lane >> 3) * 4096 + (((lane & 7) ^ ((lane >> 4) & 1) ^ (i << 1)) * 16) + i * (32768 - 1024);
  const unsigned lds0 = (unsigned)(size_t)(LAS unsigned char*)smem;
  const unsigned slotA = LDS_W + w * 16384, slotB = slotA + 8192, slotT = w < 3 ? 36864 + w * 8192 : 81920;
  unsigned dso;
#define DMA_W(ck, slot, i) do { \
    if (((i) & 3) == 0) asm volatile("s_add_u32 m0, %1, %2\n\ts_add_u32 %0, %3, %4\n\tbuffer_load_dwordx4 %5, %6, %0 offen lds" \
                 : "=&s"(dso) : "s"(lds0 + (slot)), "i"((i) * 1024), "s"(wsp_s + (((ck) + kr) & 7) * 128), "i"((i) * 32768), "v"(dv[0]), "s"(rwsp) : "scc", "memory"); \
    else asm volatile("buffer_load_dwordx4 %0, %1, %2 offen offset:%3 lds" :: "v"(dv[(i) & 3]), "s"(rwsp), "s"(dso), "i"(((i) & 3) * 1024) : "memory"); } while (0)
  auto dma_sel = [&](auto CK, unsigned slot, int i) __attribute__((always_inline)) {
    constexpr int ck = decltype(CK)::value;
    switch (i) {
      case 0: DMA_W(ck, slot, 0); break; case 1: DMA_W(ck, slot, 1); break; case 2: DMA_W(ck, slot, 2); break; case 3: DMA_W(ck, slot, 3); break;
      case 4: DMA_W(ck, slot, 4); break; case 5: DMA_W(ck, slot, 5); break; case 6: DMA_W(ck, slot, 6); break; default: DMA_W(ck, slot, 7); break;
    }
  };
  v4u cA[2][8], cB[2][8];
  f32x4 gs[2];
  v4u sbf;
  const v4u ones4 = {ONES, ONES, ONES, ONES};
#pragma unroll
  for (int k = 0; k < 2; ++k) { cA[0][k] = CV_A(0, k); cB[0][k] = CV_B(0, k); }
  {
    const float* gp = (const float*)(smem + LDS_GS) + w * 32 + q * 8;
    gs[0] = *(const f32x4*)gp; gs[1] = *(const f32x4*)(gp + 4);
    sbf = *(const v4u*)(smem + LDS_GS + 512 + w * 64 + q * 16);
  }
  SB();
  f32x4 cacc[8];
  v2u ue[4];
  {
    f32x4 cb4[4];
    v4u wo_[2];
    float wl, wh;
    f32x2 cp[4];
#pragma unroll
    for (int t = 0; t < 8; ++t) cacc[t] = f32x4{0.f, 0.f, 0.f, 0.f};
    sfor<144>([&](auto G) __attribute__((always_inline)) {
      constexpr int g = decltype(G)::value, ee = g / 36, j = g % 36;
      if constexpr (ee == 0 && j < 6) { cA[0][2 + j] = CV_A(0, 2 + j); cB[0][2 + j] = CV_B(0, 2 + j); }
      cacc[CVO.pp[j] + CVO.qq[j]] = mfma(cA[ee & 1][CVO.qq[j]], cB[ee & 1][CVO.pp[j]], cacc[CVO.pp[j] + CVO.qq[j]]);
      constexpr int jn = ee == 0 ? j - 20 : (j & 1) ? j >> 1 : -1;
      if constexpr (ee < 3 && jn >= 0 && jn < 16) {
        if constexpr (jn & 1) cB[(ee + 1) & 1][jn >> 1] = CV_B(ee + 1, jn >> 1);
        else cA[(ee + 1) & 1][jn >> 1] = CV_A(ee + 1, jn >> 1);
      }
      if constexpr (ee == 3 && j < 4) ue[j] = *(const v2u*)(smem + LDS_U + cch * 4096 + (eh * 4 + j) * 512 + (c * 16 + q * 4) * 2);
      if constexpr (g >= 6 && g < 30) {
        constexpr int i = (g - 6) / 12, jj = ((g - 6) / 3) & 3, ph = (g - 6) % 3;
        if constexpr (ph == 0) { wl = bflo(wt[i][jj]) * gs[jj >> 1][(jj & 1) * 2]; PIN(wl); }
        if constexpr (ph == 1) { wh = bfhi(wt[i][jj]) * gs[jj >> 1][(jj & 1) * 2 + 1]; PIN(wh); }
        if constexpr (ph == 2) {
          wo_[i][jj] = pk2(wl, wh);
          if constexpr (jj == 3) st128<SC1>(wo_[i], rws, l16 + i * 16384, WS_WP + wp_fr);
          else PIN(wo_[i][jj]);
        }
      }
      if constexpr (g == 4 || g == 5) cb4[2 * (g - 4) + 1] = mfma(sbf, wt[g - 4], f32x4{0.f, 0.f, 0.f, 0.f});
      if constexpr (g == 19 || g == 31) cb4[g == 19 ? 0 : 2] = mfma(ones4, wo_[g == 31], f32x4{0.f, 0.f, 0.f, 0.f});
      if constexpr (g == 38) *(f32x4*)(smem + LDS_S + 4096 + (w * 16 + c) * 16) = f32x4{cb4[0][0], cb4[1][0], cb4[2][0], cb4[3][0]};
      if constexpr (g == 46) bar();
      if constexpr (g == 47) {
#pragma unroll
        for (int k = 0; k < 4; ++k) cp[k] = *(const f32x2*)(smem + LDS_S + 4096 + (k * 16 + c) * 16 + (q & 1) * 8);
      }
      if constexpr (g == 54) {
        float s0 = cp[0][0] + cp[1][0], s1 = cp[2][0] + cp[3][0];
        PIN(s0); PIN(s1);
        s0 += s1;
        PIN(s0);
        float s2 = cp[0][1] + cp[1][1], s3 = cp[2][1] + cp[3][1];
        PIN(s2); PIN(s3);
        st64<SC1>(v2u{__float_as_uint(s0), __float_as_uint(s2 + s3)}, rws, tid < 32 ? q * 512 + c * 8 : OOB, WS_CSB + wpr * 1024 + wkg * 128 + wp_oob);
      }
      if constexpr (g == 1 || g == 5) st128<SC1>(mk, rws, l16 + (g == 5) * 1024, WS_X1T + dk * 4096 + eh * 2048);
      if constexpr (g == 9) __builtin_amdgcn_raw_buffer_store_b32(~0u, rws, lane * 4, WS_ST2 + ((mt * 2 + kh) * 16 + nt * 2 + uh) * 256, SC1);
      if constexpr (g == 42) { if (b == 0 && tid == 0) *(unsigned*)(p.ws + WS_EPOCH) = tag; }
      if constexpr (g == 36) STAMP(1, 40);
      if constexpr (g == 72) STAMP(1, 41);
      if constexpr (g == FLAG_AT) {
        STAMP(1, 42);
        asm volatile("s_waitcnt vmcnt(%0)" ::"i"((FLAG_AT - 57 + 5) / 6) : "memory");
        __builtin_amdgcn_s_barrier();
        if (tid < 8) hint_add(p.ws, 256 + tid * 16 + (idx & 15));
        STAMP(1, 43);
      }
      if constexpr (g % 6 == 3) DMA_W(1 + g / 48, g < 48 ? slotT : g < 96 ? slotA : slotB, (g / 6) & 7);
      SB();
    });
  }
  STAMP(1, 2);
  {
    f32x4* red = (f32x4*)(smem + LDS_X) + w * 256 + lane;
    f32x4 keep[4];
    if (eh) {
#pragma unroll
      for (int t = 0; t < 4; ++t) { red[t * 64] = cacc[t]; keep[t] = cacc[4 + t]; PIN(keep[t]); }
    } else {
#pragma unroll
      for (int t = 0; t < 4; ++t) { red[t * 64] = cacc[4 + t]; keep[t] = cacc[t]; PIN(keep[t]); }
    }
    f32x2 pre[4][2];
#pragma unroll
    for (int t = 0; t < 4; ++t) {
      pre[t][0] = unpk(ue[t][0]) + lo2(keep[t]);
      pre[t][1] = unpk(ue[t][1]) + hi2(keep[t]);
      PIN(pre[t][0]); PIN(pre[t][1]);
    }
    const unsigned xoff = ((c >> 1) * 32 + ((q & 1) * 4 + 2 * (c & 1) + (q >> 1)) * 4) * 2;
    bar();
    const f32x4* rd = (const f32x4*)(smem + LDS_X) + (w ^ 1) * 256 + lane;
    f32x4 oth[4];
#pragma unroll
    for (int t = 0; t < 4; ++t) oth[t] = rd[t * 64];
#pragma unroll
    for (int t = 0; t < 4; ++t) {
      v2u o;
      o[0] = nomark(pk2(pre[t][0] + lo2(oth[t])));
      o[1] = pk2(pre[t][1] + hi2(oth[t]));
      st64<SC1>(o, rws, xoff + t * 512, WS_X1T + (d0 + cch) * 4096 + eh * 2048);
    }
  }
  STAMP(1, 3);
  SB();
  v4u wf[3][8];
  const unsigned rot = uh * 4;
#define LOAD_WP(half, k, i) ld128<SC1>(rws, l16 + ((i) & 3) * 1024, WS_WP + (((half) * 44 + ub_own + (k)) * 16 + kh * 8 + (((i) & 4) ^ rot)) * 1024 + ((k) == 2 ? oob3 : 0u))
#define LOAD_XS(i) ld128<SC1>(rws, wsp_v, WS_X1T + ((i) * 32 + w * 8) * 4096 + mt * 128)
  v4u wq[8][8];
  int rd_off[4];
#pragma unroll
  for (int tu = 0; tu < 4; ++tu) rd_off[tu] = perm(tu ^ (2 * kh), c) * 128 + ((q ^ (c >> 1)) * 16);
#define READ_W(slot, f) (*(const v4u*)(smem + (slot) + (rd_off[(f) & 3] ^ (((f) >> 2) * 64))))
  {
    const int wr_off = (lane >> 3) * 128 + (((lane & 7) ^ ((lane >> 4) & 1)) * 16);
    asm volatile("s_waitcnt vmcnt(12)" ::: "memory");
#pragma unroll
    for (int f = 0; f < 8; ++f) wq[1][f] = READ_W(slotT, f);
#pragma unroll
    for (int i = 0; i < 8; ++i) *(v4u*)(smem + slotT + i * 1024 + (wr_off ^ ((i & 3) * 32))) = wr[0][i];
#pragma unroll
    for (int f = 0; f < 8; ++f) wq[2][f] = READ_W(slotA, f);
#pragma unroll
    for (int f = 0; f < 8; ++f) wq[0][f] = READ_W(slotT, f);
    asm volatile("s_waitcnt vmcnt(4)" ::: "memory");
#pragma unroll
    for (int f = 0; f < 8; ++f) wq[3][f] = READ_W(slotB, f);
    asm volatile("s_waitcnt lgkmcnt(0)" ::: "memory");
#pragma unroll
    for (int f = 0; f < 32; ++f) PINA(wq[f >> 3][f & 7]);
  }
  v4u xs[16];
  const unsigned char* tb[2][4];
  unsigned char* xdst;
  {
    const int row = 8 * q + ((lane >> 2) & 3);
    const int sg = ((row >> 1) & 1) | (((row >> 3) & 1) << 1);
#pragma unroll
    for (int tu = 0; tu < 4; ++tu) {
      const unsigned char* t0 = smem + LDS_A + kh * 32768 + row * 128 + ((tu ^ (2 * kh) ^ sg) * 32) + (lane & 3) * 8;
      tb[0][tu] = t0 + uh * 16384;
      tb[1][tu] = t0 + (uh ^ 1) * 16384;
    }
    const int rl = w * 8 + (lane >> 3), chn = lane & 7;
    const int sg2 = ((rl >> 1) & 1) | (((rl >> 3) & 1) << 1);
    xdst = smem + LDS_A + rl * 128 + (((chn >> 1) ^ sg2) * 32) + (chn & 1) * 16;
  }
  SB();
  unsigned z2o[3];
#pragma unroll
  for (int k = 0; k < 3; ++k) { z2o[k] = l16 + WS_Z2 + ((ub_own + k) * 64 + mt * 2 + kh) * 1024 + (k == 2 ? oob3 : 0u); PIN(z2o[k]); }
  v2u cz[2][3];
  f32x2 csb[2][3];
  float gbv, c1v[2];
  unsigned bspq[2];
#pragma unroll
  for (int t = 0; t < 2; ++t) bspq[t] = p.bsp[mt * 64 + perm(2 * kh + t, c)];
#define LOAD_CZ(hf, kc) ld64<SC1>(rws, lane * 8, WS_CSB + ((hf) * 44 + ub_own + (kc)) * 512 + ((kc) == 2 ? oob3 : 0u))
  {
    STAMP(1, 20);
    if (w == 0) hint_wait(rws, c * 128, WS_CNT + (256 + xcd * 16) * 128, 16u);
    __builtin_amdgcn_s_barrier();
    MEMFENCE();
    STAMP(1, 21);
    unsigned gq[4];
#pragma unroll
    for (int i = 0; i < 6; ++i) cz[i / 3][i % 3] = LOAD_CZ(i / 3, i % 3);
#pragma unroll
    for (int i = 0; i < 4; ++i) gq[i] = __builtin_amdgcn_raw_buffer_load_b32(rws, lane * 8, WS_MODG + (i * 3072 + 5 * 512 + nt * 64) * 8, SC1);
    SB();
#pragma unroll
    for (int i = 0; i < WF_EARLY; ++i)
#pragma unroll
      for (int k = 0; k < 3; ++k) wf[k][i] = LOAD_WP(1, k, i);
#pragma unroll
    for (int i = 0; i < 16; ++i) xs[i] = LOAD_XS(i);
    SB();
#pragma unroll
    for (int k = 0; k < 3; ++k) {
      const float y = xrow_sum4(__uint_as_float(cz[1][k][0]), __uint_as_float(cz[1][k][1]), __uint_as_float(cz[1][k][0]), __uint_as_float(cz[1][k][1]));
      auto sw = __builtin_amdgcn_permlane16_swap(__float_as_uint(y), __float_as_uint(y), false, false);
      csb[1][k] = f32x2{__uint_as_float(sw[0]), __uint_as_float(sw[1])};
      PIN(csb[1][k]);
    }
    c1v[0] = xrow_sum4(__uint_as_float(cz[0][0][0]), __uint_as_float(cz[0][0][1]), __uint_as_float(cz[0][1][0]), __uint_as_float(cz[0][1][1]));
    c1v[1] = xrow_sum4(__uint_as_float(cz[0][2][0]), __uint_as_float(cz[0][2][1]), __uint_as_float(cz[0][2][0]), __uint_as_float(cz[0][2][1]));
    PIN(c1v[0]); PIN(c1v[1]);
    gbv = rbf((__uint_as_float(gq[0]) + __uint_as_float(gq[1])) + (__uint_as_float(gq[2]) + __uint_as_float(gq[3])));
    PIN(gbv);
    SB();
    STAMP(1, 44);
    unsigned m = 0;
#pragma unroll
    for (int i = 0; i < 16; ++i) {
      m = macc(m, xs[i]); *(v4u*)(xdst + i * 4096) = xs[i];
      if (WF_EARLY * 3 + i < 24) wf[(WF_EARLY * 3 + i) % 3][(WF_EARLY * 3 + i) / 3] = LOAD_WP(1, (WF_EARLY * 3 + i) % 3, (WF_EARLY * 3 + i) / 3);
      SB();
    }
    PIN(m);
    STAMP(1, 45);
    RETRY_BEGIN(m)
#pragma unroll
      for (int i = 0; i < 16; ++i) xs[i] = LOAD_XS(i);
      SB();
#pragma unroll
      for (int i = 0; i < 16; ++i) { m = macc(m, xs[i]); *(v4u*)(xdst + i * 4096) = xs[i]; }
    RETRY_END(m)
  }
  STAMP(1, 4);
  bar();
  STAMP(1, 5);
#define AHALF(tu, i, h) trread(tb[(i) >> 2][tu] + ((i) & 3) * 4096 + (h) * 512)
  f32x4 acc[3][4];
  v4u wg[3][8];
  v2u ah[2][4][2];
  float* st = (float*)(smem + LDS_S);
  float* fin = st + 256 + w * 64;
  f32x4* xch = (f32x4*)(smem + LDS_X);
  f32x4 r4[2], tm[3][2];
  {
    f32x4 s1[4], s2[4], m4[2];
    v2u ar[3][2][2];
    float pa[4], pb[4];
    constexpr int ORD[20] = {0, 1, 2, 3, 4, 5, 6, 7, 16, 17, 18, 19, 8, 9, 10, 11, 12, 13, 14, 15};
    int dgo[2], rdo[2];
    dgo[0] = w * 64 + kh * 32 + c; dgo[1] = w * 64 + (kh ^ 1) * 32 + c;
    rdo[0] = (kh * 32 + (lane & 31)) * 4; rdo[1] = rdo[0] * 4 + (lane & 3) * 4;
    PIN(dgo[0]); PIN(dgo[1]); PIN(rdo[0]); PIN(rdo[1]);
    const bool diag = (c >> 2) == q;
#define GFRAG(gi, t, h) AHALF(((gi) < 8 ? 2 : 0) + (t), (gi) < 16 ? (gi) & 7 : (gi) - 16, h)
#pragma unroll
    for (int n = 0; n < 2; ++n)
#pragma unroll
      for (int e = 0; e < 4; ++e) ar[n][e >> 1][e & 1] = GFRAG(ORD[n], e >> 1, e & 1);
    SB();
    sfor<20 * 6>([&](auto G) __attribute__((always_inline)) {
      constexpr int n = decltype(G)::value / 6, j = decltype(G)::value % 6, gi = ORD[n], t = j / 3, k = j % 3;
      constexpr int tu = (gi < 8 ? 2 : 0) + t, i = gi < 16 ? gi & 7 : gi - 16;
      const v4u a = {ar[n % 3][t][0][0], ar[n % 3][t][0][1], ar[n % 3][t][1][0], ar[n % 3][t][1][1]};
      const f32x4 z4 = {0.f, 0.f, 0.f, 0.f};
      if constexpr (gi < 16) acc[k][tu] = mfma(a, wf[k][i], gi == 0 ? z4 : acc[k][tu]);
      if constexpr ((gi >= 4 && gi < 8) || gi >= 16) {
        if constexpr (k == 0) s1[tu] = mfma(ones4, a, (gi == 4 || gi == 16) ? z4 : s1[tu]);
        if constexpr (k == 1) s2[tu] = mfma(a, a, (gi == 4 || gi == 16) ? z4 : s2[tu]);
      }
      if constexpr (j < 4 && n + 2 < 20) ar[(n + 2) % 3][j >> 1][j & 1] = GFRAG(ORD[n + 2], j >> 1, j & 1);
      if constexpr (n >= 18 && j < 4) ah[0][(n - 18) * 2 + (j >> 1)][j & 1] = AHALF((n - 18) * 2 + (j >> 1), 0, j & 1);
      if constexpr (n == 4 && j == 0) USE(wf[2][7]);
      if constexpr (n >= 3 && n < 11 && j == 4) DMA_W(4, slotA, n - 3);
      if constexpr (n >= 11 && n < 19 && j == 4) DMA_W(5, slotB, n - 11);
      if constexpr ((n == 8 || n == 9) && (j & 1) == 0) {
        constexpr int x = (n - 8) * 3 + (j >> 1);
        xch[(w * 6 + (x % 3) * 2 + x / 3) * 64 + lane] = acc[x % 3][2 + x / 3];
      }
      if constexpr (n == 10 && j == 4) bar();
      if constexpr (n == 11) acc[j % 3][j / 3] = xch[((w ^ 1) * 6 + (j % 3) * 2 + j / 3) * 64 + lane];
      if constexpr (n == 12 && j == 3) {
        float s1v[4];
#pragma unroll
        for (int x = 0; x < 4; ++x) { s1v[x] = s1[x][0]; PIN(s1v[x]); }
        if (diag) {
#pragma unroll
          for (int x = 0; x < 4; ++x) {
            *(float*)(smem + LDS_S + dgo[x >> 1] * 4 + (x & 1) * 64) = s1v[x];
            *(f32x4*)(smem + LDS_S + 2048 + dgo[x >> 1] * 16 + (x & 1) * 256) = s2[x];
          }
        }
      }
      if constexpr (n == 13 && j == 5) bar();
      if constexpr (n == 14 && j < 4) {
        pa[j] = *(const float*)(smem + LDS_S + rdo[0] + j * 256); pb[j] = *(const float*)(smem + LDS_S + 2048 + rdo[1] + j * 1024);
      }
      if constexpr (n == 15 && j == 1) { pa[0] += pa[1]; pa[2] += pa[3]; PIN(pa[0]); PIN(pa[2]); }
      if constexpr (n == 15 && j == 2) { pb[0] += pb[1]; pb[2] += pb[3]; PIN(pb[0]); PIN(pb[2]); }
      if constexpr (n == 15 && j == 3) { pa[0] = (pa[0] + pa[2]) * (1.0f / D); PIN(pa[0]); }
      if constexpr (n == 15 && j == 4) { pb[0] = (pb[0] + pb[2]) * (1.0f / D); PIN(pb[0]); }
      if constexpr (n == 15 && j == 5) { pb[0] = fmaxf(__builtin_fmaf(-pa[0], pa[0], pb[0]), 0.f); PIN(pb[0]); }
      if constexpr (n == 16 && j == 0) { pb[0] = __builtin_amdgcn_rsqf(pb[0] + EPS); PIN(pb[0]); }
      if constexpr (n == 16 && j == 1) fin[lane] = lane < 32 ? pb[0] : -pb[0] * pa[0];
      if constexpr (n == 16 && (j == 2 || j == 3)) { m4[j - 2] = *(const f32x4*)(fin + 32 + (j - 2) * 16 + q * 4); r4[j - 2] = *(const f32x4*)(fin + (j - 2) * 16 + q * 4); }
      if constexpr (n >= 18) {
        constexpr int x = (n - 18) * 6 + j, kk = x >> 2, tt = (x >> 1) & 1, h = (x & 1) * 2;
        float t0 = __builtin_fmaf(m4[tt][h], csb[1][kk][0], csb[1][kk][1]);
        PIN(t0);
        float t1 = __builtin_fmaf(m4[tt][h + 1], csb[1][kk][0], csb[1][kk][1]);
        PIN(t1);
        tm[kk][tt][h] = t0; tm[kk][tt][h + 1] = t1;
      }
      if constexpr (n >= 12 && n < 15 && j == 5) st128<SC1>(mk, rws, z2o[n - 12], 0);
      if constexpr (n == 7 && j == 5) STAMP(1, 46);
      if constexpr (n == 11 && j == 5) STAMP(1, 47);
      SB();
    });
  }
#pragma unroll
  for (int k = 0; k < 6; ++k) PINA(acc[k >> 1][k & 1]);
  STAMP(1, 6);
  f32x4 z2v[3][2];
  {
    f32x4 rs[2], rq[2];
#pragma unroll
    for (int t = 0; t < 2; ++t) {
#pragma unroll
      for (int k = 0; k < 3; ++k) {
        const f32x4 a = acc[k][t];
        const f32x2 zl = gelu2(fma2(lo2(a), lo2(r4[t]), lo2(tm[k][t])));
        const f32x2 zh = gelu2(fma2(hi2(a), hi2(r4[t]), hi2(tm[k][t])));
        z2v[k][t] = f32x4{zl[0], zl[1], zh[0], zh[1]};
        if (k == 0) { rs[t] = z2v[k][t]; rq[t] = z2v[k][t] * z2v[k][t]; }
        else { rs[t] += z2v[k][t]; rq[t] += z2v[k][t] * z2v[k][t]; }
        if (((t * 3 + k) & 1) == 0) {
#pragma unroll
          for (int f = (t * 3 + k) * 4; f < (t * 3 + k) * 4 + 8; ++f) wg[f % 3][f / 3] = LOAD_WP(0, f % 3, f / 3);
        } else {
          PIN(z2v[k][t]);
          SB();
        }
      }
    }
    STAMP(1, 50);
    float v8[8], v4[4], v2[2];
    const bool b3 = c & 8, b2 = c & 4, b1 = c & 2, b0 = c & 1;
#pragma unroll
    for (int r_ = 0; r_ < 4; ++r_) {
      const float a0 = rs[0][r_] + dpp<0x128>(rs[0][r_]), a1 = rs[1][r_] + dpp<0x128>(rs[1][r_]);
      const float q0 = rq[0][r_] + dpp<0x128>(rq[0][r_]), q1 = rq[1][r_] + dpp<0x128>(rq[1][r_]);
      v8[2 * r_] = b3 ? a1 : a0; v8[2 * r_ + 1] = b3 ? q1 : q0;
    }
#pragma unroll
    for (int j = 0; j < 4; ++j) {
      const float x0 = v8[j] + dpp<0x141>(v8[j]), x1 = v8[4 + j] + dpp<0x141>(v8[4 + j]);
      v4[j] = b2 ? x1 : x0;
    }
#pragma unroll
    for (int j = 0; j < 2; ++j) {
      const float x0 = v4[j] + dpp<0x4E>(v4[j]), x1 = v4[2 + j] + dpp<0x4E>(v4[2 + j]);
      v2[j] = b1 ? x1 : x0;
    }
    const float x0 = v2[0] + dpp<0xB1>(v2[0]), x1 = v2[1] + dpp<0xB1>(v2[1]);
    const float outv = b0 ? x1 : x0;
    __builtin_amdgcn_raw_buffer_store_b32(nomark(__float_as_uint(outv)), rws, lane * 4, WS_ST2 + ((mt * 2 + kh) * 16 + nt * 2 + uh) * 256, SC1);
  }
  STAMP(1, 7);
  SB();
#pragma unroll
  for (int tu = 0; tu < 4; ++tu)
#pragma unroll
    for (int k = 0; k < 3; ++k) acc[k][tu] = f32x4{0.f, 0.f, 0.f, 0.f};
  const int k30 = w * 6 - (w >> 1) * (w & 1);
  const unsigned oob6 = (unsigned)(w >> 1) << 30;
  v2u x1h[2][2];
  int x1o;
  {
    const int row = 8 * q + ((lane >> 2) & 3);
    const int sg = ((row >> 1) & 1) | (((row >> 3) & 1) << 1);
    x1o = LDS_A + nt * 8192 + row * 128 + ((w ^ sg) * 32) + (lane & 3) * 8;
  }
  v4u wo[6][4];
#define LOAD_WO(f) wo[(f) >> 2][(f) & 3] = ld128<SC1>(rws, l16 + (((f) >> 2) & 3) * 1024, WS_WOT + ((nt * 4 + ((f) & 3)) * 22 + k30 + (((f) >> 2) & 4)) * 1024 + ((f) >= 20 ? oob6 : 0u))
  f32x4 pt[4];
  float* c1s = st + 768 + w * 96;
#define LOAD_PT(i) asf(ld128<SC1>(rws, l16 + (i) * 1024, WS_ST2 + (mt * 2 + kh) * 4096))
  SB();
#pragma unroll
  for (int i = 0; i < 8; ++i) {
#pragma unroll
    for (int j = 0; j < 12; ++j) {
      const int tu = j / 3, k = j % 3;
      const v4u a = {ah[i & 1][tu][0][0], ah[i & 1][tu][0][1], ah[i & 1][tu][1][0], ah[i & 1][tu][1][1]};
      acc[k][tu] = mfma(wg[k][i], a, acc[k][tu]);
      if (j < 8 && i < 7) ah[(i + 1) & 1][j >> 1][j & 1] = AHALF(j >> 1, i + 1, j & 1);
      if (j < 4 && i == 7) x1h[j >> 1][j & 1] = trread(smem + x1o + (j >> 1) * 4096 + (j & 1) * 512);
      if (i == 0 && j == 0) asm volatile("s_waitcnt vmcnt(24)" ::: "memory");
      if (i < 2 && j >= 4) wq[4 + i][j - 4] = READ_W(i == 0 ? slotA : slotB, j - 4);
      if (i == 3 && j == 0) {
#pragma unroll
        for (int f = 0; f < 16; ++f) PINA(wq[4 + (f >> 3)][f & 7]);
      }
      if (i == 4 && j == 0) { USE(wg[0][7]); USE(wg[1][7]); USE(wg[2][7]); }
      if ((i == 2 || i == 3) && j % 3 == 2) dma_sel(IC<6>{}, slotA, (i - 2) * 4 + j / 3);
      if ((i == 4 || i == 5) && j % 3 == 2) dma_sel(IC<7>{}, slotB, (i - 4) * 4 + j / 3);
      if (j >= 8 && i == 6) pt[j - 8] = LOAD_PT(j - 8);
      if (i == 7 && j >= 8) LOAD_WO(j - 8);
      if (i == 5 && j == 0) {
        asm volatile("s_waitcnt vmcnt(30)" ::: "memory");
        __builtin_amdgcn_s_barrier();
      }
      if (i == 5 && j == 1) st[1408 + lane] = gbv;
      if (i == 5 && j == 9) c1s[lane] = c1v[0];
      if (i == 5 && j == 10) c1s[64 + (lane & 31)] = c1v[1];
      SB();
    }
  }
  STAMP(1, 8);
#pragma unroll
  for (int k = 0; k < 3; ++k)
#pragma unroll
    for (int t = 0; t < 2; ++t) xch[(w * 6 + k * 2 + t) * 64 + lane] = acc[k][2 + t];
  SB();
  {
    unsigned m = 0;
#pragma unroll
    for (int i = 0; i < 4; ++i) m = macc4(m, __builtin_bit_cast(v4u, pt[i]));
    RETRY_BEGIN(m)
#pragma unroll
      for (int i = 0; i < 4; ++i) { pt[i] = LOAD_PT(i); m = macc4(m, __builtin_bit_cast(v4u, pt[i])); }
    RETRY_END(m)
    if (tid < 4) hint_add(p.ws, 64 + (nt * 4 + (mt >> 3)) * 4 + tid);
    const f32x4 sx = (pt[0] + pt[1]) + (pt[2] + pt[3]);
    const float tv = xrow_sum4(sx[0], sx[2], sx[1], sx[3]);
    auto sw = __builtin_amdgcn_permlane32_swap(__float_as_uint(tv), __float_as_uint(tv), false, false);
    const float mm = __uint_as_float(sw[0]) * (1.0f / FH);
    const float r = __builtin_amdgcn_rsqf(fmaxf(__uint_as_float(sw[1]) * (1.0f / FH) - mm * mm, 0.f) + EPS);
    float* f2 = st + 512 + w * 64;
    f2[(q >> 1) * 32 + 2 * c + (q & 1)] = (q >> 1) ? -r * mm : r;
    f32x4 r4[2], m4[2];
#pragma unroll
    for (int t = 0; t < 2; ++t) { r4[t] = *(const f32x4*)(f2 + q * 8 + t * 4); m4[t] = *(const f32x4*)(f2 + 32 + q * 8 + t * 4); }
#pragma unroll
    for (int k = 0; k < 3; ++k) {
      v4u o;
#pragma unroll
      for (int t = 0; t < 2; ++t) {
        o[2 * t] = nomark(pk2(fma2(lo2(z2v[k][t]), lo2(r4[t]), lo2(m4[t]))));
        o[2 * t + 1] = pk2(fma2(hi2(z2v[k][t]), hi2(r4[t]), hi2(m4[t])));
      }
      st128<SC1>(o, rws, z2o[k], 0);
    }
  }
  STAMP(1, 9);
  SB();
  f32x4 z1f[3][2];
  constexpr int RD = 12;
  v4u zf[RD];
#define LOAD_Z(n) ld128<SC1>(rws, zv[((n) >> 1) & 7] + ((n) & 1) * 1024, WS_Z2 + ((((n) < 48 ? ub_oth : ub_own) + ((n) >> 4) % 3) * 64 + w * 16) * 1024 + (((n) >> 4) % 3 == 2 ? oob3 : 0u))
  unsigned zv[8];
#pragma unroll
  for (int i = 0; i < 8; ++i) { zv[i] = l16 + ((i + kr) & 7) * 2048; PIN(zv[i]); }
  {
    bar();
    STAMP(1, 34);
#pragma unroll
    for (int k = 0; k < 6; ++k) PINA(acc[k >> 1][k & 1]);
    f32x4 oth[3][2], cs1[3], bs1[3];
    float r1[2], m1[2];
#pragma unroll
    for (int t = 0; t < 2; ++t) { r1[t] = fin[t * 16 + c]; m1[t] = fin[32 + t * 16 + c]; }
#pragma unroll
    for (int k = 0; k < 3; ++k) {
      cs1[k] = *(const f32x4*)(c1s + k * 32 + q * 4); bs1[k] = *(const f32x4*)(c1s + k * 32 + 16 + q * 4);
#pragma unroll
      for (int t = 0; t < 2; ++t) oth[k][t] = xch[((w ^ 1) * 6 + k * 2 + t) * 64 + lane];
    }
#pragma unroll
    for (int i = 4; i < WO0; ++i) LOAD_WO(i);
    SB();
    f32x2 z1a[6][2];
#pragma unroll
    for (int i = 0; i < 6; ++i) {
      const int k = i >> 1, t = i & 1;
      const f32x4 a = acc[k][t] + oth[k][t];
      z1a[i][0] = fma2(lo2(a), bc2(r1[t]), fma2(lo2(cs1[k]), bc2(m1[t]), lo2(bs1[k])));
      z1a[i][1] = fma2(hi2(a), bc2(r1[t]), fma2(hi2(cs1[k]), bc2(m1[t]), hi2(bs1[k])));
    }
#pragma unroll
    for (int i = 0; i < GELU_PRE; ++i) {
      const f32x2 lo = gelu2(z1a[i][0]), hi = gelu2(z1a[i][1]);
      z1f[i >> 1][i & 1] = f32x4{lo[0], lo[1], hi[0], hi[1]};
      PIN(z1f[i >> 1][i & 1]);
    }
#pragma unroll
    for (int i = GELU_PRE; i < 6; ++i) { PIN(z1a[i][0]); PIN(z1a[i][1]); }
    SB();
    STAMP(1, 10);
    hint_wait(rws, 0u, WS_CNT + (64 + (nt * 4 + w) * 4 + (xcd >> 1)) * 128, 8u);
    STAMP(1, 22);
#pragma unroll
    for (int n = 0; n < RD; ++n) zf[n] = LOAD_Z(n);
    SB();
#pragma unroll
    for (int i = GELU_PRE; i < 6; ++i) {
      const f32x2 lo = gelu2(z1a[i][0]), hi = gelu2(z1a[i][1]);
      z1f[i >> 1][i & 1] = f32x4{lo[0], lo[1], hi[0], hi[1]};
      PIN(z1f[i >> 1][i & 1]);
    }
  }
  f32x4* xb = (f32x4*)(smem + LDS_A) + lane;
  f32x4* yb = (f32x4*)(smem + LDS_A + 49152) + lane;
  f32x4 ac2[2][4], ini[4], yo[2];
  const float bs0 = bf2f(bspq[0]), bs1_ = bf2f(bspq[1]);
  const f32x4 bq0 = {bs0, bs0, bs0, bs0}, bq1 = {bs1_, bs1_, bs1_, bs1_}, z4 = {0.f, 0.f, 0.f, 0.f};
  unsigned voff[2];
#pragma unroll
  for (int t = 0; t < 2; ++t) voff[t] = WS_V + (mt * 4 + 2 * kh + t) * 22 * 1024;
  float ev[4];
  unsigned eo0;
  SB();
  STAMP(1, 11);
  asm volatile("s_waitcnt vmcnt(30)" ::: "memory");
  auto v_store = [&](int k, int t, unsigned o0, unsigned o1) __attribute__((always_inline)) {
    const int uu = ub_own + k;
    st64<SC1>(v2u{o0, o1}, rws, l16, voff[t] + (uu >> 1) * 1024 + (uu & 1) * 8 + (k == 2 ? oob3 : 0u));
  };
  sfor<6>([&](auto P) __attribute__((always_inline)) {
    constexpr int ps = decltype(P)::value, cur = ps & 1, prv = cur ^ 1;
    unsigned zm = 0;
    sfor<64>([&](auto G) __attribute__((always_inline)) {
      constexpr int g = decltype(G)::value, ks = g >> 2, tu = g & 3, ck = ks >> 1, k2 = ks & 1, n = ps * 16 + ks;
      const f32x4 cin = ks > 0 ? ac2[cur][tu] : ps >= 3 ? ini[tu] : tu == 0 ? bq0 : tu == 1 ? bq1 : z4;
      ac2[cur][tu] = mfma(zf[n % RD], wq[ck][k2 * 4 + tu], cin);
      if constexpr (tu == 1) { zm = macc(zm, zf[n % RD]); PIN(zm); }
      if constexpr (tu == 3) {
        if constexpr (n + RD < 96) zf[n % RD] = LOAD_Z(n + RD);
        else if constexpr (WO0 + n + RD - 96 < 24) LOAD_WO(WO0 + n + RD - 96);
      }
      if constexpr (ps == 0 && g < 32 && (g & 1) == 0) wq[6 + (g >> 4)][(g >> 1) & 7] = READ_W(g < 16 ? slotA : slotB, (g >> 1) & 7);
      if constexpr (ps >= 1 && ps <= 3 && g >= 2 && g < 18 && (g & 3) == 2) xb[((w * 3 + ps - 1) * 4 + (g >> 2)) * 64] = ac2[prv][g >> 2];
      if constexpr (ps >= 4 && (g == 2 || g == 6)) yb[((w * 3 + ps - 4) * 2 + (g >> 2)) * 64] = ac2[prv][2 + (g >> 2)];
      if constexpr (ps >= 1 && g == (ps >= 4 ? 12 : 24)) {
        if constexpr (ps == 4) asm volatile("s_waitcnt vmcnt(20)" ::: "memory");
        bar();
      }
      if constexpr (ps >= 4 && (g == 13 || g == 14)) yo[g - 13] = yb[(((w ^ 1) * 3 + ps - 4) * 2 + g - 13) * 64];
      if constexpr (ps >= 4 && g >= 22 && g < 42) {
        constexpr int t = (g - 22) / 10, ph = (g - 22) % 10;
        if constexpr (ph < 4) { ev[ph] = ac2[prv][t][ph] + yo[t][ph]; PIN(ev[ph]); }
        else if constexpr (ph < 8) { ev[ph - 4] *= z1f[ps - 4][t][ph - 4]; PIN(ev[ph - 4]); }
        else if constexpr (ph == 8) { eo0 = nomark(pk2(ev[0], ev[1])); PIN(eo0); }
        else v_store(ps - 4, t, eo0, pk2(ev[2], ev[3]));
      }
      if constexpr (ps >= 2 && ps <= 4 && g >= 28 && g < 44 && (g & 3) == 0) ini[(g - 28) >> 2] = xb[(((w ^ 2) * 3 + ps - 2) * 4 + ((g - 28) >> 2)) * 64];
      if constexpr (ps == 0 && (g == 21 || g == 37 || g == 53)) st128<SC1>(mk, rws, l16, WS_V + ((mt * 4 + w) * 22 + pr0 + (g - 21) / 16) * 1024 + (g == 53 ? oob3 : 0u));
      if constexpr (ps == 5 && g == HINT4_G) { if (tid < 2) hint_add(p.ws, 192 + mt * 2 + tid); }
      if constexpr (g == 0 && ps >= 1 && ps < 5) STAMP(1, 55 + ps);
      SB();
    });
    if (__builtin_expect(any_mark(zm), 0)) {
#pragma unroll
      for (int tu = 0; tu < 4; ++tu) ac2[cur][tu] = ps >= 3 ? xb[(((w ^ 2) * 3 + ps - 3) * 4 + tu) * 64] : tu == 0 ? bq0 : tu == 1 ? bq1 : z4;
      sfor<16>([&](auto K) __attribute__((always_inline)) {
        constexpr int ks = decltype(K)::value, ck = ks >> 1, k2 = ks & 1;
        v4u zz;
        unsigned sp = 0;
        do { MEMFENCE(); zz = LOAD_Z(ps * 16 + ks); } while (any_mark(macc(0u, zz)) & (++sp <= SPIN_CAP));
#pragma unroll
        for (int tu = 0; tu < 4; ++tu) ac2[cur][tu] = mfma(zz, wq[ck][k2 * 4 + tu], ac2[cur][tu]);
      });
    }
  });
  STAMP(1, 12);
  {
#pragma unroll
    for (int t = 0; t < 2; ++t) yb[((w * 3 + 2) * 2 + t) * 64] = ac2[1][2 + t];
    PIN(ac2[1][0]); PIN(ac2[1][1]);
    bar();
    STAMP(1, 52);
#pragma unroll
    for (int t = 0; t < 2; ++t) yo[t] = yb[(((w ^ 1) * 3 + 2) * 2 + t) * 64];
#pragma unroll
    for (int t = 0; t < 2; ++t) {
      const f32x4 g = (ac2[1][t] + yo[t]) * z1f[2][t];
      v_store(2, t, nomark(pk2(lo2(g))), pk2(hi2(g)));
    }
  }
  STAMP(1, 13);
  SB();
  f32x4 gb4[4];
  f32x2 x1f[2][4];
#pragma unroll
  for (int i = 0; i < 4; ++i) gb4[i] = *(const f32x4*)(st + 1408 + (i >> 1) * 32 + q * 8 + (i & 1) * 4);
#pragma unroll
  for (int i = 0; i < 8; ++i) { x1f[i >> 2][i & 3] = unpk(x1h[i >> 2][(i >> 1) & 1][i & 1]); PIN(x1f[i >> 2][i & 3]); }
  STAMP(1, 53);
  if (w == 0) hint_wait(rws, 0u, WS_CNT + (192 + mt * 2 + (xcd & 1)) * 128, 8u);
  __builtin_amdgcn_s_barrier();
  MEMFENCE();
  STAMP(1, 23);
  f32x4 a3[4][4];
  {
    unsigned spins = 0;
    for (;;) {
      MEMFENCE();
      v4u vs[6][4];
#define LOAD_V(f) vs[(f) >> 2][(f) & 3] = ld128<SC1>(rws, l16 + (((f) >> 2) & 3) * 1024, WS_V + ((mt * 4 + (((f) & 3) ^ w)) * 22 + k30 + (((f) >> 2) & 4)) * 1024 + ((f) >= 20 ? oob6 : 0u))
#pragma unroll
      for (int f = 0; f < 12; ++f) LOAD_V(f);
      SB();
      unsigned m = 0;
      sfor<96>([&](auto G) __attribute__((always_inline)) {
        constexpr int g = decltype(G)::value, ks = g >> 4, tu = (g >> 2) & 3, e = g & 3;
        a3[e][tu] = mfma(wo[ks][e], vs[ks][tu], ks == 0 ? f32x4{0.f, 0.f, 0.f, 0.f} : a3[e][tu]);
        if constexpr (e == 0 && 12 + (g >> 2) < 24) LOAD_V(12 + (g >> 2));
        if constexpr (e == 1) { m = macc(m, vs[ks][tu]); PIN(m); }
        SB();
      });
      if (!(any_mark(m) & (++spins <= SPIN_CAP))) break;
    }
  }
  STAMP(1, 14);
  {
    f32x4* red = (f32x4*)(smem + LDS_W);
#pragma unroll
    for (int tu = 1; tu < 4; ++tu)
#pragma unroll
      for (int e = 0; e < 4; ++e) red[((w * 3 + tu - 1) * 4 + e) * 64 + lane] = a3[e][tu];
#pragma unroll
    for (int e = 0; e < 4; ++e) PIN(a3[e][0]);
    bar();
    STAMP(1, 54);
    f32x4 o3[4][3];
#pragma unroll
    for (int e = 0; e < 4; ++e)
#pragma unroll
      for (int rel = 1; rel < 4; ++rel) o3[e][rel - 1] = red[(((w ^ rel) * 3 + rel - 1) * 4 + e) * 64 + lane];
    const rsrc_t rout = mkrs(p.out, L * D * 2);
    SB();
#pragma unroll
    for (int pp = 0; pp < 2; ++pp) {
      v4u o;
#pragma unroll
      for (int h = 0; h < 2; ++h) {
        const int e = pp * 2 + h;
        const f32x4 y = (a3[e][0] + o3[e][0]) + (o3[e][1] + o3[e][2]);
        o[2 * h] = pk2(fma2(lo2(y), lo2(gb4[e]), x1f[pp][2 * h]));
        o[2 * h + 1] = pk2(fma2(hi2(y), hi2(gb4[e]), x1f[pp][2 * h + 1]));
      }
      st128<0>(o, rout, perm(w, c) * 1024 + q * 16 + pp * 64, mt * 64 * 1024 + nt * 128);
    }
  }
  STAMP(1, 15);
  STAMP_FLUSH(1);
  asm volatile("adaln_code_end:");
}

extern "C" void kernel_launch(void* const* d_in, const int* in_sizes, int n_in, void* d_out, int out_size, void* d_ws,
                              size_t ws_size, hipStream_t stream) {
  Params p;
  p.u = (const u16*)d_in[0];
  p.lar = (const u16*)d_in[1];
  p.aim = (const u16*)d_in[2];
  p.cre = (const u16*)d_in[3];
  p.cim = (const u16*)d_in[4];
  p.ldt = (const u16*)d_in[5];
  p.dsk = (const u16*)d_in[6];
  p.win = (const u16*)d_in[8];
  p.wsp = (const u16*)d_in[9];
  p.bsp = (const u16*)d_in[10];
  p.wout = (const u16*)d_in[11];
  p.c = (const u16*)d_in[12];
  p.wmod = (const u16*)d_in[13];
  p.out = (u16*)d_out;
  p.ws = (unsigned char*)d_ws;
  (void)hipFuncSetAttribute((const void*)adaln_mega, hipFuncAttributeMaxDynamicSharedMemorySize, MEGA_LDS);
  adaln_mega<<<256, 256, MEGA_LDS, stream>>>(p);
}
```

```cpp
#include <hip/hip_runtime.h>
#include <hip/hip_bf16.h>
#include <stdint.h>
#include <utility>

#define LAS __attribute__((address_space(3)))
typedef unsigned v4u __attribute__((ext_vector_type(4)));
typedef unsigned v2u __attribute__((ext_vector_type(2)));
typedef float f32x4 __attribute__((ext_vector_type(4)));
typedef float f32x2 __attribute__((ext_vector_type(2)));
typedef short bf16x8 __attribute__((ext_vector_type(8)));
typedef _Float16 f16x8 __attribute__((ext_vector_type(8)));
typedef _Float16 f16x2 __attribute__((ext_vector_type(2)));
typedef __bf16 bf16x2_t __attribute__((ext_vector_type(2)));
typedef unsigned short u16;
typedef __amdgpu_buffer_rsrc_t rsrc_t;

#define STAMP_DECL do { } while (0)
#define STAMP(k, i) do { } while (0)
#define STAMP_FLUSH(k) do { } while (0)

#define OPT_A 1
#define OPT_B 1
#define OPT_C 2
#define OPT_D 1
#define OPT_E 2
#define OPT_F 1
constexpr int L = 2048, D = 512, NM = 128, FH = 688, FF = 1376;
constexpr float EPS = 1e-5f;
constexpr int SC1 = 16;
constexpr unsigned OOB = 0x40000000u;
constexpr unsigned SPIN_CAP = 1u << 14;

constexpr unsigned WS_EPOCH = 0;
constexpr unsigned WS_CNT = 4096;
constexpr unsigned WS_SR = WS_CNT + 512 * 128;
constexpr unsigned WS_SB = WS_SR + 2048 * 8;
constexpr unsigned WS_MODG = WS_SB + 2048 * 8;
constexpr unsigned WS_CSB = WS_MODG + 3072 * 32;
constexpr unsigned WS_ST2 = WS_CSB + 88 * 16 * 32;
constexpr unsigned WS_UG = 1u << 20;
static_assert(WS_ST2 + 32 * 2 * 16 * 32 * 8 <= WS_UG, "small buffers");
constexpr unsigned WS_X1T = WS_UG + 256 * 2048 * 8;
constexpr unsigned WS_WP = WS_X1T + 512 * 2048 * 2;
constexpr unsigned WS_WOT = WS_WP + 88 * 16 * 1024;
constexpr unsigned WS_Z2 = WS_WOT + 32 * 22 * 1024;
constexpr unsigned WS_V = WS_Z2 + 44 * 64 * 1024;
constexpr unsigned WS_END = WS_V + 128 * 22 * 1024;

struct Params {
  const u16 *u, *lar, *aim, *cre, *cim, *ldt, *dsk, *win, *wsp, *bsp, *wout, *c, *wmod;
  u16* out;
  unsigned char* ws;
};

#define DI __device__ __forceinline__
template <int V> struct IC { static constexpr int value = V; };
template <class F, int... I> DI void sfor_(F&& f, std::integer_sequence<int, I...>) { int d[] = {(f(IC<I>{}), 0)...}; (void)d; }
template <int N, class F> DI void sfor(F&& f) { sfor_(f, std::make_integer_sequence<int, N>{}); }
DI float bf2f(unsigned b) { return __builtin_bit_cast(float, b << 16); }
DI float bflo(unsigned w) { return __builtin_bit_cast(float, w << 16); }
DI float bfhi(unsigned w) { return __builtin_bit_cast(float, w & 0xFFFF0000u); }
DI f32x2 unpk(unsigned w) { return f32x2{bflo(w), bfhi(w)}; }
DI unsigned pk2(f32x2 v) { return __builtin_bit_cast(unsigned, __builtin_convertvector(v, bf16x2_t)); }
DI unsigned pk2(float lo, float hi) { return pk2(f32x2{lo, hi}); }
DI unsigned nomark(unsigned x) { return x < 0xFFFFFFFEu ? x : 0xFFFFFFFEu; }
DI float rbf(float x) { return bflo(pk2(x, 0.f)); }
DI f32x2 fma2(f32x2 a, f32x2 b, f32x2 c) { return __builtin_elementwise_fma(a, b, c); }
DI f32x2 lo2(f32x4 v) { return f32x2{v[0], v[1]}; }
DI f32x2 hi2(f32x4 v) { return f32x2{v[2], v[3]}; }
DI f32x2 bc2(float x) { return f32x2{x, x}; }
DI f32x4 mfma(v4u a, v4u b, f32x4 c) {
  return __builtin_amdgcn_mfma_f32_16x16x32_bf16(__builtin_bit_cast(bf16x8, a), __builtin_bit_cast(bf16x8, b), c, 0, 0, 0);
}
DI f32x4 mfma_h(v4u a, v4u b, f32x4 c) {
  return __builtin_amdgcn_mfma_f32_16x16x32_f16(__builtin_bit_cast(f16x8, a), __builtin_bit_cast(f16x8, b), c, 0, 0, 0);
}
DI float dot2(unsigned a, unsigned b, float acc) {
  return __builtin_amdgcn_fdot2_f32_bf16(__builtin_bit_cast(bf16x2_t, a), __builtin_bit_cast(bf16x2_t, b), acc, false);
}
constexpr unsigned ONES = 0x3F803F80u;
DI int perm(int u, int c) { return 32 * (u >> 1) + 8 * (c >> 2) + 4 * (u & 1) + (c & 3); }
template <int CTRL> DI float dpp(float x) {
  return __builtin_bit_cast(float, __builtin_amdgcn_mov_dpp(__builtin_bit_cast(int, x), CTRL, 0xf, 0xf, true));
}
DI float row16_sum(float x) {
  x += dpp<0xB1>(x); x += dpp<0x4E>(x); x += dpp<0x141>(x); x += dpp<0x128>(x);
  return x;
}
DI float row16_max(float x) {
  x = fmaxf(x, dpp<0xB1>(x)); x = fmaxf(x, dpp<0x4E>(x)); x = fmaxf(x, dpp<0x141>(x)); x = fmaxf(x, dpp<0x128>(x));
  return x;
}
DI float xrow_sum(float x) {
  auto s = __builtin_amdgcn_permlane16_swap(__float_as_uint(x), __float_as_uint(x), false, false);
  x = __uint_as_float(s[0]) + __uint_as_float(s[1]);
  auto t = __builtin_amdgcn_permlane32_swap(__float_as_uint(x), __float_as_uint(x), false, false);
  return __uint_as_float(t[0]) + __uint_as_float(t[1]);
}
DI float xrow_sum4(float a, float b, float c, float d) {
  auto s = __builtin_amdgcn_permlane16_swap(__float_as_uint(a), __float_as_uint(b), false, false);
  auto t = __builtin_amdgcn_permlane16_swap(__float_as_uint(c), __float_as_uint(d), false, false);
  const float x = __uint_as_float(s[0]) + __uint_as_float(s[1]), y = __uint_as_float(t[0]) + __uint_as_float(t[1]);
  auto z = __builtin_amdgcn_permlane32_swap(__float_as_uint(x), __float_as_uint(y), false, false);
  return __uint_as_float(z[0]) + __uint_as_float(z[1]);
}
DI float xrow_max(float x) {
  auto s = __builtin_amdgcn_permlane16_swap(__float_as_uint(x), __float_as_uint(x), false, false);
  x = fmaxf(__uint_as_float(s[0]), __uint_as_float(s[1]));
  auto t = __builtin_amdgcn_permlane32_swap(__float_as_uint(x), __float_as_uint(x), false, false);
  return fmaxf(__uint_as_float(t[0]), __uint_as_float(t[1]));
}
DI f32x2 gelu2(f32x2 x) {
  const float k0 = -2.0f * 0.7978845608028654f * 1.4426950408889634f, k1 = k0 * 0.044715f;
  const f32x2 a = x * fma2(x * x, bc2(k1), bc2(k0));
  const f32x2 e = f32x2{__builtin_amdgcn_exp2f(a[0]), __builtin_amdgcn_exp2f(a[1])} + bc2(1.0f);
  return x * f32x2{__builtin_amdgcn_rcpf(e[0]), __builtin_amdgcn_rcpf(e[1])};
}
DI rsrc_t mkrs(const void* ptr, unsigned bytes) { return __builtin_amdgcn_make_buffer_rsrc((void*)ptr, 0, (int)bytes, 0x00020000); }
template <int AUX> DI v4u ld128(rsrc_t rs, unsigned v, unsigned s) { return __builtin_amdgcn_raw_buffer_load_b128(rs, (int)v, (int)s, AUX); }
template <int AUX> DI v2u ld64(rsrc_t rs, unsigned v, unsigned s) { return __builtin_amdgcn_raw_buffer_load_b64(rs, (int)v, (int)s, AUX); }
template <int AUX> DI void st128(v4u x, rsrc_t rs, unsigned v, unsigned s) { __builtin_amdgcn_raw_buffer_store_b128(x, rs, (int)(v + s), 0, AUX); }
template <int AUX> DI void st64(v2u x, rsrc_t rs, unsigned v, unsigned s) { __builtin_amdgcn_raw_buffer_store_b64(x, rs, (int)v, (int)s, AUX); }
DI f32x4 asf(v4u x) { return __builtin_bit_cast(f32x4, x); }
DI float sum4(f32x4 x) { return (x[0] + x[1]) + (x[2] + x[3]); }
DI unsigned umax3(unsigned a, unsigned b, unsigned c) { unsigned m = a > b ? a : b; return m > c ? m : c; }
DI unsigned macc4(unsigned m, v4u x) { return umax3(umax3(m, x[0], x[1]), x[2], x[3]); }
DI unsigned macc(unsigned m, v4u x) { return umax3(m, x[0], x[2]); }
DI bool any_mark(unsigned m) { return __builtin_amdgcn_ballot_w64(m == 0xFFFFFFFFu) != 0ull; }
#define SB() __builtin_amdgcn_sched_barrier(0)
#define PIN(x) asm volatile("" : "+v"(x))
#define PINA(x) asm volatile("" : "+a"(x))
#define USE(x) asm volatile("" ::"v"(x))
#define RETRY_BEGIN(m) if (__builtin_expect(any_mark(m), 0)) { unsigned sp_ = 0; do { MEMFENCE(); m = 0;
#define RETRY_END(m) } while (any_mark(m) & (++sp_ <= SPIN_CAP)); }
#define MEMFENCE() asm volatile("" ::: "memory")
DI void bar() { asm volatile("s_waitcnt lgkmcnt(0)" ::: "memory"); __builtin_amdgcn_s_barrier(); asm volatile("" ::: "memory"); }

DI f32x2 cmul(f32x2 a, f32x2 b) { return fma2(bc2(a[1]), f32x2{-b[1], b[0]}, bc2(a[0]) * b); }
DI f32x2 cexp_p(float are2, float rev_hi, float rev_lo, float p) {
  const float mag = __builtin_amdgcn_exp2f(are2 * p);
  const float hi = rev_hi * p;
  const float fr = (hi - __builtin_rintf(hi)) + rev_lo * p;
  return f32x2{mag * __builtin_amdgcn_cosf(fr), mag * __builtin_amdgcn_sinf(fr)};
}
DI float exp_acc(float x) {
  const float hi = x * 1.4426950408889634f;
  const float lo = __builtin_fmaf(x, 1.4426950408889634f, -hi) + x * 1.925963033500011e-8f;
  const float e = __builtin_amdgcn_exp2f(hi);
  return __builtin_fmaf(e, lo * 0.6931471805599453f, e);
}
DI unsigned pkh(float a, float b) {
  f16x2 v = {(_Float16)a, (_Float16)b};
  return __builtin_bit_cast(unsigned, v);
}

constexpr int CV_RK = 4160;
constexpr int CV_H = 2 * CV_RK;
constexpr int CV_CH = CV_H + 4608;
constexpr int LDS_A = 0;
constexpr int LDS_U = 2 * CV_CH;
constexpr int LDS_GS = LDS_U + 8192;
constexpr int LDS_TAB = 36864;
constexpr int KP_CH = 13840;
constexpr int LDS_KP = LDS_TAB + 2 * 64 * 528 + 16;
constexpr int LDS_LN = 0, LDS_MR = 8448;
constexpr int LDS_WI = 145472, LDS_WO = LDS_WI + 10240;
constexpr int LDS_X = 65536;
constexpr int LDS_S = LDS_X + 24576;
constexpr int LDS_W = LDS_S + 6144;
constexpr int MEGA_LDS = LDS_W + 65536;
static_assert(LDS_WO + 4608 <= MEGA_LDS && LDS_KP + 2 * KP_CH <= LDS_WI && LDS_S >= 73728, "lds");
static_assert(LDS_GS + 1280 <= LDS_TAB && LDS_KP + 2 * KP_CH <= MEGA_LDS && MEGA_LDS <= 160 * 1024, "lds");
#define FLAG_AT 84
static_assert(FLAG_AT > 77 && FLAG_AT < 88, "flag position");
#define WF_EARLY 8
#define WO0 12
#define WSP_P0 4
#define HINT4_KS 15
#define ZDEPTH 3
#define GELU_IN 0
#define EPI_W 0
#define MOD_POS 0
#define POLL3 0
#define GELU_PRE 4
#define HINT4_G 50
#define RELABEL 1
#define CKMAP(ck) (RELABEL ? 2 * ((ck) & 3) + ((ck) >> 2) : (ck))
#define POLL_AT 130
#define MOD_MID 1
#define IN_AUX 0
#define WSP_AUX 0

template <class T> DI T ldsr(unsigned a) { return *(const LAS T*)(size_t)a; }
template <class T> DI void ldsw(unsigned a, T v) { *(LAS T*)(size_t)a = v; }
DI v2u trread(const unsigned char* ptr) {
  typedef short v4i16_t __attribute__((ext_vector_type(4)));
  return __builtin_bit_cast(v2u, __builtin_amdgcn_ds_read_tr16_b64_v4i16((LAS v4i16_t*)ptr));
}
DI v2u trread_a(unsigned a) {
  typedef short v4i16_t __attribute__((ext_vector_type(4)));
  return __builtin_bit_cast(v2u, __builtin_amdgcn_ds_read_tr16_b64_v4i16((LAS v4i16_t*)(size_t)a));
}
struct CvOrder { int qq[36], pp[36]; };
constexpr CvOrder cv_order() {
  CvOrder o{};
  int n = 0;
  for (int m = 0; m < 8; ++m) {
    for (int j = 0; j < m; ++j) if (m + j < 8) { o.qq[n] = m; o.pp[n] = j; ++n; }
    for (int j = 0; j <= m; ++j) if (m + j < 8) { o.qq[n] = j; o.pp[n] = m; ++n; }
  }
  return o;
}
constexpr CvOrder CVO = cv_order();

typedef __attribute__((address_space(1))) unsigned gu32;
DI unsigned tx(unsigned m, unsigned x, unsigned tag) { return __builtin_amdgcn_bitop3_b32(m, x, tag, 0xF6); }
DI unsigned tacc(unsigned m, v4u x, unsigned tag) { return tx(tx(m, x[1], tag), x[3], tag); }
DI bool any_nz(unsigned m) { return __builtin_amdgcn_ballot_w64(m != 0u) != 0ull; }
DI void hint_add(unsigned char* ws, int line) { (void)__hip_atomic_fetch_add((gu32*)(ws + WS_CNT + line * 128), 1u, __ATOMIC_RELAXED, __HIP_MEMORY_SCOPE_AGENT); }
DI void hint_wait(rsrc_t rws, unsigned voff, unsigned soff, unsigned want) {
  unsigned sp = 0;
  for (;;) {
    MEMFENCE();
    const unsigned v = __builtin_amdgcn_raw_buffer_load_b32(rws, (int)voff, (int)soff, SC1);
    if (!((__builtin_amdgcn_ballot_w64(v < want) != 0ull) & (++sp <= SPIN_CAP))) break;
    __builtin_amdgcn_s_sleep(OPT_E);
  }
  MEMFENCE();
}

extern "C" __global__ void __launch_bounds__(256, 1) adaln_mega(Params p) {
  extern __shared__ __attribute__((aligned(16))) unsigned char smem[];
  const int tid = threadIdx.x, lane = tid & 63, w = __builtin_amdgcn_readfirstlane(tid >> 6);
  const int b = blockIdx.x, xcd = b & 7, idx = b >> 3;
  const int pb = xcd * 32 + idx;
  const unsigned l16 = lane * 16;
  STAMP_DECL;
  STAMP(1, 0);
  v2u codepf;
  {
    unsigned long long pc;
    int rel_m, rel_e;
    asm volatile("s_getpc_b64 %0\n\ts_mov_b32 %1, adaln_mega@rel32@lo+4\n\ts_mov_b32 %2, adaln_code_end@rel32@lo+12" : "=s"(pc), "=s"(rel_m), "=s"(rel_e));
    const rsrc_t rcode = mkrs((const void*)(pc + (long long)rel_m), rel_e > rel_m ? (unsigned)(rel_e - rel_m) & ~15u : 0u);
    codepf = ld64<0>(rcode, tid * 8, idx * 2048);
  }
  SB();
  const rsrc_t ru = mkrs(p.u, L * D * 2);
  v4u xr[2];
  xr[0] = ld128<IN_AUX>(ru, l16, (pb * 8 + w * 2) * 1024);
  xr[1] = ld128<IN_AUX>(ru, l16 + 1024, (pb * 8 + w * 2) * 1024);
  const int mkg = b & 3, mcg = b >> 2;
  const unsigned cq = p.c[mkg * 128 + (tid & 3) * 32 + (tid >> 3)];
  const unsigned epoch = *(const unsigned*)(p.ws + WS_EPOCH);
  const rsrc_t rws = mkrs(p.ws, WS_END);
  __builtin_amdgcn_raw_buffer_store_b32(0u, rws, tid < 2 ? tid * 32768u : OOB, WS_CNT + b * 128, SC1);
  SB();
  const int cch = w >> 1, eh = w & 1, ns = tid & 127;
  const unsigned q_cre = p.cre[pb * 256 + tid], q_cim = p.cim[pb * 256 + tid], q_lar = p.lar[pb * 256 + tid], q_aim = p.aim[pb * 256 + tid];
  const unsigned q_ldt = p.ldt[pb * 2 + cch], q_dsk = p.dsk[pb * 2 + cch];
  v4u xm[4];
#define LOAD_XM() { \
  const rsrc_t rmod = mkrs(p.wmod, D * 3072 * 2); \
_Pragma("unroll") \
  for (int i = 0; i < 4; ++i) xm[i] = ld128<IN_AUX>(rmod, (tid >> 3) * 6144 + (tid & 7) * 16, (mkg * 128 + i * 32) * 6144 + mcg * 128 + (b < 192 ? 0u : OOB)); }
  SB();
  LOAD_XM();
  SB();
  STAMP(1, 24);
  const int c = lane & 15, q = lane >> 4;
  const unsigned lds0 = (unsigned)(size_t)(LAS unsigned char*)smem;
  unsigned pos4[9];
  {
    const float f4 = (float)(8188 - tid * 32);
#pragma unroll
    for (int j = 0; j < 10; j += 2) {
      const unsigned pp = pk2(bc2(f4) - f32x2{4.f * j, 4.f * j + 4.f});
      const int p0 = (int)bflo(pp), p1 = (int)bfhi(pp);
      pos4[j] = p0 + ((p0 >> 5) << 2) + lds0 + LDS_KP;
      PIN(pos4[j]);
      if (j + 1 < 9) { pos4[j + 1] = p1 + ((p1 >> 5) << 2) + lds0 + LDS_KP; PIN(pos4[j + 1]); }
    }
  }
  unsigned co[2][4];
  {
    const unsigned b0 = (eh ? 1152 + 576 * q : 144 * (q >> 1) + 4 * (q & 1)) * 4 + 36 * c + lds0 + LDS_KP + cch * KP_CH;
    const unsigned b1 = (q < 2 ? 288 + 144 * q : 568 + 4 * q) * 4 + 36 * c + lds0 + LDS_KP + cch * KP_CH;
    const unsigned s0 = eh ? 576 : 4, s1 = q < 2 ? 8 : 576;
#pragma unroll
    for (int r = 0; r < 4; ++r) { co[0][r] = b0 + r * s0; co[1][r] = b1 + r * s1; PIN(co[0][r]); PIN(co[1][r]); }
  }
  *(unsigned*)(smem + LDS_KP - 16 + cch * KP_CH + (lane & 3) * 4) = 0u;
  unsigned a_pad, a_wi, a_wo, a_tab, gm_off, gc_off;
  a_pad = lds0 + cch * CV_CH + (lane < 32 ? CV_H + l16 : ((lane >> 1) & 1) * CV_RK + 4096 + (lane & 1) * 16);
  a_wi = lds0 + LDS_WI + (w * 32 + 8 * q + ((lane >> 2) & 3)) * 80 + (lane & 3) * 16;
  a_wo = lds0 + LDS_WO + (8 * q + ((lane >> 2) & 3)) * 144 + (w >> 1) * 64 + (lane & 3) * 16 + (w & 1) * 8;
  a_tab = lds0 + LDS_TAB + cch * (64 * 528) + c * 528 + q * 16;
  gm_off = ((4 - cch) * 512 + mkg * 128 + ns) * 8;
  gc_off = (((lane & 7) > 5 ? 0 : ((lane & 7) >> 1)) * 512 + pb * 2 + (lane & 1)) * 8;
  PIN(a_pad); PIN(a_wi); PIN(a_wo); PIN(a_tab); PIN(gm_off); PIN(gc_off);
  unsigned ab[4], bb, dv[4], rdo4[4], xoff;
#pragma unroll
  for (int i = 0; i < 4; ++i) {
    ab[i] = lds0 + cch * CV_CH + (1 - (c & 1)) * CV_RK + 2 * (2030 - 2 * (c >> 1) + 8 * q) - eh * 256 - 192 - 512 - 1024 * i;
    dv[i] = (lane >> 3) * 4096 + (((lane & 7) ^ ((lane >> 4) & 1) ^ (i << 1)) * 16) + i * (32768 - 1024);
    rdo4[i] = lds0 + LDS_W + w * 16384 + ((perm((i & 1) * 2 ^ (2 * (w & 1)), c) * 128 + ((q ^ (c >> 1)) * 16)) ^ ((i >> 1) * 64));
    PIN(ab[i]); PIN(dv[i]); PIN(rdo4[i]);
  }
  bb = lds0 + cch * CV_CH + CV_H + 2 * (256 - 16 + 16 * c + 8 * q) - eh * 256 - 192;
  xoff = WS_X1T + (pb * 2 + cch) * 4096 + eh * 2048 + ((c >> 1) * 32 + ((q & 1) * 4 + 2 * (c & 1) + (q >> 1)) * 4) * 2;
  PIN(bb); PIN(xoff);
  SB();
  STAMP(1, 26);
  {
    *(v4u*)(smem + LDS_LN + (w * 2) * 1040 + l16) = xr[0];
    *(v4u*)(smem + LDS_LN + (w * 2 + 1) * 1040 + l16) = xr[1];
    float sm[2], sq[2];
#pragma unroll
    for (int i = 0; i < 2; ++i) {
      sm[i] = dot2(xr[i][0], ONES, dot2(xr[i][1], ONES, dot2(xr[i][2], ONES, dot2(xr[i][3], ONES, 0.f))));
      sq[i] = dot2(xr[i][0], xr[i][0], dot2(xr[i][1], xr[i][1], dot2(xr[i][2], xr[i][2], dot2(xr[i][3], xr[i][3], 0.f))));
    }
    const float tv = row16_sum(xrow_sum4(sm[0], sm[1], sq[0], sq[1]));
    auto sw = __builtin_amdgcn_permlane32_swap(__float_as_uint(tv), __float_as_uint(tv), false, false);
    const float m = __uint_as_float(sw[0]) * (1.0f / D);
    const float r = __builtin_amdgcn_rsqf(fmaxf(__uint_as_float(sw[1]) * (1.0f / D) - m * m, 0.f) + EPS);
    ((float*)(smem + LDS_GS + 1088))[(q >> 1) * 8 + w * 2 + (q & 1)] = (q >> 1) ? -m * r : r;
  }
  const unsigned tag = epoch * 2654435761u + 0x9E3779B9u;
  STAMP(1, 28);
  asm volatile("s_waitcnt vmcnt(%0)" ::"i"(MOD_POS == 0 ? 8 : 4) : "memory");
  bar();
  STAMP(1, 16);
  {
    unsigned ua[4][2];
#pragma unroll
    for (int i = 0; i < 4; ++i) {
      const unsigned char* src = smem + LDS_LN + (tid & 3) * 2080 + (i * 64 + (tid >> 2)) * 4;
      ua[i][0] = *(const unsigned*)src; ua[i][1] = *(const unsigned*)(src + 1040);
    }
    const unsigned lnv = ((const unsigned*)(smem + LDS_GS + 1088))[c];
    SB();
#pragma unroll
    for (int i = 0; i < 4; ++i) st128<SC1>(v4u{ua[i][0], tag, ua[i][1], tag}, rws, (tid >> 2) * 16384 + (tid & 3) * 16, WS_UG + i * 64 * 16384 + pb * 64);
    st64<SC1>(v2u{lnv, tag}, rws, tid < 16 ? (c >> 3) * 16384 + (c & 7) * 8 : OOB, WS_SR + pb * 64);
  }
  STAMP(1, 17);
  SB();
  const unsigned wp_oob = b < 176 ? 0u : OOB;
  const int wpr = b >> 2, wkg = b & 3;
  v4u xi[2], xo;
  {
    const rsrc_t rwin = mkrs(p.win, D * FF * 2), rwout = mkrs(p.wout, FH * D * 2);
    const int hf = wpr >= 22, pp = wpr - 22 * hf;
    const unsigned v = ((tid >> 2) * (FF * 2) + (tid & 3) * 16) | ((pp == 21 ? tid & 2 : 0) << 29);
#pragma unroll
    for (int i = 0; i < 2; ++i) xi[i] = ld128<IN_AUX>(rwin, v, (wkg * 128 + i * 64) * (FF * 2) + (hf * FH + pp * 32) * 2 + wp_oob);
    xo = ld128<IN_AUX>(rwout, (tid >> 3) * 1024 + (tid & 7) * 16, idx * 32 * 1024 + xcd * 128);
  }
  SB();
  const int d0 = pb * 2, dk = d0 + cch;
  const int mt = (xcd >> 1) * 8 + (idx & 7), nt = (xcd & 1) * 4 + (idx >> 3);
  const int big = (nt + 2) >> 3;
  const int np = 3 - big, pr0 = 3 * nt - big * (nt - 6);
  const int kh = w & 1, uh = w >> 1;
  const int ub_own = pr0 * 2 + uh * np, ub_oth = pr0 * 2 + (uh ^ 1) * np;
  const unsigned oob3 = (unsigned)big << 30;
  const int kr = 2 * (idx >> 3) + (xcd & 1) + (OPT_B == 0 ? (idx & 7) : OPT_B == 1 ? 4 * (idx & 1) : OPT_B == 2 ? 2 * (idx & 3) : 0);
  const rsrc_t rwsp = mkrs(p.wsp, L * L * 2);
  const unsigned wp_fr = (wpr * 2 * 16 + wkg * 4 + w) * 1024 + wp_oob;
  v4u wr[1][8];
  const unsigned wsp_v = (lane >> 3) * 4096 + (lane & 7) * 16, wsp_s = mt * 64 * 4096 + w * 1024;
#define LOAD_WSP1(ck, i) wr[ck][i] = ld128<WSP_AUX>(rwsp, wsp_v + (((ck) + kr) & 7) * 128, wsp_s + (i) * 8 * 4096)
#define LOAD_WSPN(n) LOAD_WSP1((n) >> 3, (n) & 7)
  const v4u mk = {~0u, ~0u, ~0u, ~0u};
  const unsigned tag_s = __builtin_amdgcn_readfirstlane(tag);
  {
    const float dt = rbf(exp_acc(bf2f(q_ldt)));
    const float cre = bf2f(q_cre) * dt, cim = bf2f(q_cim) * dt;
    const float are2 = -rbf(exp_acc(bf2f(q_lar))) * dt * 1.4426950408889634f;
    const float aim = bf2f(q_aim) * dt;
    const float rev_hi = aim * 0.15915494309189535f;
    const float rev_lo = __builtin_fmaf(aim, 0.15915494309189535f, -rev_hi) + aim * 6.4206383e-9f;
    int ex = __builtin_amdgcn_frexp_expf(xrow_max(row16_max(fmaxf(fabsf(cre), fabsf(cim))))) + 1;
    ex = ex < -100 ? -100 : ex;
    const float scale = __builtin_amdgcn_ldexpf(1.f, -ex);
    ((float*)(smem + LDS_GS + 1024))[w] = __builtin_amdgcn_ldexpf(2.f, ex);
    const f32x2 c0 = {cre * scale, cim * scale};
    const f32x2 e1 = cexp_p(are2, rev_hi, rev_lo, 1.f), e8 = cexp_p(are2, rev_hi, rev_lo, 8.f), e64 = cexp_p(are2, rev_hi, rev_lo, 64.f);
    unsigned char* tn = smem + LDS_TAB + cch * (64 * 528) + ns * 4;
#define TROW(r, z) *(unsigned*)(tn + (r) * 528) = pkh((z)[0], -(z)[1])
    SB();
    const f32x2 e128 = cmul(e64, e64), e2 = cmul(e1, e1), e256 = cmul(e128, e128), e4 = cmul(e2, e2), e512 = cmul(e256, e256);
    f32x2 f0 = {1.f, 0.f}, f1 = e64;
    f32x2 ca = cmul(c0, e512), cb = cmul(ca, e512);
#pragma unroll
    for (int j = 0; j < 8; ++j) {
      *(unsigned*)(tn + (48 + j) * 528) = pkh(f0[0], f0[1]);
      *(unsigned*)(tn + (56 + j) * 528) = pkh(f1[0], f1[1]);
      TROW(32 + j, cb);
      f0 = j == 0 ? e8 : cmul(f0, e8); f1 = cmul(f1, e8); cb = cmul(cb, e128);
      if (j < 4) {
        const f32x2 z = cmul(ca, e4);
        TROW(24 + j, ca); TROW(28 + j, z);
        ca = cmul(ca, e128);
      }
      if (j == 3 || j == 7) {
        PIN(f0); PIN(f1); PIN(cb);
        if (WSP_P0 >= 4) { LOAD_WSPN(j >> 2 << 1); LOAD_WSPN((j >> 2 << 1) + 1); }
        SB();
      }
    }
    TROW(40, cb);
    SB();
  {
    const float cv = bf2f(cq);
    const float sl = rbf(cv * __builtin_amdgcn_rcpf(1.0f + __builtin_amdgcn_exp2f(cv * -1.4426950408889634f)));
    const float sc[4] = {dpp<0x00>(sl), dpp<0x55>(sl), dpp<0xAA>(sl), dpp<0xFF>(sl)};
    f32x2 a[4];
#pragma unroll
    for (int i = 0; i < 4; ++i)
#pragma unroll
      for (int j = 0; j < 4; ++j) a[j] = i == 0 ? unpk(xm[i][j]) * bc2(sc[i]) : fma2(unpk(xm[i][j]), bc2(sc[i]), a[j]);
    float* red = (float*)(smem + LDS_MR) + (tid >> 3) * 68 + (tid & 7) * 8;
    *(f32x4*)red = f32x4{a[0][0], a[0][1], a[1][0], a[1][1]};
    *(f32x4*)(red + 4) = f32x4{a[2][0], a[2][1], a[3][0], a[3][1]};
  }
    bar();
    float ms8[8];
#pragma unroll
    for (int i = 0; i < 8; ++i) ms8[i] = ((const float*)(smem + LDS_MR))[(q * 8 + i) * 68 + w * 16 + c];
    SB();
    f32x2 t1[8];
    t1[1] = e1; t1[2] = e2; t1[4] = e4;
    t1[3] = cmul(e2, e1); t1[5] = cmul(e4, e1); t1[6] = cmul(e2, e4); t1[7] = cmul(t1[6], e1);
    const f32x2 c1 = cmul(c0, e128), c2 = cmul(c0, e256), c3 = cmul(c2, e128);
    TROW(0, c0); TROW(8, c1); TROW(16, c2); TROW(20, c3);
    {
    const float s = xrow_sum(((ms8[0] + ms8[1]) + (ms8[2] + ms8[3])) + ((ms8[4] + ms8[5]) + (ms8[6] + ms8[7])));
    st64<SC1>(v2u{__float_as_uint(s), tag}, rws, q == 0 ? c * 8 : OOB, WS_MODG + (mkg * 3072 + mcg * 64 + w * 16) * 8 + (b < 192 ? 0u : OOB));
    }
    SB();
#pragma unroll
    for (int v = 1; v < 8; ++v) {
      const f32x2 z0 = cmul(t1[v], c0), z1 = cmul(t1[v], c1);
      TROW(v, z0); TROW(8 + v, z1);
      if ((v & 1) == 0) {
        const f32x2 z2 = cmul(t1[v], c2), z3 = cmul(t1[v], c3);
        TROW(16 + v / 2, z2); TROW(20 + v / 2, z3);
        if (v < 6 && WSP_P0 >= 8) { LOAD_WSPN(v + 2); LOAD_WSPN(v + 3); }
        SB();
      }
    }
  }
  STAMP(1, 27);
#pragma unroll
  for (int i = 0; i < 2; ++i) *(v4u*)(smem + LDS_WI + (i * 64 + (tid >> 2)) * 80 + (tid & 3) * 16) = xi[i];
  *(v4u*)(smem + LDS_WO + (tid >> 3) * 144 + (tid & 7) * 16) = xo;
  STAMP(1, 29);
  bar();
  STAMP(1, 18);
  SB();
  v4u gr[4], gb_[4];
  v2u gm[4], gc[4];
#define LOAD_GS() _Pragma("unroll") for (int i_ = 0; i_ < 4; ++i_) { gr[i_] = ld128<SC1>(rws, tid * 16, WS_SR + i_ * 4096); gb_[i_] = ld128<SC1>(rws, tid * 16, WS_SB + i_ * 4096); }
#define LOAD_GM() _Pragma("unroll") for (int i_ = 0; i_ < 4; ++i_) { gm[i_] = ld64<SC1>(rws, gm_off, WS_MODG + i_ * 24576); gc[i_] = ld64<SC1>(rws, gc_off, WS_MODG + i_ * 24576); }
  v4u ug[4], wt[2];
#define LOAD_UG() _Pragma("unroll") for (int i_ = 0; i_ < 4; ++i_) ug[i_] = ld128<SC1>(rws, tid * 16, WS_UG + pb * 16384 + i_ * 4096)
  {
    v4u fa[2][8], fb[8];
    const unsigned ta = a_tab + eh * (32 * 528);
#pragma unroll
    for (int ks = 0; ks < 3; ++ks) { fb[ks] = ldsr<v4u>(a_tab + 48 * 528 + ks * 64); fa[0][ks] = ldsr<v4u>(ta + ks * 64); fa[1][ks] = ldsr<v4u>(a_tab + 16 * 528 + ks * 64); }
    SB();
    LOAD_UG();
    SB();
    f32x4 kk[2][2];
    f32x2 us;
    v2u wh_[6];
#pragma unroll
    for (int i = 0; i < 4; ++i) kk[i >> 1][i & 1] = f32x4{0.f, 0.f, 0.f, 0.f};
#pragma unroll
    for (int ks = 0; ks < 8; ++ks) {
      if (ks < 5) { fb[3 + ks] = ldsr<v4u>(a_tab + 48 * 528 + (3 + ks) * 64); fa[0][3 + ks] = ldsr<v4u>(ta + (3 + ks) * 64); fa[1][3 + ks] = ldsr<v4u>(a_tab + 16 * 528 + (3 + ks) * 64); }
      if (ks == 5) { us = *(const f32x2*)(smem + LDS_GS + 1024 + cch * 8); wh_[4] = trread_a(a_wo); wh_[5] = trread_a(a_wo + 576); }
      if (ks == 6) { wh_[0] = trread_a(a_wi); wh_[1] = trread_a(a_wi + 320); wh_[2] = trread_a(a_wi + 8); wh_[3] = trread_a(a_wi + 328); }
      kk[0][ks >> 2] = mfma_h(fa[0][ks], fb[ks], kk[0][ks >> 2]); kk[1][ks >> 2] = mfma_h(fa[1][ks], fb[ks], kk[1][ks >> 2]);
      SB();
    }
    STAMP(1, 37);
    wt[0] = v4u{wh_[0][0], wh_[0][1], wh_[1][0], wh_[1][1]};
    wt[1] = v4u{wh_[2][0], wh_[2][1], wh_[3][0], wh_[3][1]};
    st128<SC1>(v4u{wh_[4][0], wh_[4][1], wh_[5][0], wh_[5][1]}, rws, l16, WS_WOT + ((xcd * 4 + w) * 22 + idx) * 1024 + wp_oob);
    LOAD_GS();
    SB();
#pragma unroll
    for (int i = 0; i < 4; ++i) PINA(kk[i >> 1][i & 1]);
    f32x4 k0 = kk[0][0] * us[0] + kk[0][1] * us[1];
    const f32x4 k1 = kk[1][0] * us[0] + kk[1][1] * us[1];
    k0[0] += ns == 0 ? bf2f(q_dsk) : 0.f;
#pragma unroll
    for (int r = 0; r < 4; ++r) { ldsw<float>(co[0][r], k0[r]); ldsw<float>(co[1][r], k1[r]); }
  }
  STAMP(1, 31);
  bar();
  STAMP(1, 19);
  {
    float v[2][9];
#pragma unroll
    for (int j = 0; j < 9; ++j) v[0][j] = ldsr<float>(pos4[j]);
    LOAD_GM();
#pragma unroll
    for (int j = 0; j < 9; ++j) v[1][j] = ldsr<float>(pos4[j] + KP_CH);
    ldsw<v4u>(a_pad, v4u{0u, 0u, 0u, 0u});
    SB();
    STAMP(1, 36);
#pragma unroll
    for (int ch = 0; ch < 2; ++ch) {
      unsigned char* rk = smem + ch * CV_CH + tid * 16;
#pragma unroll
      for (int sft = 0; sft < 2; ++sft)
        *(v4u*)(rk + sft * CV_RK) = v4u{pk2(v[ch][sft], v[ch][sft + 1]), pk2(v[ch][sft + 2], v[ch][sft + 3]), pk2(v[ch][sft + 4], v[ch][sft + 5]), pk2(v[ch][sft + 6], v[ch][sft + 7])};
    }
#pragma unroll
    for (int i = WSP_P0; i < 8; ++i) LOAD_WSPN(i);
    st128<SC1>(mk, rws, l16, WS_X1T + dk * 4096 + eh * 2048);
    st128<SC1>(mk, rws, l16 + 1024, WS_X1T + dk * 4096 + eh * 2048);
    __builtin_amdgcn_raw_buffer_store_b32(~0u, rws, lane * 4, WS_ST2 + ((mt * 2 + kh) * 16 + nt * 2 + uh) * 256, SC1);
    SB();
    STAMP(1, 32);
    unsigned m = 0;
#pragma unroll
    for (int i = 0; i < 4; ++i) { m = tacc(m, ug[i], tag); m = tacc(m, gr[i], tag); m = tacc(m, gb_[i], tag); }
    if (__builtin_expect(any_nz(m), 0)) {
      unsigned sp = 0;
      do {
        MEMFENCE(); LOAD_UG(); LOAD_GS(); m = 0;
#pragma unroll
        for (int i = 0; i < 4; ++i) { m = tacc(m, ug[i], tag); m = tacc(m, gr[i], tag); m = tacc(m, gb_[i], tag); }
      } while (any_nz(m) & (++sp <= SPIN_CAP));
    }
    f32x2 nn[4][2];
#pragma unroll
    for (int i = 0; i < 4; ++i) {
      nn[i][0] = fma2(unpk(ug[i][0]), bc2(__uint_as_float(gr[i][0])), bc2(__uint_as_float(gb_[i][0])));
      nn[i][1] = fma2(unpk(ug[i][2]), bc2(__uint_as_float(gr[i][2])), bc2(__uint_as_float(gb_[i][2])));
      PIN(nn[i][0]); PIN(nn[i][1]);
      *(unsigned*)(smem + LDS_U + i * 1024 + tid * 4) = __builtin_amdgcn_perm(ug[i][2], ug[i][0], 0x05040100u);
      *(unsigned*)(smem + LDS_U + 4096 + i * 1024 + tid * 4) = __builtin_amdgcn_perm(ug[i][2], ug[i][0], 0x07060302u);
    }
    SB();
    STAMP(1, 33);
    m = 0;
#pragma unroll
    for (int i = 0; i < 4; ++i) m = tx(tx(m, gm[i][1], tag), gc[i][1], tag);
    if (__builtin_expect(any_nz(m), 0)) {
      unsigned sp = 0;
      do {
        __builtin_amdgcn_s_sleep(3);
        MEMFENCE(); LOAD_GM(); m = 0;
#pragma unroll
        for (int i = 0; i < 4; ++i) m = tx(tx(m, gm[i][1], tag), gc[i][1], tag);
      } while (any_nz(m) & (++sp <= SPIN_CAP));
    }
    const float mv = rbf((__uint_as_float(gm[0][0]) + __uint_as_float(gm[1][0])) + (__uint_as_float(gm[2][0]) + __uint_as_float(gm[3][0])));
    *(u16*)(smem + LDS_GS + tid * 2) = (u16)pk2(mv + (cch ? 0.f : 1.0f), 0.f);
    const float cv = rbf((__uint_as_float(gc[0][0]) + __uint_as_float(gc[1][0])) + (__uint_as_float(gc[2][0]) + __uint_as_float(gc[3][0])));
    const float gav = dpp<0x104>(cv), gav2 = dpp<0x102>(cv);
    const float hbv = cv * gav, hsv = rbf(1.0f + cv) * gav2;
    const f32x2 hb = {__builtin_bit_cast(float, __builtin_amdgcn_readlane(__builtin_bit_cast(int, hbv), 0)), __builtin_bit_cast(float, __builtin_amdgcn_readlane(__builtin_bit_cast(int, hbv), 1))};
    const f32x2 hs = {__builtin_bit_cast(float, __builtin_amdgcn_readlane(__builtin_bit_cast(int, hsv), 2)), __builtin_bit_cast(float, __builtin_amdgcn_readlane(__builtin_bit_cast(int, hsv), 3))};
    SB();
    STAMP(1, 38);
#pragma unroll
    for (int i = 0; i < 4; ++i) {
      const f32x2 h0 = fma2(nn[i][0], hs, hb), h1 = fma2(nn[i][1], hs, hb);
      *(unsigned*)(smem + CV_H + 512 + i * 1024 + tid * 4) = pk2(h0[0], h1[0]);
      *(unsigned*)(smem + CV_CH + CV_H + 512 + i * 1024 + tid * 4) = pk2(h0[1], h1[1]);
    }
  }
  asm volatile("" ::"v"(codepf));
  STAMP(1, 39);
  bar();
  STAMP(1, 1);
#define CV_A(ee, k) ({ const unsigned a_ = ab[(k) >> 1] + 192 - 64 * (ee) + 512 * (1 - ((k) & 1)); v4u{ldsr<unsigned>(a_), ldsr<unsigned>(a_ + 4), ldsr<unsigned>(a_ + 8), ldsr<unsigned>(a_ + 12)}; })
#define CV_B(ee, k) ldsr<v4u>(bb + 192 - 64 * (ee) + 512 * (k))
  const unsigned slotA = LDS_W + w * 16384, slotB = slotA + 8192;
  v4u wq[8][8];
#define READ_W(dl, f) ldsr<v4u>(rdo4[(((f) >> 1) & 1) + ((f) >> 2) * 2] + (dl) + ((f) & 1) * 512)
  unsigned dso;
#define DMA_W(ck, slot, i) do { \
    if (((i) & 3) == 0) asm volatile("s_add_u32 m0, %1, %2\n\ts_add_u32 %0, %3, %4\n\tbuffer_load_dwordx4 %5, %6, %0 offen lds" \
                 : "=&s"(dso) : "s"(lds0 + (slot)), "i"((i) * 1024), "s"(wsp_s + ((CKMAP(ck) + kr) & 7) * 128), "i"((i) * 32768), "v"(dv[0]), "s"(rwsp) : "scc", "memory"); \
    else asm volatile("buffer_load_dwordx4 %0, %1, %2 offen offset:%3 lds" :: "v"(dv[(i) & 3]), "s"(rwsp), "s"(dso), "i"(((i) & 3) * 1024) : "memory"); } while (0)
  auto dma_sel = [&](auto CK, unsigned slot, int i) __attribute__((always_inline)) {
    constexpr int ck = decltype(CK)::value;
    switch (i) {
      case 0: DMA_W(ck, slot, 0); break; case 1: DMA_W(ck, slot, 1); break; case 2: DMA_W(ck, slot, 2); break; case 3: DMA_W(ck, slot, 3); break;
      case 4: DMA_W(ck, slot, 4); break; case 5: DMA_W(ck, slot, 5); break; case 6: DMA_W(ck, slot, 6); break; default: DMA_W(ck, slot, 7); break;
    }
  };
  v4u cA[2][8], cB[2][8];
  float gs[8];
  v4u gbf, sbf;
  v4u ones4 = {ONES, ONES, ONES, ONES};
  PIN(ones4);
#pragma unroll
  for (int k = 0; k < 2; ++k) { cA[0][k] = CV_A(0, k); cB[0][k] = CV_B(0, k); }
  {
    gbf = *(const v4u*)(smem + LDS_GS + w * 64 + q * 16);
    sbf = *(const v4u*)(smem + LDS_GS + 256 + w * 64 + q * 16);
  }
  SB();
  f32x4 cacc[8];
  v2u ue[4];
  float uf[16];
  unsigned flagv;
  unsigned wro = lds0 + slotA + (lane >> 3) * 128 + (((lane & 7) ^ ((lane >> 4) & 1)) * 16);
  PIN(wro);
  {
    f32x4 cb4[4];
    v4u wo_[2];
    float wl, wh;
    f32x2 cp[4];
#pragma unroll
    for (int t = 0; t < 8; ++t) cacc[t] = f32x4{0.f, 0.f, 0.f, 0.f};
    sfor<144>([&](auto G) __attribute__((always_inline)) {
      constexpr int g = decltype(G)::value, ee = g / 36, j = g % 36;
      if constexpr (ee == 0 && j < 6) { cA[0][2 + j] = CV_A(0, 2 + j); cB[0][2 + j] = CV_B(0, 2 + j); }
      cacc[CVO.pp[j] + CVO.qq[j]] = mfma(cA[ee & 1][CVO.qq[j]], cB[ee & 1][CVO.pp[j]], cacc[CVO.pp[j] + CVO.qq[j]]);
      constexpr int jn = ee == 0 ? j - 20 : (j & 1) ? j >> 1 : -1;
      if constexpr (ee < 3 && jn >= 0 && jn < 16) {
        if constexpr (jn & 1) cB[(ee + 1) & 1][jn >> 1] = CV_B(ee + 1, jn >> 1);
        else cA[(ee + 1) & 1][jn >> 1] = CV_A(ee + 1, jn >> 1);
      }
      if constexpr (ee == 3 && j < 4) ue[j] = *(const v2u*)(smem + LDS_U + cch * 4096 + (eh * 4 + j) * 512 + (c * 16 + q * 4) * 2);
      if constexpr (g >= 6 && g < 30) {
        constexpr int i = (g - 6) / 12, jj = ((g - 6) / 3) & 3, ph = (g - 6) % 3;
        if constexpr (ph == 0) { if constexpr (i == 0) gs[2 * jj] = bflo(gbf[jj]); wl = bflo(wt[i][jj]) * gs[2 * jj]; PIN(wl); }
        if constexpr (ph == 1) { if constexpr (i == 0) gs[2 * jj + 1] = bfhi(gbf[jj]); wh = bfhi(wt[i][jj]) * gs[2 * jj + 1]; PIN(wh); }
        if constexpr (ph == 2) {
          wo_[i][jj] = pk2(wl, wh);
          if constexpr (jj == 3) st128<SC1>(wo_[i], rws, l16 + i * 16384, WS_WP + wp_fr);
          else PIN(wo_[i][jj]);
        }
      }
      if constexpr (g == 4 || g == 5) cb4[2 * (g - 4) + 1] = mfma(sbf, wt[g - 4], f32x4{0.f, 0.f, 0.f, 0.f});
      if constexpr (g == 19 || g == 31) cb4[g == 19 ? 0 : 2] = mfma(ones4, wo_[g == 31], f32x4{0.f, 0.f, 0.f, 0.f});
      if constexpr (g == 33) *(f32x4*)(smem + LDS_S + 4096 + (w * 16 + c) * 16) = f32x4{cb4[0][0], cb4[1][0], cb4[2][0], cb4[3][0]};
      if constexpr (g == 38) bar();
      if constexpr (g == 39) {
#pragma unroll
        for (int k = 0; k < 4; ++k) cp[k] = *(const f32x2*)(smem + LDS_S + 4096 + (k * 16 + c) * 16 + (q & 1) * 8);
      }
      if constexpr (g == 45) {
        float s0 = cp[0][0] + cp[1][0], s1 = cp[2][0] + cp[3][0];
        PIN(s0); PIN(s1);
        s0 += s1;
        PIN(s0);
        float s2 = cp[0][1] + cp[1][1], s3 = cp[2][1] + cp[3][1];
        PIN(s2); PIN(s3);
        st64<SC1>(v2u{__float_as_uint(s0), __float_as_uint(s2 + s3)}, rws, tid < 32 ? q * 512 + c * 8 : OOB, WS_CSB + wpr * 1024 + wkg * 128 + wp_oob);
      }
      if constexpr (g == 36) STAMP(1, 40);
      if constexpr (g == 72) STAMP(1, 41);
      if constexpr (g == FLAG_AT) {
        STAMP(1, 42);
        asm volatile("s_waitcnt vmcnt(7)" ::: "memory");
        __builtin_amdgcn_s_barrier();
        if (tid < 8) hint_add(p.ws, 256 + tid * 16 + (idx & 15));
        STAMP(1, 43);
      }
      if constexpr (g % 5 == 2 && g < 80) DMA_W(1 + g / 40, g < 40 ? slotA : slotB, (g / 5) & 7);
      if constexpr (g == 124) asm volatile("s_waitcnt vmcnt(0)" ::: "memory");
      if constexpr (g == POLL_AT) flagv = __builtin_amdgcn_raw_buffer_load_b32(rws, w == 0 ? c * 128 : OOB, WS_CNT + (256 + xcd * 16) * 128, SC1);
      if constexpr (g >= 88 && g < 104 && (g & 1) == 0) wq[1][(g - 88) >> 1] = READ_W(0, (g - 88) >> 1);
      if constexpr (g >= 108 && g < 140 && (g & 3) == 0) ldsw<v4u>((wro ^ ((((g - 108) >> 2) & 3) * 32)) + ((g - 108) >> 2) * 1024, wr[0][(g - 108) >> 2]);
      if constexpr (g >= 125 && g < 141 && (g & 1) == 1) wq[2][(g - 125) >> 1] = READ_W(8192, (g - 125) >> 1);
      if constexpr (g >= 128 && g < 144) { uf[g - 128] = ((g & 1) ? bfhi : bflo)(ue[(g - 128) >> 2][((g - 128) >> 1) & 1]); PIN(uf[g - 128]); }
      SB();
    });
  }
  STAMP(1, 2);
  unsigned late;
  {
    f32x4* red = (f32x4*)(smem + LDS_X) + w * 256 + lane;
    f32x4 keep[4];
    if (eh) {
#pragma unroll
      for (int t = 0; t < 4; ++t) red[t * 64] = cacc[t];
      SB();
#pragma unroll
      for (int t = 0; t < 4; ++t) { PINA(cacc[4 + t]); keep[t] = cacc[4 + t]; PIN(keep[t]); }
    } else {
#pragma unroll
      for (int t = 0; t < 4; ++t) red[t * 64] = cacc[4 + t];
      SB();
#pragma unroll
      for (int t = 0; t < 4; ++t) { PINA(cacc[t]); keep[t] = cacc[t]; PIN(keep[t]); }
    }
    f32x2 pre[4][2];
#pragma unroll
    for (int t = 0; t < 4; ++t) {
      pre[t][0] = f32x2{uf[4 * t], uf[4 * t + 1]} + lo2(keep[t]);
      pre[t][1] = f32x2{uf[4 * t + 2], uf[4 * t + 3]} + hi2(keep[t]);
      PIN(pre[t][0]); PIN(pre[t][1]);
    }
    if (w == 0) *(unsigned*)(smem + LDS_S) = __builtin_amdgcn_ballot_w64(flagv < 16u) != 0ull;
    bar();
    late = *(const unsigned*)(smem + LDS_S);
    const f32x4* rd = (const f32x4*)(smem + LDS_X) + (w ^ 1) * 256 + lane;
    f32x4 oth[4];
#pragma unroll
    for (int t = 0; t < 4; ++t) oth[t] = rd[t * 64];
#pragma unroll
    for (int t = 0; t < 4; ++t) {
      v2u o;
      o[0] = nomark(pk2(pre[t][0] + lo2(oth[t])));
      o[1] = pk2(pre[t][1] + hi2(oth[t]));
      st64<SC1>(o, rws, xoff + t * 512, 0);
    }
  }
  STAMP(1, 3);
  SB();
  v4u wf[3][8];
  const unsigned rot = uh * 4;
#define LOAD_WP(half, k, i) ld128<SC1>(rws, l16 + ((i) & 3) * 1024, WS_WP + (((half) * 44 + ub_own + (k)) * 16 + kh * 8 + (((i) & 4) ^ rot)) * 1024 + ((k) == 2 ? oob3 : 0u))
#define LOAD_XS(i) ld128<SC1>(rws, wsp_v, WS_X1T + ((i) * 32 + w * 8) * 4096 + mt * 128)
  v4u xs[16];
  unsigned tb[2][4], xdst;
  {
    const int row = 8 * q + ((lane >> 2) & 3);
    const int sg = ((row >> 1) & 1) | (((row >> 3) & 1) << 1);
#pragma unroll
    for (int tu = 0; tu < 4; ++tu) {
      const unsigned t0 = lds0 + LDS_A + kh * 32768 + row * 128 + ((tu ^ (2 * kh) ^ sg) * 32) + (lane & 3) * 8;
      tb[0][tu] = t0 + uh * 16384;
      tb[1][tu] = t0 + (uh ^ 1) * 16384;
    }
    const int rl = w * 8 + (lane >> 3), chn = lane & 7;
    const int sg2 = ((rl >> 1) & 1) | (((rl >> 3) & 1) << 1);
    xdst = lds0 + LDS_A + rl * 128 + (((chn >> 1) ^ sg2) * 32) + (chn & 1) * 16;
  }
  SB();
  unsigned z2o[3];
#pragma unroll
  for (int k = 0; k < 3; ++k) { z2o[k] = l16 + WS_Z2 + ((ub_own + k) * 64 + mt * 2 + kh) * 1024 + (k == 2 ? oob3 : 0u); }
  v2u cz[2][3];
  f32x2 csb[2][3];
  float gbv, c1v[2];
  unsigned gq[4];
  unsigned bspq[2];
#define LOAD_CZ(hf, kc) ld64<SC1>(rws, lane * 8, WS_CSB + ((hf) * 44 + ub_own + (kc)) * 512 + ((kc) == 2 ? oob3 : 0u))
  {
    STAMP(1, 20);
    if (__builtin_amdgcn_readfirstlane(late)) {
      if (w == 0) hint_wait(rws, c * 128, WS_CNT + (256 + xcd * 16) * 128, 16u);
      __builtin_amdgcn_s_barrier();
    }
    MEMFENCE();
    STAMP(1, 21);
#pragma unroll
    for (int i = 0; i < 3; ++i)
#pragma unroll
      for (int k = 0; k < 3; ++k) wf[k][i] = LOAD_WP(1, k, i);
    SB();
#pragma unroll
  for (int f = 0; f < 8; ++f) wq[0][f] = READ_W(0, f);
#pragma unroll
  for (int f = 0; f < 24; ++f) PINA(wq[f >> 3][f & 7]);
#pragma unroll
    for (int tu = 0; tu < 8; ++tu) PIN(tb[tu >> 2][tu & 3]);
    PIN(xdst);
#pragma unroll
    for (int k = 0; k < 3; ++k) PIN(z2o[k]);
    SB();
#pragma unroll
    for (int i = 3; i < WF_EARLY; ++i)
#pragma unroll
      for (int k = 0; k < 3; ++k) wf[k][i] = LOAD_WP(1, k, i);
    SB();
#pragma unroll
    for (int i = 0; i < 16; ++i) xs[i] = LOAD_XS(i);
    SB();
    STAMP(1, 44);
    unsigned m = 0;
#pragma unroll
    for (int i = 0; i < 16; ++i) {
      m = macc(m, xs[i]); ldsw<v4u>(xdst + i * 4096, xs[i]);
      if (WF_EARLY * 3 + i < 24) wf[(WF_EARLY * 3 + i) % 3][(WF_EARLY * 3 + i) / 3] = LOAD_WP(1, (WF_EARLY * 3 + i) % 3, (WF_EARLY * 3 + i) / 3);
      SB();
    }
    PIN(m);
    STAMP(1, 45);
    RETRY_BEGIN(m)
#pragma unroll
      for (int i = 0; i < 16; ++i) xs[i] = LOAD_XS(i);
      SB();
#pragma unroll
      for (int i = 0; i < 16; ++i) { m = macc(m, xs[i]); ldsw<v4u>(xdst + i * 4096, xs[i]); }
    RETRY_END(m)
  }
  STAMP(1, 4);
  bar();
  STAMP(1, 5);
#define AHALF(tu, i, h) trread_a(tb[(i) >> 2][tu] + ((i) & 3) * 4096 + (h) * 512)
  f32x4 acc[3][4];
  v4u wg[3][8];
  v2u ah[2][4][2];
  float* st = (float*)(smem + LDS_S);
  float* fin = st + 256 + w * 64;
  f32x4* xch = (f32x4*)(smem + LDS_X);
  f32x4 r4[2], tm[3][2];
  {
    f32x4 s1[4], s2[4], m4[2];
    v2u ar[3][2][2];
    float pa[4], pb[4];
    constexpr int ORD[20] = {0, 1, 2, 3, 4, 5, 6, 7, 16, 17, 18, 19, 8, 9, 10, 11, 12, 13, 14, 15};
    int dgo[2], rdo[2];
    dgo[0] = w * 64 + kh * 32 + c; dgo[1] = w * 64 + (kh ^ 1) * 32 + c;
    rdo[0] = (kh * 32 + (lane & 31)) * 4; rdo[1] = rdo[0] * 4 + (lane & 3) * 4;
    PIN(dgo[0]); PIN(dgo[1]); PIN(rdo[0]); PIN(rdo[1]);
    const bool diag = (c >> 2) == q;
#define GFRAG(gi, t, h) AHALF(((gi) < 8 ? 2 : 0) + (t), (gi) < 16 ? (gi) & 7 : (gi) - 16, h)
#pragma unroll
    for (int n = 0; n < 2; ++n)
#pragma unroll
      for (int e = 0; e < 4; ++e) ar[n][e >> 1][e & 1] = GFRAG(ORD[n], e >> 1, e & 1);
    SB();
    sfor<20 * 6>([&](auto G) __attribute__((always_inline)) {
      constexpr int n = decltype(G)::value / 6, j = decltype(G)::value % 6, gi = ORD[n], t = j / 3, k = j % 3;
      constexpr int tu = (gi < 8 ? 2 : 0) + t, i = gi < 16 ? gi & 7 : gi - 16;
      const v4u a = {ar[n % 3][t][0][0], ar[n % 3][t][0][1], ar[n % 3][t][1][0], ar[n % 3][t][1][1]};
      const f32x4 z4 = {0.f, 0.f, 0.f, 0.f};
      if constexpr (gi < 16) acc[k][tu] = mfma(a, wf[k][i], gi == 0 ? z4 : acc[k][tu]);
      if constexpr ((gi >= 4 && gi < 8) || gi >= 16) {
        if constexpr (k == 0) s1[tu] = mfma(ones4, a, (gi == 4 || gi == 16) ? z4 : s1[tu]);
        if constexpr (k == 1) s2[tu] = mfma(a, a, (gi == 4 || gi == 16) ? z4 : s2[tu]);
      }
      if constexpr (j < 4 && n + 2 < 20) ar[(n + 2) % 3][j >> 1][j & 1] = GFRAG(ORD[n + 2], j >> 1, j & 1);
      if constexpr (n >= 18 && j < 4) ah[0][(n - 18) * 2 + (j >> 1)][j & 1] = AHALF((n - 18) * 2 + (j >> 1), 0, j & 1);
      if constexpr (n < 4 && (j == 1 || j == 4)) DMA_W(3, slotB, n * 2 + (j == 4));
      if constexpr (n == 4) cz[j / 3][j % 3] = LOAD_CZ(j / 3, j % 3);
      if constexpr (n == 5 && j < 4) gq[j] = __builtin_amdgcn_raw_buffer_load_b32(rws, lane * 8, WS_MODG + (j * 3072 + 5 * 512 + nt * 64) * 8, SC1);
      if constexpr (n == 5 && j >= 4) bspq[j - 4] = p.bsp[mt * 64 + perm(2 * kh + j - 4, c)];
      if constexpr (n == 8 && j == 1) USE(bspq[1]);
      if constexpr (n >= 8 && n < 12 && (j == 2 || j == 5)) DMA_W(4, slotA, (n - 8) * 2 + (j == 5));
      if constexpr (n >= 12 && j == 4) DMA_W(5, slotB, n - 12);
      if constexpr ((n == 8 || n == 9) && (j & 1) == 0) {
        constexpr int x = (n - 8) * 3 + (j >> 1);
        xch[(w * 6 + (x % 3) * 2 + x / 3) * 64 + lane] = acc[x % 3][2 + x / 3];
      }
      if constexpr (n == 10 && j == 0) {
#pragma unroll
    for (int k = 0; k < 3; ++k) {
      const float y = xrow_sum4(__uint_as_float(cz[1][k][0]), __uint_as_float(cz[1][k][1]), __uint_as_float(cz[1][k][0]), __uint_as_float(cz[1][k][1]));
      auto sw = __builtin_amdgcn_permlane16_swap(__float_as_uint(y), __float_as_uint(y), false, false);
      csb[1][k] = f32x2{__uint_as_float(sw[0]), __uint_as_float(sw[1])};
      PIN(csb[1][k]);
    }
    c1v[0] = xrow_sum4(__uint_as_float(cz[0][0][0]), __uint_as_float(cz[0][0][1]), __uint_as_float(cz[0][1][0]), __uint_as_float(cz[0][1][1]));
    c1v[1] = xrow_sum4(__uint_as_float(cz[0][2][0]), __uint_as_float(cz[0][2][1]), __uint_as_float(cz[0][2][0]), __uint_as_float(cz[0][2][1]));
    PIN(c1v[0]); PIN(c1v[1]);
    gbv = rbf((__uint_as_float(gq[0]) + __uint_as_float(gq[1])) + (__uint_as_float(gq[2]) + __uint_as_float(gq[3])));
    PIN(gbv);
      }
      if constexpr ((n == 8 || n == 9) && j >= 2) wq[3][(n - 8) * 4 + j - 2] = READ_W(8192, (n - 8) * 4 + j - 2);
      if constexpr (n == 10 && j == 5) {
#pragma unroll
        for (int f = 0; f < 8; ++f) PINA(wq[3][f]);
      }
      if constexpr (n == 10 && j == 4) bar();
      if constexpr (n == 11) acc[j % 3][j / 3] = xch[((w ^ 1) * 6 + (j % 3) * 2 + j / 3) * 64 + lane];
      if constexpr (n == 12 && j == 3) {
        float s1v[4];
#pragma unroll
        for (int x = 0; x < 4; ++x) { s1v[x] = s1[x][0]; PIN(s1v[x]); }
        if (diag) {
#pragma unroll
          for (int x = 0; x < 4; ++x) {
            *(float*)(smem + LDS_S + dgo[x >> 1] * 4 + (x & 1) * 64) = s1v[x];
            *(f32x4*)(smem + LDS_S + 2048 + dgo[x >> 1] * 16 + (x & 1) * 256) = s2[x];
          }
        }
      }
      if constexpr (n == 13 && j == 5) bar();
      if constexpr (n == 14 && j < 4) {
        pa[j] = *(const float*)(smem + LDS_S + rdo[0] + j * 256); pb[j] = *(const float*)(smem + LDS_S + 2048 + rdo[1] + j * 1024);
      }
      if constexpr (n == 15 && j == 1) { pa[0] += pa[1]; pa[2] += pa[3]; PIN(pa[0]); PIN(pa[2]); }
      if constexpr (n == 15 && j == 2) { pb[0] += pb[1]; pb[2] += pb[3]; PIN(pb[0]); PIN(pb[2]); }
      if constexpr (n == 15 && j == 3) { pa[0] = (pa[0] + pa[2]) * (1.0f / D); PIN(pa[0]); }
      if constexpr (n == 15 && j == 4) { pb[0] = (pb[0] + pb[2]) * (1.0f / D); PIN(pb[0]); }
      if constexpr (n == 15 && j == 5) { pb[0] = fmaxf(__builtin_fmaf(-pa[0], pa[0], pb[0]), 0.f); PIN(pb[0]); }
      if constexpr (n == 16 && j == 0) { pb[0] = __builtin_amdgcn_rsqf(pb[0] + EPS); PIN(pb[0]); }
      if constexpr (n == 16 && j == 1) fin[lane] = lane < 32 ? pb[0] : -pb[0] * pa[0];
      if constexpr (n == 16 && (j == 2 || j == 3)) { m4[j - 2] = *(const f32x4*)(fin + 32 + (j - 2) * 16 + q * 4); r4[j - 2] = *(const f32x4*)(fin + (j - 2) * 16 + q * 4); }
      if constexpr (n >= 18) {
        constexpr int x = (n - 18) * 6 + j, kk = x >> 2, tt = (x >> 1) & 1, h = (x & 1) * 2;
        float t0 = __builtin_fmaf(m4[tt][h], csb[1][kk][0], csb[1][kk][1]);
        PIN(t0);
        float t1 = __builtin_fmaf(m4[tt][h + 1], csb[1][kk][0], csb[1][kk][1]);
        PIN(t1);
        tm[kk][tt][h] = t0; tm[kk][tt][h + 1] = t1;
      }
      if constexpr (n >= 12 && n < 15 && j == 5) st128<SC1>(mk, rws, z2o[n - 12], 0);
      if constexpr (n == 7 && j == 5) STAMP(1, 46);
      if constexpr (n == 11 && j == 5) STAMP(1, 47);
      SB();
    });
  }
#pragma unroll
  for (int k = 0; k < 6; ++k) PINA(acc[k >> 1][k & 1]);
  STAMP(1, 6);
  f32x4 z2v[3][2];
  {
    f32x4 rs[2], rq[2];
#pragma unroll
    for (int t = 0; t < 2; ++t) {
#pragma unroll
      for (int k = 0; k < 3; ++k) {
        const f32x4 a = acc[k][t];
        const f32x2 zl = gelu2(fma2(lo2(a), lo2(r4[t]), lo2(tm[k][t])));
        const f32x2 zh = gelu2(fma2(hi2(a), hi2(r4[t]), hi2(tm[k][t])));
        z2v[k][t] = f32x4{zl[0], zl[1], zh[0], zh[1]};
        if (k == 0) { rs[t] = z2v[k][t]; rq[t] = z2v[k][t] * z2v[k][t]; }
        else { rs[t] += z2v[k][t]; rq[t] += z2v[k][t] * z2v[k][t]; }
        if (((t * 3 + k) & 1) == 0) {
#pragma unroll
          for (int f = (t * 3 + k) * 4; f < (t * 3 + k) * 4 + 8; ++f) wg[f % 3][f / 3] = LOAD_WP(0, f % 3, f / 3);
        } else {
          PIN(z2v[k][t]);
          SB();
        }
      }
    }
    STAMP(1, 50);
    float v8[8], v4[4], v2[2];
    const bool b3 = c & 8, b2 = c & 4, b1 = c & 2, b0 = c & 1;
#pragma unroll
    for (int r_ = 0; r_ < 4; ++r_) {
      const float a0 = rs[0][r_] + dpp<0x128>(rs[0][r_]), a1 = rs[1][r_] + dpp<0x128>(rs[1][r_]);
      const float q0 = rq[0][r_] + dpp<0x128>(rq[0][r_]), q1 = rq[1][r_] + dpp<0x128>(rq[1][r_]);
      v8[2 * r_] = b3 ? a1 : a0; v8[2 * r_ + 1] = b3 ? q1 : q0;
    }
#pragma unroll
    for (int j = 0; j < 4; ++j) {
      const float x0 = v8[j] + dpp<0x141>(v8[j]), x1 = v8[4 + j] + dpp<0x141>(v8[4 + j]);
      v4[j] = b2 ? x1 : x0;
    }
#pragma unroll
    for (int j = 0; j < 2; ++j) {
      const float x0 = v4[j] + dpp<0x4E>(v4[j]), x1 = v4[2 + j] + dpp<0x4E>(v4[2 + j]);
      v2[j] = b1 ? x1 : x0;
    }
    const float x0 = v2[0] + dpp<0xB1>(v2[0]), x1 = v2[1] + dpp<0xB1>(v2[1]);
    const float outv = b0 ? x1 : x0;
    __builtin_amdgcn_raw_buffer_store_b32(nomark(__float_as_uint(outv)), rws, lane * 4, WS_ST2 + ((mt * 2 + kh) * 16 + nt * 2 + uh) * 256, SC1);
  }
  STAMP(1, 7);
  SB();
#pragma unroll
  for (int tu = 0; tu < 4; ++tu)
#pragma unroll
    for (int k = 0; k < 3; ++k) acc[k][tu] = f32x4{0.f, 0.f, 0.f, 0.f};
  const int k30 = w * 6 - (w >> 1) * (w & 1);
  const unsigned oob6 = (unsigned)(w >> 1) << 30;
  v2u x1h[2][2];
  int x1o;
  {
    const int row = 8 * q + ((lane >> 2) & 3);
    const int sg = ((row >> 1) & 1) | (((row >> 3) & 1) << 1);
    x1o = LDS_A + nt * 8192 + row * 128 + ((w ^ sg) * 32) + (lane & 3) * 8;
  }
  v4u wo[6][4];
#define LOAD_WO(f) wo[(f) >> 2][(f) & 3] = ld128<SC1>(rws, l16 + (((f) >> 2) & 3) * 1024, WS_WOT + ((nt * 4 + ((f) & 3)) * 22 + k30 + (((f) >> 2) & 4)) * 1024 + ((f) >= 20 ? oob6 : 0u))
  f32x4 pt[4];
  float* c1s = st + 768 + w * 96;
#define LOAD_PT(i) asf(ld128<SC1>(rws, l16 + (i) * 1024, WS_ST2 + (mt * 2 + kh) * 4096))
  SB();
#pragma unroll
  for (int i = 0; i < 8; ++i) {
#pragma unroll
    for (int j = 0; j < 12; ++j) {
      const int tu = j / 3, k = j % 3;
      const v4u a = {ah[i & 1][tu][0][0], ah[i & 1][tu][0][1], ah[i & 1][tu][1][0], ah[i & 1][tu][1][1]};
      acc[k][tu] = mfma(wg[k][i], a, acc[k][tu]);
      if (j < 8 && i < 7) ah[(i + 1) & 1][j >> 1][j & 1] = AHALF(j >> 1, i + 1, j & 1);
      if (j < 4 && i == 7) x1h[j >> 1][j & 1] = trread(smem + x1o + (j >> 1) * 4096 + (j & 1) * 512);
      if (i == 0 && j == 0) asm volatile("s_waitcnt vmcnt(24)" ::: "memory");
      if (i < 2 && j >= 4) wq[4 + i][j - 4] = READ_W(i * 8192, j - 4);
      if (i == 3 && j == 0) {
#pragma unroll
        for (int f = 0; f < 16; ++f) PINA(wq[4 + (f >> 3)][f & 7]);
      }
      if (i == 4 && j == 0) { USE(wg[0][7]); USE(wg[1][7]); USE(wg[2][7]); }
      if ((i == 2 || i == 3) && j % 3 == 2) dma_sel(IC<6>{}, slotA, (i - 2) * 4 + j / 3);
      if ((i == 4 || i == 5) && j % 3 == 2) dma_sel(IC<7>{}, slotB, (i - 4) * 4 + j / 3);
      if (j >= 8 && i == 6) pt[j - 8] = LOAD_PT(j - 8);
      if (i == 7 && j >= 8) LOAD_WO(j - 8);
      if (i == 5 && j == 0) {
        asm volatile("s_waitcnt vmcnt(30)" ::: "memory");
        __builtin_amdgcn_s_barrier();
      }
      if (i == 5 && j == 1) st[1408 + lane] = gbv;
      if (i == 5 && j == 9) c1s[lane] = c1v[0];
      if (i == 5 && j == 10) c1s[64 + (lane & 31)] = c1v[1];
      SB();
    }
  }
  STAMP(1, 8);
#pragma unroll
  for (int k = 0; k < 3; ++k)
#pragma unroll
    for (int t = 0; t < 2; ++t) xch[(w * 6 + k * 2 + t) * 64 + lane] = acc[k][2 + t];
  SB();
  {
    unsigned m = 0;
#pragma unroll
    for (int i = 0; i < 4; ++i) m = macc4(m, __builtin_bit_cast(v4u, pt[i]));
    RETRY_BEGIN(m)
#pragma unroll
      for (int i = 0; i < 4; ++i) { pt[i] = LOAD_PT(i); m = macc4(m, __builtin_bit_cast(v4u, pt[i])); }
    RETRY_END(m)
    if (tid < 4) hint_add(p.ws, 64 + (nt * 4 + (mt >> 3)) * 4 + tid);
    const f32x4 sx = (pt[0] + pt[1]) + (pt[2] + pt[3]);
    const float tv = xrow_sum4(sx[0], sx[2], sx[1], sx[3]);
    auto sw = __builtin_amdgcn_permlane32_swap(__float_as_uint(tv), __float_as_uint(tv), false, false);
    const float mm = __uint_as_float(sw[0]) * (1.0f / FH);
    const float r = __builtin_amdgcn_rsqf(fmaxf(__uint_as_float(sw[1]) * (1.0f / FH) - mm * mm, 0.f) + EPS);
    float* f2 = st + 512 + w * 64;
    f2[(q >> 1) * 32 + 2 * c + (q & 1)] = (q >> 1) ? -r * mm : r;
    f32x4 r4[2], m4[2];
#pragma unroll
    for (int t = 0; t < 2; ++t) { r4[t] = *(const f32x4*)(f2 + q * 8 + t * 4); m4[t] = *(const f32x4*)(f2 + 32 + q * 8 + t * 4); }
#pragma unroll
    for (int k = 0; k < 3; ++k) {
      v4u o;
#pragma unroll
      for (int t = 0; t < 2; ++t) {
        o[2 * t] = nomark(pk2(fma2(lo2(z2v[k][t]), lo2(r4[t]), lo2(m4[t]))));
        o[2 * t + 1] = pk2(fma2(hi2(z2v[k][t]), hi2(r4[t]), hi2(m4[t])));
      }
      st128<SC1>(o, rws, z2o[k], 0);
    }
  }
  STAMP(1, 9);
  SB();
  f32x4 z1f[3][2];
  constexpr int RD = 12;
  v4u zf[RD];
#define LOAD_Z(n) ld128<SC1>(rws, zv[((n) >> 1) & 7] + ((n) & 1) * 1024, WS_Z2 + ((((n) < 48 ? ub_oth : ub_own) + ((n) >> 4) % 3) * 64 + w * 16) * 1024 + (((n) >> 4) % 3 == 2 ? oob3 : 0u))
  unsigned zv[8];
#pragma unroll
  for (int i = 0; i < 8; ++i) { zv[i] = l16 + ((CKMAP(i) + kr) & 7) * 2048; PIN(zv[i]); }
  {
    bar();
    STAMP(1, 34);
#pragma unroll
    for (int k = 0; k < 6; ++k) PINA(acc[k >> 1][k & 1]);
    f32x4 oth[3][2], cs1[3], bs1[3];
    float r1[2], m1[2];
#pragma unroll
    for (int t = 0; t < 2; ++t) { r1[t] = fin[t * 16 + c]; m1[t] = fin[32 + t * 16 + c]; }
#pragma unroll
    for (int k = 0; k < 3; ++k) {
      cs1[k] = *(const f32x4*)(c1s + k * 32 + q * 4); bs1[k] = *(const f32x4*)(c1s + k * 32 + 16 + q * 4);
#pragma unroll
      for (int t = 0; t < 2; ++t) oth[k][t] = xch[((w ^ 1) * 6 + k * 2 + t) * 64 + lane];
    }
#pragma unroll
    for (int i = 4; i < WO0; ++i) LOAD_WO(i);
    SB();
    f32x2 z1a[6][2];
#pragma unroll
    for (int i = 0; i < 6; ++i) {
      const int k = i >> 1, t = i & 1;
      const f32x4 a = acc[k][t] + oth[k][t];
      z1a[i][0] = fma2(lo2(a), bc2(r1[t]), fma2(lo2(cs1[k]), bc2(m1[t]), lo2(bs1[k])));
      z1a[i][1] = fma2(hi2(a), bc2(r1[t]), fma2(hi2(cs1[k]), bc2(m1[t]), hi2(bs1[k])));
    }
#pragma unroll
    for (int i = 0; i < GELU_PRE; ++i) {
      const f32x2 lo = gelu2(z1a[i][0]), hi = gelu2(z1a[i][1]);
      z1f[i >> 1][i & 1] = f32x4{lo[0], lo[1], hi[0], hi[1]};
      PIN(z1f[i >> 1][i & 1]);
    }
#pragma unroll
    for (int i = GELU_PRE; i < 6; ++i) { PIN(z1a[i][0]); PIN(z1a[i][1]); }
    SB();
    STAMP(1, 10);
    hint_wait(rws, 0u, WS_CNT + (64 + (nt * 4 + w) * 4 + (xcd >> 1)) * 128, 8u);
    STAMP(1, 22);
#pragma unroll
    for (int n = 0; n < RD; ++n) zf[n] = LOAD_Z(n);
    SB();
#pragma unroll
    for (int i = GELU_PRE; i < 6; ++i) {
      const f32x2 lo = gelu2(z1a[i][0]), hi = gelu2(z1a[i][1]);
      z1f[i >> 1][i & 1] = f32x4{lo[0], lo[1], hi[0], hi[1]};
      PIN(z1f[i >> 1][i & 1]);
    }
  }
  f32x4* xb = (f32x4*)(smem + LDS_A) + lane;
  f32x4* yb = (f32x4*)(smem + LDS_A + 49152) + lane;
  f32x4 ac2[2][4], ini[4], yo[2];
  const float bs0 = bf2f(bspq[0]), bs1_ = bf2f(bspq[1]);
  const f32x4 bq0 = {bs0, bs0, bs0, bs0}, bq1 = {bs1_, bs1_, bs1_, bs1_}, z4 = {0.f, 0.f, 0.f, 0.f};
  unsigned voff[2];
#pragma unroll
  for (int t = 0; t < 2; ++t) voff[t] = WS_V + (mt * 4 + 2 * kh + t) * 22 * 1024;
  float ev[4];
  unsigned eo0;
  SB();
  STAMP(1, 11);
  asm volatile("s_waitcnt vmcnt(30)" ::: "memory");
  auto v_store = [&](int k, int t, unsigned o0, unsigned o1) __attribute__((always_inline)) {
    const int uu = ub_own + k;
    st64<SC1>(v2u{o0, o1}, rws, l16, voff[t] + (uu >> 1) * 1024 + (uu & 1) * 8 + (k == 2 ? oob3 : 0u));
  };
  sfor<6>([&](auto P) __attribute__((always_inline)) {
    constexpr int ps = decltype(P)::value, cur = ps & 1, prv = cur ^ 1;
    unsigned zm = 0;
    sfor<64>([&](auto G) __attribute__((always_inline)) {
      constexpr int g = decltype(G)::value, ks = g >> 2, tu = g & 3, ck = ks >> 1, k2 = ks & 1, n = ps * 16 + ks;
      const f32x4 cin = ks > 0 ? ac2[cur][tu] : ps >= 3 ? ini[tu] : tu == 0 ? bq0 : tu == 1 ? bq1 : z4;
      ac2[cur][tu] = mfma(zf[n % RD], wq[ck][k2 * 4 + tu], cin);
      if constexpr (tu == 1) { zm = macc(zm, zf[n % RD]); PIN(zm); }
      if constexpr (tu == 3) {
        if constexpr (n + RD < 96) zf[n % RD] = LOAD_Z(n + RD);
        else if constexpr (WO0 + n + RD - 96 < 24) LOAD_WO(WO0 + n + RD - 96);
      }
      if constexpr (ps == 0 && g < 32 && (g & 1) == 0) wq[6 + (g >> 4)][(g >> 1) & 7] = READ_W((g >> 4) * 8192, (g >> 1) & 7);
      if constexpr (ps >= 1 && ps <= 3 && g >= 2 && g < 18 && (g & 3) == 2) xb[((w * 3 + ps - 1) * 4 + (g >> 2)) * 64] = ac2[prv][g >> 2];
      if constexpr (ps >= 4 && (g == 2 || g == 6)) yb[((w * 3 + ps - 4) * 2 + (g >> 2)) * 64] = ac2[prv][2 + (g >> 2)];
      if constexpr (ps >= 1 && g == (ps >= 4 ? 12 : 24)) {
        if constexpr (ps == 4) asm volatile("s_waitcnt vmcnt(20)" ::: "memory");
        bar();
      }
      if constexpr (ps >= 4 && (g == 13 || g == 14)) yo[g - 13] = yb[(((w ^ 1) * 3 + ps - 4) * 2 + g - 13) * 64];
      if constexpr (ps >= 4 && g >= 22 && g < 42) {
        constexpr int t = (g - 22) / 10, ph = (g - 22) % 10;
        if constexpr (ph < 4) { ev[ph] = ac2[prv][t][ph] + yo[t][ph]; PIN(ev[ph]); }
        else if constexpr (ph < 8) { ev[ph - 4] *= z1f[ps - 4][t][ph - 4]; PIN(ev[ph - 4]); }
        else if constexpr (ph == 8) { eo0 = nomark(pk2(ev[0], ev[1])); PIN(eo0); }
        else v_store(ps - 4, t, eo0, pk2(ev[2], ev[3]));
      }
      if constexpr (ps >= 2 && ps <= 4 && g >= 28 && g < 44 && (g & 3) == 0) ini[(g - 28) >> 2] = xb[(((w ^ 2) * 3 + ps - 2) * 4 + ((g - 28) >> 2)) * 64];
      if constexpr (ps == 0 && (g == 21 || g == 37 || g == 53)) st128<SC1>(mk, rws, l16, WS_V + ((mt * 4 + w) * 22 + pr0 + (g - 21) / 16) * 1024 + (g == 53 ? oob3 : 0u));
      if constexpr (ps == 5 && g == HINT4_G) { if (tid < 2) hint_add(p.ws, 192 + mt * 2 + tid); }
      if constexpr (g == 0 && ps >= 1 && ps < 5) STAMP(1, 55 + ps);
      SB();
    });
    if (__builtin_expect(any_mark(zm), 0)) {
#pragma unroll
      for (int tu = 0; tu < 4; ++tu) ac2[cur][tu] = ps >= 3 ? xb[(((w ^ 2) * 3 + ps - 3) * 4 + tu) * 64] : tu == 0 ? bq0 : tu == 1 ? bq1 : z4;
      sfor<16>([&](auto K) __attribute__((always_inline)) {
        constexpr int ks = decltype(K)::value, ck = ks >> 1, k2 = ks & 1;
        v4u zz;
        unsigned sp = 0;
        do { MEMFENCE(); zz = LOAD_Z(ps * 16 + ks); } while (any_mark(macc(0u, zz)) & (++sp <= SPIN_CAP));
#pragma unroll
        for (int tu = 0; tu < 4; ++tu) ac2[cur][tu] = mfma(zz, wq[ck][k2 * 4 + tu], ac2[cur][tu]);
      });
    }
  });
  STAMP(1, 12);
  {
#pragma unroll
    for (int t = 0; t < 2; ++t) yb[((w * 3 + 2) * 2 + t) * 64] = ac2[1][2 + t];
    PIN(ac2[1][0]); PIN(ac2[1][1]);
    bar();
    STAMP(1, 52);
#pragma unroll
    for (int t = 0; t < 2; ++t) yo[t] = yb[(((w ^ 1) * 3 + 2) * 2 + t) * 64];
#pragma unroll
    for (int t = 0; t < 2; ++t) {
      const f32x4 g = (ac2[1][t] + yo[t]) * z1f[2][t];
      v_store(2, t, nomark(pk2(lo2(g))), pk2(hi2(g)));
    }
  }
  STAMP(1, 13);
  SB();
  f32x4 gb4[4];
  f32x2 x1f[2][4];
#pragma unroll
  for (int i = 0; i < 4; ++i) gb4[i] = *(const f32x4*)(st + 1408 + (i >> 1) * 32 + q * 8 + (i & 1) * 4);
#pragma unroll
  for (int i = 0; i < 8; ++i) { x1f[i >> 2][i & 3] = unpk(x1h[i >> 2][(i >> 1) & 1][i & 1]); PIN(x1f[i >> 2][i & 3]); }
  STAMP(1, 53);
  if (w == 0) hint_wait(rws, 0u, WS_CNT + (192 + mt * 2 + (xcd & 1)) * 128, 8u);
  __builtin_amdgcn_s_barrier();
  MEMFENCE();
  STAMP(1, 23);
  f32x4 a3[4][4];
  {
    unsigned spins = 0;
    for (;;) {
      MEMFENCE();
      v4u vs[6][4];
#define LOAD_V(f) vs[(f) >> 2][(f) & 3] = ld128<SC1>(rws, l16 + (((f) >> 2) & 3) * 1024, WS_V + ((mt * 4 + (((f) & 3) ^ w)) * 22 + k30 + (((f) >> 2) & 4)) * 1024 + ((f) >= 20 ? oob6 : 0u))
#pragma unroll
      for (int f = 0; f < 12; ++f) LOAD_V(f);
      SB();
      unsigned m = 0;
      sfor<96>([&](auto G) __attribute__((always_inline)) {
        constexpr int g = decltype(G)::value, ks = g >> 4, tu = (g >> 2) & 3, e = g & 3;
        a3[e][tu] = mfma(wo[ks][e], vs[ks][tu], ks == 0 ? f32x4{0.f, 0.f, 0.f, 0.f} : a3[e][tu]);
        if constexpr (e == 0 && 12 + (g >> 2) < 24) LOAD_V(12 + (g >> 2));
        if constexpr (e == 1) { m = macc(m, vs[ks][tu]); PIN(m); }
        SB();
      });
      if (!(any_mark(m) & (++spins <= SPIN_CAP))) break;
    }
  }
  STAMP(1, 14);
  {
    f32x4* red = (f32x4*)(smem + LDS_W);
#pragma unroll
    for (int tu = 1; tu < 4; ++tu)
#pragma unroll
      for (int e = 0; e < 4; ++e) red[((w * 3 + tu - 1) * 4 + e) * 64 + lane] = a3[e][tu];
#pragma unroll
    for (int e = 0; e < 4; ++e) PIN(a3[e][0]);
    bar();
    STAMP(1, 54);
    f32x4 o3[4][3];
#pragma unroll
    for (int e = 0; e < 4; ++e)
#pragma unroll
      for (int rel = 1; rel < 4; ++rel) o3[e][rel - 1] = red[(((w ^ rel) * 3 + rel - 1) * 4 + e) * 64 + lane];
    const rsrc_t rout = mkrs(p.out, L * D * 2);
    SB();
#pragma unroll
    for (int pp = 0; pp < 2; ++pp) {
      v4u o;
#pragma unroll
      for (int h = 0; h < 2; ++h) {
        const int e = pp * 2 + h;
        const f32x4 y = (a3[e][0] + o3[e][0]) + (o3[e][1] + o3[e][2]);
        o[2 * h] = pk2(fma2(lo2(y), lo2(gb4[e]), x1f[pp][2 * h]));
        o[2 * h + 1] = pk2(fma2(hi2(y), hi2(gb4[e]), x1f[pp][2 * h + 1]));
      }
      st128<0>(o, rout, perm(w, c) * 1024 + q * 16 + pp * 64, mt * 64 * 1024 + nt * 128);
    }
  }
  if (b == 0 && tid == 0) *(unsigned*)(p.ws + WS_EPOCH) = tag_s;
  STAMP(1, 15);
  STAMP_FLUSH(1);
  asm volatile("adaln_code_end:");
}

extern "C" void kernel_launch(void* const* d_in, const int* in_sizes, int n_in, void* d_out, int out_size, void* d_ws,
                              size_t ws_size, hipStream_t stream) {
  Params p;
  p.u = (const u16*)d_in[0];
  p.lar = (const u16*)d_in[1];
  p.aim = (const u16*)d_in[2];
  p.cre = (const u16*)d_in[3];
  p.cim = (const u16*)d_in[4];
  p.ldt = (const u16*)d_in[5];
  p.dsk = (const u16*)d_in[6];
  p.win = (const u16*)d_in[8];
  p.wsp = (const u16*)d_in[9];
  p.bsp = (const u16*)d_in[10];
  p.wout = (const u16*)d_in[11];
  p.c = (const u16*)d_in[12];
  p.wmod = (const u16*)d_in[13];
  p.out = (u16*)d_out;
  p.ws = (unsigned char*)d_ws;
  (void)hipFuncSetAttribute((const void*)adaln_mega, hipFuncAttributeMaxDynamicSharedMemorySize, MEGA_LDS);
  adaln_mega<<<256, 256, MEGA_LDS, stream>>>(p);
}
```

```cpp
#define LATE_SLEEP 10
#include <hip/hip_runtime.h>
#include <hip/hip_bf16.h>
#include <stdint.h>
#include <utility>

#define LAS __attribute__((address_space(3)))
typedef unsigned v4u __attribute__((ext_vector_type(4)));
typedef unsigned v2u __attribute__((ext_vector_type(2)));
typedef float f32x4 __attribute__((ext_vector_type(4)));
typedef float f32x2 __attribute__((ext_vector_type(2)));
typedef short bf16x8 __attribute__((ext_vector_type(8)));
typedef _Float16 f16x8 __attribute__((ext_vector_type(8)));
typedef _Float16 f16x2 __attribute__((ext_vector_type(2)));
typedef __bf16 bf16x2_t __attribute__((ext_vector_type(2)));
typedef unsigned short u16;
typedef __amdgpu_buffer_rsrc_t rsrc_t;

#define STAMP_DECL do { } while (0)
#define STAMP(k, i) do { } while (0)
#define STAMP_FLUSH(k) do { } while (0)

#define OPT_A 1
#define OPT_B 1
#define OPT_C 2
#define OPT_D 1
#define OPT_E 2
#define OPT_F 1
constexpr int L = 2048, D = 512, NM = 128, FH = 688, FF = 1376;
constexpr float EPS = 1e-5f;
constexpr int SC1 = 16;
constexpr unsigned OOB = 0x40000000u;
constexpr unsigned SPIN_CAP = 1u << 14;

constexpr unsigned WS_EPOCH = 0;
constexpr unsigned WS_CNT = 4096;
constexpr unsigned WS_SR = WS_CNT + 512 * 128;
constexpr unsigned WS_SB = WS_SR + 2048 * 8;
constexpr unsigned WS_MODG = WS_SB + 2048 * 8;
constexpr unsigned WS_CSB = WS_MODG + 3072 * 32;
constexpr unsigned WS_ST2 = WS_CSB + 88 * 16 * 32;
constexpr unsigned WS_UG = 1u << 20;
static_assert(WS_ST2 + 32 * 2 * 16 * 32 * 8 <= WS_UG, "small buffers");
constexpr unsigned WS_X1T = WS_UG + 256 * 2048 * 8;
constexpr unsigned WS_WP = WS_X1T + 512 * 2048 * 2;
constexpr unsigned WS_WOT = WS_WP + 88 * 16 * 1024;
constexpr unsigned WS_Z2 = WS_WOT + 32 * 22 * 1024;
constexpr unsigned WS_V = WS_Z2 + 44 * 64 * 1024;
constexpr unsigned WS_END = WS_V + 128 * 22 * 1024;

struct Params {
  const u16 *u, *lar, *aim, *cre, *cim, *ldt, *dsk, *win, *wsp, *bsp, *wout, *c, *wmod;
  u16* out;
  unsigned char* ws;
};

#define DI __device__ __forceinline__
template <int V> struct IC { static constexpr int value = V; };
template <class F, int... I> DI void sfor_(F&& f, std::integer_sequence<int, I...>) { int d[] = {(f(IC<I>{}), 0)...}; (void)d; }
template <int N, class F> DI void sfor(F&& f) { sfor_(f, std::make_integer_sequence<int, N>{}); }
DI float bf2f(unsigned b) { return __builtin_bit_cast(float, b << 16); }
DI float bflo(unsigned w) { return __builtin_bit_cast(float, w << 16); }
DI float bfhi(unsigned w) { return __builtin_bit_cast(float, w & 0xFFFF0000u); }
DI f32x2 unpk(unsigned w) { return f32x2{bflo(w), bfhi(w)}; }
DI unsigned pk2(f32x2 v) { return __builtin_bit_cast(unsigned, __builtin_convertvector(v, bf16x2_t)); }
DI unsigned pk2(float lo, float hi) { return pk2(f32x2{lo, hi}); }
DI unsigned nomark(unsigned x) { return x < 0xFFFFFFFEu ? x : 0xFFFFFFFEu; }
DI float rbf(float x) { return bflo(pk2(x, 0.f)); }
DI f32x2 fma2(f32x2 a, f32x2 b, f32x2 c) { return __builtin_elementwise_fma(a, b, c); }
DI f32x2 lo2(f32x4 v) { return f32x2{v[0], v[1]}; }
DI f32x2 hi2(f32x4 v) { return f32x2{v[2], v[3]}; }
DI f32x2 bc2(float x) { return f32x2{x, x}; }
DI f32x4 mfma(v4u a, v4u b, f32x4 c) {
  return __builtin_amdgcn_mfma_f32_16x16x32_bf16(__builtin_bit_cast(bf16x8, a), __builtin_bit_cast(bf16x8, b), c, 0, 0, 0);
}
DI f32x4 mfma_h(v4u a, v4u b, f32x4 c) {
  return __builtin_amdgcn_mfma_f32_16x16x32_f16(__builtin_bit_cast(f16x8, a), __builtin_bit_cast(f16x8, b), c, 0, 0, 0);
}
DI float dot2(unsigned a, unsigned b, float acc) {
  return __builtin_amdgcn_fdot2_f32_bf16(__builtin_bit_cast(bf16x2_t, a), __builtin_bit_cast(bf16x2_t, b), acc, false);
}
constexpr unsigned ONES = 0x3F803F80u;
DI int perm(int u, int c) { return 32 * (u >> 1) + 8 * (c >> 2) + 4 * (u & 1) + (c & 3); }
template <int CTRL> DI float dpp(float x) {
  return __builtin_bit_cast(float, __builtin_amdgcn_mov_dpp(__builtin_bit_cast(int, x), CTRL, 0xf, 0xf, true));
}
DI float row16_sum(float x) {
  x += dpp<0xB1>(x); x += dpp<0x4E>(x); x += dpp<0x141>(x); x += dpp<0x128>(x);
  return x;
}
DI float row16_max(float x) {
  x = fmaxf(x, dpp<0xB1>(x)); x = fmaxf(x, dpp<0x4E>(x)); x = fmaxf(x, dpp<0x141>(x)); x = fmaxf(x, dpp<0x128>(x));
  return x;
}
DI float xrow_sum(float x) {
  auto s = __builtin_amdgcn_permlane16_swap(__float_as_uint(x), __float_as_uint(x), false, false);
  x = __uint_as_float(s[0]) + __uint_as_float(s[1]);
  auto t = __builtin_amdgcn_permlane32_swap(__float_as_uint(x), __float_as_uint(x), false, false);
  return __uint_as_float(t[0]) + __uint_as_float(t[1]);
}
DI float xrow_sum4(float a, float b, float c, float d) {
  auto s = __builtin_amdgcn_permlane16_swap(__float_as_uint(a), __float_as_uint(b), false, false);
  auto t = __builtin_amdgcn_permlane16_swap(__float_as_uint(c), __float_as_uint(d), false, false);
  const float x = __uint_as_float(s[0]) + __uint_as_float(s[1]), y = __uint_as_float(t[0]) + __uint_as_float(t[1]);
  auto z = __builtin_amdgcn_permlane32_swap(__float_as_uint(x), __float_as_uint(y), false, false);
  return __uint_as_float(z[0]) + __uint_as_float(z[1]);
}
DI float xrow_max(float x) {
  auto s = __builtin_amdgcn_permlane16_swap(__float_as_uint(x), __float_as_uint(x), false, false);
  x = fmaxf(__uint_as_float(s[0]), __uint_as_float(s[1]));
  auto t = __builtin_amdgcn_permlane32_swap(__float_as_uint(x), __float_as_uint(x), false, false);
  return fmaxf(__uint_as_float(t[0]), __uint_as_float(t[1]));
}
DI f32x2 gelu2(f32x2 x) {
  const float k0 = -2.0f * 0.7978845608028654f * 1.4426950408889634f, k1 = k0 * 0.044715f;
  const f32x2 a = x * fma2(x * x, bc2(k1), bc2(k0));
  const f32x2 e = f32x2{__builtin_amdgcn_exp2f(a[0]), __builtin_amdgcn_exp2f(a[1])} + bc2(1.0f);
  return x * f32x2{__builtin_amdgcn_rcpf(e[0]), __builtin_amdgcn_rcpf(e[1])};
}
DI rsrc_t mkrs(const void* ptr, unsigned bytes) { return __builtin_amdgcn_make_buffer_rsrc((void*)ptr, 0, (int)bytes, 0x00020000); }
template <int AUX> DI v4u ld128(rsrc_t rs, unsigned v, unsigned s) { return __builtin_amdgcn_raw_buffer_load_b128(rs, (int)v, (int)s, AUX); }
template <int AUX> DI v2u ld64(rsrc_t rs, unsigned v, unsigned s) { return __builtin_amdgcn_raw_buffer_load_b64(rs, (int)v, (int)s, AUX); }
template <int AUX> DI void st128(v4u x, rsrc_t rs, unsigned v, unsigned s) { __builtin_amdgcn_raw_buffer_store_b128(x, rs, (int)(v + s), 0, AUX); }
template <int AUX> DI void st64(v2u x, rsrc_t rs, unsigned v, unsigned s) { __builtin_amdgcn_raw_buffer_store_b64(x, rs, (int)v, (int)s, AUX); }
DI f32x4 asf(v4u x) { return __builtin_bit_cast(f32x4, x); }
DI float sum4(f32x4 x) { return (x[0] + x[1]) + (x[2] + x[3]); }
DI unsigned umax3(unsigned a, unsigned b, unsigned c) { unsigned m = a > b ? a : b; return m > c ? m : c; }
DI unsigned macc4(unsigned m, v4u x) { return umax3(umax3(m, x[0], x[1]), x[2], x[3]); }
DI unsigned macc(unsigned m, v4u x) { return umax3(m, x[0], x[2]); }
DI bool any_mark(unsigned m) { return __builtin_amdgcn_ballot_w64(m == 0xFFFFFFFFu) != 0ull; }
#define SB() __builtin_amdgcn_sched_barrier(0)
#define PIN(x) asm volatile("" : "+v"(x))
#define PINA(x) asm volatile("" : "+a"(x))
#define USE(x) asm volatile("" ::"v"(x))
#define RETRY_BEGIN(m) if (__builtin_expect(any_mark(m), 0)) { unsigned sp_ = 0; do { MEMFENCE(); m = 0;
#define RETRY_END(m) } while (any_mark(m) & (++sp_ <= SPIN_CAP)); }
#define MEMFENCE() asm volatile("" ::: "memory")
DI void bar() { asm volatile("s_waitcnt lgkmcnt(0)" ::: "memory"); __builtin_amdgcn_s_barrier(); asm volatile("" ::: "memory"); }

DI f32x2 cmul(f32x2 a, f32x2 b) { return fma2(bc2(a[1]), f32x2{-b[1], b[0]}, bc2(a[0]) * b); }
DI f32x2 cexp_p(float are2, float rev_hi, float rev_lo, float p) {
  const float mag = __builtin_amdgcn_exp2f(are2 * p);
  const float hi = rev_hi * p;
  const float fr = (hi - __builtin_rintf(hi)) + rev_lo * p;
  return f32x2{mag * __builtin_amdgcn_cosf(fr), mag * __builtin_amdgcn_sinf(fr)};
}
DI float exp_acc(float x) {
  const float hi = x * 1.4426950408889634f;
  const float lo = __builtin_fmaf(x, 1.4426950408889634f, -hi) + x * 1.925963033500011e-8f;
  const float e = __builtin_amdgcn_exp2f(hi);
  return __builtin_fmaf(e, lo * 0.6931471805599453f, e);
}
DI unsigned pkh(float a, float b) {
  f16x2 v = {(_Float16)a, (_Float16)b};
  return __builtin_bit_cast(unsigned, v);
}

constexpr int CV_RK = 4160;
constexpr int CV_H = 2 * CV_RK;
constexpr int CV_CH = CV_H + 4608;
constexpr int LDS_A = 0;
constexpr int LDS_U = 2 * CV_CH;
constexpr int LDS_GS = LDS_U + 8192;
constexpr int LDS_TAB = 36864;
constexpr int KP_CH = 13840;
constexpr int LDS_KP = LDS_TAB + 2 * 64 * 528 + 16;
constexpr int LDS_LN = 0, LDS_MR = 8448;
constexpr int LDS_WI = 145472, LDS_WO = LDS_WI + 10240;
constexpr int LDS_X = 65536;
constexpr int LDS_S = LDS_X + 24576;
constexpr int LDS_W = LDS_S + 6144;
constexpr int MEGA_LDS = LDS_W + 65536;
static_assert(LDS_WO + 4608 <= MEGA_LDS && LDS_KP + 2 * KP_CH <= LDS_WI && LDS_S >= 73728, "lds");
static_assert(LDS_GS + 1280 <= LDS_TAB && LDS_KP + 2 * KP_CH <= MEGA_LDS && MEGA_LDS <= 160 * 1024, "lds");
#define FLAG_AT 64
static_assert(FLAG_AT > 45 && FLAG_AT < 78 && FLAG_AT % 5 != 2, "flag position");
#define WF_EARLY 4
#define WO0 12
#define WSP_P0 4
#define HINT4_KS 15
#define ZDEPTH 3
#define GELU_IN 0
#define EPI_W 0
#define MOD_POS 0
#define POLL3 0
#define GELU_PRE 4
#define HINT4_G 50
#define RELABEL 1
#define CKMAP(ck) (RELABEL ? 2 * ((ck) & 3) + ((ck) >> 2) : (ck))
#define POLL_AT 134
#define MOD_MID 1
#define IN_AUX 0
#define WSP_AUX 0

template <class T> DI T ldsr(unsigned a) { return *(const LAS T*)(size_t)a; }
template <class T> DI void ldsw(unsigned a, T v) { *(LAS T*)(size_t)a = v; }
DI v2u trread(const unsigned char* ptr) {
  typedef short v4i16_t __attribute__((ext_vector_type(4)));
  return __builtin_bit_cast(v2u, __builtin_amdgcn_ds_read_tr16_b64_v4i16((LAS v4i16_t*)ptr));
}
DI v2u trread_a(unsigned a) {
  typedef short v4i16_t __attribute__((ext_vector_type(4)));
  return __builtin_bit_cast(v2u, __builtin_amdgcn_ds_read_tr16_b64_v4i16((LAS v4i16_t*)(size_t)a));
}
struct CvOrder { int qq[36], pp[36]; };
constexpr CvOrder cv_order() {
  CvOrder o{};
  int n = 0;
  for (int m = 0; m < 8; ++m) {
    for (int j = 0; j < m; ++j) if (m + j < 8) { o.qq[n] = m; o.pp[n] = j; ++n; }
    for (int j = 0; j <= m; ++j) if (m + j < 8) { o.qq[n] = j; o.pp[n] = m; ++n; }
  }
  return o;
}
constexpr CvOrder CVO = cv_order();

typedef __attribute__((address_space(1))) unsigned gu32;
DI unsigned tx(unsigned m, unsigned x, unsigned tag) { return __builtin_amdgcn_bitop3_b32(m, x, tag, 0xF6); }
DI unsigned tacc(unsigned m, v4u x, unsigned tag) { return tx(tx(m, x[1], tag), x[3], tag); }
DI bool any_nz(unsigned m) { return __builtin_amdgcn_ballot_w64(m != 0u) != 0ull; }
DI void hint_add(unsigned char* ws, int line) { (void)__hip_atomic_fetch_add((gu32*)(ws + WS_CNT + line * 128), 1u, __ATOMIC_RELAXED, __HIP_MEMORY_SCOPE_AGENT); }
DI void hint_wait(rsrc_t rws, unsigned voff, unsigned soff, unsigned want) {
  unsigned sp = 0;
  for (;;) {
    MEMFENCE();
    const unsigned v = __builtin_amdgcn_raw_buffer_load_b32(rws, (int)voff, (int)soff, SC1);
    if (!((__builtin_amdgcn_ballot_w64(v < want) != 0ull) & (++sp <= SPIN_CAP))) break;
    __builtin_amdgcn_s_sleep(OPT_E);
  }
  MEMFENCE();
}

extern "C" __global__ void __launch_bounds__(256, 1) adaln_mega(Params p) {
  extern __shared__ __attribute__((aligned(16))) unsigned char smem[];
  const int tid = threadIdx.x, lane = tid & 63, w = __builtin_amdgcn_readfirstlane(tid >> 6);
  const int b = blockIdx.x, xcd = b & 7, idx = b >> 3;
  const int pb = xcd * 32 + idx;
  const unsigned l16 = lane * 16;
  STAMP_DECL;
  STAMP(1, 0);
  v2u codepf;
  {
    unsigned long long pc;
    int rel_m, rel_e;
    asm volatile("s_getpc_b64 %0\n\ts_mov_b32 %1, adaln_mega@rel32@lo+4\n\ts_mov_b32 %2, adaln_code_end@rel32@lo+12" : "=s"(pc), "=s"(rel_m), "=s"(rel_e));
    const rsrc_t rcode = mkrs((const void*)(pc + (long long)rel_m), rel_e > rel_m ? (unsigned)(rel_e - rel_m) & ~15u : 0u);
    codepf = ld64<0>(rcode, tid * 8, idx * 2048);
  }
  SB();
  const rsrc_t ru = mkrs(p.u, L * D * 2);
  v4u xr[2];
  xr[0] = ld128<IN_AUX>(ru, l16, (pb * 8 + w * 2) * 1024);
  xr[1] = ld128<IN_AUX>(ru, l16 + 1024, (pb * 8 + w * 2) * 1024);
  const int mkg = b & 3, mcg = b >> 2;
  const unsigned cq = p.c[mkg * 128 + (tid & 3) * 32 + (tid >> 3)];
  const unsigned epoch = *(const unsigned*)(p.ws + WS_EPOCH);
  const rsrc_t rws = mkrs(p.ws, WS_END);
  __builtin_amdgcn_raw_buffer_store_b32(0u, rws, tid < 2 ? tid * 32768u : OOB, WS_CNT + b * 128, SC1);
  SB();
  const int cch = w >> 1, eh = w & 1, ns = tid & 127;
  const unsigned q_cre = p.cre[pb * 256 + tid], q_cim = p.cim[pb * 256 + tid], q_lar = p.lar[pb * 256 + tid], q_aim = p.aim[pb * 256 + tid];
  const unsigned q_ldt = p.ldt[pb * 2 + cch], q_dsk = p.dsk[pb * 2 + cch];
  v4u xm[4];
#define LOAD_XM() { \
  const rsrc_t rmod = mkrs(p.wmod, D * 3072 * 2); \
_Pragma("unroll") \
  for (int i = 0; i < 4; ++i) xm[i] = ld128<IN_AUX>(rmod, (tid >> 3) * 6144 + (tid & 7) * 16, (mkg * 128 + i * 32) * 6144 + mcg * 128 + (b < 192 ? 0u : OOB)); }
  SB();
  LOAD_XM();
  SB();
  STAMP(1, 24);
  const int c = lane & 15, q = lane >> 4;
  const unsigned lds0 = (unsigned)(size_t)(LAS unsigned char*)smem;
  unsigned pos4[9];
  {
    const float f4 = (float)(8188 - tid * 32);
#pragma unroll
    for (int j = 0; j < 10; j += 2) {
      const unsigned pp = pk2(bc2(f4) - f32x2{4.f * j, 4.f * j + 4.f});
      const int p0 = (int)bflo(pp), p1 = (int)bfhi(pp);
      pos4[j] = p0 + ((p0 >> 5) << 2) + lds0 + LDS_KP;
      PIN(pos4[j]);
      if (j + 1 < 9) { pos4[j + 1] = p1 + ((p1 >> 5) << 2) + lds0 + LDS_KP; PIN(pos4[j + 1]); }
    }
  }
  unsigned co[2][4];
  {
    const unsigned b0 = (eh ? 1152 + 576 * q : 144 * (q >> 1) + 4 * (q & 1)) * 4 + 36 * c + lds0 + LDS_KP + cch * KP_CH;
    const unsigned b1 = (q < 2 ? 288 + 144 * q : 568 + 4 * q) * 4 + 36 * c + lds0 + LDS_KP + cch * KP_CH;
    const unsigned s0 = eh ? 576 : 4, s1 = q < 2 ? 8 : 576;
#pragma unroll
    for (int r = 0; r < 4; ++r) { co[0][r] = b0 + r * s0; co[1][r] = b1 + r * s1; PIN(co[0][r]); PIN(co[1][r]); }
  }
  *(unsigned*)(smem + LDS_KP - 16 + cch * KP_CH + (lane & 3) * 4) = 0u;
  unsigned a_pad, a_wi, a_wo, a_tab, gm_off, gc_off;
  a_pad = lds0 + cch * CV_CH + (lane < 32 ? CV_H + l16 : ((lane >> 1) & 1) * CV_RK + 4096 + (lane & 1) * 16);
  a_wi = lds0 + LDS_WI + (w * 32 + 8 * q + ((lane >> 2) & 3)) * 80 + (lane & 3) * 16;
  a_wo = lds0 + LDS_WO + (8 * q + ((lane >> 2) & 3)) * 144 + (w >> 1) * 64 + (lane & 3) * 16 + (w & 1) * 8;
  a_tab = lds0 + LDS_TAB + cch * (64 * 528) + c * 528 + q * 16;
  gm_off = ((4 - cch) * 512 + mkg * 128 + ns) * 8;
  gc_off = (((lane & 7) > 5 ? 0 : ((lane & 7) >> 1)) * 512 + pb * 2 + (lane & 1)) * 8;
  PIN(a_pad); PIN(a_wi); PIN(a_wo); PIN(a_tab); PIN(gm_off); PIN(gc_off);
  unsigned ab[4], bb, dv[4], rdo4[4], xoff;
#pragma unroll
  for (int i = 0; i < 4; ++i) {
    ab[i] = lds0 + cch * CV_CH + (1 - (c & 1)) * CV_RK + 2 * (2030 - 2 * (c >> 1) + 8 * q) - eh * 256 - 192 - 512 - 1024 * i;
    dv[i] = (lane >> 3) * 4096 + (((lane & 7) ^ ((lane >> 4) & 1) ^ (i << 1)) * 16) + i * (32768 - 1024);
    rdo4[i] = lds0 + LDS_W + w * 16384 + ((perm((i & 1) * 2 ^ (2 * (w & 1)), c) * 128 + ((q ^ (c >> 1)) * 16)) ^ ((i >> 1) * 64));
    PIN(ab[i]); PIN(dv[i]); PIN(rdo4[i]);
  }
  bb = lds0 + cch * CV_CH + CV_H + 2 * (256 - 16 + 16 * c + 8 * q) - eh * 256 - 192;
  xoff = WS_X1T + (pb * 2 + cch) * 4096 + eh * 2048 + ((c >> 1) * 32 + ((q & 1) * 4 + 2 * (c & 1) + (q >> 1)) * 4) * 2;
  PIN(bb); PIN(xoff);
  SB();
  STAMP(1, 26);
  {
    *(v4u*)(smem + LDS_LN + (w * 2) * 1040 + l16) = xr[0];
    *(v4u*)(smem + LDS_LN + (w * 2 + 1) * 1040 + l16) = xr[1];
    float sm[2], sq[2];
#pragma unroll
    for (int i = 0; i < 2; ++i) {
      sm[i] = dot2(xr[i][0], ONES, dot2(xr[i][1], ONES, dot2(xr[i][2], ONES, dot2(xr[i][3], ONES, 0.f))));
      sq[i] = dot2(xr[i][0], xr[i][0], dot2(xr[i][1], xr[i][1], dot2(xr[i][2], xr[i][2], dot2(xr[i][3], xr[i][3], 0.f))));
    }
    const float tv = row16_sum(xrow_sum4(sm[0], sm[1], sq[0], sq[1]));
    auto sw = __builtin_amdgcn_permlane32_swap(__float_as_uint(tv), __float_as_uint(tv), false, false);
    const float m = __uint_as_float(sw[0]) * (1.0f / D);
    const float r = __builtin_amdgcn_rsqf(fmaxf(__uint_as_float(sw[1]) * (1.0f / D) - m * m, 0.f) + EPS);
    ((float*)(smem + LDS_GS + 1088))[(q >> 1) * 8 + w * 2 + (q & 1)] = (q >> 1) ? -m * r : r;
  }
  const unsigned tag = epoch * 2654435761u + 0x9E3779B9u;
  STAMP(1, 28);
  asm volatile("s_waitcnt vmcnt(%0)" ::"i"(MOD_POS == 0 ? 8 : 4) : "memory");
  bar();
  STAMP(1, 16);
  {
    unsigned ua[4][2];
#pragma unroll
    for (int i = 0; i < 4; ++i) {
      const unsigned char* src = smem + LDS_LN + (tid & 3) * 2080 + (i * 64 + (tid >> 2)) * 4;
      ua[i][0] = *(const unsigned*)src; ua[i][1] = *(const unsigned*)(src + 1040);
    }
    const unsigned lnv = ((const unsigned*)(smem + LDS_GS + 1088))[c];
    SB();
#pragma unroll
    for (int i = 0; i < 4; ++i) st128<SC1>(v4u{ua[i][0], tag, ua[i][1], tag}, rws, (tid >> 2) * 16384 + (tid & 3) * 16, WS_UG + i * 64 * 16384 + pb * 64);
    st64<SC1>(v2u{lnv, tag}, rws, tid < 16 ? (c >> 3) * 16384 + (c & 7) * 8 : OOB, WS_SR + pb * 64);
  }
  STAMP(1, 17);
  SB();
  const unsigned wp_oob = b < 176 ? 0u : OOB;
  const int wpr = b >> 2, wkg = b & 3;
  v4u xi[2], xo;
  {
    const rsrc_t rwin = mkrs(p.win, D * FF * 2), rwout = mkrs(p.wout, FH * D * 2);
    const int hf = wpr >= 22, pp = wpr - 22 * hf;
    const unsigned v = ((tid >> 2) * (FF * 2) + (tid & 3) * 16) | ((pp == 21 ? tid & 2 : 0) << 29);
#pragma unroll
    for (int i = 0; i < 2; ++i) xi[i] = ld128<IN_AUX>(rwin, v, (wkg * 128 + i * 64) * (FF * 2) + (hf * FH + pp * 32) * 2 + wp_oob);
    xo = ld128<IN_AUX>(rwout, (tid >> 3) * 1024 + (tid & 7) * 16, idx * 32 * 1024 + xcd * 128);
  }
  SB();
  const int d0 = pb * 2, dk = d0 + cch;
  const int mt = (xcd >> 1) * 8 + (idx & 7), nt = (xcd & 1) * 4 + (idx >> 3);
  const int big = (nt + 2) >> 3;
  const int np = 3 - big, pr0 = 3 * nt - big * (nt - 6);
  const int kh = w & 1, uh = w >> 1;
  const int ub_own = pr0 * 2 + uh * np, ub_oth = pr0 * 2 + (uh ^ 1) * np;
  const unsigned oob3 = (unsigned)big << 30;
  const int kr = 2 * (idx >> 3) + (xcd & 1) + (OPT_B == 0 ? (idx & 7) : OPT_B == 1 ? 4 * (idx & 1) : OPT_B == 2 ? 2 * (idx & 3) : 0);
  const rsrc_t rwsp = mkrs(p.wsp, L * L * 2);
  const unsigned wp_fr = (wpr * 2 * 16 + wkg * 4 + w) * 1024 + wp_oob;
  v4u wr[1][8];
  const unsigned wsp_v = (lane >> 3) * 4096 + (lane & 7) * 16, wsp_s = mt * 64 * 4096 + w * 1024;
#define LOAD_WSP1(ck, i) wr[ck][i] = ld128<WSP_AUX>(rwsp, wsp_v + (((ck) + kr) & 7) * 128, wsp_s + (i) * 8 * 4096)
#define LOAD_WSPN(n) LOAD_WSP1((n) >> 3, (n) & 7)
  const v4u mk = {~0u, ~0u, ~0u, ~0u};
  const unsigned tag_s = __builtin_amdgcn_readfirstlane(tag);
  {
    const float dt = rbf(exp_acc(bf2f(q_ldt)));
    const float cre = bf2f(q_cre) * dt, cim = bf2f(q_cim) * dt;
    const float are2 = -rbf(exp_acc(bf2f(q_lar))) * dt * 1.4426950408889634f;
    const float aim = bf2f(q_aim) * dt;
    const float rev_hi = aim * 0.15915494309189535f;
    const float rev_lo = __builtin_fmaf(aim, 0.15915494309189535f, -rev_hi) + aim * 6.4206383e-9f;
    int ex = __builtin_amdgcn_frexp_expf(xrow_max(row16_max(fmaxf(fabsf(cre), fabsf(cim))))) + 1;
    ex = ex < -100 ? -100 : ex;
    const float scale = __builtin_amdgcn_ldexpf(1.f, -ex);
    ((float*)(smem + LDS_GS + 1024))[w] = __builtin_amdgcn_ldexpf(2.f, ex);
    const f32x2 c0 = {cre * scale, cim * scale};
    const f32x2 e1 = cexp_p(are2, rev_hi, rev_lo, 1.f), e8 = cexp_p(are2, rev_hi, rev_lo, 8.f), e64 = cexp_p(are2, rev_hi, rev_lo, 64.f);
    unsigned char* tn = smem + LDS_TAB + cch * (64 * 528) + ns * 4;
#define TROW(r, z) *(unsigned*)(tn + (r) * 528) = pkh((z)[0], -(z)[1])
    SB();
    const f32x2 e128 = cmul(e64, e64), e2 = cmul(e1, e1), e256 = cmul(e128, e128), e4 = cmul(e2, e2), e512 = cmul(e256, e256);
    f32x2 f0 = {1.f, 0.f}, f1 = e64;
    f32x2 ca = cmul(c0, e512), cb = cmul(ca, e512);
#pragma unroll
    for (int j = 0; j < 8; ++j) {
      *(unsigned*)(tn + (48 + j) * 528) = pkh(f0[0], f0[1]);
      *(unsigned*)(tn + (56 + j) * 528) = pkh(f1[0], f1[1]);
      TROW(32 + j, cb);
      f0 = j == 0 ? e8 : cmul(f0, e8); f1 = cmul(f1, e8); cb = cmul(cb, e128);
      if (j < 4) {
        const f32x2 z = cmul(ca, e4);
        TROW(24 + j, ca); TROW(28 + j, z);
        ca = cmul(ca, e128);
      }
      if (j == 3 || j == 7) {
        PIN(f0); PIN(f1); PIN(cb);
        if (WSP_P0 >= 4) { LOAD_WSPN(j >> 2 << 1); LOAD_WSPN((j >> 2 << 1) + 1); }
        SB();
      }
    }
    TROW(40, cb);
    SB();
  {
    const float cv = bf2f(cq);
    const float sl = rbf(cv * __builtin_amdgcn_rcpf(1.0f + __builtin_amdgcn_exp2f(cv * -1.4426950408889634f)));
    const float sc[4] = {dpp<0x00>(sl), dpp<0x55>(sl), dpp<0xAA>(sl), dpp<0xFF>(sl)};
    f32x2 a[4];
#pragma unroll
    for (int i = 0; i < 4; ++i)
#pragma unroll
      for (int j = 0; j < 4; ++j) a[j] = i == 0 ? unpk(xm[i][j]) * bc2(sc[i]) : fma2(unpk(xm[i][j]), bc2(sc[i]), a[j]);
    float* red = (float*)(smem + LDS_MR) + (tid >> 3) * 68 + (tid & 7) * 8;
    *(f32x4*)red = f32x4{a[0][0], a[0][1], a[1][0], a[1][1]};
    *(f32x4*)(red + 4) = f32x4{a[2][0], a[2][1], a[3][0], a[3][1]};
  }
    bar();
    float ms8[8];
#pragma unroll
    for (int i = 0; i < 8; ++i) ms8[i] = ((const float*)(smem + LDS_MR))[(q * 8 + i) * 68 + w * 16 + c];
    SB();
    f32x2 t1[8];
    t1[1] = e1; t1[2] = e2; t1[4] = e4;
    t1[3] = cmul(e2, e1); t1[5] = cmul(e4, e1); t1[6] = cmul(e2, e4); t1[7] = cmul(t1[6], e1);
    const f32x2 c1 = cmul(c0, e128), c2 = cmul(c0, e256), c3 = cmul(c2, e128);
    TROW(0, c0); TROW(8, c1); TROW(16, c2); TROW(20, c3);
    {
    const float s = xrow_sum(((ms8[0] + ms8[1]) + (ms8[2] + ms8[3])) + ((ms8[4] + ms8[5]) + (ms8[6] + ms8[7])));
    st64<SC1>(v2u{__float_as_uint(s), tag}, rws, q == 0 ? c * 8 : OOB, WS_MODG + (mkg * 3072 + mcg * 64 + w * 16) * 8 + (b < 192 ? 0u : OOB));
    }
    SB();
#pragma unroll
    for (int v = 1; v < 8; ++v) {
      const f32x2 z0 = cmul(t1[v], c0), z1 = cmul(t1[v], c1);
      TROW(v, z0); TROW(8 + v, z1);
      if ((v & 1) == 0) {
        const f32x2 z2 = cmul(t1[v], c2), z3 = cmul(t1[v], c3);
        TROW(16 + v / 2, z2); TROW(20 + v / 2, z3);
        if (v < 6 && WSP_P0 >= 8) { LOAD_WSPN(v + 2); LOAD_WSPN(v + 3); }
        SB();
      }
    }
  }
  STAMP(1, 27);
#pragma unroll
  for (int i = 0; i < 2; ++i) *(v4u*)(smem + LDS_WI + (i * 64 + (tid >> 2)) * 80 + (tid & 3) * 16) = xi[i];
  *(v4u*)(smem + LDS_WO + (tid >> 3) * 144 + (tid & 7) * 16) = xo;
  STAMP(1, 29);
  bar();
  STAMP(1, 18);
  SB();
  v4u gr[4], gb_[4];
  v2u gm[4], gc[4];
#define LOAD_GS() _Pragma("unroll") for (int i_ = 0; i_ < 4; ++i_) { gr[i_] = ld128<SC1>(rws, tid * 16, WS_SR + i_ * 4096); gb_[i_] = ld128<SC1>(rws, tid * 16, WS_SB + i_ * 4096); }
#define LOAD_GM() _Pragma("unroll") for (int i_ = 0; i_ < 4; ++i_) { gm[i_] = ld64<SC1>(rws, gm_off, WS_MODG + i_ * 24576); gc[i_] = ld64<SC1>(rws, gc_off, WS_MODG + i_ * 24576); }
  v4u ug[4], wt[2];
#define LOAD_UG() _Pragma("unroll") for (int i_ = 0; i_ < 4; ++i_) ug[i_] = ld128<SC1>(rws, tid * 16, WS_UG + pb * 16384 + i_ * 4096)
  {
    v4u fa[2][8], fb[8];
    const unsigned ta = a_tab + eh * (32 * 528);
#pragma unroll
    for (int ks = 0; ks < 3; ++ks) { fb[ks] = ldsr<v4u>(a_tab + 48 * 528 + ks * 64); fa[0][ks] = ldsr<v4u>(ta + ks * 64); fa[1][ks] = ldsr<v4u>(a_tab + 16 * 528 + ks * 64); }
    SB();
    LOAD_UG();
    SB();
    f32x4 kk[2][2];
    f32x2 us;
    v2u wh_[6];
#pragma unroll
    for (int i = 0; i < 4; ++i) kk[i >> 1][i & 1] = f32x4{0.f, 0.f, 0.f, 0.f};
#pragma unroll
    for (int ks = 0; ks < 8; ++ks) {
      if (ks < 5) { fb[3 + ks] = ldsr<v4u>(a_tab + 48 * 528 + (3 + ks) * 64); fa[0][3 + ks] = ldsr<v4u>(ta + (3 + ks) * 64); fa[1][3 + ks] = ldsr<v4u>(a_tab + 16 * 528 + (3 + ks) * 64); }
      if (ks == 5) { us = *(const f32x2*)(smem + LDS_GS + 1024 + cch * 8); wh_[4] = trread_a(a_wo); wh_[5] = trread_a(a_wo + 576); }
      if (ks == 6) { wh_[0] = trread_a(a_wi); wh_[1] = trread_a(a_wi + 320); wh_[2] = trread_a(a_wi + 8); wh_[3] = trread_a(a_wi + 328); }
      kk[0][ks >> 2] = mfma_h(fa[0][ks], fb[ks], kk[0][ks >> 2]); kk[1][ks >> 2] = mfma_h(fa[1][ks], fb[ks], kk[1][ks >> 2]);
      SB();
    }
    STAMP(1, 37);
    wt[0] = v4u{wh_[0][0], wh_[0][1], wh_[1][0], wh_[1][1]};
    wt[1] = v4u{wh_[2][0], wh_[2][1], wh_[3][0], wh_[3][1]};
    st128<SC1>(v4u{wh_[4][0], wh_[4][1], wh_[5][0], wh_[5][1]}, rws, l16, WS_WOT + ((xcd * 4 + w) * 22 + idx) * 1024 + wp_oob);
    LOAD_GS();
    SB();
#pragma unroll
    for (int i = 0; i < 4; ++i) PINA(kk[i >> 1][i & 1]);
    f32x4 k0 = kk[0][0] * us[0] + kk[0][1] * us[1];
    const f32x4 k1 = kk[1][0] * us[0] + kk[1][1] * us[1];
    k0[0] += ns == 0 ? bf2f(q_dsk) : 0.f;
#pragma unroll
    for (int r = 0; r < 4; ++r) { ldsw<float>(co[0][r], k0[r]); ldsw<float>(co[1][r], k1[r]); }
  }
  STAMP(1, 31);
  bar();
  STAMP(1, 19);
  {
    float v[2][9];
#pragma unroll
    for (int j = 0; j < 9; ++j) v[0][j] = ldsr<float>(pos4[j]);
    LOAD_GM();
#pragma unroll
    for (int j = 0; j < 9; ++j) v[1][j] = ldsr<float>(pos4[j] + KP_CH);
    ldsw<v4u>(a_pad, v4u{0u, 0u, 0u, 0u});
    SB();
    STAMP(1, 36);
#pragma unroll
    for (int ch = 0; ch < 2; ++ch) {
      unsigned char* rk = smem + ch * CV_CH + tid * 16;
#pragma unroll
      for (int sft = 0; sft < 2; ++sft)
        *(v4u*)(rk + sft * CV_RK) = v4u{pk2(v[ch][sft], v[ch][sft + 1]), pk2(v[ch][sft + 2], v[ch][sft + 3]), pk2(v[ch][sft + 4], v[ch][sft + 5]), pk2(v[ch][sft + 6], v[ch][sft + 7])};
    }
#pragma unroll
    for (int i = WSP_P0; i < 8; ++i) LOAD_WSPN(i);
    st128<SC1>(mk, rws, l16, WS_X1T + dk * 4096 + eh * 2048);
    st128<SC1>(mk, rws, l16 + 1024, WS_X1T + dk * 4096 + eh * 2048);
    __builtin_amdgcn_raw_buffer_store_b32(~0u, rws, lane * 4, WS_ST2 + ((mt * 2 + kh) * 16 + nt * 2 + uh) * 256, SC1);
    SB();
    STAMP(1, 32);
    unsigned m = 0;
#pragma unroll
    for (int i = 0; i < 4; ++i) { m = tacc(m, ug[i], tag); m = tacc(m, gr[i], tag); m = tacc(m, gb_[i], tag); }
    if (__builtin_expect(any_nz(m), 0)) {
      unsigned sp = 0;
      do {
        MEMFENCE(); LOAD_UG(); LOAD_GS(); m = 0;
#pragma unroll
        for (int i = 0; i < 4; ++i) { m = tacc(m, ug[i], tag); m = tacc(m, gr[i], tag); m = tacc(m, gb_[i], tag); }
      } while (any_nz(m) & (++sp <= SPIN_CAP));
    }
    f32x2 nn[4][2];
#pragma unroll
    for (int i = 0; i < 4; ++i) {
      nn[i][0] = fma2(unpk(ug[i][0]), bc2(__uint_as_float(gr[i][0])), bc2(__uint_as_float(gb_[i][0])));
      nn[i][1] = fma2(unpk(ug[i][2]), bc2(__uint_as_float(gr[i][2])), bc2(__uint_as_float(gb_[i][2])));
      PIN(nn[i][0]); PIN(nn[i][1]);
      *(unsigned*)(smem + LDS_U + i * 1024 + tid * 4) = __builtin_amdgcn_perm(ug[i][2], ug[i][0], 0x05040100u);
      *(unsigned*)(smem + LDS_U + 4096 + i * 1024 + tid * 4) = __builtin_amdgcn_perm(ug[i][2], ug[i][0], 0x07060302u);
    }
    SB();
    STAMP(1, 33);
    m = 0;
#pragma unroll
    for (int i = 0; i < 4; ++i) m = tx(tx(m, gm[i][1], tag), gc[i][1], tag);
    if (__builtin_expect(any_nz(m), 0)) {
      unsigned sp = 0;
      do {
        __builtin_amdgcn_s_sleep(3);
        MEMFENCE(); LOAD_GM(); m = 0;
#pragma unroll
        for (int i = 0; i < 4; ++i) m = tx(tx(m, gm[i][1], tag), gc[i][1], tag);
      } while (any_nz(m) & (++sp <= SPIN_CAP));
    }
    const float mv = rbf((__uint_as_float(gm[0][0]) + __uint_as_float(gm[1][0])) + (__uint_as_float(gm[2][0]) + __uint_as_float(gm[3][0])));
    *(u16*)(smem + LDS_GS + tid * 2) = (u16)pk2(mv + (cch ? 0.f : 1.0f), 0.f);
    const float cv = rbf((__uint_as_float(gc[0][0]) + __uint_as_float(gc[1][0])) + (__uint_as_float(gc[2][0]) + __uint_as_float(gc[3][0])));
    const float gav = dpp<0x104>(cv), gav2 = dpp<0x102>(cv);
    const float hbv = cv * gav, hsv = rbf(1.0f + cv) * gav2;
    const f32x2 hb = {__builtin_bit_cast(float, __builtin_amdgcn_readlane(__builtin_bit_cast(int, hbv), 0)), __builtin_bit_cast(float, __builtin_amdgcn_readlane(__builtin_bit_cast(int, hbv), 1))};
    const f32x2 hs = {__builtin_bit_cast(float, __builtin_amdgcn_readlane(__builtin_bit_cast(int, hsv), 2)), __builtin_bit_cast(float, __builtin_amdgcn_readlane(__builtin_bit_cast(int, hsv), 3))};
    SB();
    STAMP(1, 38);
#pragma unroll
    for (int i = 0; i < 4; ++i) {
      const f32x2 h0 = fma2(nn[i][0], hs, hb), h1 = fma2(nn[i][1], hs, hb);
      *(unsigned*)(smem + CV_H + 512 + i * 1024 + tid * 4) = pk2(h0[0], h1[0]);
      *(unsigned*)(smem + CV_CH + CV_H + 512 + i * 1024 + tid * 4) = pk2(h0[1], h1[1]);
    }
  }
  asm volatile("" ::"v"(codepf));
  STAMP(1, 39);
  bar();
  STAMP(1, 1);
#define CV_A(ee, k) ({ const unsigned a_ = ab[(k) >> 1] + 192 - 64 * (ee) + 512 * (1 - ((k) & 1)); v4u{ldsr<unsigned>(a_), ldsr<unsigned>(a_ + 4), ldsr<unsigned>(a_ + 8), ldsr<unsigned>(a_ + 12)}; })
#define CV_B(ee, k) ldsr<v4u>(bb + 192 - 64 * (ee) + 512 * (k))
  const unsigned slotA = LDS_W + w * 16384, slotB = slotA + 8192;
  v4u wq[8][8];
#define READ_W(dl, f) ldsr<v4u>(rdo4[(((f) >> 1) & 1) + ((f) >> 2) * 2] + (dl) + ((f) & 1) * 512)
  unsigned dso;
#define DMA_W(ck, slot, i) do { \
    if (((i) & 3) == 0) asm volatile("s_add_u32 m0, %1, %2\n\ts_add_u32 %0, %3, %4\n\tbuffer_load_dwordx4 %5, %6, %0 offen lds" \
                 : "=&s"(dso) : "s"(lds0 + (slot)), "i"((i) * 1024), "s"(wsp_s + ((CKMAP(ck) + kr) & 7) * 128), "i"((i) * 32768), "v"(dv[0]), "s"(rwsp) : "scc", "memory"); \
    else asm volatile("buffer_load_dwordx4 %0, %1, %2 offen offset:%3 lds" :: "v"(dv[(i) & 3]), "s"(rwsp), "s"(dso), "i"(((i) & 3) * 1024) : "memory"); } while (0)
  auto dma_sel = [&](auto CK, unsigned slot, int i) __attribute__((always_inline)) {
    constexpr int ck = decltype(CK)::value;
    switch (i) {
      case 0: DMA_W(ck, slot, 0); break; case 1: DMA_W(ck, slot, 1); break; case 2: DMA_W(ck, slot, 2); break; case 3: DMA_W(ck, slot, 3); break;
      case 4: DMA_W(ck, slot, 4); break; case 5: DMA_W(ck, slot, 5); break; case 6: DMA_W(ck, slot, 6); break; default: DMA_W(ck, slot, 7); break;
    }
  };
  v4u cA[2][8], cB[2][8];
  float gs[8];
  v4u gbf, sbf;
  v4u ones4 = {ONES, ONES, ONES, ONES};
  PIN(ones4);
#pragma unroll
  for (int k = 0; k < 2; ++k) { cA[0][k] = CV_A(0, k); cB[0][k] = CV_B(0, k); }
  {
    gbf = *(const v4u*)(smem + LDS_GS + w * 64 + q * 16);
    sbf = *(const v4u*)(smem + LDS_GS + 256 + w * 64 + q * 16);
  }
  SB();
  f32x4 cacc[8];
  v2u ue[4];
  float uf[16];
  unsigned flagv;
  unsigned wro = lds0 + slotA + (lane >> 3) * 128 + (((lane & 7) ^ ((lane >> 4) & 1)) * 16);
  PIN(wro);
  {
    f32x4 cb4[4];
    v4u wo_[2];
    float wl, wh;
    f32x2 cp[4];
#pragma unroll
    for (int t = 0; t < 8; ++t) cacc[t] = f32x4{0.f, 0.f, 0.f, 0.f};
    sfor<144>([&](auto G) __attribute__((always_inline)) {
      constexpr int g = decltype(G)::value, ee = g / 36, j = g % 36;
      if constexpr (ee == 0 && j < 6) { cA[0][2 + j] = CV_A(0, 2 + j); cB[0][2 + j] = CV_B(0, 2 + j); }
      cacc[CVO.pp[j] + CVO.qq[j]] = mfma(cA[ee & 1][CVO.qq[j]], cB[ee & 1][CVO.pp[j]], cacc[CVO.pp[j] + CVO.qq[j]]);
      constexpr int jn = ee == 0 ? j - 20 : (j & 1) ? j >> 1 : -1;
      if constexpr (ee < 3 && jn >= 0 && jn < 16) {
        if constexpr (jn & 1) cB[(ee + 1) & 1][jn >> 1] = CV_B(ee + 1, jn >> 1);
        else cA[(ee + 1) & 1][jn >> 1] = CV_A(ee + 1, jn >> 1);
      }
      if constexpr (ee == 3 && j < 4) ue[j] = *(const v2u*)(smem + LDS_U + cch * 4096 + (eh * 4 + j) * 512 + (c * 16 + q * 4) * 2);
      if constexpr (g >= 6 && g < 30) {
        constexpr int i = (g - 6) / 12, jj = ((g - 6) / 3) & 3, ph = (g - 6) % 3;
        if constexpr (ph == 0) { if constexpr (i == 0) gs[2 * jj] = bflo(gbf[jj]); wl = bflo(wt[i][jj]) * gs[2 * jj]; PIN(wl); }
        if constexpr (ph == 1) { if constexpr (i == 0) gs[2 * jj + 1] = bfhi(gbf[jj]); wh = bfhi(wt[i][jj]) * gs[2 * jj + 1]; PIN(wh); }
        if constexpr (ph == 2) {
          wo_[i][jj] = pk2(wl, wh);
          if constexpr (jj == 3) st128<SC1>(wo_[i], rws, l16 + i * 16384, WS_WP + wp_fr);
          else PIN(wo_[i][jj]);
        }
      }
      if constexpr (g == 4 || g == 5) cb4[2 * (g - 4) + 1] = mfma(sbf, wt[g - 4], f32x4{0.f, 0.f, 0.f, 0.f});
      if constexpr (g == 19 || g == 31) cb4[g == 19 ? 0 : 2] = mfma(ones4, wo_[g == 31], f32x4{0.f, 0.f, 0.f, 0.f});
      if constexpr (g == 33) *(f32x4*)(smem + LDS_S + 4096 + (w * 16 + c) * 16) = f32x4{cb4[0][0], cb4[1][0], cb4[2][0], cb4[3][0]};
      if constexpr (g == 38) bar();
      if constexpr (g == 39) {
#pragma unroll
        for (int k = 0; k < 4; ++k) cp[k] = *(const f32x2*)(smem + LDS_S + 4096 + (k * 16 + c) * 16 + (q & 1) * 8);
      }
      if constexpr (g == 45) {
        float s0 = cp[0][0] + cp[1][0], s1 = cp[2][0] + cp[3][0];
        PIN(s0); PIN(s1);
        s0 += s1;
        PIN(s0);
        float s2 = cp[0][1] + cp[1][1], s3 = cp[2][1] + cp[3][1];
        PIN(s2); PIN(s3);
        st64<SC1>(v2u{__float_as_uint(s0), __float_as_uint(s2 + s3)}, rws, tid < 32 ? q * 512 + c * 8 : OOB, WS_CSB + wpr * 1024 + wkg * 128 + wp_oob);
      }
      if constexpr (g == 36) STAMP(1, 40);
      if constexpr (g == 72) STAMP(1, 41);
      if constexpr (g == FLAG_AT) {
        STAMP(1, 42);
        asm volatile("s_waitcnt vmcnt(%0)" ::"i"((FLAG_AT - 32 + 4) / 5 + 1) : "memory");
        __builtin_amdgcn_s_barrier();
        if (tid < 8) hint_add(p.ws, 256 + tid * 16 + (idx & 15));
        STAMP(1, 43);
      }
      if constexpr (g % 5 == 2 && g < 80) DMA_W(1 + g / 40, g < 40 ? slotA : slotB, (g / 5) & 7);
      if constexpr (g == 88) asm volatile("s_waitcnt vmcnt(8)" ::: "memory");
      if constexpr (g == 124) asm volatile("s_waitcnt vmcnt(0)" ::: "memory");
      if constexpr (g == POLL_AT) flagv = __builtin_amdgcn_raw_buffer_load_b32(rws, w == 0 ? c * 128 : OOB, WS_CNT + (256 + xcd * 16) * 128, SC1);
      if constexpr (g >= 88 && g < 104 && (g & 1) == 0) wq[1][(g - 88) >> 1] = READ_W(0, (g - 88) >> 1);
      if constexpr (g >= 108 && g < 140 && (g & 3) == 0) ldsw<v4u>((wro ^ ((((g - 108) >> 2) & 3) * 32)) + ((g - 108) >> 2) * 1024, wr[0][(g - 108) >> 2]);
      if constexpr (g >= 125 && g < 141 && (g & 1) == 1) wq[2][(g - 125) >> 1] = READ_W(8192, (g - 125) >> 1);
      if constexpr (g >= 128 && g < 144) { uf[g - 128] = ((g & 1) ? bfhi : bflo)(ue[(g - 128) >> 2][((g - 128) >> 1) & 1]); PIN(uf[g - 128]); }
      SB();
    });
  }
  STAMP(1, 2);
  unsigned late;
  {
    f32x4* red = (f32x4*)(smem + LDS_X) + w * 256 + lane;
    f32x4 keep[4];
    if (eh) {
#pragma unroll
      for (int t = 0; t < 4; ++t) red[t * 64] = cacc[t];
      SB();
#pragma unroll
      for (int t = 0; t < 4; ++t) { PINA(cacc[4 + t]); keep[t] = cacc[4 + t]; PIN(keep[t]); }
    } else {
#pragma unroll
      for (int t = 0; t < 4; ++t) red[t * 64] = cacc[4 + t];
      SB();
#pragma unroll
      for (int t = 0; t < 4; ++t) { PINA(cacc[t]); keep[t] = cacc[t]; PIN(keep[t]); }
    }
    f32x2 pre[4][2];
#pragma unroll
    for (int t = 0; t < 4; ++t) {
      pre[t][0] = f32x2{uf[4 * t], uf[4 * t + 1]} + lo2(keep[t]);
      pre[t][1] = f32x2{uf[4 * t + 2], uf[4 * t + 3]} + hi2(keep[t]);
      PIN(pre[t][0]); PIN(pre[t][1]);
    }
    if (w == 0) *(unsigned*)(smem + LDS_S) = __builtin_amdgcn_ballot_w64(flagv < 16u) != 0ull;
    bar();
    late = *(const unsigned*)(smem + LDS_S);
    const f32x4* rd = (const f32x4*)(smem + LDS_X) + (w ^ 1) * 256 + lane;
    f32x4 oth[4];
#pragma unroll
    for (int t = 0; t < 4; ++t) oth[t] = rd[t * 64];
#pragma unroll
    for (int t = 0; t < 4; ++t) {
      v2u o;
      o[0] = nomark(pk2(pre[t][0] + lo2(oth[t])));
      o[1] = pk2(pre[t][1] + hi2(oth[t]));
      st64<SC1>(o, rws, xoff + t * 512, 0);
    }
  }
  STAMP(1, 3);
  SB();
  v4u wf[3][8];
  const unsigned rot = uh * 4;
#define LOAD_WP(half, k, i) ld128<SC1>(rws, l16 + ((i) & 3) * 1024, WS_WP + (((half) * 44 + ub_own + (k)) * 16 + kh * 8 + (((i) & 4) ^ rot)) * 1024 + ((k) == 2 ? oob3 : 0u))
#define LOAD_XS(i) ld128<SC1>(rws, wsp_v, WS_X1T + ((i) * 32 + w * 8) * 4096 + mt * 128)
  v4u xs[16];
  unsigned tb[2][4], xdst;
  {
    const int row = 8 * q + ((lane >> 2) & 3);
    const int sg = ((row >> 1) & 1) | (((row >> 3) & 1) << 1);
#pragma unroll
    for (int tu = 0; tu < 4; ++tu) {
      const unsigned t0 = lds0 + LDS_A + kh * 32768 + row * 128 + ((tu ^ (2 * kh) ^ sg) * 32) + (lane & 3) * 8;
      tb[0][tu] = t0 + uh * 16384;
      tb[1][tu] = t0 + (uh ^ 1) * 16384;
    }
    const int rl = w * 8 + (lane >> 3), chn = lane & 7;
    const int sg2 = ((rl >> 1) & 1) | (((rl >> 3) & 1) << 1);
    xdst = lds0 + LDS_A + rl * 128 + (((chn >> 1) ^ sg2) * 32) + (chn & 1) * 16;
  }
  SB();
  unsigned z2o[3];
#pragma unroll
  for (int k = 0; k < 3; ++k) { z2o[k] = l16 + WS_Z2 + ((ub_own + k) * 64 + mt * 2 + kh) * 1024 + (k == 2 ? oob3 : 0u); }
  v2u cz[2][3];
  f32x2 csb[2][3];
  float gbv, c1v[2];
  unsigned gq[4];
  unsigned bspq[2];
#define LOAD_CZ(hf, kc) ld64<SC1>(rws, lane * 8, WS_CSB + ((hf) * 44 + ub_own + (kc)) * 512 + ((kc) == 2 ? oob3 : 0u))
  {
    STAMP(1, 20);
    if (__builtin_amdgcn_readfirstlane(late)) {
      if (w == 0) { hint_wait(rws, c * 128, WS_CNT + (256 + xcd * 16) * 128, 16u); __builtin_amdgcn_s_sleep(LATE_SLEEP); }
      __builtin_amdgcn_s_barrier();
    }
    MEMFENCE();
    STAMP(1, 21);
#pragma unroll
    for (int i = 0; i < 3; ++i)
#pragma unroll
      for (int k = 0; k < 3; ++k) wf[k][i] = LOAD_WP(1, k, i);
    SB();
#pragma unroll
  for (int f = 0; f < 8; ++f) wq[0][f] = READ_W(0, f);
#pragma unroll
  for (int f = 0; f < 24; ++f) PINA(wq[f >> 3][f & 7]);
#pragma unroll
    for (int tu = 0; tu < 8; ++tu) PIN(tb[tu >> 2][tu & 3]);
    PIN(xdst);
#pragma unroll
    for (int k = 0; k < 3; ++k) PIN(z2o[k]);
    SB();
#pragma unroll
    for (int i = 3; i < WF_EARLY; ++i)
#pragma unroll
      for (int k = 0; k < 3; ++k) wf[k][i] = LOAD_WP(1, k, i);
    SB();
#pragma unroll
    for (int i = 0; i < 16; ++i) xs[i] = LOAD_XS(i);
    SB();
    STAMP(1, 44);
    unsigned m = 0;
#pragma unroll
    for (int i = 0; i < 16; ++i) {
      m = macc(m, xs[i]); ldsw<v4u>(xdst + i * 4096, xs[i]);
      if (WF_EARLY * 3 + i < 24) wf[(WF_EARLY * 3 + i) % 3][(WF_EARLY * 3 + i) / 3] = LOAD_WP(1, (WF_EARLY * 3 + i) % 3, (WF_EARLY * 3 + i) / 3);
      SB();
    }
    PIN(m);
    STAMP(1, 45);
    RETRY_BEGIN(m)
#pragma unroll
      for (int i = 0; i < 16; ++i) xs[i] = LOAD_XS(i);
      SB();
#pragma unroll
      for (int i = 0; i < 16; ++i) { m = macc(m, xs[i]); ldsw<v4u>(xdst + i * 4096, xs[i]); }
    RETRY_END(m)
  }
  STAMP(1, 4);
  bar();
  STAMP(1, 5);
#define AHALF(tu, i, h) trread_a(tb[(i) >> 2][tu] + ((i) & 3) * 4096 + (h) * 512)
  f32x4 acc[3][4];
  v4u wg[3][8];
  v2u ah[2][4][2];
  float* st = (float*)(smem + LDS_S);
  float* fin = st + 256 + w * 64;
  f32x4* xch = (f32x4*)(smem + LDS_X);
  f32x4 r4[2], tm[3][2];
  {
    f32x4 s1[4], s2[4], m4[2];
    v2u ar[3][2][2];
    float pa[4], pb[4];
    constexpr int ORD[20] = {0, 1, 2, 3, 4, 5, 6, 7, 16, 17, 18, 19, 8, 9, 10, 11, 12, 13, 14, 15};
    int dgo[2], rdo[2];
    dgo[0] = w * 64 + kh * 32 + c; dgo[1] = w * 64 + (kh ^ 1) * 32 + c;
    rdo[0] = (kh * 32 + (lane & 31)) * 4; rdo[1] = rdo[0] * 4 + (lane & 3) * 4;
    PIN(dgo[0]); PIN(dgo[1]); PIN(rdo[0]); PIN(rdo[1]);
    const bool diag = (c >> 2) == q;
#define GFRAG(gi, t, h) AHALF(((gi) < 8 ? 2 : 0) + (t), (gi) < 16 ? (gi) & 7 : (gi) - 16, h)
#pragma unroll
    for (int n = 0; n < 2; ++n)
#pragma unroll
      for (int e = 0; e < 4; ++e) ar[n][e >> 1][e & 1] = GFRAG(ORD[n], e >> 1, e & 1);
    SB();
    sfor<20 * 6>([&](auto G) __attribute__((always_inline)) {
      constexpr int n = decltype(G)::value / 6, j = decltype(G)::value % 6, gi = ORD[n], t = j / 3, k = j % 3;
      constexpr int tu = (gi < 8 ? 2 : 0) + t, i = gi < 16 ? gi & 7 : gi - 16;
      const v4u a = {ar[n % 3][t][0][0], ar[n % 3][t][0][1], ar[n % 3][t][1][0], ar[n % 3][t][1][1]};
      const f32x4 z4 = {0.f, 0.f, 0.f, 0.f};
      if constexpr (gi < 16) acc[k][tu] = mfma(a, wf[k][i], gi == 0 ? z4 : acc[k][tu]);
      if constexpr ((gi >= 4 && gi < 8) || gi >= 16) {
        if constexpr (k == 0) s1[tu] = mfma(ones4, a, (gi == 4 || gi == 16) ? z4 : s1[tu]);
        if constexpr (k == 1) s2[tu] = mfma(a, a, (gi == 4 || gi == 16) ? z4 : s2[tu]);
      }
      if constexpr (j < 4 && n + 2 < 20) ar[(n + 2) % 3][j >> 1][j & 1] = GFRAG(ORD[n + 2], j >> 1, j & 1);
      if constexpr (n >= 18 && j < 4) ah[0][(n - 18) * 2 + (j >> 1)][j & 1] = AHALF((n - 18) * 2 + (j >> 1), 0, j & 1);
      if constexpr (n < 4 && (j == 1 || j == 4)) DMA_W(3, slotB, n * 2 + (j == 4));
      if constexpr (n == 4) cz[j / 3][j % 3] = LOAD_CZ(j / 3, j % 3);
      if constexpr (n == 5 && j < 4) gq[j] = __builtin_amdgcn_raw_buffer_load_b32(rws, lane * 8, WS_MODG + (j * 3072 + 5 * 512 + nt * 64) * 8, SC1);
      if constexpr (n == 5 && j >= 4) bspq[j - 4] = p.bsp[mt * 64 + perm(2 * kh + j - 4, c)];
      if constexpr (n == 8 && j == 1) USE(bspq[1]);
      if constexpr (n >= 8 && n < 12 && (j == 2 || j == 5)) DMA_W(4, slotA, (n - 8) * 2 + (j == 5));
      if constexpr (n >= 12 && j == 4) DMA_W(5, slotB, n - 12);
      if constexpr ((n == 8 || n == 9) && (j & 1) == 0) {
        constexpr int x = (n - 8) * 3 + (j >> 1);
        xch[(w * 6 + (x % 3) * 2 + x / 3) * 64 + lane] = acc[x % 3][2 + x / 3];
      }
      if constexpr (n == 10 && j == 0) {
#pragma unroll
    for (int k = 0; k < 3; ++k) {
      const float y = xrow_sum4(__uint_as_float(cz[1][k][0]), __uint_as_float(cz[1][k][1]), __uint_as_float(cz[1][k][0]), __uint_as_float(cz[1][k][1]));
      auto sw = __builtin_amdgcn_permlane16_swap(__float_as_uint(y), __float_as_uint(y), false, false);
      csb[1][k] = f32x2{__uint_as_float(sw[0]), __uint_as_float(sw[1])};
      PIN(csb[1][k]);
    }
    c1v[0] = xrow_sum4(__uint_as_float(cz[0][0][0]), __uint_as_float(cz[0][0][1]), __uint_as_float(cz[0][1][0]), __uint_as_float(cz[0][1][1]));
    c1v[1] = xrow_sum4(__uint_as_float(cz[0][2][0]), __uint_as_float(cz[0][2][1]), __uint_as_float(cz[0][2][0]), __uint_as_float(cz[0][2][1]));
    PIN(c1v[0]); PIN(c1v[1]);
    gbv = rbf((__uint_as_float(gq[0]) + __uint_as_float(gq[1])) + (__uint_as_float(gq[2]) + __uint_as_float(gq[3])));
    PIN(gbv);
      }
      if constexpr ((n == 8 || n == 9) && j >= 2) wq[3][(n - 8) * 4 + j - 2] = READ_W(8192, (n - 8) * 4 + j - 2);
      if constexpr (n == 10 && j == 5) {
#pragma unroll
        for (int f = 0; f < 8; ++f) PINA(wq[3][f]);
      }
      if constexpr (n == 10 && j == 4) bar();
      if constexpr (n == 11) acc[j % 3][j / 3] = xch[((w ^ 1) * 6 + (j % 3) * 2 + j / 3) * 64 + lane];
      if constexpr (n == 12 && j == 3) {
        float s1v[4];
#pragma unroll
        for (int x = 0; x < 4; ++x) { s1v[x] = s1[x][0]; PIN(s1v[x]); }
        if (diag) {
#pragma unroll
          for (int x = 0; x < 4; ++x) {
            *(float*)(smem + LDS_S + dgo[x >> 1] * 4 + (x & 1) * 64) = s1v[x];
            *(f32x4*)(smem + LDS_S + 2048 + dgo[x >> 1] * 16 + (x & 1) * 256) = s2[x];
          }
        }
      }
      if constexpr (n == 13 && j == 5) bar();
      if constexpr (n == 14 && j < 4) {
        pa[j] = *(const float*)(smem + LDS_S + rdo[0] + j * 256); pb[j] = *(const float*)(smem + LDS_S + 2048 + rdo[1] + j * 1024);
      }
      if constexpr (n == 15 && j == 1) { pa[0] += pa[1]; pa[2] += pa[3]; PIN(pa[0]); PIN(pa[2]); }
      if constexpr (n == 15 && j == 2) { pb[0] += pb[1]; pb[2] += pb[3]; PIN(pb[0]); PIN(pb[2]); }
      if constexpr (n == 15 && j == 3) { pa[0] = (pa[0] + pa[2]) * (1.0f / D); PIN(pa[0]); }
      if constexpr (n == 15 && j == 4) { pb[0] = (pb[0] + pb[2]) * (1.0f / D); PIN(pb[0]); }
      if constexpr (n == 15 && j == 5) { pb[0] = fmaxf(__builtin_fmaf(-pa[0], pa[0], pb[0]), 0.f); PIN(pb[0]); }
      if constexpr (n == 16 && j == 0) { pb[0] = __builtin_amdgcn_rsqf(pb[0] + EPS); PIN(pb[0]); }
      if constexpr (n == 16 && j == 1) fin[lane] = lane < 32 ? pb[0] : -pb[0] * pa[0];
      if constexpr (n == 16 && (j == 2 || j == 3)) { m4[j - 2] = *(const f32x4*)(fin + 32 + (j - 2) * 16 + q * 4); r4[j - 2] = *(const f32x4*)(fin + (j - 2) * 16 + q * 4); }
      if constexpr (n >= 18) {
        constexpr int x = (n - 18) * 6 + j, kk = x >> 2, tt = (x >> 1) & 1, h = (x & 1) * 2;
        float t0 = __builtin_fmaf(m4[tt][h], csb[1][kk][0], csb[1][kk][1]);
        PIN(t0);
        float t1 = __builtin_fmaf(m4[tt][h + 1], csb[1][kk][0], csb[1][kk][1]);
        PIN(t1);
        tm[kk][tt][h] = t0; tm[kk][tt][h + 1] = t1;
      }
      if constexpr (n >= 12 && n < 15 && j == 5) st128<SC1>(mk, rws, z2o[n - 12], 0);
      if constexpr (n == 7 && j == 5) STAMP(1, 46);
      if constexpr (n == 11 && j == 5) STAMP(1, 47);
      SB();
    });
  }
#pragma unroll
  for (int k = 0; k < 6; ++k) PINA(acc[k >> 1][k & 1]);
  STAMP(1, 6);
  f32x4 z2v[3][2];
  {
    f32x4 rs[2], rq[2];
#pragma unroll
    for (int t = 0; t < 2; ++t) {
#pragma unroll
      for (int k = 0; k < 3; ++k) {
        const f32x4 a = acc[k][t];
        const f32x2 zl = gelu2(fma2(lo2(a), lo2(r4[t]), lo2(tm[k][t])));
        const f32x2 zh = gelu2(fma2(hi2(a), hi2(r4[t]), hi2(tm[k][t])));
        z2v[k][t] = f32x4{zl[0], zl[1], zh[0], zh[1]};
        if (k == 0) { rs[t] = z2v[k][t]; rq[t] = z2v[k][t] * z2v[k][t]; }
        else { rs[t] += z2v[k][t]; rq[t] += z2v[k][t] * z2v[k][t]; }
        if (((t * 3 + k) & 1) == 0) {
#pragma unroll
          for (int f = (t * 3 + k) * 4; f < (t * 3 + k) * 4 + 8; ++f) wg[f % 3][f / 3] = LOAD_WP(0, f % 3, f / 3);
        } else {
          PIN(z2v[k][t]);
          SB();
        }
      }
    }
    STAMP(1, 50);
    float v8[8], v4[4], v2[2];
    const bool b3 = c & 8, b2 = c & 4, b1 = c & 2, b0 = c & 1;
#pragma unroll
    for (int r_ = 0; r_ < 4; ++r_) {
      const float a0 = rs[0][r_] + dpp<0x128>(rs[0][r_]), a1 = rs[1][r_] + dpp<0x128>(rs[1][r_]);
      const float q0 = rq[0][r_] + dpp<0x128>(rq[0][r_]), q1 = rq[1][r_] + dpp<0x128>(rq[1][r_]);
      v8[2 * r_] = b3 ? a1 : a0; v8[2 * r_ + 1] = b3 ? q1 : q0;
    }
#pragma unroll
    for (int j = 0; j < 4; ++j) {
      const float x0 = v8[j] + dpp<0x141>(v8[j]), x1 = v8[4 + j] + dpp<0x141>(v8[4 + j]);
      v4[j] = b2 ? x1 : x0;
    }
#pragma unroll
    for (int j = 0; j < 2; ++j) {
      const float x0 = v4[j] + dpp<0x4E>(v4[j]), x1 = v4[2 + j] + dpp<0x4E>(v4[2 + j]);
      v2[j] = b1 ? x1 : x0;
    }
    const float x0 = v2[0] + dpp<0xB1>(v2[0]), x1 = v2[1] + dpp<0xB1>(v2[1]);
    const float outv = b0 ? x1 : x0;
    __builtin_amdgcn_raw_buffer_store_b32(nomark(__float_as_uint(outv)), rws, lane * 4, WS_ST2 + ((mt * 2 + kh) * 16 + nt * 2 + uh) * 256, SC1);
  }
  STAMP(1, 7);
  SB();
#pragma unroll
  for (int tu = 0; tu < 4; ++tu)
#pragma unroll
    for (int k = 0; k < 3; ++k) acc[k][tu] = f32x4{0.f, 0.f, 0.f, 0.f};
  const int k30 = w * 6 - (w >> 1) * (w & 1);
  const unsigned oob6 = (unsigned)(w >> 1) << 30;
  v2u x1h[2][2];
  int x1o;
  {
    const int row = 8 * q + ((lane >> 2) & 3);
    const int sg = ((row >> 1) & 1) | (((row >> 3) & 1) << 1);
    x1o = LDS_A + nt * 8192 + row * 128 + ((w ^ sg) * 32) + (lane & 3) * 8;
  }
  v4u wo[6][4];
#define LOAD_WO(f) wo[(f) >> 2][(f) & 3] = ld128<SC1>(rws, l16 + (((f) >> 2) & 3) * 1024, WS_WOT + ((nt * 4 + ((f) & 3)) * 22 + k30 + (((f) >> 2) & 4)) * 1024 + ((f) >= 20 ? oob6 : 0u))
  f32x4 pt[4];
  float* c1s = st + 768 + w * 96;
#define LOAD_PT(i) asf(ld128<SC1>(rws, l16 + (i) * 1024, WS_ST2 + (mt * 2 + kh) * 4096))
  SB();
#pragma unroll
  for (int i = 0; i < 8; ++i) {
#pragma unroll
    for (int j = 0; j < 12; ++j) {
      const int tu = j / 3, k = j % 3;
      const v4u a = {ah[i & 1][tu][0][0], ah[i & 1][tu][0][1], ah[i & 1][tu][1][0], ah[i & 1][tu][1][1]};
      acc[k][tu] = mfma(wg[k][i], a, acc[k][tu]);
      if (j < 8 && i < 7) ah[(i + 1) & 1][j >> 1][j & 1] = AHALF(j >> 1, i + 1, j & 1);
      if (j < 4 && i == 7) x1h[j >> 1][j & 1] = trread(smem + x1o + (j >> 1) * 4096 + (j & 1) * 512);
      if (i == 0 && j == 0) asm volatile("s_waitcnt vmcnt(24)" ::: "memory");
      if (i < 2 && j >= 4) wq[4 + i][j - 4] = READ_W(i * 8192, j - 4);
      if (i == 3 && j == 0) {
#pragma unroll
        for (int f = 0; f < 16; ++f) PINA(wq[4 + (f >> 3)][f & 7]);
      }
      if (i == 4 && j == 0) { USE(wg[0][7]); USE(wg[1][7]); USE(wg[2][7]); }
      if ((i == 2 || i == 3) && j % 3 == 2) dma_sel(IC<6>{}, slotA, (i - 2) * 4 + j / 3);
      if ((i == 4 || i == 5) && j % 3 == 2) dma_sel(IC<7>{}, slotB, (i - 4) * 4 + j / 3);
      if (j >= 8 && i == 6) pt[j - 8] = LOAD_PT(j - 8);
      if (i == 7 && j >= 8) LOAD_WO(j - 8);
      if (i == 5 && j == 0) {
        asm volatile("s_waitcnt vmcnt(30)" ::: "memory");
        __builtin_amdgcn_s_barrier();
      }
      if (i == 5 && j == 1) st[1408 + lane] = gbv;
      if (i == 5 && j == 9) c1s[lane] = c1v[0];
      if (i == 5 && j == 10) c1s[64 + (lane & 31)] = c1v[1];
      SB();
    }
  }
  STAMP(1, 8);
#pragma unroll
  for (int k = 0; k < 3; ++k)
#pragma unroll
    for (int t = 0; t < 2; ++t) xch[(w * 6 + k * 2 + t) * 64 + lane] = acc[k][2 + t];
  SB();
  {
    unsigned m = 0;
#pragma unroll
    for (int i = 0; i < 4; ++i) m = macc4(m, __builtin_bit_cast(v4u, pt[i]));
    RETRY_BEGIN(m)
#pragma unroll
      for (int i = 0; i < 4; ++i) { pt[i] = LOAD_PT(i); m = macc4(m, __builtin_bit_cast(v4u, pt[i])); }
    RETRY_END(m)
    if (tid < 4) hint_add(p.ws, 64 + (nt * 4 + (mt >> 3)) * 4 + tid);
    const f32x4 sx = (pt[0] + pt[1]) + (pt[2] + pt[3]);
    const float tv = xrow_sum4(sx[0], sx[2], sx[1], sx[3]);
    auto sw = __builtin_amdgcn_permlane32_swap(__float_as_uint(tv), __float_as_uint(tv), false, false);
    const float mm = __uint_as_float(sw[0]) * (1.0f / FH);
    const float r = __builtin_amdgcn_rsqf(fmaxf(__uint_as_float(sw[1]) * (1.0f / FH) - mm * mm, 0.f) + EPS);
    float* f2 = st + 512 + w * 64;
    f2[(q >> 1) * 32 + 2 * c + (q & 1)] = (q >> 1) ? -r * mm : r;
    f32x4 r4[2], m4[2];
#pragma unroll
    for (int t = 0; t < 2; ++t) { r4[t] = *(const f32x4*)(f2 + q * 8 + t * 4); m4[t] = *(const f32x4*)(f2 + 32 + q * 8 + t * 4); }
#pragma unroll
    for (int k = 0; k < 3; ++k) {
      v4u o;
#pragma unroll
      for (int t = 0; t < 2; ++t) {
        o[2 * t] = nomark(pk2(fma2(lo2(z2v[k][t]), lo2(r4[t]), lo2(m4[t]))));
        o[2 * t + 1] = pk2(fma2(hi2(z2v[k][t]), hi2(r4[t]), hi2(m4[t])));
      }
      st128<SC1>(o, rws, z2o[k], 0);
    }
  }
  STAMP(1, 9);
  SB();
  f32x4 z1f[3][2];
  constexpr int RD = 12;
  v4u zf[RD];
#define LOAD_Z(n) ld128<SC1>(rws, zv[((n) >> 1) & 7] + ((n) & 1) * 1024, WS_Z2 + ((((n) < 48 ? ub_oth : ub_own) + ((n) >> 4) % 3) * 64 + w * 16) * 1024 + (((n) >> 4) % 3 == 2 ? oob3 : 0u))
  unsigned zv[8];
#pragma unroll
  for (int i = 0; i < 8; ++i) { zv[i] = l16 + ((CKMAP(i) + kr) & 7) * 2048; PIN(zv[i]); }
  {
    bar();
    STAMP(1, 34);
#pragma unroll
    for (int k = 0; k < 6; ++k) PINA(acc[k >> 1][k & 1]);
    f32x4 oth[3][2], cs1[3], bs1[3];
    float r1[2], m1[2];
#pragma unroll
    for (int t = 0; t < 2; ++t) { r1[t] = fin[t * 16 + c]; m1[t] = fin[32 + t * 16 + c]; }
#pragma unroll
    for (int k = 0; k < 3; ++k) {
      cs1[k] = *(const f32x4*)(c1s + k * 32 + q * 4); bs1[k] = *(const f32x4*)(c1s + k * 32 + 16 + q * 4);
#pragma unroll
      for (int t = 0; t < 2; ++t) oth[k][t] = xch[((w ^ 1) * 6 + k * 2 + t) * 64 + lane];
    }
#pragma unroll
    for (int i = 4; i < WO0; ++i) LOAD_WO(i);
    SB();
    f32x2 z1a[6][2];
#pragma unroll
    for (int i = 0; i < 6; ++i) {
      const int k = i >> 1, t = i & 1;
      const f32x4 a = acc[k][t] + oth[k][t];
      z1a[i][0] = fma2(lo2(a), bc2(r1[t]), fma2(lo2(cs1[k]), bc2(m1[t]), lo2(bs1[k])));
      z1a[i][1] = fma2(hi2(a), bc2(r1[t]), fma2(hi2(cs1[k]), bc2(m1[t]), hi2(bs1[k])));
    }
#pragma unroll
    for (int i = 0; i < GELU_PRE; ++i) {
      const f32x2 lo = gelu2(z1a[i][0]), hi = gelu2(z1a[i][1]);
      z1f[i >> 1][i & 1] = f32x4{lo[0], lo[1], hi[0], hi[1]};
      PIN(z1f[i >> 1][i & 1]);
    }
#pragma unroll
    for (int i = GELU_PRE; i < 6; ++i) { PIN(z1a[i][0]); PIN(z1a[i][1]); }
    SB();
    STAMP(1, 10);
    hint_wait(rws, 0u, WS_CNT + (64 + (nt * 4 + w) * 4 + (xcd >> 1)) * 128, 8u);
    STAMP(1, 22);
#pragma unroll
    for (int n = 0; n < RD; ++n) zf[n] = LOAD_Z(n);
    SB();
#pragma unroll
    for (int i = GELU_PRE; i < 6; ++i) {
      const f32x2 lo = gelu2(z1a[i][0]), hi = gelu2(z1a[i][1]);
      z1f[i >> 1][i & 1] = f32x4{lo[0], lo[1], hi[0], hi[1]};
      PIN(z1f[i >> 1][i & 1]);
    }
  }
  f32x4* xb = (f32x4*)(smem + LDS_A) + lane;
  f32x4* yb = (f32x4*)(smem + LDS_A + 49152) + lane;
  f32x4 ac2[2][4], ini[4], yo[2];
  const float bs0 = bf2f(bspq[0]), bs1_ = bf2f(bspq[1]);
  const f32x4 bq0 = {bs0, bs0, bs0, bs0}, bq1 = {bs1_, bs1_, bs1_, bs1_}, z4 = {0.f, 0.f, 0.f, 0.f};
  unsigned voff[2];
#pragma unroll
  for (int t = 0; t < 2; ++t) voff[t] = WS_V + (mt * 4 + 2 * kh + t) * 22 * 1024;
  float ev[4];
  unsigned eo0;
  SB();
  STAMP(1, 11);
  asm volatile("s_waitcnt vmcnt(30)" ::: "memory");
  auto v_store = [&](int k, int t, unsigned o0, unsigned o1) __attribute__((always_inline)) {
    const int uu = ub_own + k;
    st64<SC1>(v2u{o0, o1}, rws, l16, voff[t] + (uu >> 1) * 1024 + (uu & 1) * 8 + (k == 2 ? oob3 : 0u));
  };
  sfor<6>([&](auto P) __attribute__((always_inline)) {
    constexpr int ps = decltype(P)::value, cur = ps & 1, prv = cur ^ 1;
    unsigned zm = 0;
    sfor<64>([&](auto G) __attribute__((always_inline)) {
      constexpr int g = decltype(G)::value, ks = g >> 2, tu = g & 3, ck = ks >> 1, k2 = ks & 1, n = ps * 16 + ks;
      const f32x4 cin = ks > 0 ? ac2[cur][tu] : ps >= 3 ? ini[tu] : tu == 0 ? bq0 : tu == 1 ? bq1 : z4;
      ac2[cur][tu] = mfma(zf[n % RD], wq[ck][k2 * 4 + tu], cin);
      if constexpr (tu == 1) { zm = macc(zm, zf[n % RD]); PIN(zm); }
      if constexpr (tu == 3) {
        if constexpr (n + RD < 96) zf[n % RD] = LOAD_Z(n + RD);
        else if constexpr (WO0 + n + RD - 96 < 24) LOAD_WO(WO0 + n + RD - 96);
      }
      if constexpr (ps == 0 && g < 32 && (g & 1) == 0) wq[6 + (g >> 4)][(g >> 1) & 7] = READ_W((g >> 4) * 8192, (g >> 1) & 7);
      if constexpr (ps >= 1 && ps <= 3 && g >= 2 && g < 18 && (g & 3) == 2) xb[((w * 3 + ps - 1) * 4 + (g >> 2)) * 64] = ac2[prv][g >> 2];
      if constexpr (ps >= 4 && (g == 2 || g == 6)) yb[((w * 3 + ps - 4) * 2 + (g >> 2)) * 64] = ac2[prv][2 + (g >> 2)];
      if constexpr (ps >= 1 && g == (ps >= 4 ? 12 : 24)) {
        if constexpr (ps == 4) asm volatile("s_waitcnt vmcnt(20)" ::: "memory");
        bar();
      }
      if constexpr (ps >= 4 && (g == 13 || g == 14)) yo[g - 13] = yb[(((w ^ 1) * 3 + ps - 4) * 2 + g - 13) * 64];
      if constexpr (ps >= 4 && g >= 22 && g < 42) {
        constexpr int t = (g - 22) / 10, ph = (g - 22) % 10;
        if constexpr (ph < 4) { ev[ph] = ac2[prv][t][ph] + yo[t][ph]; PIN(ev[ph]); }
        else if constexpr (ph < 8) { ev[ph - 4] *= z1f[ps - 4][t][ph - 4]; PIN(ev[ph - 4]); }
        else if constexpr (ph == 8) { eo0 = nomark(pk2(ev[0], ev[1])); PIN(eo0); }
        else v_store(ps - 4, t, eo0, pk2(ev[2], ev[3]));
      }
      if constexpr (ps >= 2 && ps <= 4 && g >= 28 && g < 44 && (g & 3) == 0) ini[(g - 28) >> 2] = xb[(((w ^ 2) * 3 + ps - 2) * 4 + ((g - 28) >> 2)) * 64];
      if constexpr (ps == 0 && (g == 21 || g == 37 || g == 53)) st128<SC1>(mk, rws, l16, WS_V + ((mt * 4 + w) * 22 + pr0 + (g - 21) / 16) * 1024 + (g == 53 ? oob3 : 0u));
      if constexpr (ps == 5 && g == HINT4_G) { if (tid < 2) hint_add(p.ws, 192 + mt * 2 + tid); }
      if constexpr (g == 0 && ps >= 1 && ps < 5) STAMP(1, 55 + ps);
      SB();
    });
    if (__builtin_expect(any_mark(zm), 0)) {
#pragma unroll
      for (int tu = 0; tu < 4; ++tu) ac2[cur][tu] = ps >= 3 ? xb[(((w ^ 2) * 3 + ps - 3) * 4 + tu) * 64] : tu == 0 ? bq0 : tu == 1 ? bq1 : z4;
      sfor<16>([&](auto K) __attribute__((always_inline)) {
        constexpr int ks = decltype(K)::value, ck = ks >> 1, k2 = ks & 1;
        v4u zz;
        unsigned sp = 0;
        do { MEMFENCE(); zz = LOAD_Z(ps * 16 + ks); } while (any_mark(macc(0u, zz)) & (++sp <= SPIN_CAP));
#pragma unroll
        for (int tu = 0; tu < 4; ++tu) ac2[cur][tu] = mfma(zz, wq[ck][k2 * 4 + tu], ac2[cur][tu]);
      });
    }
  });
  STAMP(1, 12);
  {
#pragma unroll
    for (int t = 0; t < 2; ++t) yb[((w * 3 + 2) * 2 + t) * 64] = ac2[1][2 + t];
    PIN(ac2[1][0]); PIN(ac2[1][1]);
    bar();
    STAMP(1, 52);
#pragma unroll
    for (int t = 0; t < 2; ++t) yo[t] = yb[(((w ^ 1) * 3 + 2) * 2 + t) * 64];
#pragma unroll
    for (int t = 0; t < 2; ++t) {
      const f32x4 g = (ac2[1][t] + yo[t]) * z1f[2][t];
      v_store(2, t, nomark(pk2(lo2(g))), pk2(hi2(g)));
    }
  }
  STAMP(1, 13);
  SB();
  f32x4 gb4[4];
  f32x2 x1f[2][4];
#pragma unroll
  for (int i = 0; i < 4; ++i) gb4[i] = *(const f32x4*)(st + 1408 + (i >> 1) * 32 + q * 8 + (i & 1) * 4);
#pragma unroll
  for (int i = 0; i < 8; ++i) { x1f[i >> 2][i & 3] = unpk(x1h[i >> 2][(i >> 1) & 1][i & 1]); PIN(x1f[i >> 2][i & 3]); }
  STAMP(1, 53);
  if (w == 0) hint_wait(rws, 0u, WS_CNT + (192 + mt * 2 + (xcd & 1)) * 128, 8u);
  __builtin_amdgcn_s_barrier();
  MEMFENCE();
  STAMP(1, 23);
  f32x4 a3[4][4];
  {
    unsigned spins = 0;
    for (;;) {
      MEMFENCE();
      v4u vs[6][4];
#define LOAD_V(f) vs[(f) >> 2][(f) & 3] = ld128<SC1>(rws, l16 + (((f) >> 2) & 3) * 1024, WS_V + ((mt * 4 + (((f) & 3) ^ w)) * 22 + k30 + (((f) >> 2) & 4)) * 1024 + ((f) >= 20 ? oob6 : 0u))
#pragma unroll
      for (int f = 0; f < 12; ++f) LOAD_V(f);
      SB();
      unsigned m = 0;
      sfor<96>([&](auto G) __attribute__((always_inline)) {
        constexpr int g = decltype(G)::value, ks = g >> 4, tu = (g >> 2) & 3, e = g & 3;
        a3[e][tu] = mfma(wo[ks][e], vs[ks][tu], ks == 0 ? f32x4{0.f, 0.f, 0.f, 0.f} : a3[e][tu]);
        if constexpr (e == 0 && 12 + (g >> 2) < 24) LOAD_V(12 + (g >> 2));
        if constexpr (e == 1) { m = macc(m, vs[ks][tu]); PIN(m); }
        SB();
      });
      if (!(any_mark(m) & (++spins <= SPIN_CAP))) break;
    }
  }
  STAMP(1, 14);
  {
    f32x4* red = (f32x4*)(smem + LDS_W);
#pragma unroll
    for (int tu = 1; tu < 4; ++tu)
#pragma unroll
      for (int e = 0; e < 4; ++e) red[((w * 3 + tu - 1) * 4 + e) * 64 + lane] = a3[e][tu];
#pragma unroll
    for (int e = 0; e < 4; ++e) PIN(a3[e][0]);
    bar();
    STAMP(1, 54);
    f32x4 o3[4][3];
#pragma unroll
    for (int e = 0; e < 4; ++e)
#pragma unroll
      for (int rel = 1; rel < 4; ++rel) o3[e][rel - 1] = red[(((w ^ rel) * 3 + rel - 1) * 4 + e) * 64 + lane];
    const rsrc_t rout = mkrs(p.out, L * D * 2);
    SB();
#pragma unroll
    for (int pp = 0; pp < 2; ++pp) {
      v4u o;
#pragma unroll
      for (int h = 0; h < 2; ++h) {
        const int e = pp * 2 + h;
        const f32x4 y = (a3[e][0] + o3[e][0]) + (o3[e][1] + o3[e][2]);
        o[2 * h] = pk2(fma2(lo2(y), lo2(gb4[e]), x1f[pp][2 * h]));
        o[2 * h + 1] = pk2(fma2(hi2(y), hi2(gb4[e]), x1f[pp][2 * h + 1]));
      }
      st128<0>(o, rout, perm(w, c) * 1024 + q * 16 + pp * 64, mt * 64 * 1024 + nt * 128);
    }
  }
  if (b == 0 && tid == 0) *(unsigned*)(p.ws + WS_EPOCH) = tag_s;
  STAMP(1, 15);
  STAMP_FLUSH(1);
  asm volatile("adaln_code_end:");
}

extern "C" void kernel_launch(void* const* d_in, const int* in_sizes, int n_in, void* d_out, int out_size, void* d_ws,
                              size_t ws_size, hipStream_t stream) {
  Params p;
  p.u = (const u16*)d_in[0];
  p.lar = (const u16*)d_in[1];
  p.aim = (const u16*)d_in[2];
  p.cre = (const u16*)d_in[3];
  p.cim = (const u16*)d_in[4];
  p.ldt = (const u16*)d_in[5];
  p.dsk = (const u16*)d_in[6];
  p.win = (const u16*)d_in[8];
  p.wsp = (const u16*)d_in[9];
  p.bsp = (const u16*)d_in[10];
  p.wout = (const u16*)d_in[11];
  p.c = (const u16*)d_in[12];
  p.wmod = (const u16*)d_in[13];
  p.out = (u16*)d_out;
  p.ws = (unsigned char*)d_ws;
  (void)hipFuncSetAttribute((const void*)adaln_mega, hipFuncAttributeMaxDynamicSharedMemorySize, MEGA_LDS);
  adaln_mega<<<256, 256, MEGA_LDS, stream>>>(p);
}
```

```cpp
#define HINT4_G 290
#include <hip/hip_runtime.h>
#include <hip/hip_bf16.h>
#include <stdint.h>
#include <utility>

#define LAS __attribute__((address_space(3)))
typedef unsigned v4u __attribute__((ext_vector_type(4)));
typedef unsigned v2u __attribute__((ext_vector_type(2)));
typedef float f32x4 __attribute__((ext_vector_type(4)));
typedef float f32x2 __attribute__((ext_vector_type(2)));
typedef short bf16x8 __attribute__((ext_vector_type(8)));
typedef _Float16 f16x8 __attribute__((ext_vector_type(8)));
typedef _Float16 f16x2 __attribute__((ext_vector_type(2)));
typedef __bf16 bf16x2_t __attribute__((ext_vector_type(2)));
typedef unsigned short u16;
typedef __amdgpu_buffer_rsrc_t rsrc_t;

#define STAMP_DECL do { } while (0)
#define STAMP(k, i) do { } while (0)
#define STAMP_FLUSH(k) do { } while (0)

#define OPT_A 1
#define OPT_B 1
#define OPT_C 2
#define OPT_D 1
#define OPT_E 2
#define OPT_F 1
constexpr int L = 2048, D = 512, NM = 128, FH = 688, FF = 1376;
constexpr float EPS = 1e-5f;
constexpr int SC1 = 16;
constexpr unsigned OOB = 0x40000000u;
constexpr unsigned SPIN_CAP = 1u << 14;

constexpr unsigned WS_EPOCH = 0;
constexpr unsigned WS_CNT = 4096;
constexpr unsigned WS_SR = WS_CNT + 512 * 128;
constexpr unsigned WS_SB = WS_SR + 2048 * 8;
constexpr unsigned WS_MODG = WS_SB + 2048 * 8;
constexpr unsigned WS_CSB = WS_MODG + 3072 * 32;
constexpr unsigned WS_ST2 = WS_CSB + 88 * 16 * 32;
constexpr unsigned WS_UG = 1u << 20;
static_assert(WS_ST2 + 32 * 2 * 16 * 32 * 8 <= WS_UG, "small buffers");
constexpr unsigned WS_X1T = WS_UG + 256 * 2048 * 8;
constexpr unsigned WS_WP = WS_X1T + 512 * 2048 * 2;
constexpr unsigned WS_WOT = WS_WP + 88 * 16 * 1024;
constexpr unsigned WS_Z2 = WS_WOT + 32 * 22 * 1024;
constexpr unsigned WS_V = WS_Z2 + 44 * 64 * 1024;
constexpr unsigned WS_END = WS_V + 128 * 22 * 1024;

struct Params {
  const u16 *u, *lar, *aim, *cre, *cim, *ldt, *dsk, *win, *wsp, *bsp, *wout, *c, *wmod;
  u16* out;
  unsigned char* ws;
};

#define DI __device__ __forceinline__
template <int V> struct IC { static constexpr int value = V; };
template <class F, int... I> DI void sfor_(F&& f, std::integer_sequence<int, I...>) { int d[] = {(f(IC<I>{}), 0)...}; (void)d; }
template <int N, class F> DI void sfor(F&& f) { sfor_(f, std::make_integer_sequence<int, N>{}); }
DI float bf2f(unsigned b) { return __builtin_bit_cast(float, b << 16); }
DI float bflo(unsigned w) { return __builtin_bit_cast(float, w << 16); }
DI float bfhi(unsigned w) { return __builtin_bit_cast(float, w & 0xFFFF0000u); }
DI f32x2 unpk(unsigned w) { return f32x2{bflo(w), bfhi(w)}; }
DI unsigned pk2(f32x2 v) { return __builtin_bit_cast(unsigned, __builtin_convertvector(v, bf16x2_t)); }
DI unsigned pk2(float lo, float hi) { return pk2(f32x2{lo, hi}); }
DI unsigned nomark(unsigned x) { return x < 0xFFFFFFFEu ? x : 0xFFFFFFFEu; }
DI float rbf(float x) { return bflo(pk2(x, 0.f)); }
DI f32x2 fma2(f32x2 a, f32x2 b, f32x2 c) { return __builtin_elementwise_fma(a, b, c); }
DI f32x2 lo2(f32x4 v) { return f32x2{v[0], v[1]}; }
DI f32x2 hi2(f32x4 v) { return f32x2{v[2], v[3]}; }
DI f32x2 bc2(float x) { return f32x2{x, x}; }
DI f32x4 mfma(v4u a, v4u b, f32x4 c) {
  return __builtin_amdgcn_mfma_f32_16x16x32_bf16(__builtin_bit_cast(bf16x8, a), __builtin_bit_cast(bf16x8, b), c, 0, 0, 0);
}
DI f32x4 mfma_h(v4u a, v4u b, f32x4 c) {
  return __builtin_amdgcn_mfma_f32_16x16x32_f16(__builtin_bit_cast(f16x8, a), __builtin_bit_cast(f16x8, b), c, 0, 0, 0);
}
DI float dot2(unsigned a, unsigned b, float acc) {
  return __builtin_amdgcn_fdot2_f32_bf16(__builtin_bit_cast(bf16x2_t, a), __builtin_bit_cast(bf16x2_t, b), acc, false);
}
constexpr unsigned ONES = 0x3F803F80u;
DI int perm(int u, int c) { return 32 * (u >> 1) + 8 * (c >> 2) + 4 * (u & 1) + (c & 3); }
template <int CTRL> DI float dpp(float x) {
  return __builtin_bit_cast(float, __builtin_amdgcn_mov_dpp(__builtin_bit_cast(int, x), CTRL, 0xf, 0xf, true));
}
DI float row16_sum(float x) {
  x += dpp<0xB1>(x); x += dpp<0x4E>(x); x += dpp<0x141>(x); x += dpp<0x128>(x);
  return x;
}
DI float row16_max(float x) {
  x = fmaxf(x, dpp<0xB1>(x)); x = fmaxf(x, dpp<0x4E>(x)); x = fmaxf(x, dpp<0x141>(x)); x = fmaxf(x, dpp<0x128>(x));
  return x;
}
DI float xrow_sum(float x) {
  auto s = __builtin_amdgcn_permlane16_swap(__float_as_uint(x), __float_as_uint(x), false, false);
  x = __uint_as_float(s[0]) + __uint_as_float(s[1]);
  auto t = __builtin_amdgcn_permlane32_swap(__float_as_uint(x), __float_as_uint(x), false, false);
  return __uint_as_float(t[0]) + __uint_as_float(t[1]);
}
DI float xrow_sum4(float a, float b, float c, float d) {
  auto s = __builtin_amdgcn_permlane16_swap(__float_as_uint(a), __float_as_uint(b), false, false);
  auto t = __builtin_amdgcn_permlane16_swap(__float_as_uint(c), __float_as_uint(d), false, false);
  const float x = __uint_as_float(s[0]) + __uint_as_float(s[1]), y = __uint_as_float(t[0]) + __uint_as_float(t[1]);
  auto z = __builtin_amdgcn_permlane32_swap(__float_as_uint(x), __float_as_uint(y), false, false);
  return __uint_as_float(z[0]) + __uint_as_float(z[1]);
}
DI float xrow_max(float x) {
  auto s = __builtin_amdgcn_permlane16_swap(__float_as_uint(x), __float_as_uint(x), false, false);
  x = fmaxf(__uint_as_float(s[0]), __uint_as_float(s[1]));
  auto t = __builtin_amdgcn_permlane32_swap(__float_as_uint(x), __float_as_uint(x), false, false);
  return fmaxf(__uint_as_float(t[0]), __uint_as_float(t[1]));
}
DI f32x2 gelu2(f32x2 x) {
  const float k0 = -2.0f * 0.7978845608028654f * 1.4426950408889634f, k1 = k0 * 0.044715f;
  const f32x2 a = x * fma2(x * x, bc2(k1), bc2(k0));
  const f32x2 e = f32x2{__builtin_amdgcn_exp2f(a[0]), __builtin_amdgcn_exp2f(a[1])} + bc2(1.0f);
  return x * f32x2{__builtin_amdgcn_rcpf(e[0]), __builtin_amdgcn_rcpf(e[1])};
}
DI rsrc_t mkrs(const void* ptr, unsigned bytes) { return __builtin_amdgcn_make_buffer_rsrc((void*)ptr, 0, (int)bytes, 0x00020000); }
template <int AUX> DI v4u ld128(rsrc_t rs, unsigned v, unsigned s) { return __builtin_amdgcn_raw_buffer_load_b128(rs, (int)v, (int)s, AUX); }
template <int AUX> DI v2u ld64(rsrc_t rs, unsigned v, unsigned s) { return __builtin_amdgcn_raw_buffer_load_b64(rs, (int)v, (int)s, AUX); }
template <int AUX> DI void st128(v4u x, rsrc_t rs, unsigned v, unsigned s) { __builtin_amdgcn_raw_buffer_store_b128(x, rs, (int)(v + s), 0, AUX); }
template <int AUX> DI void st64(v2u x, rsrc_t rs, unsigned v, unsigned s) { __builtin_amdgcn_raw_buffer_store_b64(x, rs, (int)v, (int)s, AUX); }
DI f32x4 asf(v4u x) { return __builtin_bit_cast(f32x4, x); }
DI float sum4(f32x4 x) { return (x[0] + x[1]) + (x[2] + x[3]); }
DI unsigned umax3(unsigned a, unsigned b, unsigned c) { unsigned m = a > b ? a : b; return m > c ? m : c; }
DI unsigned macc4(unsigned m, v4u x) { return umax3(umax3(m, x[0], x[1]), x[2], x[3]); }
DI unsigned macc(unsigned m, v4u x) { return umax3(m, x[0], x[2]); }
DI bool any_mark(unsigned m) { return __builtin_amdgcn_ballot_w64(m == 0xFFFFFFFFu) != 0ull; }
#define SB() __builtin_amdgcn_sched_barrier(0)
#define PIN(x) asm volatile("" : "+v"(x))
#define PINA(x) asm volatile("" : "+a"(x))
#define USE(x) asm volatile("" ::"v"(x))
#define RETRY_BEGIN(m) if (__builtin_expect(any_mark(m), 0)) { unsigned sp_ = 0; do { MEMFENCE(); m = 0;
#define RETRY_END(m) } while (any_mark(m) & (++sp_ <= SPIN_CAP)); }
#define MEMFENCE() asm volatile("" ::: "memory")
DI void bar() { asm volatile("s_waitcnt lgkmcnt(0)" ::: "memory"); __builtin_amdgcn_s_barrier(); asm volatile("" ::: "memory"); }

DI f32x2 cmul(f32x2 a, f32x2 b) { return fma2(bc2(a[1]), f32x2{-b[1], b[0]}, bc2(a[0]) * b); }
DI f32x2 cexp_p(float are2, float rev_hi, float rev_lo, float p) {
  const float mag = __builtin_amdgcn_exp2f(are2 * p);
  const float hi = rev_hi * p;
  const float fr = (hi - __builtin_rintf(hi)) + rev_lo * p;
  return f32x2{mag * __builtin_amdgcn_cosf(fr), mag * __builtin_amdgcn_sinf(fr)};
}
DI float exp_acc(float x) {
  const float hi = x * 1.4426950408889634f;
  const float lo = __builtin_fmaf(x, 1.4426950408889634f, -hi) + x * 1.925963033500011e-8f;
  const float e = __builtin_amdgcn_exp2f(hi);
  return __builtin_fmaf(e, lo * 0.6931471805599453f, e);
}
DI unsigned pkh(float a, float b) {
  f16x2 v = {(_Float16)a, (_Float16)b};
  return __builtin_bit_cast(unsigned, v);
}

constexpr int CV_RK = 4160;
constexpr int CV_H = 2 * CV_RK;
constexpr int CV_CH = CV_H + 4608;
constexpr int LDS_A = 0;
constexpr int LDS_U = 2 * CV_CH;
constexpr int LDS_GS = LDS_U + 8192;
constexpr int LDS_TAB = 36864;
constexpr int KP_CH = 13840;
constexpr int LDS_KP = LDS_TAB + 2 * 64 * 528 + 16;
constexpr int LDS_LN = 0, LDS_MR = 8448;
constexpr int LDS_WI = 145472, LDS_WO = LDS_WI + 10240;
constexpr int LDS_X = 65536;
constexpr int LDS_S = LDS_X + 24576;
constexpr int LDS_W = LDS_S + 6144;
constexpr int MEGA_LDS = LDS_W + 65536;
static_assert(LDS_WO + 4608 <= MEGA_LDS && LDS_KP + 2 * KP_CH <= LDS_WI && LDS_S >= 73728, "lds");
static_assert(LDS_GS + 1280 <= LDS_TAB && LDS_KP + 2 * KP_CH <= MEGA_LDS && MEGA_LDS <= 160 * 1024, "lds");
#define FLAG_AT 64
static_assert(FLAG_AT > 45 && FLAG_AT < 78 && FLAG_AT % 5 != 2, "flag position");
#define WF_EARLY 5
#define WO0 12
#define WSP_P0 4
#define HINT4_KS 15
#define ZDEPTH 3
#define GELU_IN 0
#define EPI_W 0
#define MOD_POS 0
#define POLL3 0
#define GELU_PRE 4
static_assert(HINT4_G > 4 * 64 + 12, "behind the marker barrier");
#define POLL4_G 45
#define RELABEL 1
#define CKMAP(ck) (RELABEL ? 2 * ((ck) & 3) + ((ck) >> 2) : (ck))
#define POLL_AT 134
#define MOD_MID 1
#define LATE_SLEEP 6
#define IN_AUX 0
#define WSP_AUX 0

template <class T> DI T ldsr(unsigned a) { return *(const LAS T*)(size_t)a; }
template <class T> DI void ldsw(unsigned a, T v) { *(LAS T*)(size_t)a = v; }
DI v2u trread(const unsigned char* ptr) {
  typedef short v4i16_t __attribute__((ext_vector_type(4)));
  return __builtin_bit_cast(v2u, __builtin_amdgcn_ds_read_tr16_b64_v4i16((LAS v4i16_t*)ptr));
}
DI v2u trread_a(unsigned a) {
  typedef short v4i16_t __attribute__((ext_vector_type(4)));
  return __builtin_bit_cast(v2u, __builtin_amdgcn_ds_read_tr16_b64_v4i16((LAS v4i16_t*)(size_t)a));
}
struct CvOrder { int qq[36], pp[36]; };
constexpr CvOrder cv_order() {
  CvOrder o{};
  int n = 0;
  for (int m = 0; m < 8; ++m) {
    for (int j = 0; j < m; ++j) if (m + j < 8) { o.qq[n] = m; o.pp[n] = j; ++n; }
    for (int j = 0; j <= m; ++j) if (m + j < 8) { o.qq[n] = j; o.pp[n] = m; ++n; }
  }
  return o;
}
constexpr CvOrder CVO = cv_order();

typedef __attribute__((address_space(1))) unsigned gu32;
DI unsigned tx(unsigned m, unsigned x, unsigned tag) { return __builtin_amdgcn_bitop3_b32(m, x, tag, 0xF6); }
DI unsigned tacc(unsigned m, v4u x, unsigned tag) { return tx(tx(m, x[1], tag), x[3], tag); }
DI bool any_nz(unsigned m) { return __builtin_amdgcn_ballot_w64(m != 0u) != 0ull; }
DI void hint_add(unsigned char* ws, int line) { (void)__hip_atomic_fetch_add((gu32*)(ws + WS_CNT + line * 128), 1u, __ATOMIC_RELAXED, __HIP_MEMORY_SCOPE_AGENT); }
DI void hint_wait(rsrc_t rws, unsigned voff, unsigned soff, unsigned want) {
  unsigned sp = 0;
  for (;;) {
    MEMFENCE();
    const unsigned v = __builtin_amdgcn_raw_buffer_load_b32(rws, (int)voff, (int)soff, SC1);
    if (!((__builtin_amdgcn_ballot_w64(v < want) != 0ull) & (++sp <= SPIN_CAP))) break;
    __builtin_amdgcn_s_sleep(OPT_E);
  }
  MEMFENCE();
}

extern "C" __global__ void __launch_bounds__(256, 1) adaln_mega(Params p) {
  extern __shared__ __attribute__((aligned(16))) unsigned char smem[];
  const int tid = threadIdx.x, lane = tid & 63, w = __builtin_amdgcn_readfirstlane(tid >> 6);
  const int b = blockIdx.x, xcd = b & 7, idx = b >> 3;
  const int pb = xcd * 32 + idx;
  const unsigned l16 = lane * 16;
  STAMP_DECL;
  STAMP(1, 0);
  v2u codepf;
  {
    unsigned long long pc;
    int rel_m, rel_e;
    asm volatile("s_getpc_b64 %0\n\ts_mov_b32 %1, adaln_mega@rel32@lo+4\n\ts_mov_b32 %2, adaln_code_end@rel32@lo+12" : "=s"(pc), "=s"(rel_m), "=s"(rel_e));
    const rsrc_t rcode = mkrs((const void*)(pc + (long long)rel_m), rel_e > rel_m ? (unsigned)(rel_e - rel_m) & ~15u : 0u);
    codepf = ld64<0>(rcode, tid * 8, idx * 2048);
  }
  SB();
  const rsrc_t ru = mkrs(p.u, L * D * 2);
  v4u xr[2];
  xr[0] = ld128<IN_AUX>(ru, l16, (pb * 8 + w * 2) * 1024);
  xr[1] = ld128<IN_AUX>(ru, l16 + 1024, (pb * 8 + w * 2) * 1024);
  const int mkg = b & 3, mcg = b >> 2;
  const unsigned cq = p.c[mkg * 128 + (tid & 3) * 32 + (tid >> 3)];
  const unsigned epoch = *(const unsigned*)(p.ws + WS_EPOCH);
  const rsrc_t rws = mkrs(p.ws, WS_END);
  __builtin_amdgcn_raw_buffer_store_b32(0u, rws, tid < 2 ? tid * 32768u : OOB, WS_CNT + b * 128, SC1);
  SB();
  const int cch = w >> 1, eh = w & 1, ns = tid & 127;
  const unsigned q_cre = p.cre[pb * 256 + tid], q_cim = p.cim[pb * 256 + tid], q_lar = p.lar[pb * 256 + tid], q_aim = p.aim[pb * 256 + tid];
  const unsigned q_ldt = p.ldt[pb * 2 + cch], q_dsk = p.dsk[pb * 2 + cch];
  v4u xm[4];
#define LOAD_XM() { \
  const rsrc_t rmod = mkrs(p.wmod, D * 3072 * 2); \
_Pragma("unroll") \
  for (int i = 0; i < 4; ++i) xm[i] = ld128<IN_AUX>(rmod, (tid >> 3) * 6144 + (tid & 7) * 16, (mkg * 128 + i * 32) * 6144 + mcg * 128 + (b < 192 ? 0u : OOB)); }
  SB();
  LOAD_XM();
  SB();
  STAMP(1, 24);
  const int c = lane & 15, q = lane >> 4;
  const unsigned lds0 = (unsigned)(size_t)(LAS unsigned char*)smem;
  unsigned pos4[9];
  {
    const float f4 = (float)(8188 - tid * 32);
#pragma unroll
    for (int j = 0; j < 10; j += 2) {
      const unsigned pp = pk2(bc2(f4) - f32x2{4.f * j, 4.f * j + 4.f});
      const int p0 = (int)bflo(pp), p1 = (int)bfhi(pp);
      pos4[j] = p0 + ((p0 >> 5) << 2) + lds0 + LDS_KP;
      PIN(pos4[j]);
      if (j + 1 < 9) { pos4[j + 1] = p1 + ((p1 >> 5) << 2) + lds0 + LDS_KP; PIN(pos4[j + 1]); }
    }
  }
  unsigned co[2][4];
  {
    const unsigned b0 = (eh ? 1152 + 576 * q : 144 * (q >> 1) + 4 * (q & 1)) * 4 + 36 * c + lds0 + LDS_KP + cch * KP_CH;
    const unsigned b1 = (q < 2 ? 288 + 144 * q : 568 + 4 * q) * 4 + 36 * c + lds0 + LDS_KP + cch * KP_CH;
    const unsigned s0 = eh ? 576 : 4, s1 = q < 2 ? 8 : 576;
#pragma unroll
    for (int r = 0; r < 4; ++r) { co[0][r] = b0 + r * s0; co[1][r] = b1 + r * s1; PIN(co[0][r]); PIN(co[1][r]); }
  }
  *(unsigned*)(smem + LDS_KP - 16 + cch * KP_CH + (lane & 3) * 4) = 0u;
  unsigned a_pad, a_wi, a_wo, a_tab, gm_off, gc_off;
  a_pad = lds0 + cch * CV_CH + (lane < 32 ? CV_H + l16 : ((lane >> 1) & 1) * CV_RK + 4096 + (lane & 1) * 16);
  a_wi = lds0 + LDS_WI + (w * 32 + 8 * q + ((lane >> 2) & 3)) * 80 + (lane & 3) * 16;
  a_wo = lds0 + LDS_WO + (8 * q + ((lane >> 2) & 3)) * 144 + (w >> 1) * 64 + (lane & 3) * 16 + (w & 1) * 8;
  a_tab = lds0 + LDS_TAB + cch * (64 * 528) + c * 528 + q * 16;
  gm_off = ((4 - cch) * 512 + mkg * 128 + ns) * 8;
  gc_off = (((lane & 7) > 5 ? 0 : ((lane & 7) >> 1)) * 512 + pb * 2 + (lane & 1)) * 8;
  PIN(a_pad); PIN(a_wi); PIN(a_wo); PIN(a_tab); PIN(gm_off); PIN(gc_off);
  unsigned ab[4], bb, dv[4], rdo4[4], xoff;
#pragma unroll
  for (int i = 0; i < 4; ++i) {
    ab[i] = lds0 + cch * CV_CH + (1 - (c & 1)) * CV_RK + 2 * (2030 - 2 * (c >> 1) + 8 * q) - eh * 256 - 192 - 512 - 1024 * i;
    dv[i] = (lane >> 3) * 4096 + (((lane & 7) ^ ((lane >> 4) & 1) ^ (i << 1)) * 16) + i * (32768 - 1024);
    rdo4[i] = lds0 + LDS_W + w * 16384 + ((perm((i & 1) * 2 ^ (2 * (w & 1)), c) * 128 + ((q ^ (c >> 1)) * 16)) ^ ((i >> 1) * 64));
    PIN(ab[i]); PIN(dv[i]); PIN(rdo4[i]);
  }
  bb = lds0 + cch * CV_CH + CV_H + 2 * (256 - 16 + 16 * c + 8 * q) - eh * 256 - 192;
  xoff = WS_X1T + (pb * 2 + cch) * 4096 + eh * 2048 + ((c >> 1) * 32 + ((q & 1) * 4 + 2 * (c & 1) + (q >> 1)) * 4) * 2;
  PIN(bb); PIN(xoff);
  SB();
  STAMP(1, 26);
  {
    *(v4u*)(smem + LDS_LN + (w * 2) * 1040 + l16) = xr[0];
    *(v4u*)(smem + LDS_LN + (w * 2 + 1) * 1040 + l16) = xr[1];
    float sm[2], sq[2];
#pragma unroll
    for (int i = 0; i < 2; ++i) {
      sm[i] = dot2(xr[i][0], ONES, dot2(xr[i][1], ONES, dot2(xr[i][2], ONES, dot2(xr[i][3], ONES, 0.f))));
      sq[i] = dot2(xr[i][0], xr[i][0], dot2(xr[i][1], xr[i][1], dot2(xr[i][2], xr[i][2], dot2(xr[i][3], xr[i][3], 0.f))));
    }
    const float tv = row16_sum(xrow_sum4(sm[0], sm[1], sq[0], sq[1]));
    auto sw = __builtin_amdgcn_permlane32_swap(__float_as_uint(tv), __float_as_uint(tv), false, false);
    const float m = __uint_as_float(sw[0]) * (1.0f / D);
    const float r = __builtin_amdgcn_rsqf(fmaxf(__uint_as_float(sw[1]) * (1.0f / D) - m * m, 0.f) + EPS);
    ((float*)(smem + LDS_GS + 1088))[(q >> 1) * 8 + w * 2 + (q & 1)] = (q >> 1) ? -m * r : r;
  }
  const unsigned tag = epoch * 2654435761u + 0x9E3779B9u;
  STAMP(1, 28);
  asm volatile("s_waitcnt vmcnt(%0)" ::"i"(MOD_POS == 0 ? 8 : 4) : "memory");
  bar();
  STAMP(1, 16);
  {
    unsigned ua[4][2];
#pragma unroll
    for (int i = 0; i < 4; ++i) {
      const unsigned char* src = smem + LDS_LN + (tid & 3) * 2080 + (i * 64 + (tid >> 2)) * 4;
      ua[i][0] = *(const unsigned*)src; ua[i][1] = *(const unsigned*)(src + 1040);
    }
    const unsigned lnv = ((const unsigned*)(smem + LDS_GS + 1088))[c];
    SB();
#pragma unroll
    for (int i = 0; i < 4; ++i) st128<SC1>(v4u{ua[i][0], tag, ua[i][1], tag}, rws, (tid >> 2) * 16384 + (tid & 3) * 16, WS_UG + i * 64 * 16384 + pb * 64);
    st64<SC1>(v2u{lnv, tag}, rws, tid < 16 ? (c >> 3) * 16384 + (c & 7) * 8 : OOB, WS_SR + pb * 64);
  }
  STAMP(1, 17);
  SB();
  const unsigned wp_oob = b < 176 ? 0u : OOB;
  const int wpr = b >> 2, wkg = b & 3;
  v4u xi[2], xo;
  {
    const rsrc_t rwin = mkrs(p.win, D * FF * 2), rwout = mkrs(p.wout, FH * D * 2);
    const int hf = wpr >= 22, pp = wpr - 22 * hf;
    const unsigned v = ((tid >> 2) * (FF * 2) + (tid & 3) * 16) | ((pp == 21 ? tid & 2 : 0) << 29);
#pragma unroll
    for (int i = 0; i < 2; ++i) xi[i] = ld128<IN_AUX>(rwin, v, (wkg * 128 + i * 64) * (FF * 2) + (hf * FH + pp * 32) * 2 + wp_oob);
    xo = ld128<IN_AUX>(rwout, (tid >> 3) * 1024 + (tid & 7) * 16, idx * 32 * 1024 + xcd * 128);
  }
  SB();
  const int d0 = pb * 2, dk = d0 + cch;
  const int mt = (xcd >> 1) * 8 + (idx & 7), nt = (xcd & 1) * 4 + (idx >> 3);
  const int big = (nt + 2) >> 3;
  const int np = 3 - big, pr0 = 3 * nt - big * (nt - 6);
  const int kh = w & 1, uh = w >> 1;
  const int uo0 = pr0 * 2 + uh * (4 - 2 * big), ut0 = pr0 * 2 + (uh ^ 1) * (4 - 2 * big), uo2 = pr0 * 2 + 2 + uh, ut2 = pr0 * 2 + 2 + (uh ^ 1);
#define UO(k) ((k) == 2 ? uo2 : uo0 + (k))
#define UT(k) ((k) == 2 ? ut2 : ut0 + (k))
  const unsigned oob3 = (unsigned)big << 30;
  const int kr = 2 * (idx >> 3) + (xcd & 1) + (OPT_B == 0 ? (idx & 7) : OPT_B == 1 ? 4 * (idx & 1) : OPT_B == 2 ? 2 * (idx & 3) : 0);
  const rsrc_t rwsp = mkrs(p.wsp, L * L * 2);
  const unsigned wp_fr = (wpr * 2 * 16 + wkg * 4 + w) * 1024 + wp_oob;
  v4u wr[1][8];
  const unsigned wsp_v = (lane >> 3) * 4096 + (lane & 7) * 16, wsp_s = mt * 64 * 4096 + w * 1024;
#define LOAD_WSP1(ck, i) wr[ck][i] = ld128<WSP_AUX>(rwsp, wsp_v + (((ck) + kr) & 7) * 128, wsp_s + (i) * 8 * 4096)
#define LOAD_WSPN(n) LOAD_WSP1((n) >> 3, (n) & 7)
  const v4u mk = {~0u, ~0u, ~0u, ~0u};
  const unsigned tag_s = __builtin_amdgcn_readfirstlane(tag);
  {
    const float dt = rbf(exp_acc(bf2f(q_ldt)));
    const float cre = bf2f(q_cre) * dt, cim = bf2f(q_cim) * dt;
    const float are2 = -rbf(exp_acc(bf2f(q_lar))) * dt * 1.4426950408889634f;
    const float aim = bf2f(q_aim) * dt;
    const float rev_hi = aim * 0.15915494309189535f;
    const float rev_lo = __builtin_fmaf(aim, 0.15915494309189535f, -rev_hi) + aim * 6.4206383e-9f;
    int ex = __builtin_amdgcn_frexp_expf(xrow_max(row16_max(fmaxf(fabsf(cre), fabsf(cim))))) + 1;
    ex = ex < -100 ? -100 : ex;
    const float scale = __builtin_amdgcn_ldexpf(1.f, -ex);
    ((float*)(smem + LDS_GS + 1024))[w] = __builtin_amdgcn_ldexpf(2.f, ex);
    const f32x2 c0 = {cre * scale, cim * scale};
    const f32x2 e1 = cexp_p(are2, rev_hi, rev_lo, 1.f), e8 = cexp_p(are2, rev_hi, rev_lo, 8.f), e64 = cexp_p(are2, rev_hi, rev_lo, 64.f);
    unsigned char* tn = smem + LDS_TAB + cch * (64 * 528) + ns * 4;
#define TROW(r, z) *(unsigned*)(tn + (r) * 528) = pkh((z)[0], -(z)[1])
    SB();
    const f32x2 e128 = cmul(e64, e64), e2 = cmul(e1, e1), e256 = cmul(e128, e128), e4 = cmul(e2, e2), e512 = cmul(e256, e256);
    f32x2 f0 = {1.f, 0.f}, f1 = e64;
    f32x2 ca = cmul(c0, e512), cb = cmul(ca, e512);
#pragma unroll
    for (int j = 0; j < 8; ++j) {
      *(unsigned*)(tn + (48 + j) * 528) = pkh(f0[0], f0[1]);
      *(unsigned*)(tn + (56 + j) * 528) = pkh(f1[0], f1[1]);
      TROW(32 + j, cb);
      f0 = j == 0 ? e8 : cmul(f0, e8); f1 = cmul(f1, e8); cb = cmul(cb, e128);
      if (j < 4) {
        const f32x2 z = cmul(ca, e4);
        TROW(24 + j, ca); TROW(28 + j, z);
        ca = cmul(ca, e128);
      }
      if (j == 3 || j == 7) {
        PIN(f0); PIN(f1); PIN(cb);
        if (WSP_P0 >= 4) { LOAD_WSPN(j >> 2 << 1); LOAD_WSPN((j >> 2 << 1) + 1); }
        SB();
      }
    }
    TROW(40, cb);
    SB();
  {
    const float cv = bf2f(cq);
    const float sl = rbf(cv * __builtin_amdgcn_rcpf(1.0f + __builtin_amdgcn_exp2f(cv * -1.4426950408889634f)));
    const float sc[4] = {dpp<0x00>(sl), dpp<0x55>(sl), dpp<0xAA>(sl), dpp<0xFF>(sl)};
    f32x2 a[4];
#pragma unroll
    for (int i = 0; i < 4; ++i)
#pragma unroll
      for (int j = 0; j < 4; ++j) a[j] = i == 0 ? unpk(xm[i][j]) * bc2(sc[i]) : fma2(unpk(xm[i][j]), bc2(sc[i]), a[j]);
    float* red = (float*)(smem + LDS_MR) + (tid >> 3) * 68 + (tid & 7) * 8;
    *(f32x4*)red = f32x4{a[0][0], a[0][1], a[1][0], a[1][1]};
    *(f32x4*)(red + 4) = f32x4{a[2][0], a[2][1], a[3][0], a[3][1]};
  }
    bar();
    float ms8[8];
#pragma unroll
    for (int i = 0; i < 8; ++i) ms8[i] = ((const float*)(smem + LDS_MR))[(q * 8 + i) * 68 + w * 16 + c];
    SB();
    f32x2 t1[8];
    t1[1] = e1; t1[2] = e2; t1[4] = e4;
    t1[3] = cmul(e2, e1); t1[5] = cmul(e4, e1); t1[6] = cmul(e2, e4); t1[7] = cmul(t1[6], e1);
    const f32x2 c1 = cmul(c0, e128), c2 = cmul(c0, e256), c3 = cmul(c2, e128);
    TROW(0, c0); TROW(8, c1); TROW(16, c2); TROW(20, c3);
    {
    const float s = xrow_sum(((ms8[0] + ms8[1]) + (ms8[2] + ms8[3])) + ((ms8[4] + ms8[5]) + (ms8[6] + ms8[7])));
    st64<SC1>(v2u{__float_as_uint(s), tag}, rws, q == 0 ? c * 8 : OOB, WS_MODG + (mkg * 3072 + mcg * 64 + w * 16) * 8 + (b < 192 ? 0u : OOB));
    }
    SB();
#pragma unroll
    for (int v = 1; v < 8; ++v) {
      const f32x2 z0 = cmul(t1[v], c0), z1 = cmul(t1[v], c1);
      TROW(v, z0); TROW(8 + v, z1);
      if ((v & 1) == 0) {
        const f32x2 z2 = cmul(t1[v], c2), z3 = cmul(t1[v], c3);
        TROW(16 + v / 2, z2); TROW(20 + v / 2, z3);
        if (v < 6 && WSP_P0 >= 8) { LOAD_WSPN(v + 2); LOAD_WSPN(v + 3); }
        SB();
      }
    }
  }
  STAMP(1, 27);
#pragma unroll
  for (int i = 0; i < 2; ++i) *(v4u*)(smem + LDS_WI + (i * 64 + (tid >> 2)) * 80 + (tid & 3) * 16) = xi[i];
  *(v4u*)(smem + LDS_WO + (tid >> 3) * 144 + (tid & 7) * 16) = xo;
  STAMP(1, 29);
  bar();
  STAMP(1, 18);
  SB();
  v4u gr[4], gb_[4];
  v2u gm[4], gc[4];
#define LOAD_GS() _Pragma("unroll") for (int i_ = 0; i_ < 4; ++i_) { gr[i_] = ld128<SC1>(rws, tid * 16, WS_SR + i_ * 4096); gb_[i_] = ld128<SC1>(rws, tid * 16, WS_SB + i_ * 4096); }
#define LOAD_GM() _Pragma("unroll") for (int i_ = 0; i_ < 4; ++i_) { gm[i_] = ld64<SC1>(rws, gm_off, WS_MODG + i_ * 24576); gc[i_] = ld64<SC1>(rws, gc_off, WS_MODG + i_ * 24576); }
  v4u ug[4], wt[2];
#define LOAD_UG() _Pragma("unroll") for (int i_ = 0; i_ < 4; ++i_) ug[i_] = ld128<SC1>(rws, tid * 16, WS_UG + pb * 16384 + i_ * 4096)
  {
    v4u fa[2][8], fb[8];
    const unsigned ta = a_tab + eh * (32 * 528);
#pragma unroll
    for (int ks = 0; ks < 3; ++ks) { fb[ks] = ldsr<v4u>(a_tab + 48 * 528 + ks * 64); fa[0][ks] = ldsr<v4u>(ta + ks * 64); fa[1][ks] = ldsr<v4u>(a_tab + 16 * 528 + ks * 64); }
    SB();
    LOAD_UG();
    SB();
    f32x4 kk[2][2];
    f32x2 us;
    v2u wh_[6];
#pragma unroll
    for (int i = 0; i < 4; ++i) kk[i >> 1][i & 1] = f32x4{0.f, 0.f, 0.f, 0.f};
#pragma unroll
    for (int ks = 0; ks < 8; ++ks) {
      if (ks < 5) { fb[3 + ks] = ldsr<v4u>(a_tab + 48 * 528 + (3 + ks) * 64); fa[0][3 + ks] = ldsr<v4u>(ta + (3 + ks) * 64); fa[1][3 + ks] = ldsr<v4u>(a_tab + 16 * 528 + (3 + ks) * 64); }
      if (ks == 5) { us = *(const f32x2*)(smem + LDS_GS + 1024 + cch * 8); wh_[4] = trread_a(a_wo); wh_[5] = trread_a(a_wo + 576); }
      if (ks == 6) { wh_[0] = trread_a(a_wi); wh_[1] = trread_a(a_wi + 320); wh_[2] = trread_a(a_wi + 8); wh_[3] = trread_a(a_wi + 328); }
      kk[0][ks >> 2] = mfma_h(fa[0][ks], fb[ks], kk[0][ks >> 2]); kk[1][ks >> 2] = mfma_h(fa[1][ks], fb[ks], kk[1][ks >> 2]);
      SB();
    }
    STAMP(1, 37);
    wt[0] = v4u{wh_[0][0], wh_[0][1], wh_[1][0], wh_[1][1]};
    wt[1] = v4u{wh_[2][0], wh_[2][1], wh_[3][0], wh_[3][1]};
    st128<SC1>(v4u{wh_[4][0], wh_[4][1], wh_[5][0], wh_[5][1]}, rws, l16, WS_WOT + ((xcd * 4 + w) * 22 + idx) * 1024 + wp_oob);
    LOAD_GS();
    SB();
#pragma unroll
    for (int i = 0; i < 4; ++i) PINA(kk[i >> 1][i & 1]);
    f32x4 k0 = kk[0][0] * us[0] + kk[0][1] * us[1];
    const f32x4 k1 = kk[1][0] * us[0] + kk[1][1] * us[1];
    k0[0] += ns == 0 ? bf2f(q_dsk) : 0.f;
#pragma unroll
    for (int r = 0; r < 4; ++r) { ldsw<float>(co[0][r], k0[r]); ldsw<float>(co[1][r], k1[r]); }
  }
  STAMP(1, 31);
  bar();
  STAMP(1, 19);
  {
    float v[2][9];
#pragma unroll
    for (int j = 0; j < 9; ++j) v[0][j] = ldsr<float>(pos4[j]);
    LOAD_GM();
#pragma unroll
    for (int j = 0; j < 9; ++j) v[1][j] = ldsr<float>(pos4[j] + KP_CH);
    ldsw<v4u>(a_pad, v4u{0u, 0u, 0u, 0u});
    SB();
    STAMP(1, 36);
#pragma unroll
    for (int ch = 0; ch < 2; ++ch) {
      unsigned char* rk = smem + ch * CV_CH + tid * 16;
#pragma unroll
      for (int sft = 0; sft < 2; ++sft)
        *(v4u*)(rk + sft * CV_RK) = v4u{pk2(v[ch][sft], v[ch][sft + 1]), pk2(v[ch][sft + 2], v[ch][sft + 3]), pk2(v[ch][sft + 4], v[ch][sft + 5]), pk2(v[ch][sft + 6], v[ch][sft + 7])};
    }
#pragma unroll
    for (int i = WSP_P0; i < 8; ++i) LOAD_WSPN(i);
    st128<SC1>(mk, rws, l16, WS_X1T + dk * 4096 + eh * 2048);
    st128<SC1>(mk, rws, l16 + 1024, WS_X1T + dk * 4096 + eh * 2048);
    __builtin_amdgcn_raw_buffer_store_b32(~0u, rws, lane * 4, WS_ST2 + ((mt * 2 + kh) * 16 + nt * 2 + uh) * 256, SC1);
    SB();
    STAMP(1, 32);
    unsigned m = 0;
#pragma unroll
    for (int i = 0; i < 4; ++i) { m = tacc(m, ug[i], tag); m = tacc(m, gr[i], tag); m = tacc(m, gb_[i], tag); }
    if (__builtin_expect(any_nz(m), 0)) {
      unsigned sp = 0;
      do {
        MEMFENCE(); LOAD_UG(); LOAD_GS(); m = 0;
#pragma unroll
        for (int i = 0; i < 4; ++i) { m = tacc(m, ug[i], tag); m = tacc(m, gr[i], tag); m = tacc(m, gb_[i], tag); }
      } while (any_nz(m) & (++sp <= SPIN_CAP));
    }
    f32x2 nn[4][2];
#pragma unroll
    for (int i = 0; i < 4; ++i) {
      nn[i][0] = fma2(unpk(ug[i][0]), bc2(__uint_as_float(gr[i][0])), bc2(__uint_as_float(gb_[i][0])));
      nn[i][1] = fma2(unpk(ug[i][2]), bc2(__uint_as_float(gr[i][2])), bc2(__uint_as_float(gb_[i][2])));
      PIN(nn[i][0]); PIN(nn[i][1]);
      *(unsigned*)(smem + LDS_U + i * 1024 + tid * 4) = __builtin_amdgcn_perm(ug[i][2], ug[i][0], 0x05040100u);
      *(unsigned*)(smem + LDS_U + 4096 + i * 1024 + tid * 4) = __builtin_amdgcn_perm(ug[i][2], ug[i][0], 0x07060302u);
    }
    SB();
    STAMP(1, 33);
    m = 0;
#pragma unroll
    for (int i = 0; i < 4; ++i) m = tx(tx(m, gm[i][1], tag), gc[i][1], tag);
    if (__builtin_expect(any_nz(m), 0)) {
      unsigned sp = 0;
      do {
        __builtin_amdgcn_s_sleep(3);
        MEMFENCE(); LOAD_GM(); m = 0;
#pragma unroll
        for (int i = 0; i < 4; ++i) m = tx(tx(m, gm[i][1], tag), gc[i][1], tag);
      } while (any_nz(m) & (++sp <= SPIN_CAP));
    }
    const float mv = rbf((__uint_as_float(gm[0][0]) + __uint_as_float(gm[1][0])) + (__uint_as_float(gm[2][0]) + __uint_as_float(gm[3][0])));
    *(u16*)(smem + LDS_GS + tid * 2) = (u16)pk2(mv + (cch ? 0.f : 1.0f), 0.f);
    const float cv = rbf((__uint_as_float(gc[0][0]) + __uint_as_float(gc[1][0])) + (__uint_as_float(gc[2][0]) + __uint_as_float(gc[3][0])));
    const float gav = dpp<0x104>(cv), gav2 = dpp<0x102>(cv);
    const float hbv = cv * gav, hsv = rbf(1.0f + cv) * gav2;
    const f32x2 hb = {__builtin_bit_cast(float, __builtin_amdgcn_readlane(__builtin_bit_cast(int, hbv), 0)), __builtin_bit_cast(float, __builtin_amdgcn_readlane(__builtin_bit_cast(int, hbv), 1))};
    const f32x2 hs = {__builtin_bit_cast(float, __builtin_amdgcn_readlane(__builtin_bit_cast(int, hsv), 2)), __builtin_bit_cast(float, __builtin_amdgcn_readlane(__builtin_bit_cast(int, hsv), 3))};
    SB();
    STAMP(1, 38);
#pragma unroll
    for (int i = 0; i < 4; ++i) {
      const f32x2 h0 = fma2(nn[i][0], hs, hb), h1 = fma2(nn[i][1], hs, hb);
      *(unsigned*)(smem + CV_H + 512 + i * 1024 + tid * 4) = pk2(h0[0], h1[0]);
      *(unsigned*)(smem + CV_CH + CV_H + 512 + i * 1024 + tid * 4) = pk2(h0[1], h1[1]);
    }
  }
  asm volatile("" ::"v"(codepf));
  STAMP(1, 39);
  bar();
  STAMP(1, 1);
#define CV_A(ee, k) ({ const unsigned a_ = ab[(k) >> 1] + 192 - 64 * (ee) + 512 * (1 - ((k) & 1)); v4u{ldsr<unsigned>(a_), ldsr<unsigned>(a_ + 4), ldsr<unsigned>(a_ + 8), ldsr<unsigned>(a_ + 12)}; })
#define CV_B(ee, k) ldsr<v4u>(bb + 192 - 64 * (ee) + 512 * (k))
  const unsigned slotA = LDS_W + w * 16384, slotB = slotA + 8192;
  v4u wq[8][8];
#define READ_W(dl, f) ldsr<v4u>(rdo4[(((f) >> 1) & 1) + ((f) >> 2) * 2] + (dl) + ((f) & 1) * 512)
  unsigned dso;
#define DMA_W(ck, slot, i) do { \
    if (((i) & 3) == 0) asm volatile("s_add_u32 m0, %1, %2\n\ts_add_u32 %0, %3, %4\n\tbuffer_load_dwordx4 %5, %6, %0 offen lds" \
                 : "=&s"(dso) : "s"(lds0 + (slot)), "i"((i) * 1024), "s"(wsp_s + ((CKMAP(ck) + kr) & 7) * 128), "i"((i) * 32768), "v"(dv[0]), "s"(rwsp) : "scc", "memory"); \
    else asm volatile("buffer_load_dwordx4 %0, %1, %2 offen offset:%3 lds" :: "v"(dv[(i) & 3]), "s"(rwsp), "s"(dso), "i"(((i) & 3) * 1024) : "memory"); } while (0)
  auto dma_sel = [&](auto CK, unsigned slot, int i) __attribute__((always_inline)) {
    constexpr int ck = decltype(CK)::value;
    switch (i) {
      case 0: DMA_W(ck, slot, 0); break; case 1: DMA_W(ck, slot, 1); break; case 2: DMA_W(ck, slot, 2); break; case 3: DMA_W(ck, slot, 3); break;
      case 4: DMA_W(ck, slot, 4); break; case 5: DMA_W(ck, slot, 5); break; case 6: DMA_W(ck, slot, 6); break; default: DMA_W(ck, slot, 7); break;
    }
  };
  v4u cA[2][8], cB[2][8];
  float gs[8];
  v4u gbf, sbf;
  v4u ones4 = {ONES, ONES, ONES, ONES};
  PIN(ones4);
#pragma unroll
  for (int k = 0; k < 2; ++k) { cA[0][k] = CV_A(0, k); cB[0][k] = CV_B(0, k); }
  {
    gbf = *(const v4u*)(smem + LDS_GS + w * 64 + q * 16);
    sbf = *(const v4u*)(smem + LDS_GS + 256 + w * 64 + q * 16);
  }
  SB();
  f32x4 cacc[8];
  v2u ue[4];
  float uf[16];
  unsigned flagv;
  unsigned wro = lds0 + slotA + (lane >> 3) * 128 + (((lane & 7) ^ ((lane >> 4) & 1)) * 16);
  PIN(wro);
  {
    f32x4 cb4[4];
    v4u wo_[2];
    float wl, wh;
    f32x2 cp[4];
#pragma unroll
    for (int t = 0; t < 8; ++t) cacc[t] = f32x4{0.f, 0.f, 0.f, 0.f};
    sfor<144>([&](auto G) __attribute__((always_inline)) {
      constexpr int g = decltype(G)::value, ee = g / 36, j = g % 36;
      if constexpr (ee == 0 && j < 6) { cA[0][2 + j] = CV_A(0, 2 + j); cB[0][2 + j] = CV_B(0, 2 + j); }
      cacc[CVO.pp[j] + CVO.qq[j]] = mfma(cA[ee & 1][CVO.qq[j]], cB[ee & 1][CVO.pp[j]], cacc[CVO.pp[j] + CVO.qq[j]]);
      constexpr int jn = ee == 0 ? j - 20 : (j & 1) ? j >> 1 : -1;
      if constexpr (ee < 3 && jn >= 0 && jn < 16) {
        if constexpr (jn & 1) cB[(ee + 1) & 1][jn >> 1] = CV_B(ee + 1, jn >> 1);
        else cA[(ee + 1) & 1][jn >> 1] = CV_A(ee + 1, jn >> 1);
      }
      if constexpr (ee == 3 && j < 4) ue[j] = *(const v2u*)(smem + LDS_U + cch * 4096 + (eh * 4 + j) * 512 + (c * 16 + q * 4) * 2);
      if constexpr (g >= 6 && g < 30) {
        constexpr int i = (g - 6) / 12, jj = ((g - 6) / 3) & 3, ph = (g - 6) % 3;
        if constexpr (ph == 0) { if constexpr (i == 0) gs[2 * jj] = bflo(gbf[jj]); wl = bflo(wt[i][jj]) * gs[2 * jj]; PIN(wl); }
        if constexpr (ph == 1) { if constexpr (i == 0) gs[2 * jj + 1] = bfhi(gbf[jj]); wh = bfhi(wt[i][jj]) * gs[2 * jj + 1]; PIN(wh); }
        if constexpr (ph == 2) {
          wo_[i][jj] = pk2(wl, wh);
          if constexpr (jj == 3) st128<SC1>(wo_[i], rws, l16 + i * 16384, WS_WP + wp_fr);
          else PIN(wo_[i][jj]);
        }
      }
      if constexpr (g == 4 || g == 5) cb4[2 * (g - 4) + 1] = mfma(sbf, wt[g - 4], f32x4{0.f, 0.f, 0.f, 0.f});
      if constexpr (g == 19 || g == 31) cb4[g == 19 ? 0 : 2] = mfma(ones4, wo_[g == 31], f32x4{0.f, 0.f, 0.f, 0.f});
      if constexpr (g == 33) *(f32x4*)(smem + LDS_S + 4096 + (w * 16 + c) * 16) = f32x4{cb4[0][0], cb4[1][0], cb4[2][0], cb4[3][0]};
      if constexpr (g == 38) bar();
      if constexpr (g == 39) {
#pragma unroll
        for (int k = 0; k < 4; ++k) cp[k] = *(const f32x2*)(smem + LDS_S + 4096 + (k * 16 + c) * 16 + (q & 1) * 8);
      }
      if constexpr (g == 45) {
        float s0 = cp[0][0] + cp[1][0], s1 = cp[2][0] + cp[3][0];
        PIN(s0); PIN(s1);
        s0 += s1;
        PIN(s0);
        float s2 = cp[0][1] + cp[1][1], s3 = cp[2][1] + cp[3][1];
        PIN(s2); PIN(s3);
        st64<SC1>(v2u{__float_as_uint(s0), __float_as_uint(s2 + s3)}, rws, tid < 32 ? q * 512 + c * 8 : OOB, WS_CSB + wpr * 1024 + wkg * 128 + wp_oob);
      }
      if constexpr (g == 36) STAMP(1, 40);
      if constexpr (g == 72) STAMP(1, 41);
      if constexpr (g == FLAG_AT) {
        STAMP(1, 42);
        asm volatile("s_waitcnt vmcnt(%0)" ::"i"((FLAG_AT - 32 + 4) / 5 + 1) : "memory");
        __builtin_amdgcn_s_barrier();
        if (tid < 8) hint_add(p.ws, 256 + tid * 16 + (idx & 15));
        STAMP(1, 43);
      }
      if constexpr (g % 5 == 2 && g < 80) DMA_W(1 + g / 40, g < 40 ? slotA : slotB, (g / 5) & 7);
      if constexpr (g == 88) asm volatile("s_waitcnt vmcnt(8)" ::: "memory");
      if constexpr (g == 124) asm volatile("s_waitcnt vmcnt(0)" ::: "memory");
      if constexpr (g == POLL_AT) flagv = __builtin_amdgcn_raw_buffer_load_b32(rws, w == 0 ? c * 128 : OOB, WS_CNT + (256 + xcd * 16) * 128, SC1);
      if constexpr (g >= 88 && g < 104 && (g & 1) == 0) wq[1][(g - 88) >> 1] = READ_W(0, (g - 88) >> 1);
      if constexpr (g >= 108 && g < 140 && (g & 3) == 0) ldsw<v4u>((wro ^ ((((g - 108) >> 2) & 3) * 32)) + ((g - 108) >> 2) * 1024, wr[0][(g - 108) >> 2]);
      if constexpr (g >= 125 && g < 141 && (g & 1) == 1) wq[2][(g - 125) >> 1] = READ_W(8192, (g - 125) >> 1);
      if constexpr (g >= 128 && g < 144) { uf[g - 128] = ((g & 1) ? bfhi : bflo)(ue[(g - 128) >> 2][((g - 128) >> 1) & 1]); PIN(uf[g - 128]); }
      SB();
    });
  }
  STAMP(1, 2);
  unsigned late;
  {
    f32x4* red = (f32x4*)(smem + LDS_X) + w * 256 + lane;
    f32x4 keep[4];
    if (eh) {
#pragma unroll
      for (int t = 0; t < 4; ++t) red[t * 64] = cacc[t];
      SB();
#pragma unroll
      for (int t = 0; t < 4; ++t) { PINA(cacc[4 + t]); keep[t] = cacc[4 + t]; PIN(keep[t]); }
    } else {
#pragma unroll
      for (int t = 0; t < 4; ++t) red[t * 64] = cacc[4 + t];
      SB();
#pragma unroll
      for (int t = 0; t < 4; ++t) { PINA(cacc[t]); keep[t] = cacc[t]; PIN(keep[t]); }
    }
    f32x2 pre[4][2];
#pragma unroll
    for (int t = 0; t < 4; ++t) {
      pre[t][0] = f32x2{uf[4 * t], uf[4 * t + 1]} + lo2(keep[t]);
      pre[t][1] = f32x2{uf[4 * t + 2], uf[4 * t + 3]} + hi2(keep[t]);
      PIN(pre[t][0]); PIN(pre[t][1]);
    }
    if (w == 0) *(unsigned*)(smem + LDS_S) = __builtin_amdgcn_ballot_w64(flagv < 16u) != 0ull;
    bar();
    late = *(const unsigned*)(smem + LDS_S);
    const f32x4* rd = (const f32x4*)(smem + LDS_X) + (w ^ 1) * 256 + lane;
    f32x4 oth[4];
#pragma unroll
    for (int t = 0; t < 4; ++t) oth[t] = rd[t * 64];
#pragma unroll
    for (int t = 0; t < 4; ++t) {
      v2u o;
      o[0] = nomark(pk2(pre[t][0] + lo2(oth[t])));
      o[1] = pk2(pre[t][1] + hi2(oth[t]));
      st64<SC1>(o, rws, xoff + t * 512, 0);
    }
  }
  STAMP(1, 3);
  SB();
  v4u wf[3][8];
  const unsigned rot = uh * 4;
#define LOAD_WP(half, k, i) ld128<SC1>(rws, l16 + ((i) & 3) * 1024, WS_WP + (((half) * 44 + UO(k)) * 16 + kh * 8 + (((i) & 4) ^ rot)) * 1024 + ((k) == 2 ? oob3 : 0u))
#define LOAD_XS(i) ld128<SC1>(rws, wsp_v, WS_X1T + ((i) * 32 + w * 8) * 4096 + mt * 128)
  v4u xs[16];
  unsigned tb[2][4], xdst;
  {
    const int row = 8 * q + ((lane >> 2) & 3);
    const int sg = ((row >> 1) & 1) | (((row >> 3) & 1) << 1);
#pragma unroll
    for (int tu = 0; tu < 4; ++tu) {
      const unsigned t0 = lds0 + LDS_A + kh * 32768 + row * 128 + ((tu ^ (2 * kh) ^ sg) * 32) + (lane & 3) * 8;
      tb[0][tu] = t0 + uh * 16384;
      tb[1][tu] = t0 + (uh ^ 1) * 16384;
    }
    const int rl = w * 8 + (lane >> 3), chn = lane & 7;
    const int sg2 = ((rl >> 1) & 1) | (((rl >> 3) & 1) << 1);
    xdst = lds0 + LDS_A + rl * 128 + (((chn >> 1) ^ sg2) * 32) + (chn & 1) * 16;
  }
  SB();
  unsigned z2o[3];
#pragma unroll
  for (int k = 0; k < 3; ++k) { z2o[k] = l16 + WS_Z2 + (UO(k) * 64 + mt * 2 + kh) * 1024 + (k == 2 ? oob3 : 0u); }
  v2u cz[2][3];
  f32x2 csb[2][3];
  float gbv, c1v[2];
  unsigned gq[4];
  unsigned bspq[2];
#define LOAD_CZ(hf, kc) ld64<SC1>(rws, lane * 8, WS_CSB + ((hf) * 44 + UO(kc)) * 512 + ((kc) == 2 ? oob3 : 0u))
  {
    STAMP(1, 20);
    if (__builtin_amdgcn_readfirstlane(late)) {
      if (w == 0) { hint_wait(rws, c * 128, WS_CNT + (256 + xcd * 16) * 128, 16u); __builtin_amdgcn_s_sleep(LATE_SLEEP); }
      __builtin_amdgcn_s_barrier();
    }
    MEMFENCE();
    STAMP(1, 21);
#pragma unroll
    for (int i = 0; i < 3; ++i)
#pragma unroll
      for (int k = 0; k < 3; ++k) wf[k][i] = LOAD_WP(1, k, i);
    SB();
#pragma unroll
  for (int f = 0; f < 8; ++f) wq[0][f] = READ_W(0, f);
#pragma unroll
  for (int f = 0; f < 24; ++f) PINA(wq[f >> 3][f & 7]);
#pragma unroll
    for (int tu = 0; tu < 8; ++tu) PIN(tb[tu >> 2][tu & 3]);
    PIN(xdst);
#pragma unroll
    for (int k = 0; k < 3; ++k) PIN(z2o[k]);
    SB();
#pragma unroll
    for (int i = 3; i < WF_EARLY; ++i)
#pragma unroll
      for (int k = 0; k < 3; ++k) wf[k][i] = LOAD_WP(1, k, i);
    SB();
#pragma unroll
    for (int i = 0; i < 16; ++i) xs[i] = LOAD_XS(i);
    SB();
    STAMP(1, 44);
    unsigned m = 0;
#pragma unroll
    for (int i = 0; i < 16; ++i) {
      m = macc(m, xs[i]); ldsw<v4u>(xdst + i * 4096, xs[i]);
      if (WF_EARLY * 3 + i < 24) wf[(WF_EARLY * 3 + i) % 3][(WF_EARLY * 3 + i) / 3] = LOAD_WP(1, (WF_EARLY * 3 + i) % 3, (WF_EARLY * 3 + i) / 3);
      SB();
    }
    PIN(m);
    STAMP(1, 45);
    RETRY_BEGIN(m)
#pragma unroll
      for (int i = 0; i < 16; ++i) xs[i] = LOAD_XS(i);
      SB();
#pragma unroll
      for (int i = 0; i < 16; ++i) { m = macc(m, xs[i]); ldsw<v4u>(xdst + i * 4096, xs[i]); }
    RETRY_END(m)
  }
  STAMP(1, 4);
  bar();
  STAMP(1, 5);
#define AHALF(tu, i, h) trread_a(tb[(i) >> 2][tu] + ((i) & 3) * 4096 + (h) * 512)
  f32x4 acc[3][4];
  v4u wg[3][8];
  v2u ah[2][4][2];
  float* st = (float*)(smem + LDS_S);
  float* fin = st + 256 + w * 64;
  f32x4* xch = (f32x4*)(smem + LDS_X);
  f32x4 r4[2], tm[3][2];
  {
    f32x4 s1[4], s2[4], m4[2];
    v2u ar[3][2][2];
    float pa[4], pb[4];
    constexpr int ORD[20] = {0, 1, 2, 3, 4, 5, 6, 7, 16, 17, 18, 19, 8, 9, 10, 11, 12, 13, 14, 15};
    int dgo[2], rdo[2];
    dgo[0] = w * 64 + kh * 32 + c; dgo[1] = w * 64 + (kh ^ 1) * 32 + c;
    rdo[0] = (kh * 32 + (lane & 31)) * 4; rdo[1] = rdo[0] * 4 + (lane & 3) * 4;
    PIN(dgo[0]); PIN(dgo[1]); PIN(rdo[0]); PIN(rdo[1]);
    const bool diag = (c >> 2) == q;
#define GFRAG(gi, t, h) AHALF(((gi) < 8 ? 2 : 0) + (t), (gi) < 16 ? (gi) & 7 : (gi) - 16, h)
#pragma unroll
    for (int n = 0; n < 2; ++n)
#pragma unroll
      for (int e = 0; e < 4; ++e) ar[n][e >> 1][e & 1] = GFRAG(ORD[n], e >> 1, e & 1);
    SB();
    sfor<20 * 6>([&](auto G) __attribute__((always_inline)) {
      constexpr int n = decltype(G)::value / 6, j = decltype(G)::value % 6, gi = ORD[n], t = j / 3, k = j % 3;
      constexpr int tu = (gi < 8 ? 2 : 0) + t, i = gi < 16 ? gi & 7 : gi - 16;
      const v4u a = {ar[n % 3][t][0][0], ar[n % 3][t][0][1], ar[n % 3][t][1][0], ar[n % 3][t][1][1]};
      const f32x4 z4 = {0.f, 0.f, 0.f, 0.f};
      if constexpr (gi < 16) acc[k][tu] = mfma(a, wf[k][i], gi == 0 ? z4 : acc[k][tu]);
      if constexpr ((gi >= 4 && gi < 8) || gi >= 16) {
        if constexpr (k == 0) s1[tu] = mfma(ones4, a, (gi == 4 || gi == 16) ? z4 : s1[tu]);
        if constexpr (k == 1) s2[tu] = mfma(a, a, (gi == 4 || gi == 16) ? z4 : s2[tu]);
      }
      if constexpr (j < 4 && n + 2 < 20) ar[(n + 2) % 3][j >> 1][j & 1] = GFRAG(ORD[n + 2], j >> 1, j & 1);
      if constexpr (n >= 18 && j < 4) ah[0][(n - 18) * 2 + (j >> 1)][j & 1] = AHALF((n - 18) * 2 + (j >> 1), 0, j & 1);
      if constexpr (n < 4 && (j == 1 || j == 4)) DMA_W(3, slotB, n * 2 + (j == 4));
      if constexpr (n == 4) cz[j / 3][j % 3] = LOAD_CZ(j / 3, j % 3);
      if constexpr (n == 5 && j < 4) gq[j] = __builtin_amdgcn_raw_buffer_load_b32(rws, lane * 8, WS_MODG + (j * 3072 + 5 * 512 + nt * 64) * 8, SC1);
      if constexpr (n == 5 && j >= 4) bspq[j - 4] = p.bsp[mt * 64 + perm(2 * kh + j - 4, c)];
      if constexpr (n == 8 && j == 1) USE(bspq[1]);
      if constexpr (n >= 8 && n < 12 && (j == 2 || j == 5)) DMA_W(4, slotA, (n - 8) * 2 + (j == 5));
      if constexpr (n >= 12 && j == 4) DMA_W(5, slotB, n - 12);
      if constexpr ((n == 8 || n == 9) && (j & 1) == 0) {
        constexpr int x = (n - 8) * 3 + (j >> 1);
        xch[(w * 6 + (x % 3) * 2 + x / 3) * 64 + lane] = acc[x % 3][2 + x / 3];
      }
      if constexpr (n == 10 && j == 0) {
#pragma unroll
    for (int k = 0; k < 3; ++k) {
      const float y = xrow_sum4(__uint_as_float(cz[1][k][0]), __uint_as_float(cz[1][k][1]), __uint_as_float(cz[1][k][0]), __uint_as_float(cz[1][k][1]));
      auto sw = __builtin_amdgcn_permlane16_swap(__float_as_uint(y), __float_as_uint(y), false, false);
      csb[1][k] = f32x2{__uint_as_float(sw[0]), __uint_as_float(sw[1])};
      PIN(csb[1][k]);
    }
    c1v[0] = xrow_sum4(__uint_as_float(cz[0][0][0]), __uint_as_float(cz[0][0][1]), __uint_as_float(cz[0][1][0]), __uint_as_float(cz[0][1][1]));
    c1v[1] = xrow_sum4(__uint_as_float(cz[0][2][0]), __uint_as_float(cz[0][2][1]), __uint_as_float(cz[0][2][0]), __uint_as_float(cz[0][2][1]));
    PIN(c1v[0]); PIN(c1v[1]);
    gbv = rbf((__uint_as_float(gq[0]) + __uint_as_float(gq[1])) + (__uint_as_float(gq[2]) + __uint_as_float(gq[3])));
    PIN(gbv);
      }
      if constexpr ((n == 8 || n == 9) && j >= 2) wq[3][(n - 8) * 4 + j - 2] = READ_W(8192, (n - 8) * 4 + j - 2);
      if constexpr (n == 10 && j == 5) {
#pragma unroll
        for (int f = 0; f < 8; ++f) PINA(wq[3][f]);
      }
      if constexpr (n == 10 && j == 4) bar();
      if constexpr (n == 11) acc[j % 3][j / 3] = xch[((w ^ 1) * 6 + (j % 3) * 2 + j / 3) * 64 + lane];
      if constexpr (n == 12 && j == 3) {
        float s1v[4];
#pragma unroll
        for (int x = 0; x < 4; ++x) { s1v[x] = s1[x][0]; PIN(s1v[x]); }
        if (diag) {
#pragma unroll
          for (int x = 0; x < 4; ++x) {
            *(float*)(smem + LDS_S + dgo[x >> 1] * 4 + (x & 1) * 64) = s1v[x];
            *(f32x4*)(smem + LDS_S + 2048 + dgo[x >> 1] * 16 + (x & 1) * 256) = s2[x];
          }
        }
      }
      if constexpr (n == 13 && j == 5) bar();
      if constexpr (n == 14 && j < 4) {
        pa[j] = *(const float*)(smem + LDS_S + rdo[0] + j * 256); pb[j] = *(const float*)(smem + LDS_S + 2048 + rdo[1] + j * 1024);
      }
      if constexpr (n == 15 && j == 1) { pa[0] += pa[1]; pa[2] += pa[3]; PIN(pa[0]); PIN(pa[2]); }
      if constexpr (n == 15 && j == 2) { pb[0] += pb[1]; pb[2] += pb[3]; PIN(pb[0]); PIN(pb[2]); }
      if constexpr (n == 15 && j == 3) { pa[0] = (pa[0] + pa[2]) * (1.0f / D); PIN(pa[0]); }
      if constexpr (n == 15 && j == 4) { pb[0] = (pb[0] + pb[2]) * (1.0f / D); PIN(pb[0]); }
      if constexpr (n == 15 && j == 5) { pb[0] = fmaxf(__builtin_fmaf(-pa[0], pa[0], pb[0]), 0.f); PIN(pb[0]); }
      if constexpr (n == 16 && j == 0) { pb[0] = __builtin_amdgcn_rsqf(pb[0] + EPS); PIN(pb[0]); }
      if constexpr (n == 16 && j == 1) fin[lane] = lane < 32 ? pb[0] : -pb[0] * pa[0];
      if constexpr (n == 16 && (j == 2 || j == 3)) { m4[j - 2] = *(const f32x4*)(fin + 32 + (j - 2) * 16 + q * 4); r4[j - 2] = *(const f32x4*)(fin + (j - 2) * 16 + q * 4); }
      if constexpr (n >= 18) {
        constexpr int x = (n - 18) * 6 + j, kk = x >> 2, tt = (x >> 1) & 1, h = (x & 1) * 2;
        float t0 = __builtin_fmaf(m4[tt][h], csb[1][kk][0], csb[1][kk][1]);
        PIN(t0);
        float t1 = __builtin_fmaf(m4[tt][h + 1], csb[1][kk][0], csb[1][kk][1]);
        PIN(t1);
        tm[kk][tt][h] = t0; tm[kk][tt][h + 1] = t1;
      }
      if constexpr (n >= 12 && n < 15 && j == 5) st128<SC1>(mk, rws, z2o[n - 12], 0);
      if constexpr (n == 7 && j == 5) STAMP(1, 46);
      if constexpr (n == 11 && j == 5) STAMP(1, 47);
      SB();
    });
  }
#pragma unroll
  for (int k = 0; k < 6; ++k) PINA(acc[k >> 1][k & 1]);
  STAMP(1, 6);
  f32x4 z2v[3][2];
  {
    f32x4 rs[2], rq[2];
#pragma unroll
    for (int t = 0; t < 2; ++t) {
#pragma unroll
      for (int k = 0; k < 3; ++k) {
        const f32x4 a = acc[k][t];
        const f32x2 zl = gelu2(fma2(lo2(a), lo2(r4[t]), lo2(tm[k][t])));
        const f32x2 zh = gelu2(fma2(hi2(a), hi2(r4[t]), hi2(tm[k][t])));
        z2v[k][t] = f32x4{zl[0], zl[1], zh[0], zh[1]};
        if (k == 0) { rs[t] = z2v[k][t]; rq[t] = z2v[k][t] * z2v[k][t]; }
        else { rs[t] += z2v[k][t]; rq[t] += z2v[k][t] * z2v[k][t]; }
        if (((t * 3 + k) & 1) == 0) {
#pragma unroll
          for (int f = (t * 3 + k) * 4; f < (t * 3 + k) * 4 + 8; ++f) wg[f % 3][f / 3] = LOAD_WP(0, f % 3, f / 3);
        } else {
          PIN(z2v[k][t]);
          SB();
        }
      }
    }
    STAMP(1, 50);
    float v8[8], v4[4], v2[2];
    const bool b3 = c & 8, b2 = c & 4, b1 = c & 2, b0 = c & 1;
#pragma unroll
    for (int r_ = 0; r_ < 4; ++r_) {
      const float a0 = rs[0][r_] + dpp<0x128>(rs[0][r_]), a1 = rs[1][r_] + dpp<0x128>(rs[1][r_]);
      const float q0 = rq[0][r_] + dpp<0x128>(rq[0][r_]), q1 = rq[1][r_] + dpp<0x128>(rq[1][r_]);
      v8[2 * r_] = b3 ? a1 : a0; v8[2 * r_ + 1] = b3 ? q1 : q0;
    }
#pragma unroll
    for (int j = 0; j < 4; ++j) {
      const float x0 = v8[j] + dpp<0x141>(v8[j]), x1 = v8[4 + j] + dpp<0x141>(v8[4 + j]);
      v4[j] = b2 ? x1 : x0;
    }
#pragma unroll
    for (int j = 0; j < 2; ++j) {
      const float x0 = v4[j] + dpp<0x4E>(v4[j]), x1 = v4[2 + j] + dpp<0x4E>(v4[2 + j]);
      v2[j] = b1 ? x1 : x0;
    }
    const float x0 = v2[0] + dpp<0xB1>(v2[0]), x1 = v2[1] + dpp<0xB1>(v2[1]);
    const float outv = b0 ? x1 : x0;
    __builtin_amdgcn_raw_buffer_store_b32(nomark(__float_as_uint(outv)), rws, lane * 4, WS_ST2 + ((mt * 2 + kh) * 16 + nt * 2 + uh) * 256, SC1);
  }
  STAMP(1, 7);
  SB();
#pragma unroll
  for (int tu = 0; tu < 4; ++tu)
#pragma unroll
    for (int k = 0; k < 3; ++k) acc[k][tu] = f32x4{0.f, 0.f, 0.f, 0.f};
  unsigned k3v[6];
#pragma unroll
  for (int i = 0; i < 6; ++i) {
    const int e3[6] = {0, 2, 3, 5, 1, 4};
    k3v[i] = l16 + (w < 3 ? (w * 6 + e3[i]) * 1024u : i < 4 ? (18 + i) * 1024u : OOB);
  }
  v2u x1h[2][2];
  int x1o;
  {
    const int row = 8 * q + ((lane >> 2) & 3);
    const int sg = ((row >> 1) & 1) | (((row >> 3) & 1) << 1);
    x1o = LDS_A + nt * 8192 + row * 128 + ((w ^ sg) * 32) + (lane & 3) * 8;
  }
  v4u wo[6][4];
#define LOAD_WO(f) wo[(f) >> 2][(f) & 3] = ld128<SC1>(rws, k3v[(f) >> 2], WS_WOT + (nt * 4 + ((f) & 3)) * 22 * 1024)
  f32x4 pt[4];
  float* c1s = st + 768 + w * 96;
#define LOAD_PT(i) asf(ld128<SC1>(rws, l16 + (i) * 1024, WS_ST2 + (mt * 2 + kh) * 4096))
  SB();
#pragma unroll
  for (int i = 0; i < 8; ++i) {
#pragma unroll
    for (int j = 0; j < 12; ++j) {
      const int tu = j / 3, k = j % 3;
      const v4u a = {ah[i & 1][tu][0][0], ah[i & 1][tu][0][1], ah[i & 1][tu][1][0], ah[i & 1][tu][1][1]};
      acc[k][tu] = mfma(wg[k][i], a, acc[k][tu]);
      if (j < 8 && i < 7) ah[(i + 1) & 1][j >> 1][j & 1] = AHALF(j >> 1, i + 1, j & 1);
      if (j < 4 && i == 7) x1h[j >> 1][j & 1] = trread(smem + x1o + (j >> 1) * 4096 + (j & 1) * 512);
      if (i == 0 && j == 0) asm volatile("s_waitcnt vmcnt(24)" ::: "memory");
      if (i < 2 && j >= 4) wq[4 + i][j - 4] = READ_W(i * 8192, j - 4);
      if (i == 3 && j == 0) {
#pragma unroll
        for (int f = 0; f < 16; ++f) PINA(wq[4 + (f >> 3)][f & 7]);
      }
      if (i == 4 && j == 0) { USE(wg[0][7]); USE(wg[1][7]); USE(wg[2][7]); }
      if ((i == 2 || i == 3) && j % 3 == 2) dma_sel(IC<6>{}, slotA, (i - 2) * 4 + j / 3);
      if ((i == 4 || i == 5) && j % 3 == 2) dma_sel(IC<7>{}, slotB, (i - 4) * 4 + j / 3);
      if (j >= 8 && i == 6) pt[j - 8] = LOAD_PT(j - 8);
      if (i == 7 && j >= 8) LOAD_WO(j - 8);
      if (i == 5 && j == 0) {
        asm volatile("s_waitcnt vmcnt(30)" ::: "memory");
        __builtin_amdgcn_s_barrier();
      }
      if (i == 5 && j == 1) st[1408 + lane] = gbv;
      if (i == 5 && j == 9) c1s[lane] = c1v[0];
      if (i == 5 && j == 10) c1s[64 + (lane & 31)] = c1v[1];
      SB();
    }
  }
  STAMP(1, 8);
#pragma unroll
  for (int k = 0; k < 3; ++k)
#pragma unroll
    for (int t = 0; t < 2; ++t) xch[(w * 6 + k * 2 + t) * 64 + lane] = acc[k][2 + t];
  SB();
  {
    unsigned m = 0;
#pragma unroll
    for (int i = 0; i < 4; ++i) m = macc4(m, __builtin_bit_cast(v4u, pt[i]));
    RETRY_BEGIN(m)
#pragma unroll
      for (int i = 0; i < 4; ++i) { pt[i] = LOAD_PT(i); m = macc4(m, __builtin_bit_cast(v4u, pt[i])); }
    RETRY_END(m)
    if (tid < 4) hint_add(p.ws, 64 + (nt * 4 + (mt >> 3)) * 4 + tid);
    const f32x4 sx = (pt[0] + pt[1]) + (pt[2] + pt[3]);
    const float tv = xrow_sum4(sx[0], sx[2], sx[1], sx[3]);
    auto sw = __builtin_amdgcn_permlane32_swap(__float_as_uint(tv), __float_as_uint(tv), false, false);
    const float mm = __uint_as_float(sw[0]) * (1.0f / FH);
    const float r = __builtin_amdgcn_rsqf(fmaxf(__uint_as_float(sw[1]) * (1.0f / FH) - mm * mm, 0.f) + EPS);
    float* f2 = st + 512 + w * 64;
    f2[(q >> 1) * 32 + 2 * c + (q & 1)] = (q >> 1) ? -r * mm : r;
    f32x4 r4[2], m4[2];
#pragma unroll
    for (int t = 0; t < 2; ++t) { r4[t] = *(const f32x4*)(f2 + q * 8 + t * 4); m4[t] = *(const f32x4*)(f2 + 32 + q * 8 + t * 4); }
#pragma unroll
    for (int k = 0; k < 3; ++k) {
      v4u o;
#pragma unroll
      for (int t = 0; t < 2; ++t) {
        o[2 * t] = nomark(pk2(fma2(lo2(z2v[k][t]), lo2(r4[t]), lo2(m4[t]))));
        o[2 * t + 1] = pk2(fma2(hi2(z2v[k][t]), hi2(r4[t]), hi2(m4[t])));
      }
      st128<SC1>(o, rws, z2o[k], 0);
    }
  }
  STAMP(1, 9);
  SB();
  f32x4 z1f[3][2];
  constexpr int RD = 12;
  v4u zf[RD];
#define LOAD_Z(n) ld128<SC1>(rws, zv[((n) >> 1) & 7] + ((n) & 1) * 1024, WS_Z2 + (((n) < 48 ? UT(((n) >> 4) % 3) : UO(((n) >> 4) % 3)) * 64 + w * 16) * 1024 + (((n) >> 4) % 3 == 2 ? oob3 : 0u))
  unsigned zv[8];
#pragma unroll
  for (int i = 0; i < 8; ++i) { zv[i] = l16 + ((CKMAP(i) + kr) & 7) * 2048; PIN(zv[i]); }
  {
    bar();
    STAMP(1, 34);
#pragma unroll
    for (int k = 0; k < 6; ++k) PINA(acc[k >> 1][k & 1]);
    f32x4 oth[3][2], cs1[3], bs1[3];
    float r1[2], m1[2];
#pragma unroll
    for (int t = 0; t < 2; ++t) { r1[t] = fin[t * 16 + c]; m1[t] = fin[32 + t * 16 + c]; }
#pragma unroll
    for (int k = 0; k < 3; ++k) {
      cs1[k] = *(const f32x4*)(c1s + k * 32 + q * 4); bs1[k] = *(const f32x4*)(c1s + k * 32 + 16 + q * 4);
#pragma unroll
      for (int t = 0; t < 2; ++t) oth[k][t] = xch[((w ^ 1) * 6 + k * 2 + t) * 64 + lane];
    }
#pragma unroll
    for (int i = 4; i < WO0; ++i) LOAD_WO(i);
    SB();
    f32x2 z1a[6][2];
#pragma unroll
    for (int i = 0; i < 6; ++i) {
      const int k = i >> 1, t = i & 1;
      const f32x4 a = acc[k][t] + oth[k][t];
      z1a[i][0] = fma2(lo2(a), bc2(r1[t]), fma2(lo2(cs1[k]), bc2(m1[t]), lo2(bs1[k])));
      z1a[i][1] = fma2(hi2(a), bc2(r1[t]), fma2(hi2(cs1[k]), bc2(m1[t]), hi2(bs1[k])));
    }
#pragma unroll
    for (int i = 0; i < GELU_PRE; ++i) {
      const f32x2 lo = gelu2(z1a[i][0]), hi = gelu2(z1a[i][1]);
      z1f[i >> 1][i & 1] = f32x4{lo[0], lo[1], hi[0], hi[1]};
      PIN(z1f[i >> 1][i & 1]);
    }
#pragma unroll
    for (int i = GELU_PRE; i < 6; ++i) { PIN(z1a[i][0]); PIN(z1a[i][1]); }
    SB();
    STAMP(1, 10);
    hint_wait(rws, 0u, WS_CNT + (64 + (nt * 4 + w) * 4 + (xcd >> 1)) * 128, 8u);
    STAMP(1, 22);
#pragma unroll
    for (int n = 0; n < RD; ++n) zf[n] = LOAD_Z(n);
    SB();
#pragma unroll
    for (int i = GELU_PRE; i < 6; ++i) {
      const f32x2 lo = gelu2(z1a[i][0]), hi = gelu2(z1a[i][1]);
      z1f[i >> 1][i & 1] = f32x4{lo[0], lo[1], hi[0], hi[1]};
      PIN(z1f[i >> 1][i & 1]);
    }
  }
  f32x4* xb = (f32x4*)(smem + LDS_A) + lane;
  f32x4* yb = (f32x4*)(smem + LDS_A + 49152) + lane;
  f32x4 ac2[2][4], ini[4], yo[2];
  const float bs0 = bf2f(bspq[0]), bs1_ = bf2f(bspq[1]);
  const f32x4 bq0 = {bs0, bs0, bs0, bs0}, bq1 = {bs1_, bs1_, bs1_, bs1_}, z4 = {0.f, 0.f, 0.f, 0.f};
  unsigned voff[2];
#pragma unroll
  for (int t = 0; t < 2; ++t) voff[t] = WS_V + (mt * 4 + 2 * kh + t) * 22 * 1024;
  float ev[4];
  unsigned eo0;
  SB();
  STAMP(1, 11);
  asm volatile("s_waitcnt vmcnt(30)" ::: "memory");
  auto v_store = [&](int k, int t, unsigned o0, unsigned o1) __attribute__((always_inline)) {
    const int uu = UO(k);
    st64<SC1>(v2u{o0, o1}, rws, l16, voff[t] + (uu >> 1) * 1024 + (uu & 1) * 8 + (k == 2 ? oob3 : 0u));
  };
  sfor<6>([&](auto P) __attribute__((always_inline)) {
    constexpr int ps = decltype(P)::value, cur = ps & 1, prv = cur ^ 1;
    unsigned zm = 0;
    sfor<64>([&](auto G) __attribute__((always_inline)) {
      constexpr int g = decltype(G)::value, ks = g >> 2, tu = g & 3, ck = ks >> 1, k2 = ks & 1, n = ps * 16 + ks;
      const f32x4 cin = ks > 0 ? ac2[cur][tu] : ps >= 3 ? ini[tu] : tu == 0 ? bq0 : tu == 1 ? bq1 : z4;
      ac2[cur][tu] = mfma(zf[n % RD], wq[ck][k2 * 4 + tu], cin);
      if constexpr (tu == 1) { zm = macc(zm, zf[n % RD]); PIN(zm); }
      if constexpr (tu == 3) {
        if constexpr (n + RD < 96) zf[n % RD] = LOAD_Z(n + RD);
        else if constexpr (WO0 + n + RD - 96 < 24) LOAD_WO(WO0 + n + RD - 96);
      }
      if constexpr (ps == 0 && g < 32 && (g & 1) == 0) wq[6 + (g >> 4)][(g >> 1) & 7] = READ_W((g >> 4) * 8192, (g >> 1) & 7);
      if constexpr (ps >= 1 && ps <= 3 && g >= 2 && g < 18 && (g & 3) == 2) xb[((w * 3 + ps - 1) * 4 + (g >> 2)) * 64] = ac2[prv][g >> 2];
      if constexpr (ps >= 4 && (g == 2 || g == 6)) yb[((w * 3 + ps - 4) * 2 + (g >> 2)) * 64] = ac2[prv][2 + (g >> 2)];
      if constexpr (ps >= 1 && g == (ps >= 4 ? 12 : 24)) {
        if constexpr (ps == 4) asm volatile("s_waitcnt vmcnt(20)" ::: "memory");
        bar();
      }
      if constexpr (ps >= 4 && (g == 13 || g == 14)) yo[g - 13] = yb[(((w ^ 1) * 3 + ps - 4) * 2 + g - 13) * 64];
      if constexpr (ps >= 4 && g >= 20 && g < 40) {
        constexpr int t = (g - 20) / 10, ph = (g - 20) % 10;
        if constexpr (ph < 4) { ev[ph] = ac2[prv][t][ph] + yo[t][ph]; PIN(ev[ph]); }
        else if constexpr (ph < 8) { ev[ph - 4] *= z1f[ps - 4][t][ph - 4]; PIN(ev[ph - 4]); }
        else if constexpr (ph == 8) { eo0 = nomark(pk2(ev[0], ev[1])); PIN(eo0); }
        else v_store(ps - 4, t, eo0, pk2(ev[2], ev[3]));
      }
      if constexpr (ps >= 2 && ps <= 4 && g >= 28 && g < 44 && (g & 3) == 0) ini[(g - 28) >> 2] = xb[(((w ^ 2) * 3 + ps - 2) * 4 + ((g - 28) >> 2)) * 64];
      if constexpr (ps == 0 && (g == 21 || g == 37 || g == 53)) st128<SC1>(mk, rws, l16, WS_V + ((mt * 4 + w) * 22 + pr0 + (g - 21) / 16) * 1024 + (g == 53 ? oob3 : 0u));
      if constexpr (ps * 64 + g == HINT4_G) { if (tid < 2) hint_add(p.ws, 192 + mt * 2 + tid); }
      if constexpr (ps == 5 && g == POLL4_G) flagv = __builtin_amdgcn_raw_buffer_load_b32(rws, w == 0 ? 0u : OOB, WS_CNT + (192 + mt * 2 + (xcd & 1)) * 128, SC1);
      if constexpr (g == 0 && ps >= 1 && ps < 5) STAMP(1, 55 + ps);
      SB();
    });
    if (__builtin_expect(any_mark(zm), 0)) {
#pragma unroll
      for (int tu = 0; tu < 4; ++tu) ac2[cur][tu] = ps >= 3 ? xb[(((w ^ 2) * 3 + ps - 3) * 4 + tu) * 64] : tu == 0 ? bq0 : tu == 1 ? bq1 : z4;
      sfor<16>([&](auto K) __attribute__((always_inline)) {
        constexpr int ks = decltype(K)::value, ck = ks >> 1, k2 = ks & 1;
        v4u zz;
        unsigned sp = 0;
        do { MEMFENCE(); zz = LOAD_Z(ps * 16 + ks); } while (any_mark(macc(0u, zz)) & (++sp <= SPIN_CAP));
#pragma unroll
        for (int tu = 0; tu < 4; ++tu) ac2[cur][tu] = mfma(zz, wq[ck][k2 * 4 + tu], ac2[cur][tu]);
      });
    }
  });
  STAMP(1, 12);
  {
#pragma unroll
    for (int t = 0; t < 2; ++t) yb[((w * 3 + 2) * 2 + t) * 64] = ac2[1][2 + t];
    PIN(ac2[1][0]); PIN(ac2[1][1]);
    if (w == 0) st[0] = __uint_as_float(flagv < 8u);
    bar();
    late = __float_as_uint(st[0]);
    STAMP(1, 52);
#pragma unroll
    for (int t = 0; t < 2; ++t) yo[t] = yb[(((w ^ 1) * 3 + 2) * 2 + t) * 64];
#pragma unroll
    for (int t = 0; t < 2; ++t) {
      const f32x4 g = (ac2[1][t] + yo[t]) * z1f[2][t];
      v_store(2, t, nomark(pk2(lo2(g))), pk2(hi2(g)));
    }
  }
  STAMP(1, 13);
  SB();
  f32x4 gb4[4];
  f32x2 x1f[2][4];
  STAMP(1, 53);
  if (__builtin_amdgcn_readfirstlane(late)) {
    if (w == 0) hint_wait(rws, 0u, WS_CNT + (192 + mt * 2 + (xcd & 1)) * 128, 8u);
    __builtin_amdgcn_s_barrier();
  }
  MEMFENCE();
  STAMP(1, 23);
  f32x4 a3[4][4];
  {
    unsigned spins = 0;
    for (;;) {
      MEMFENCE();
      v4u vs[6][4];
#define LOAD_V(f) vs[(f) >> 2][(f) & 3] = ld128<SC1>(rws, k3v[(f) >> 2], WS_V + (mt * 4 + (((f) & 3) ^ w)) * 22 * 1024)
#pragma unroll
      for (int f = 0; f < 12; ++f) LOAD_V(f);
      SB();
      unsigned m = 0;
      sfor<96>([&](auto G) __attribute__((always_inline)) {
        constexpr int g = decltype(G)::value, ks = g >> 4, tu = (g >> 2) & 3, e = g & 3;
        a3[e][tu] = mfma(wo[ks][e], vs[ks][tu], ks == 0 ? f32x4{0.f, 0.f, 0.f, 0.f} : a3[e][tu]);
        if constexpr (e == 0 && 12 + (g >> 2) < 24) LOAD_V(12 + (g >> 2));
        if constexpr (e == 1) { m = macc(m, vs[ks][tu]); PIN(m); }
        SB();
      });
      if (!(any_mark(m) & (++spins <= SPIN_CAP))) break;
    }
  }
#pragma unroll
  for (int i = 0; i < 4; ++i) gb4[i] = *(const f32x4*)(st + 1408 + (i >> 1) * 32 + q * 8 + (i & 1) * 4);
#pragma unroll
  for (int i = 0; i < 8; ++i) { x1f[i >> 2][i & 3] = unpk(x1h[i >> 2][(i >> 1) & 1][i & 1]); PIN(x1f[i >> 2][i & 3]); }
  STAMP(1, 14);
  {
    f32x4* red = (f32x4*)(smem + LDS_W);
#pragma unroll
    for (int tu = 1; tu < 4; ++tu)
#pragma unroll
      for (int e = 0; e < 4; ++e) red[((w * 3 + tu - 1) * 4 + e) * 64 + lane] = a3[e][tu];
#pragma unroll
    for (int e = 0; e < 4; ++e) PIN(a3[e][0]);
    bar();
    STAMP(1, 54);
    f32x4 o3[4][3];
#pragma unroll
    for (int e = 0; e < 4; ++e)
#pragma unroll
      for (int rel = 1; rel < 4; ++rel) o3[e][rel - 1] = red[(((w ^ rel) * 3 + rel - 1) * 4 + e) * 64 + lane];
    const rsrc_t rout = mkrs(p.out, L * D * 2);
    SB();
#pragma unroll
    for (int pp = 0; pp < 2; ++pp) {
      v4u o;
#pragma unroll
      for (int h = 0; h < 2; ++h) {
        const int e = pp * 2 + h;
        const f32x4 y = (a3[e][0] + o3[e][0]) + (o3[e][1] + o3[e][2]);
        o[2 * h] = pk2(fma2(lo2(y), lo2(gb4[e]), x1f[pp][2 * h]));
        o[2 * h + 1] = pk2(fma2(hi2(y), hi2(gb4[e]), x1f[pp][2 * h + 1]));
      }
      st128<0>(o, rout, perm(w, c) * 1024 + q * 16 + pp * 64, mt * 64 * 1024 + nt * 128);
    }
  }
  if (b == 0 && tid == 0) *(unsigned*)(p.ws + WS_EPOCH) = tag_s;
  STAMP(1, 15);
  STAMP_FLUSH(1);
  asm volatile("adaln_code_end:");
}

extern "C" void kernel_launch(void* const* d_in, const int* in_sizes, int n_in, void* d_out, int out_size, void* d_ws,
                              size_t ws_size, hipStream_t stream) {
  Params p;
  p.u = (const u16*)d_in[0];
  p.lar = (const u16*)d_in[1];
  p.aim = (const u16*)d_in[2];
  p.cre = (const u16*)d_in[3];
  p.cim = (const u16*)d_in[4];
  p.ldt = (const u16*)d_in[5];
  p.dsk = (const u16*)d_in[6];
  p.win = (const u16*)d_in[8];
  p.wsp = (const u16*)d_in[9];
  p.bsp = (const u16*)d_in[10];
  p.wout = (const u16*)d_in[11];
  p.c = (const u16*)d_in[12];
  p.wmod = (const u16*)d_in[13];
  p.out = (u16*)d_out;
  p.ws = (unsigned char*)d_ws;
  (void)hipFuncSetAttribute((const void*)adaln_mega, hipFuncAttributeMaxDynamicSharedMemorySize, MEGA_LDS);
  adaln_mega<<<256, 256, MEGA_LDS, stream>>>(p);
}
```

```cpp
#include <hip/hip_runtime.h>
#include <hip/hip_bf16.h>
#include <stdint.h>
#include <utility>

#define LAS __attribute__((address_space(3)))
typedef unsigned v4u __attribute__((ext_vector_type(4)));
typedef unsigned v2u __attribute__((ext_vector_type(2)));
typedef float f32x4 __attribute__((ext_vector_type(4)));
typedef float f32x2 __attribute__((ext_vector_type(2)));
typedef short bf16x8 __attribute__((ext_vector_type(8)));
typedef _Float16 f16x8 __attribute__((ext_vector_type(8)));
typedef _Float16 f16x2 __attribute__((ext_vector_type(2)));
typedef __bf16 bf16x2_t __attribute__((ext_vector_type(2)));
typedef unsigned short u16;
typedef __amdgpu_buffer_rsrc_t rsrc_t;

#define STAMP_DECL do { } while (0)
#define STAMP(k, i) do { } while (0)
#define STAMP_FLUSH(k) do { } while (0)

#define OPT_A 1
#define OPT_B 1
#define OPT_C 2
#define OPT_D 1
#define OPT_E 2
#define OPT_F 1
constexpr int L = 2048, D = 512, NM = 128, FH = 688, FF = 1376;
constexpr float EPS = 1e-5f;
constexpr int SC1 = 16;
constexpr unsigned OOB = 0x40000000u;
constexpr unsigned SPIN_CAP = 1u << 14;

constexpr unsigned WS_EPOCH = 0;
constexpr unsigned WS_CNT = 4096;
constexpr unsigned WS_SR = WS_CNT + 512 * 128;
constexpr unsigned WS_SB = WS_SR + 2048 * 8;
constexpr unsigned WS_MODG = WS_SB + 2048 * 8;
constexpr unsigned WS_CSB = WS_MODG + 3072 * 32;
constexpr unsigned WS_ST2 = WS_CSB + 88 * 16 * 32;
constexpr unsigned WS_UG = 1u << 20;
static_assert(WS_ST2 + 32 * 2 * 16 * 32 * 8 <= WS_UG, "small buffers");
constexpr unsigned WS_X1T = WS_UG + 256 * 2048 * 8;
constexpr unsigned WS_WP = WS_X1T + 512 * 2048 * 2;
constexpr unsigned WS_WOT = WS_WP + 88 * 16 * 1024;
constexpr unsigned WS_Z2 = WS_WOT + 32 * 22 * 1024;
constexpr unsigned WS_V = WS_Z2 + 44 * 64 * 1024;
constexpr unsigned WS_END = WS_V + 128 * 22 * 1024;

struct Params {
  const u16 *u, *lar, *aim, *cre, *cim, *ldt, *dsk, *win, *wsp, *bsp, *wout, *c, *wmod;
  u16* out;
  unsigned char* ws;
};

#define DI __device__ __forceinline__
template <int V> struct IC { static constexpr int value = V; };
template <class F, int... I> DI void sfor_(F&& f, std::integer_sequence<int, I...>) { int d[] = {(f(IC<I>{}), 0)...}; (void)d; }
template <int N, class F> DI void sfor(F&& f) { sfor_(f, std::make_integer_sequence<int, N>{}); }
DI float bf2f(unsigned b) { return __builtin_bit_cast(float, b << 16); }
DI float bflo(unsigned w) { return __builtin_bit_cast(float, w << 16); }
DI float bfhi(unsigned w) { return __builtin_bit_cast(float, w & 0xFFFF0000u); }
DI f32x2 unpk(unsigned w) { return f32x2{bflo(w), bfhi(w)}; }
DI unsigned pk2(f32x2 v) { return __builtin_bit_cast(unsigned, __builtin_convertvector(v, bf16x2_t)); }
DI unsigned pk2(float lo, float hi) { return pk2(f32x2{lo, hi}); }
DI unsigned nomark(unsigned x) { return x < 0xFFFFFFFEu ? x : 0xFFFFFFFEu; }
DI float rbf(float x) { return bflo(pk2(x, 0.f)); }
DI f32x2 fma2(f32x2 a, f32x2 b, f32x2 c) { return __builtin_elementwise_fma(a, b, c); }
DI f32x2 lo2(f32x4 v) { return f32x2{v[0], v[1]}; }
DI f32x2 hi2(f32x4 v) { return f32x2{v[2], v[3]}; }
DI f32x2 bc2(float x) { return f32x2{x, x}; }
DI f32x4 mfma(v4u a, v4u b, f32x4 c) {
  return __builtin_amdgcn_mfma_f32_16x16x32_bf16(__builtin_bit_cast(bf16x8, a), __builtin_bit_cast(bf16x8, b), c, 0, 0, 0);
}
DI f32x4 mfma_h(v4u a, v4u b, f32x4 c) {
  return __builtin_amdgcn_mfma_f32_16x16x32_f16(__builtin_bit_cast(f16x8, a), __builtin_bit_cast(f16x8, b), c, 0, 0, 0);
}
DI float dot2(unsigned a, unsigned b, float acc) {
  return __builtin_amdgcn_fdot2_f32_bf16(__builtin_bit_cast(bf16x2_t, a), __builtin_bit_cast(bf16x2_t, b), acc, false);
}
constexpr unsigned ONES = 0x3F803F80u;
DI int perm(int u, int c) { return 32 * (u >> 1) + 8 * (c >> 2) + 4 * (u & 1) + (c & 3); }
template <int CTRL> DI float dpp(float x) {
  return __builtin_bit_cast(float, __builtin_amdgcn_mov_dpp(__builtin_bit_cast(int, x), CTRL, 0xf, 0xf, true));
}
DI float row16_sum(float x) {
  x += dpp<0xB1>(x); x += dpp<0x4E>(x); x += dpp<0x141>(x); x += dpp<0x128>(x);
  return x;
}
DI float row16_max(float x) {
  x = fmaxf(x, dpp<0xB1>(x)); x = fmaxf(x, dpp<0x4E>(x)); x = fmaxf(x, dpp<0x141>(x)); x = fmaxf(x, dpp<0x128>(x));
  return x;
}
DI float xrow_sum(float x) {
  auto s = __builtin_amdgcn_permlane16_swap(__float_as_uint(x), __float_as_uint(x), false, false);
  x = __uint_as_float(s[0]) + __uint_as_float(s[1]);
  auto t = __builtin_amdgcn_permlane32_swap(__float_as_uint(x), __float_as_uint(x), false, false);
  return __uint_as_float(t[0]) + __uint_as_float(t[1]);
}
DI float xrow_sum4(float a, float b, float c, float d) {
  auto s = __builtin_amdgcn_permlane16_swap(__float_as_uint(a), __float_as_uint(b), false, false);
  auto t = __builtin_amdgcn_permlane16_swap(__float_as_uint(c), __float_as_uint(d), false, false);
  const float x = __uint_as_float(s[0]) + __uint_as_float(s[1]), y = __uint_as_float(t[0]) + __uint_as_float(t[1]);
  auto z = __builtin_amdgcn_permlane32_swap(__float_as_uint(x), __float_as_uint(y), false, false);
  return __uint_as_float(z[0]) + __uint_as_float(z[1]);
}
DI float xrow_max(float x) {
  auto s = __builtin_amdgcn_permlane16_swap(__float_as_uint(x), __float_as_uint(x), false, false);
  x = fmaxf(__uint_as_float(s[0]), __uint_as_float(s[1]));
  auto t = __builtin_amdgcn_permlane32_swap(__float_as_uint(x), __float_as_uint(x), false, false);
  return fmaxf(__uint_as_float(t[0]), __uint_as_float(t[1]));
}
DI f32x2 gelu2(f32x2 x) {
  const float k0 = -2.0f * 0.7978845608028654f * 1.4426950408889634f, k1 = k0 * 0.044715f;
  const f32x2 a = x * fma2(x * x, bc2(k1), bc2(k0));
  const f32x2 e = f32x2{__builtin_amdgcn_exp2f(a[0]), __builtin_amdgcn_exp2f(a[1])} + bc2(1.0f);
  return x * f32x2{__builtin_amdgcn_rcpf(e[0]), __builtin_amdgcn_rcpf(e[1])};
}
DI rsrc_t mkrs(const void* ptr, unsigned bytes) { return __builtin_amdgcn_make_buffer_rsrc((void*)ptr, 0, (int)bytes, 0x00020000); }
template <int AUX> DI v4u ld128(rsrc_t rs, unsigned v, unsigned s) { return __builtin_amdgcn_raw_buffer_load_b128(rs, (int)v, (int)s, AUX); }
template <int AUX> DI v2u ld64(rsrc_t rs, unsigned v, unsigned s) { return __builtin_amdgcn_raw_buffer_load_b64(rs, (int)v, (int)s, AUX); }
template <int AUX> DI void st128(v4u x, rsrc_t rs, unsigned v, unsigned s) { __builtin_amdgcn_raw_buffer_store_b128(x, rs, (int)(v + s), 0, AUX); }
template <int AUX> DI void st64(v2u x, rsrc_t rs, unsigned v, unsigned s) { __builtin_amdgcn_raw_buffer_store_b64(x, rs, (int)v, (int)s, AUX); }
DI f32x4 asf(v4u x) { return __builtin_bit_cast(f32x4, x); }
DI float sum4(f32x4 x) { return (x[0] + x[1]) + (x[2] + x[3]); }
DI unsigned umax3(unsigned a, unsigned b, unsigned c) { unsigned m = a > b ? a : b; return m > c ? m : c; }
DI unsigned macc4(unsigned m, v4u x) { return umax3(umax3(m, x[0], x[1]), x[2], x[3]); }
DI unsigned macc(unsigned m, v4u x) { return umax3(m, x[0], x[2]); }
DI bool any_mark(unsigned m) { return __builtin_amdgcn_ballot_w64(m == 0xFFFFFFFFu) != 0ull; }
#define SB() __builtin_amdgcn_sched_barrier(0)
#define PIN(x) asm volatile("" : "+v"(x))
#define PINA(x) asm volatile("" : "+a"(x))
#define USE(x) asm volatile("" ::"v"(x))
#define RETRY_BEGIN(m) if (__builtin_expect(any_mark(m), 0)) { unsigned sp_ = 0; do { MEMFENCE(); m = 0;
#define RETRY_END(m) } while (any_mark(m) & (++sp_ <= SPIN_CAP)); }
#define MEMFENCE() asm volatile("" ::: "memory")
DI void bar() { asm volatile("s_waitcnt lgkmcnt(0)" ::: "memory"); __builtin_amdgcn_s_barrier(); asm volatile("" ::: "memory"); }

DI f32x2 cmul(f32x2 a, f32x2 b) { return fma2(bc2(a[1]), f32x2{-b[1], b[0]}, bc2(a[0]) * b); }
DI f32x2 cexp_p(float are2, float rev_hi, float rev_lo, float p) {
  const float mag = __builtin_amdgcn_exp2f(are2 * p);
  const float hi = rev_hi * p;
  const float fr = (hi - __builtin_rintf(hi)) + rev_lo * p;
  return f32x2{mag * __builtin_amdgcn_cosf(fr), mag * __builtin_amdgcn_sinf(fr)};
}
DI float exp_acc(float x) {
  const float hi = x * 1.4426950408889634f;
  const float lo = __builtin_fmaf(x, 1.4426950408889634f, -hi) + x * 1.925963033500011e-8f;
  const float e = __builtin_amdgcn_exp2f(hi);
  return __builtin_fmaf(e, lo * 0.6931471805599453f, e);
}
DI unsigned pkh(float a, float b) {
  f16x2 v = {(_Float16)a, (_Float16)b};
  return __builtin_bit_cast(unsigned, v);
}

constexpr int CV_RK = 4160;
constexpr int CV_H = 2 * CV_RK;
constexpr int CV_CH = CV_H + 4608;
constexpr int LDS_A = 0;
constexpr int LDS_U = 2 * CV_CH;
constexpr int LDS_GS = LDS_U + 8192;
constexpr int LDS_TAB = 36864;
constexpr int KP_CH = 13840;
constexpr int LDS_KP = LDS_TAB + 2 * 64 * 528 + 16;
constexpr int LDS_LN = 0, LDS_MR = 8448;
constexpr int LDS_WI = 145472, LDS_WO = LDS_WI + 10240;
constexpr int LDS_X = 65536;
constexpr int LDS_S = LDS_X + 24576;
constexpr int LDS_W = LDS_S + 6144;
constexpr int MEGA_LDS = LDS_W + 65536;
static_assert(LDS_WO + 4608 <= MEGA_LDS && LDS_KP + 2 * KP_CH <= LDS_WI && LDS_S >= 73728, "lds");
static_assert(LDS_GS + 1280 <= LDS_TAB && LDS_KP + 2 * KP_CH <= MEGA_LDS && MEGA_LDS <= 160 * 1024, "lds");
#define FLAG_AT 64
static_assert(FLAG_AT > 45 && FLAG_AT < 78 && FLAG_AT % 5 != 2, "flag position");
#define WF_EARLY 5
#define WO0 12
#define WSP_P0 4
#define HINT4_KS 15
#define ZDEPTH 3
#define GELU_IN 0
#define EPI_W 0
#define MOD_POS 0
#define POLL3 0
#define GELU_PRE 4
#define HINT4_G 310
static_assert(HINT4_G > 4 * 64 + 12, "behind the marker barrier");
#define POLL4_G 45
#define RELABEL 1
#define CKMAP(ck) (RELABEL ? 2 * ((ck) & 3) + ((ck) >> 2) : (ck))
#define POLL_AT 134
#define MOD_MID 1
#define LATE_SLEEP 6
#define SMALL_N 3
#define IN_AUX 0
#define WSP_AUX 0

template <class T> DI T ldsr(unsigned a) { return *(const LAS T*)(size_t)a; }
template <class T> DI void ldsw(unsigned a, T v) { *(LAS T*)(size_t)a = v; }
DI v2u trread(const unsigned char* ptr) {
  typedef short v4i16_t __attribute__((ext_vector_type(4)));
  return __builtin_bit_cast(v2u, __builtin_amdgcn_ds_read_tr16_b64_v4i16((LAS v4i16_t*)ptr));
}
DI v2u trread_a(unsigned a) {
  typedef short v4i16_t __attribute__((ext_vector_type(4)));
  return __builtin_bit_cast(v2u, __builtin_amdgcn_ds_read_tr16_b64_v4i16((LAS v4i16_t*)(size_t)a));
}
struct CvOrder { int qq[36], pp[36]; };
constexpr CvOrder cv_order() {
  CvOrder o{};
  int n = 0;
  for (int m = 0; m < 8; ++m) {
    for (int j = 0; j < m; ++j) if (m + j < 8) { o.qq[n] = m; o.pp[n] = j; ++n; }
    for (int j = 0; j <= m; ++j) if (m + j < 8) { o.qq[n] = j; o.pp[n] = m; ++n; }
  }
  return o;
}
constexpr CvOrder CVO = cv_order();

typedef __attribute__((address_space(1))) unsigned gu32;
DI unsigned tx(unsigned m, unsigned x, unsigned tag) { return __builtin_amdgcn_bitop3_b32(m, x, tag, 0xF6); }
DI unsigned tacc(unsigned m, v4u x, unsigned tag) { return tx(tx(m, x[1], tag), x[3], tag); }
DI bool any_nz(unsigned m) { return __builtin_amdgcn_ballot_w64(m != 0u) != 0ull; }
DI void hint_add(unsigned char* ws, int line) { (void)__hip_atomic_fetch_add((gu32*)(ws + WS_CNT + line * 128), 1u, __ATOMIC_RELAXED, __HIP_MEMORY_SCOPE_AGENT); }
DI void hint_wait(rsrc_t rws, unsigned voff, unsigned soff, unsigned want) {
  unsigned sp = 0;
  for (;;) {
    MEMFENCE();
    const unsigned v = __builtin_amdgcn_raw_buffer_load_b32(rws, (int)voff, (int)soff, SC1);
    if (!((__builtin_amdgcn_ballot_w64(v < want) != 0ull) & (++sp <= SPIN_CAP))) break;
    __builtin_amdgcn_s_sleep(OPT_E);
  }
  MEMFENCE();
}

extern "C" __global__ void __launch_bounds__(256, 1) adaln_mega(Params p) {
  extern __shared__ __attribute__((aligned(16))) unsigned char smem[];
  const int tid = threadIdx.x, lane = tid & 63, w = __builtin_amdgcn_readfirstlane(tid >> 6);
  const int b = blockIdx.x, xcd = b & 7, idx = b >> 3;
  const int pb = xcd * 32 + idx;
  const unsigned l16 = lane * 16;
  STAMP_DECL;
  STAMP(1, 0);
  v2u codepf;
  {
    unsigned long long pc;
    int rel_m, rel_e;
    asm volatile("s_getpc_b64 %0\n\ts_mov_b32 %1, adaln_mega@rel32@lo+4\n\ts_mov_b32 %2, adaln_code_end@rel32@lo+12" : "=s"(pc), "=s"(rel_m), "=s"(rel_e));
    const rsrc_t rcode = mkrs((const void*)(pc + (long long)rel_m), rel_e > rel_m ? (unsigned)(rel_e - rel_m) & ~15u : 0u);
    codepf = ld64<0>(rcode, tid * 8, idx * 2048);
  }
  SB();
  const rsrc_t ru = mkrs(p.u, L * D * 2);
  v4u xr[2];
  xr[0] = ld128<IN_AUX>(ru, l16, (pb * 8 + w * 2) * 1024);
  xr[1] = ld128<IN_AUX>(ru, l16 + 1024, (pb * 8 + w * 2) * 1024);
  const int mkg = b & 3, mcg = b >> 2;
  const unsigned cq = p.c[mkg * 128 + (tid & 3) * 32 + (tid >> 3)];
  const unsigned epoch = *(const unsigned*)(p.ws + WS_EPOCH);
  const rsrc_t rws = mkrs(p.ws, WS_END);
  __builtin_amdgcn_raw_buffer_store_b32(0u, rws, tid < 2 ? tid * 32768u : OOB, WS_CNT + b * 128, SC1);
  SB();
  const int cch = w >> 1, eh = w & 1, ns = tid & 127;
  const unsigned q_cre = p.cre[pb * 256 + tid], q_cim = p.cim[pb * 256 + tid], q_lar = p.lar[pb * 256 + tid], q_aim = p.aim[pb * 256 + tid];
  const unsigned q_ldt = p.ldt[pb * 2 + cch], q_dsk = p.dsk[pb * 2 + cch];
  v4u xm[4];
#define LOAD_XM() { \
  const rsrc_t rmod = mkrs(p.wmod, D * 3072 * 2); \
_Pragma("unroll") \
  for (int i = 0; i < 4; ++i) xm[i] = ld128<IN_AUX>(rmod, (tid >> 3) * 6144 + (tid & 7) * 16, (mkg * 128 + i * 32) * 6144 + mcg * 128 + (b < 192 ? 0u : OOB)); }
  SB();
  LOAD_XM();
  SB();
  STAMP(1, 24);
  const int c = lane & 15, q = lane >> 4;
  const unsigned lds0 = (unsigned)(size_t)(LAS unsigned char*)smem;
  unsigned pos4[9];
  {
    const float f4 = (float)(8188 - tid * 32);
#pragma unroll
    for (int j = 0; j < 10; j += 2) {
      const unsigned pp = pk2(bc2(f4) - f32x2{4.f * j, 4.f * j + 4.f});
      const int p0 = (int)bflo(pp), p1 = (int)bfhi(pp);
      pos4[j] = p0 + ((p0 >> 5) << 2) + lds0 + LDS_KP;
      PIN(pos4[j]);
      if (j + 1 < 9) { pos4[j + 1] = p1 + ((p1 >> 5) << 2) + lds0 + LDS_KP; PIN(pos4[j + 1]); }
    }
  }
  unsigned co[2][4];
  {
    const unsigned b0 = (eh ? 1152 + 576 * q : 144 * (q >> 1) + 4 * (q & 1)) * 4 + 36 * c + lds0 + LDS_KP + cch * KP_CH;
    const unsigned b1 = (q < 2 ? 288 + 144 * q : 568 + 4 * q) * 4 + 36 * c + lds0 + LDS_KP + cch * KP_CH;
    const unsigned s0 = eh ? 576 : 4, s1 = q < 2 ? 8 : 576;
#pragma unroll
    for (int r = 0; r < 4; ++r) { co[0][r] = b0 + r * s0; co[1][r] = b1 + r * s1; PIN(co[0][r]); PIN(co[1][r]); }
  }
  *(unsigned*)(smem + LDS_KP - 16 + cch * KP_CH + (lane & 3) * 4) = 0u;
  unsigned a_pad, a_wi, a_wo, a_tab, gm_off, gc_off;
  a_pad = lds0 + cch * CV_CH + (lane < 32 ? CV_H + l16 : ((lane >> 1) & 1) * CV_RK + 4096 + (lane & 1) * 16);
  a_wi = lds0 + LDS_WI + (w * 32 + 8 * q + ((lane >> 2) & 3)) * 80 + (lane & 3) * 16;
  a_wo = lds0 + LDS_WO + (8 * q + ((lane >> 2) & 3)) * 144 + (w >> 1) * 64 + (lane & 3) * 16 + (w & 1) * 8;
  a_tab = lds0 + LDS_TAB + cch * (64 * 528) + c * 528 + q * 16;
  gm_off = ((4 - cch) * 512 + mkg * 128 + ns) * 8;
  gc_off = (((lane & 7) > 5 ? 0 : ((lane & 7) >> 1)) * 512 + pb * 2 + (lane & 1)) * 8;
  PIN(a_pad); PIN(a_wi); PIN(a_wo); PIN(a_tab); PIN(gm_off); PIN(gc_off);
  unsigned ab[4], bb, dv[4], rdo4[4], xoff;
#pragma unroll
  for (int i = 0; i < 4; ++i) {
    ab[i] = lds0 + cch * CV_CH + (1 - (c & 1)) * CV_RK + 2 * (2030 - 2 * (c >> 1) + 8 * q) - eh * 256 - 192 - 512 - 1024 * i;
    dv[i] = (lane >> 3) * 4096 + (((lane & 7) ^ ((lane >> 4) & 1) ^ (i << 1)) * 16) + i * (32768 - 1024);
    rdo4[i] = lds0 + LDS_W + w * 16384 + ((perm((i & 1) * 2 ^ (2 * (w & 1)), c) * 128 + ((q ^ (c >> 1)) * 16)) ^ ((i >> 1) * 64));
    PIN(ab[i]); PIN(dv[i]); PIN(rdo4[i]);
  }
  bb = lds0 + cch * CV_CH + CV_H + 2 * (256 - 16 + 16 * c + 8 * q) - eh * 256 - 192;
  xoff = WS_X1T + (pb * 2 + cch) * 4096 + eh * 2048 + ((c >> 1) * 32 + ((q & 1) * 4 + 2 * (c & 1) + (q >> 1)) * 4) * 2;
  PIN(bb); PIN(xoff);
  SB();
  STAMP(1, 26);
  {
    *(v4u*)(smem + LDS_LN + (w * 2) * 1040 + l16) = xr[0];
    *(v4u*)(smem + LDS_LN + (w * 2 + 1) * 1040 + l16) = xr[1];
    float sm[2], sq[2];
#pragma unroll
    for (int i = 0; i < 2; ++i) {
      sm[i] = dot2(xr[i][0], ONES, dot2(xr[i][1], ONES, dot2(xr[i][2], ONES, dot2(xr[i][3], ONES, 0.f))));
      sq[i] = dot2(xr[i][0], xr[i][0], dot2(xr[i][1], xr[i][1], dot2(xr[i][2], xr[i][2], dot2(xr[i][3], xr[i][3], 0.f))));
    }
    const float tv = row16_sum(xrow_sum4(sm[0], sm[1], sq[0], sq[1]));
    auto sw = __builtin_amdgcn_permlane32_swap(__float_as_uint(tv), __float_as_uint(tv), false, false);
    const float m = __uint_as_float(sw[0]) * (1.0f / D);
    const float r = __builtin_amdgcn_rsqf(fmaxf(__uint_as_float(sw[1]) * (1.0f / D) - m * m, 0.f) + EPS);
    ((float*)(smem + LDS_GS + 1088))[(q >> 1) * 8 + w * 2 + (q & 1)] = (q >> 1) ? -m * r : r;
  }
  const unsigned tag = epoch * 2654435761u + 0x9E3779B9u;
  STAMP(1, 28);
  asm volatile("s_waitcnt vmcnt(%0)" ::"i"(MOD_POS == 0 ? 8 : 4) : "memory");
  bar();
  STAMP(1, 16);
  {
    unsigned ua[4][2];
#pragma unroll
    for (int i = 0; i < 4; ++i) {
      const unsigned char* src = smem + LDS_LN + (tid & 3) * 2080 + (i * 64 + (tid >> 2)) * 4;
      ua[i][0] = *(const unsigned*)src; ua[i][1] = *(const unsigned*)(src + 1040);
    }
    const unsigned lnv = ((const unsigned*)(smem + LDS_GS + 1088))[c];
    SB();
#pragma unroll
    for (int i = 0; i < 4; ++i) st128<SC1>(v4u{ua[i][0], tag, ua[i][1], tag}, rws, (tid >> 2) * 16384 + (tid & 3) * 16, WS_UG + i * 64 * 16384 + pb * 64);
    st64<SC1>(v2u{lnv, tag}, rws, tid < 16 ? (c >> 3) * 16384 + (c & 7) * 8 : OOB, WS_SR + pb * 64);
  }
  STAMP(1, 17);
  SB();
  const unsigned wp_oob = b < 176 ? 0u : OOB;
  const int wpr = b >> 2, wkg = b & 3;
  v4u xi[2], xo;
  {
    const rsrc_t rwin = mkrs(p.win, D * FF * 2), rwout = mkrs(p.wout, FH * D * 2);
    const int hf = wpr >= 22, pp = wpr - 22 * hf;
    const unsigned v = ((tid >> 2) * (FF * 2) + (tid & 3) * 16) | ((pp == 21 ? tid & 2 : 0) << 29);
#pragma unroll
    for (int i = 0; i < 2; ++i) xi[i] = ld128<IN_AUX>(rwin, v, (wkg * 128 + i * 64) * (FF * 2) + (hf * FH + pp * 32) * 2 + wp_oob);
    xo = ld128<IN_AUX>(rwout, (tid >> 3) * 1024 + (tid & 7) * 16, idx * 32 * 1024 + xcd * 128);
  }
  SB();
  const int d0 = pb * 2, dk = d0 + cch;
  const int mt = (xcd >> 1) * 8 + (idx & 7), nt = (xcd & 1) * 4 + (idx >> 3);
  const int big = (nt + 2) >> 3;
  const int np = 3 - big, pr0 = 3 * nt - big * (nt - 6);
  const int kh = w & 1, uh = w >> 1;
  const int uo0 = pr0 * 2 + uh * (4 - 2 * big), ut0 = pr0 * 2 + (uh ^ 1) * (4 - 2 * big), uo2 = pr0 * 2 + 2 + uh, ut2 = pr0 * 2 + 2 + (uh ^ 1);
#define UO(k) ((k) == 2 ? uo2 : uo0 + (k))
#define UT(k) ((k) == 2 ? ut2 : ut0 + (k))
  const unsigned oob3 = (unsigned)big << 30;
  const int kr = 2 * (idx >> 3) + (xcd & 1) + (OPT_B == 0 ? (idx & 7) : OPT_B == 1 ? 4 * (idx & 1) : OPT_B == 2 ? 2 * (idx & 3) : 0);
  const rsrc_t rwsp = mkrs(p.wsp, L * L * 2);
  const unsigned wp_fr = (wpr * 2 * 16 + wkg * 4 + w) * 1024 + wp_oob;
  v4u wr[1][8];
  const unsigned wsp_v = (lane >> 3) * 4096 + (lane & 7) * 16, wsp_s = mt * 64 * 4096 + w * 1024;
#define LOAD_WSP1(ck, i) wr[ck][i] = ld128<WSP_AUX>(rwsp, wsp_v + (((ck) + kr) & 7) * 128, wsp_s + (i) * 8 * 4096)
#define LOAD_WSPN(n) LOAD_WSP1((n) >> 3, (n) & 7)
  const v4u mk = {~0u, ~0u, ~0u, ~0u};
  unsigned bspq[2];
#pragma unroll
  for (int t = 0; t < 2; ++t) bspq[t] = p.bsp[mt * 64 + perm(2 * kh + t, c)];
  const unsigned tag_s = __builtin_amdgcn_readfirstlane(tag);
  {
    const float dt = rbf(exp_acc(bf2f(q_ldt)));
    const float cre = bf2f(q_cre) * dt, cim = bf2f(q_cim) * dt;
    const float are2 = -rbf(exp_acc(bf2f(q_lar))) * dt * 1.4426950408889634f;
    const float aim = bf2f(q_aim) * dt;
    const float rev_hi = aim * 0.15915494309189535f;
    const float rev_lo = __builtin_fmaf(aim, 0.15915494309189535f, -rev_hi) + aim * 6.4206383e-9f;
    int ex = __builtin_amdgcn_frexp_expf(xrow_max(row16_max(fmaxf(fabsf(cre), fabsf(cim))))) + 1;
    ex = ex < -100 ? -100 : ex;
    const float scale = __builtin_amdgcn_ldexpf(1.f, -ex);
    ((float*)(smem + LDS_GS + 1024))[w] = __builtin_amdgcn_ldexpf(2.f, ex);
    const f32x2 c0 = {cre * scale, cim * scale};
    const f32x2 e1 = cexp_p(are2, rev_hi, rev_lo, 1.f), e8 = cexp_p(are2, rev_hi, rev_lo, 8.f), e64 = cexp_p(are2, rev_hi, rev_lo, 64.f);
    unsigned char* tn = smem + LDS_TAB + cch * (64 * 528) + ns * 4;
#define TROW(r, z) *(unsigned*)(tn + (r) * 528) = pkh((z)[0], -(z)[1])
    SB();
    const f32x2 e128 = cmul(e64, e64), e2 = cmul(e1, e1), e256 = cmul(e128, e128), e4 = cmul(e2, e2), e512 = cmul(e256, e256);
    f32x2 f0 = {1.f, 0.f}, f1 = e64;
    f32x2 ca = cmul(c0, e512), cb = cmul(ca, e512);
#pragma unroll
    for (int j = 0; j < 8; ++j) {
      *(unsigned*)(tn + (48 + j) * 528) = pkh(f0[0], f0[1]);
      *(unsigned*)(tn + (56 + j) * 528) = pkh(f1[0], f1[1]);
      TROW(32 + j, cb);
      f0 = j == 0 ? e8 : cmul(f0, e8); f1 = cmul(f1, e8); cb = cmul(cb, e128);
      if (j < 4) {
        const f32x2 z = cmul(ca, e4);
        TROW(24 + j, ca); TROW(28 + j, z);
        ca = cmul(ca, e128);
      }
      if (j == 3 || j == 7) {
        PIN(f0); PIN(f1); PIN(cb);
        if (WSP_P0 >= 4) { LOAD_WSPN(j >> 2 << 1); LOAD_WSPN((j >> 2 << 1) + 1); }
        SB();
      }
    }
    TROW(40, cb);
    SB();
  {
    const float cv = bf2f(cq);
    const float sl = rbf(cv * __builtin_amdgcn_rcpf(1.0f + __builtin_amdgcn_exp2f(cv * -1.4426950408889634f)));
    const float sc[4] = {dpp<0x00>(sl), dpp<0x55>(sl), dpp<0xAA>(sl), dpp<0xFF>(sl)};
    f32x2 a[4];
#pragma unroll
    for (int i = 0; i < 4; ++i)
#pragma unroll
      for (int j = 0; j < 4; ++j) a[j] = i == 0 ? unpk(xm[i][j]) * bc2(sc[i]) : fma2(unpk(xm[i][j]), bc2(sc[i]), a[j]);
    float* red = (float*)(smem + LDS_MR) + (tid >> 3) * 68 + (tid & 7) * 8;
    *(f32x4*)red = f32x4{a[0][0], a[0][1], a[1][0], a[1][1]};
    *(f32x4*)(red + 4) = f32x4{a[2][0], a[2][1], a[3][0], a[3][1]};
  }
    bar();
    float ms8[8];
#pragma unroll
    for (int i = 0; i < 8; ++i) ms8[i] = ((const float*)(smem + LDS_MR))[(q * 8 + i) * 68 + w * 16 + c];
    SB();
    f32x2 t1[8];
    t1[1] = e1; t1[2] = e2; t1[4] = e4;
    t1[3] = cmul(e2, e1); t1[5] = cmul(e4, e1); t1[6] = cmul(e2, e4); t1[7] = cmul(t1[6], e1);
    const f32x2 c1 = cmul(c0, e128), c2 = cmul(c0, e256), c3 = cmul(c2, e128);
    TROW(0, c0); TROW(8, c1); TROW(16, c2); TROW(20, c3);
    {
    const float s = xrow_sum(((ms8[0] + ms8[1]) + (ms8[2] + ms8[3])) + ((ms8[4] + ms8[5]) + (ms8[6] + ms8[7])));
    st64<SC1>(v2u{__float_as_uint(s), tag}, rws, q == 0 ? c * 8 : OOB, WS_MODG + (mkg * 3072 + mcg * 64 + w * 16) * 8 + (b < 192 ? 0u : OOB));
    }
    SB();
#pragma unroll
    for (int v = 1; v < 8; ++v) {
      const f32x2 z0 = cmul(t1[v], c0), z1 = cmul(t1[v], c1);
      TROW(v, z0); TROW(8 + v, z1);
      if ((v & 1) == 0) {
        const f32x2 z2 = cmul(t1[v], c2), z3 = cmul(t1[v], c3);
        TROW(16 + v / 2, z2); TROW(20 + v / 2, z3);
        if (v < 6 && WSP_P0 >= 8) { LOAD_WSPN(v + 2); LOAD_WSPN(v + 3); }
        SB();
      }
    }
  }
  STAMP(1, 27);
#pragma unroll
  for (int i = 0; i < 2; ++i) *(v4u*)(smem + LDS_WI + (i * 64 + (tid >> 2)) * 80 + (tid & 3) * 16) = xi[i];
  *(v4u*)(smem + LDS_WO + (tid >> 3) * 144 + (tid & 7) * 16) = xo;
  STAMP(1, 29);
  bar();
  STAMP(1, 18);
  SB();
  v4u gr[4], gb_[4];
  v2u gm[4], gc[4];
#define LOAD_GS() _Pragma("unroll") for (int i_ = 0; i_ < 4; ++i_) { gr[i_] = ld128<SC1>(rws, tid * 16, WS_SR + i_ * 4096); gb_[i_] = ld128<SC1>(rws, tid * 16, WS_SB + i_ * 4096); }
#define LOAD_GM() _Pragma("unroll") for (int i_ = 0; i_ < 4; ++i_) { gm[i_] = ld64<SC1>(rws, gm_off, WS_MODG + i_ * 24576); gc[i_] = ld64<SC1>(rws, gc_off, WS_MODG + i_ * 24576); }
  v4u ug[4], wt[2];
#define LOAD_UG() _Pragma("unroll") for (int i_ = 0; i_ < 4; ++i_) ug[i_] = ld128<SC1>(rws, tid * 16, WS_UG + pb * 16384 + i_ * 4096)
  {
    v4u fa[2][8], fb[8];
    const unsigned ta = a_tab + eh * (32 * 528);
#pragma unroll
    for (int ks = 0; ks < 3; ++ks) { fb[ks] = ldsr<v4u>(a_tab + 48 * 528 + ks * 64); fa[0][ks] = ldsr<v4u>(ta + ks * 64); fa[1][ks] = ldsr<v4u>(a_tab + 16 * 528 + ks * 64); }
    SB();
    LOAD_UG();
    SB();
    f32x4 kk[2][2];
    f32x2 us;
    v2u wh_[6];
#pragma unroll
    for (int i = 0; i < 4; ++i) kk[i >> 1][i & 1] = f32x4{0.f, 0.f, 0.f, 0.f};
#pragma unroll
    for (int ks = 0; ks < 8; ++ks) {
      if (ks < 5) { fb[3 + ks] = ldsr<v4u>(a_tab + 48 * 528 + (3 + ks) * 64); fa[0][3 + ks] = ldsr<v4u>(ta + (3 + ks) * 64); fa[1][3 + ks] = ldsr<v4u>(a_tab + 16 * 528 + (3 + ks) * 64); }
      if (ks == 5) { us = *(const f32x2*)(smem + LDS_GS + 1024 + cch * 8); wh_[4] = trread_a(a_wo); wh_[5] = trread_a(a_wo + 576); }
      if (ks == 6) { wh_[0] = trread_a(a_wi); wh_[1] = trread_a(a_wi + 320); wh_[2] = trread_a(a_wi + 8); wh_[3] = trread_a(a_wi + 328); }
      kk[0][ks >> 2] = mfma_h(fa[0][ks], fb[ks], kk[0][ks >> 2]); kk[1][ks >> 2] = mfma_h(fa[1][ks], fb[ks], kk[1][ks >> 2]);
      SB();
    }
    STAMP(1, 37);
    wt[0] = v4u{wh_[0][0], wh_[0][1], wh_[1][0], wh_[1][1]};
    wt[1] = v4u{wh_[2][0], wh_[2][1], wh_[3][0], wh_[3][1]};
    st128<SC1>(v4u{wh_[4][0], wh_[4][1], wh_[5][0], wh_[5][1]}, rws, l16, WS_WOT + ((xcd * 4 + w) * 22 + idx) * 1024 + wp_oob);
    LOAD_GS();
    SB();
#pragma unroll
    for (int i = 0; i < 4; ++i) PINA(kk[i >> 1][i & 1]);
    f32x4 k0 = kk[0][0] * us[0] + kk[0][1] * us[1];
    const f32x4 k1 = kk[1][0] * us[0] + kk[1][1] * us[1];
    k0[0] += ns == 0 ? bf2f(q_dsk) : 0.f;
#pragma unroll
    for (int r = 0; r < 4; ++r) { ldsw<float>(co[0][r], k0[r]); ldsw<float>(co[1][r], k1[r]); }
  }
  STAMP(1, 31);
  bar();
  STAMP(1, 19);
  {
    float v[2][9];
#pragma unroll
    for (int j = 0; j < 9; ++j) v[0][j] = ldsr<float>(pos4[j]);
    LOAD_GM();
#pragma unroll
    for (int j = 0; j < 9; ++j) v[1][j] = ldsr<float>(pos4[j] + KP_CH);
    ldsw<v4u>(a_pad, v4u{0u, 0u, 0u, 0u});
    SB();
    STAMP(1, 36);
#pragma unroll
    for (int ch = 0; ch < 2; ++ch) {
      unsigned char* rk = smem + ch * CV_CH + tid * 16;
#pragma unroll
      for (int sft = 0; sft < 2; ++sft)
        *(v4u*)(rk + sft * CV_RK) = v4u{pk2(v[ch][sft], v[ch][sft + 1]), pk2(v[ch][sft + 2], v[ch][sft + 3]), pk2(v[ch][sft + 4], v[ch][sft + 5]), pk2(v[ch][sft + 6], v[ch][sft + 7])};
    }
#pragma unroll
    for (int i = WSP_P0; i < 8; ++i) LOAD_WSPN(i);
    st128<SC1>(mk, rws, l16, WS_X1T + dk * 4096 + eh * 2048);
    st128<SC1>(mk, rws, l16 + 1024, WS_X1T + dk * 4096 + eh * 2048);
    __builtin_amdgcn_raw_buffer_store_b32(~0u, rws, lane * 4, WS_ST2 + ((mt * 2 + kh) * 16 + nt * 2 + uh) * 256, SC1);
    SB();
    STAMP(1, 32);
    unsigned m = 0;
#pragma unroll
    for (int i = 0; i < 4; ++i) { m = tacc(m, ug[i], tag); m = tacc(m, gr[i], tag); m = tacc(m, gb_[i], tag); }
    if (__builtin_expect(any_nz(m), 0)) {
      unsigned sp = 0;
      do {
        MEMFENCE(); LOAD_UG(); LOAD_GS(); m = 0;
#pragma unroll
        for (int i = 0; i < 4; ++i) { m = tacc(m, ug[i], tag); m = tacc(m, gr[i], tag); m = tacc(m, gb_[i], tag); }
      } while (any_nz(m) & (++sp <= SPIN_CAP));
    }
    f32x2 nn[4][2];
#pragma unroll
    for (int i = 0; i < 4; ++i) {
      nn[i][0] = fma2(unpk(ug[i][0]), bc2(__uint_as_float(gr[i][0])), bc2(__uint_as_float(gb_[i][0])));
      nn[i][1] = fma2(unpk(ug[i][2]), bc2(__uint_as_float(gr[i][2])), bc2(__uint_as_float(gb_[i][2])));
      PIN(nn[i][0]); PIN(nn[i][1]);
      *(unsigned*)(smem + LDS_U + i * 1024 + tid * 4) = __builtin_amdgcn_perm(ug[i][2], ug[i][0], 0x05040100u);
      *(unsigned*)(smem + LDS_U + 4096 + i * 1024 + tid * 4) = __builtin_amdgcn_perm(ug[i][2], ug[i][0], 0x07060302u);
    }
    SB();
    STAMP(1, 33);
    m = 0;
#pragma unroll
    for (int i = 0; i < 4; ++i) m = tx(tx(m, gm[i][1], tag), gc[i][1], tag);
    if (__builtin_expect(any_nz(m), 0)) {
      unsigned sp = 0;
      do {
        __builtin_amdgcn_s_sleep(3);
        MEMFENCE(); LOAD_GM(); m = 0;
#pragma unroll
        for (int i = 0; i < 4; ++i) m = tx(tx(m, gm[i][1], tag), gc[i][1], tag);
      } while (any_nz(m) & (++sp <= SPIN_CAP));
    }
    const float mv = rbf((__uint_as_float(gm[0][0]) + __uint_as_float(gm[1][0])) + (__uint_as_float(gm[2][0]) + __uint_as_float(gm[3][0])));
    *(u16*)(smem + LDS_GS + tid * 2) = (u16)pk2(mv + (cch ? 0.f : 1.0f), 0.f);
    const float cv = rbf((__uint_as_float(gc[0][0]) + __uint_as_float(gc[1][0])) + (__uint_as_float(gc[2][0]) + __uint_as_float(gc[3][0])));
    const float gav = dpp<0x104>(cv), gav2 = dpp<0x102>(cv);
    const float hbv = cv * gav, hsv = rbf(1.0f + cv) * gav2;
    const f32x2 hb = {__builtin_bit_cast(float, __builtin_amdgcn_readlane(__builtin_bit_cast(int, hbv), 0)), __builtin_bit_cast(float, __builtin_amdgcn_readlane(__builtin_bit_cast(int, hbv), 1))};
    const f32x2 hs = {__builtin_bit_cast(float, __builtin_amdgcn_readlane(__builtin_bit_cast(int, hsv), 2)), __builtin_bit_cast(float, __builtin_amdgcn_readlane(__builtin_bit_cast(int, hsv), 3))};
    SB();
    bspq[0] |= bspq[1] << 16;
    PIN(bspq[0]);
    STAMP(1, 38);
#pragma unroll
    for (int i = 0; i < 4; ++i) {
      const f32x2 h0 = fma2(nn[i][0], hs, hb), h1 = fma2(nn[i][1], hs, hb);
      *(unsigned*)(smem + CV_H + 512 + i * 1024 + tid * 4) = pk2(h0[0], h1[0]);
      *(unsigned*)(smem + CV_CH + CV_H + 512 + i * 1024 + tid * 4) = pk2(h0[1], h1[1]);
    }
  }
  asm volatile("" ::"v"(codepf));
  STAMP(1, 39);
  bar();
  STAMP(1, 1);
#define CV_A(ee, k) ({ const unsigned a_ = ab[(k) >> 1] + 192 - 64 * (ee) + 512 * (1 - ((k) & 1)); v4u{ldsr<unsigned>(a_), ldsr<unsigned>(a_ + 4), ldsr<unsigned>(a_ + 8), ldsr<unsigned>(a_ + 12)}; })
#define CV_B(ee, k) ldsr<v4u>(bb + 192 - 64 * (ee) + 512 * (k))
  const unsigned slotA = LDS_W + w * 16384, slotB = slotA + 8192;
  v4u wq[8][8];
#define READ_W(dl, f) ldsr<v4u>(rdo4[(((f) >> 1) & 1) + ((f) >> 2) * 2] + (dl) + ((f) & 1) * 512)
  unsigned dso;
#define DMA_W(ck, slot, i) do { \
    if (((i) & 3) == 0) asm volatile("s_add_u32 m0, %1, %2\n\ts_add_u32 %0, %3, %4\n\tbuffer_load_dwordx4 %5, %6, %0 offen lds" \
                 : "=&s"(dso) : "s"(lds0 + (slot)), "i"((i) * 1024), "s"(wsp_s + ((CKMAP(ck) + kr) & 7) * 128), "i"((i) * 32768), "v"(dv[0]), "s"(rwsp) : "scc", "memory"); \
    else asm volatile("buffer_load_dwordx4 %0, %1, %2 offen offset:%3 lds" :: "v"(dv[(i) & 3]), "s"(rwsp), "s"(dso), "i"(((i) & 3) * 1024) : "memory"); } while (0)
  auto dma_sel = [&](auto CK, unsigned slot, int i) __attribute__((always_inline)) {
    constexpr int ck = decltype(CK)::value;
    switch (i) {
      case 0: DMA_W(ck, slot, 0); break; case 1: DMA_W(ck, slot, 1); break; case 2: DMA_W(ck, slot, 2); break; case 3: DMA_W(ck, slot, 3); break;
      case 4: DMA_W(ck, slot, 4); break; case 5: DMA_W(ck, slot, 5); break; case 6: DMA_W(ck, slot, 6); break; default: DMA_W(ck, slot, 7); break;
    }
  };
  v4u cA[2][8], cB[2][8];
  float gs[8];
  v4u gbf, sbf;
  v4u ones4 = {ONES, ONES, ONES, ONES};
  PIN(ones4);
#pragma unroll
  for (int k = 0; k < 2; ++k) { cA[0][k] = CV_A(0, k); cB[0][k] = CV_B(0, k); }
  {
    gbf = *(const v4u*)(smem + LDS_GS + w * 64 + q * 16);
    sbf = *(const v4u*)(smem + LDS_GS + 256 + w * 64 + q * 16);
  }
  SB();
  f32x4 cacc[8];
  v2u ue[4];
  float uf[16];
  unsigned flagv;
  unsigned wro = lds0 + slotA + (lane >> 3) * 128 + (((lane & 7) ^ ((lane >> 4) & 1)) * 16);
  PIN(wro);
  {
    f32x4 cb4[4];
    v4u wo_[2];
    float wl, wh;
    f32x2 cp[4];
#pragma unroll
    for (int t = 0; t < 8; ++t) cacc[t] = f32x4{0.f, 0.f, 0.f, 0.f};
    sfor<144>([&](auto G) __attribute__((always_inline)) {
      constexpr int g = decltype(G)::value, ee = g / 36, j = g % 36;
      if constexpr (ee == 0 && j < 6) { cA[0][2 + j] = CV_A(0, 2 + j); cB[0][2 + j] = CV_B(0, 2 + j); }
      cacc[CVO.pp[j] + CVO.qq[j]] = mfma(cA[ee & 1][CVO.qq[j]], cB[ee & 1][CVO.pp[j]], cacc[CVO.pp[j] + CVO.qq[j]]);
      constexpr int jn = ee == 0 ? j - 20 : (j & 1) ? j >> 1 : -1;
      if constexpr (ee < 3 && jn >= 0 && jn < 16) {
        if constexpr (jn & 1) cB[(ee + 1) & 1][jn >> 1] = CV_B(ee + 1, jn >> 1);
        else cA[(ee + 1) & 1][jn >> 1] = CV_A(ee + 1, jn >> 1);
      }
      if constexpr (ee == 3 && j < 4) ue[j] = *(const v2u*)(smem + LDS_U + cch * 4096 + (eh * 4 + j) * 512 + (c * 16 + q * 4) * 2);
      if constexpr (g >= 6 && g < 30) {
        constexpr int i = (g - 6) / 12, jj = ((g - 6) / 3) & 3, ph = (g - 6) % 3;
        if constexpr (ph == 0) { if constexpr (i == 0) gs[2 * jj] = bflo(gbf[jj]); wl = bflo(wt[i][jj]) * gs[2 * jj]; PIN(wl); }
        if constexpr (ph == 1) { if constexpr (i == 0) gs[2 * jj + 1] = bfhi(gbf[jj]); wh = bfhi(wt[i][jj]) * gs[2 * jj + 1]; PIN(wh); }
        if constexpr (ph == 2) {
          wo_[i][jj] = pk2(wl, wh);
          if constexpr (jj == 3) st128<SC1>(wo_[i], rws, l16 + i * 16384, WS_WP + wp_fr);
          else PIN(wo_[i][jj]);
        }
      }
      if constexpr (g == 4 || g == 5) cb4[2 * (g - 4) + 1] = mfma(sbf, wt[g - 4], f32x4{0.f, 0.f, 0.f, 0.f});
      if constexpr (g == 19 || g == 31) cb4[g == 19 ? 0 : 2] = mfma(ones4, wo_[g == 31], f32x4{0.f, 0.f, 0.f, 0.f});
      if constexpr (g == 33) *(f32x4*)(smem + LDS_S + 4096 + (w * 16 + c) * 16) = f32x4{cb4[0][0], cb4[1][0], cb4[2][0], cb4[3][0]};
      if constexpr (g == 38) bar();
      if constexpr (g == 39) {
#pragma unroll
        for (int k = 0; k < 4; ++k) cp[k] = *(const f32x2*)(smem + LDS_S + 4096 + (k * 16 + c) * 16 + (q & 1) * 8);
      }
      if constexpr (g == 45) {
        float s0 = cp[0][0] + cp[1][0], s1 = cp[2][0] + cp[3][0];
        PIN(s0); PIN(s1);
        s0 += s1;
        PIN(s0);
        float s2 = cp[0][1] + cp[1][1], s3 = cp[2][1] + cp[3][1];
        PIN(s2); PIN(s3);
        st64<SC1>(v2u{__float_as_uint(s0), __float_as_uint(s2 + s3)}, rws, tid < 32 ? q * 512 + c * 8 : OOB, WS_CSB + wpr * 1024 + wkg * 128 + wp_oob);
      }
      if constexpr (g == 36) STAMP(1, 40);
      if constexpr (g == 72) STAMP(1, 41);
      if constexpr (g == FLAG_AT) {
        STAMP(1, 42);
        asm volatile("s_waitcnt vmcnt(%0)" ::"i"((FLAG_AT - 32 + 4) / 5 + 1) : "memory");
        __builtin_amdgcn_s_barrier();
        if (tid < 8) hint_add(p.ws, 256 + tid * 16 + (idx & 15));
        STAMP(1, 43);
      }
      if constexpr (g % 5 == 2 && g < 80) DMA_W(1 + g / 40, g < 40 ? slotA : slotB, (g / 5) & 7);
      if constexpr (g == 88) asm volatile("s_waitcnt vmcnt(8)" ::: "memory");
      if constexpr (g == 124) asm volatile("s_waitcnt vmcnt(0)" ::: "memory");
      if constexpr (g == POLL_AT) flagv = __builtin_amdgcn_raw_buffer_load_b32(rws, w == 0 ? c * 128 : OOB, WS_CNT + (256 + xcd * 16) * 128, SC1);
      if constexpr (g >= 88 && g < 104 && (g & 1) == 0) wq[1][(g - 88) >> 1] = READ_W(0, (g - 88) >> 1);
      if constexpr (g >= 108 && g < 140 && (g & 3) == 0) ldsw<v4u>((wro ^ ((((g - 108) >> 2) & 3) * 32)) + ((g - 108) >> 2) * 1024, wr[0][(g - 108) >> 2]);
      if constexpr (g >= 125 && g < 141 && (g & 1) == 1) wq[2][(g - 125) >> 1] = READ_W(8192, (g - 125) >> 1);
      if constexpr (g >= 128 && g < 144) { uf[g - 128] = ((g & 1) ? bfhi : bflo)(ue[(g - 128) >> 2][((g - 128) >> 1) & 1]); PIN(uf[g - 128]); }
      SB();
    });
  }
  STAMP(1, 2);
  unsigned late;
  {
    f32x4* red = (f32x4*)(smem + LDS_X) + w * 256 + lane;
    f32x4 keep[4];
    if (eh) {
#pragma unroll
      for (int t = 0; t < 4; ++t) red[t * 64] = cacc[t];
      SB();
#pragma unroll
      for (int t = 0; t < 4; ++t) { PINA(cacc[4 + t]); keep[t] = cacc[4 + t]; PIN(keep[t]); }
    } else {
#pragma unroll
      for (int t = 0; t < 4; ++t) red[t * 64] = cacc[4 + t];
      SB();
#pragma unroll
      for (int t = 0; t < 4; ++t) { PINA(cacc[t]); keep[t] = cacc[t]; PIN(keep[t]); }
    }
    f32x2 pre[4][2];
#pragma unroll
    for (int t = 0; t < 4; ++t) {
      pre[t][0] = f32x2{uf[4 * t], uf[4 * t + 1]} + lo2(keep[t]);
      pre[t][1] = f32x2{uf[4 * t + 2], uf[4 * t + 3]} + hi2(keep[t]);
      PIN(pre[t][0]); PIN(pre[t][1]);
    }
    if (w == 0) *(unsigned*)(smem + LDS_S) = __builtin_amdgcn_ballot_w64(flagv < 16u) != 0ull;
    bar();
    late = *(const unsigned*)(smem + LDS_S);
    const f32x4* rd = (const f32x4*)(smem + LDS_X) + (w ^ 1) * 256 + lane;
    f32x4 oth[4];
#pragma unroll
    for (int t = 0; t < 4; ++t) oth[t] = rd[t * 64];
#pragma unroll
    for (int t = 0; t < 4; ++t) {
      v2u o;
      o[0] = nomark(pk2(pre[t][0] + lo2(oth[t])));
      o[1] = pk2(pre[t][1] + hi2(oth[t]));
      st64<SC1>(o, rws, xoff + t * 512, 0);
    }
  }
  STAMP(1, 3);
  SB();
  v4u wf[3][8];
  const unsigned rot = uh * 4;
#define LOAD_WP(half, k, i) ld128<SC1>(rws, l16 + ((i) & 3) * 1024, WS_WP + (((half) * 44 + UO(k)) * 16 + kh * 8 + (((i) & 4) ^ rot)) * 1024 + ((k) == 2 ? oob3 : 0u))
#define LOAD_XS(i) ld128<SC1>(rws, wsp_v, WS_X1T + ((i) * 32 + w * 8) * 4096 + mt * 128)
  v4u xs[16];
  unsigned tb[2][4], xdst;
  SB();
  int x1o;
  unsigned z2o[3];
  v2u cz[2][3];
  f32x2 csb[2][3];
  float gbv, c1v[2];
  unsigned gq[4];
#define LOAD_CZ(hf, kc) ld64<SC1>(rws, lane * 8, WS_CSB + ((hf) * 44 + UO(kc)) * 512 + ((kc) == 2 ? oob3 : 0u))
  {
    STAMP(1, 20);
    if (__builtin_amdgcn_readfirstlane(late)) {
      if (w == 0) { hint_wait(rws, c * 128, WS_CNT + (256 + xcd * 16) * 128, 16u); __builtin_amdgcn_s_sleep(LATE_SLEEP); }
      __builtin_amdgcn_s_barrier();
    }
    MEMFENCE();
    STAMP(1, 21);
#pragma unroll
    for (int i = 0; i < 3; ++i)
#pragma unroll
      for (int k = 0; k < 3; ++k) wf[k][i] = LOAD_WP(1, k, i);
    SB();
#pragma unroll
  for (int f = 0; f < 8; ++f) wq[0][f] = READ_W(0, f);
#pragma unroll
  for (int f = 0; f < 24; ++f) PINA(wq[f >> 3][f & 7]);
    int lz = lane;
    PIN(lz);
  {
    const int row = 8 * (lz >> 4) + ((lz >> 2) & 3);
    const int sg = ((row >> 1) & 1) | (((row >> 3) & 1) << 1);
#pragma unroll
    for (int tu = 0; tu < 4; ++tu) {
      const unsigned t0 = lds0 + LDS_A + kh * 32768 + row * 128 + ((tu ^ (2 * kh) ^ sg) * 32) + (lz & 3) * 8;
      tb[0][tu] = t0 + uh * 16384;
      tb[1][tu] = t0 + (uh ^ 1) * 16384;
    }
    const int rl = w * 8 + (lz >> 3), chn = lz & 7;
    const int sg2 = ((rl >> 1) & 1) | (((rl >> 3) & 1) << 1);
    xdst = lds0 + LDS_A + rl * 128 + (((chn >> 1) ^ sg2) * 32) + (chn & 1) * 16;
  }
  {
    const int row = 8 * (lz >> 4) + ((lz >> 2) & 3);
    const int sg = ((row >> 1) & 1) | (((row >> 3) & 1) << 1);
    x1o = LDS_A + nt * 8192 + row * 128 + ((w ^ sg) * 32) + (lz & 3) * 8;
  }
#pragma unroll
  for (int k = 0; k < 3; ++k) { z2o[k] = lz * 16 + WS_Z2 + (UO(k) * 64 + mt * 2 + kh) * 1024 + (k == 2 ? oob3 : 0u); }
#pragma unroll
    for (int tu = 0; tu < 8; ++tu) PIN(tb[tu >> 2][tu & 3]);
    PIN(xdst); PIN(x1o);
#pragma unroll
    for (int k = 0; k < 3; ++k) PIN(z2o[k]);
    SB();
#pragma unroll
    for (int i = 3; i < WF_EARLY; ++i)
#pragma unroll
      for (int k = 0; k < 3; ++k) wf[k][i] = LOAD_WP(1, k, i);
    SB();
#pragma unroll
    for (int i = 0; i < 16; ++i) xs[i] = LOAD_XS(i);
    SB();
    STAMP(1, 44);
    unsigned m = 0;
#pragma unroll
    for (int i = 0; i < 16; ++i) {
      m = macc(m, xs[i]); ldsw<v4u>(xdst + i * 4096, xs[i]);
      if (WF_EARLY * 3 + i < 24) wf[(WF_EARLY * 3 + i) % 3][(WF_EARLY * 3 + i) / 3] = LOAD_WP(1, (WF_EARLY * 3 + i) % 3, (WF_EARLY * 3 + i) / 3);
      SB();
    }
    PIN(m);
    STAMP(1, 45);
    RETRY_BEGIN(m)
#pragma unroll
      for (int i = 0; i < 16; ++i) xs[i] = LOAD_XS(i);
      SB();
#pragma unroll
      for (int i = 0; i < 16; ++i) { m = macc(m, xs[i]); ldsw<v4u>(xdst + i * 4096, xs[i]); }
    RETRY_END(m)
  }
  STAMP(1, 4);
  bar();
  STAMP(1, 5);
#define AHALF(tu, i, h) trread_a(tb[(i) >> 2][tu] + ((i) & 3) * 4096 + (h) * 512)
  f32x4 acc[3][4];
  v4u wg[3][8];
  v2u ah[2][4][2];
  float* st = (float*)(smem + LDS_S);
  float* fin = st + 256 + w * 64;
  f32x4* xch = (f32x4*)(smem + LDS_X);
  f32x4 r4[2], tm[3][2];
  {
    f32x4 s1[4], s2[4], m4[2];
    v2u ar[3][2][2];
    float pa[4], pb[4];
    constexpr int ORD[20] = {0, 1, 2, 3, 4, 5, 6, 7, 16, 17, 18, 19, 8, 9, 10, 11, 12, 13, 14, 15};
    int dgo[2], rdo[2];
    dgo[0] = w * 64 + kh * 32 + c; dgo[1] = w * 64 + (kh ^ 1) * 32 + c;
    rdo[0] = (kh * 32 + (lane & 31)) * 4; rdo[1] = rdo[0] * 4 + (lane & 3) * 4;
    PIN(dgo[0]); PIN(dgo[1]); PIN(rdo[0]); PIN(rdo[1]);
    const bool diag = (c >> 2) == q;
#define GFRAG(gi, t, h) AHALF(((gi) < 8 ? 2 : 0) + (t), (gi) < 16 ? (gi) & 7 : (gi) - 16, h)
#pragma unroll
    for (int n = 0; n < 2; ++n)
#pragma unroll
      for (int e = 0; e < 4; ++e) ar[n][e >> 1][e & 1] = GFRAG(ORD[n], e >> 1, e & 1);
    SB();
    sfor<20 * 6>([&](auto G) __attribute__((always_inline)) {
      constexpr int n = decltype(G)::value / 6, j = decltype(G)::value % 6, gi = ORD[n], t = j / 3, k = j % 3;
      constexpr int tu = (gi < 8 ? 2 : 0) + t, i = gi < 16 ? gi & 7 : gi - 16;
      const v4u a = {ar[n % 3][t][0][0], ar[n % 3][t][0][1], ar[n % 3][t][1][0], ar[n % 3][t][1][1]};
      const f32x4 z4 = {0.f, 0.f, 0.f, 0.f};
      if constexpr (gi < 16) acc[k][tu] = mfma(a, wf[k][i], gi == 0 ? z4 : acc[k][tu]);
      if constexpr ((gi >= 4 && gi < 8) || gi >= 16) {
        if constexpr (k == 0) s1[tu] = mfma(ones4, a, (gi == 4 || gi == 16) ? z4 : s1[tu]);
        if constexpr (k == 1) s2[tu] = mfma(a, a, (gi == 4 || gi == 16) ? z4 : s2[tu]);
      }
      if constexpr (j < 4 && n + 2 < 20) ar[(n + 2) % 3][j >> 1][j & 1] = GFRAG(ORD[n + 2], j >> 1, j & 1);
      if constexpr (n >= 18 && j < 4) ah[0][(n - 18) * 2 + (j >> 1)][j & 1] = AHALF((n - 18) * 2 + (j >> 1), 0, j & 1);
      if constexpr (n < 4 && (j == 1 || j == 4)) DMA_W(3, slotB, n * 2 + (j == 4));
      if constexpr (n == SMALL_N) cz[j / 3][j % 3] = LOAD_CZ(j / 3, j % 3);
      if constexpr (n == 4 && j < 4) gq[j] = __builtin_amdgcn_raw_buffer_load_b32(rws, lane * 8, WS_MODG + (j * 3072 + 5 * 512 + nt * 64) * 8, SC1);
      if constexpr (n == 8 && j == 1) USE(gq[3]);
      if constexpr (n >= 8 && n < 12 && (j == 2 || j == 5)) DMA_W(4, slotA, (n - 8) * 2 + (j == 5));
      if constexpr (n >= 12 && j == 4) DMA_W(5, slotB, n - 12);
      if constexpr ((n == 8 || n == 9) && (j & 1) == 0) {
        constexpr int x = (n - 8) * 3 + (j >> 1);
        xch[(w * 6 + (x % 3) * 2 + x / 3) * 64 + lane] = acc[x % 3][2 + x / 3];
      }
      if constexpr (n == 10 && j == 0) {
#pragma unroll
    for (int k = 0; k < 3; ++k) {
      const float y = xrow_sum4(__uint_as_float(cz[1][k][0]), __uint_as_float(cz[1][k][1]), __uint_as_float(cz[1][k][0]), __uint_as_float(cz[1][k][1]));
      auto sw = __builtin_amdgcn_permlane16_swap(__float_as_uint(y), __float_as_uint(y), false, false);
      csb[1][k] = f32x2{__uint_as_float(sw[0]), __uint_as_float(sw[1])};
      PIN(csb[1][k]);
    }
    c1v[0] = xrow_sum4(__uint_as_float(cz[0][0][0]), __uint_as_float(cz[0][0][1]), __uint_as_float(cz[0][1][0]), __uint_as_float(cz[0][1][1]));
    c1v[1] = xrow_sum4(__uint_as_float(cz[0][2][0]), __uint_as_float(cz[0][2][1]), __uint_as_float(cz[0][2][0]), __uint_as_float(cz[0][2][1]));
    PIN(c1v[0]); PIN(c1v[1]);
    gbv = rbf((__uint_as_float(gq[0]) + __uint_as_float(gq[1])) + (__uint_as_float(gq[2]) + __uint_as_float(gq[3])));
    PIN(gbv);
      }
      if constexpr ((n == 8 || n == 9) && j >= 2) wq[3][(n - 8) * 4 + j - 2] = READ_W(8192, (n - 8) * 4 + j - 2);
      if constexpr (n == 10 && j == 5) {
#pragma unroll
        for (int f = 0; f < 8; ++f) PINA(wq[3][f]);
      }
      if constexpr (n == 10 && j == 4) bar();
      if constexpr (n == 11) acc[j % 3][j / 3] = xch[((w ^ 1) * 6 + (j % 3) * 2 + j / 3) * 64 + lane];
      if constexpr (n == 12 && j == 3) {
        float s1v[4];
#pragma unroll
        for (int x = 0; x < 4; ++x) { s1v[x] = s1[x][0]; PIN(s1v[x]); }
        if (diag) {
#pragma unroll
          for (int x = 0; x < 4; ++x) {
            *(float*)(smem + LDS_S + dgo[x >> 1] * 4 + (x & 1) * 64) = s1v[x];
            *(f32x4*)(smem + LDS_S + 2048 + dgo[x >> 1] * 16 + (x & 1) * 256) = s2[x];
          }
        }
      }
      if constexpr (n == 13 && j == 5) bar();
      if constexpr (n == 14 && j < 4) {
        pa[j] = *(const float*)(smem + LDS_S + rdo[0] + j * 256); pb[j] = *(const float*)(smem + LDS_S + 2048 + rdo[1] + j * 1024);
      }
      if constexpr (n == 15 && j == 1) { pa[0] += pa[1]; pa[2] += pa[3]; PIN(pa[0]); PIN(pa[2]); }
      if constexpr (n == 15 && j == 2) { pb[0] += pb[1]; pb[2] += pb[3]; PIN(pb[0]); PIN(pb[2]); }
      if constexpr (n == 15 && j == 3) { pa[0] = (pa[0] + pa[2]) * (1.0f / D); PIN(pa[0]); }
      if constexpr (n == 15 && j == 4) { pb[0] = (pb[0] + pb[2]) * (1.0f / D); PIN(pb[0]); }
      if constexpr (n == 15 && j == 5) { pb[0] = fmaxf(__builtin_fmaf(-pa[0], pa[0], pb[0]), 0.f); PIN(pb[0]); }
      if constexpr (n == 16 && j == 0) { pb[0] = __builtin_amdgcn_rsqf(pb[0] + EPS); PIN(pb[0]); }
      if constexpr (n == 16 && j == 1) fin[lane] = lane < 32 ? pb[0] : -pb[0] * pa[0];
      if constexpr (n == 16 && (j == 2 || j == 3)) { m4[j - 2] = *(const f32x4*)(fin + 32 + (j - 2) * 16 + q * 4); r4[j - 2] = *(const f32x4*)(fin + (j - 2) * 16 + q * 4); }
      if constexpr (n >= 18) {
        constexpr int x = (n - 18) * 6 + j, kk = x >> 2, tt = (x >> 1) & 1, h = (x & 1) * 2;
        float t0 = __builtin_fmaf(m4[tt][h], csb[1][kk][0], csb[1][kk][1]);
        PIN(t0);
        float t1 = __builtin_fmaf(m4[tt][h + 1], csb[1][kk][0], csb[1][kk][1]);
        PIN(t1);
        tm[kk][tt][h] = t0; tm[kk][tt][h + 1] = t1;
      }
      if constexpr (n >= 12 && n < 15 && j == 5) st128<SC1>(mk, rws, z2o[n - 12], 0);
      if constexpr (n == 7 && j == 5) STAMP(1, 46);
      if constexpr (n == 11 && j == 5) STAMP(1, 47);
      SB();
    });
  }
#pragma unroll
  for (int k = 0; k < 6; ++k) PINA(acc[k >> 1][k & 1]);
  STAMP(1, 6);
  f32x4 z2v[3][2];
  {
    f32x4 rs[2], rq[2];
#pragma unroll
    for (int t = 0; t < 2; ++t) {
#pragma unroll
      for (int k = 0; k < 3; ++k) {
        const f32x4 a = acc[k][t];
        const f32x2 zl = gelu2(fma2(lo2(a), lo2(r4[t]), lo2(tm[k][t])));
        const f32x2 zh = gelu2(fma2(hi2(a), hi2(r4[t]), hi2(tm[k][t])));
        z2v[k][t] = f32x4{zl[0], zl[1], zh[0], zh[1]};
        if (k == 0) { rs[t] = z2v[k][t]; rq[t] = z2v[k][t] * z2v[k][t]; }
        else { rs[t] += z2v[k][t]; rq[t] += z2v[k][t] * z2v[k][t]; }
        if (((t * 3 + k) & 1) == 0) {
#pragma unroll
          for (int f = (t * 3 + k) * 4; f < (t * 3 + k) * 4 + 8; ++f) wg[f % 3][f / 3] = LOAD_WP(0, f % 3, f / 3);
        } else {
          PIN(z2v[k][t]);
          SB();
        }
      }
    }
    STAMP(1, 50);
    float v8[8], v4[4], v2[2];
    const bool b3 = c & 8, b2 = c & 4, b1 = c & 2, b0 = c & 1;
#pragma unroll
    for (int r_ = 0; r_ < 4; ++r_) {
      const float a0 = rs[0][r_] + dpp<0x128>(rs[0][r_]), a1 = rs[1][r_] + dpp<0x128>(rs[1][r_]);
      const float q0 = rq[0][r_] + dpp<0x128>(rq[0][r_]), q1 = rq[1][r_] + dpp<0x128>(rq[1][r_]);
      v8[2 * r_] = b3 ? a1 : a0; v8[2 * r_ + 1] = b3 ? q1 : q0;
    }
#pragma unroll
    for (int j = 0; j < 4; ++j) {
      const float x0 = v8[j] + dpp<0x141>(v8[j]), x1 = v8[4 + j] + dpp<0x141>(v8[4 + j]);
      v4[j] = b2 ? x1 : x0;
    }
#pragma unroll
    for (int j = 0; j < 2; ++j) {
      const float x0 = v4[j] + dpp<0x4E>(v4[j]), x1 = v4[2 + j] + dpp<0x4E>(v4[2 + j]);
      v2[j] = b1 ? x1 : x0;
    }
    const float x0 = v2[0] + dpp<0xB1>(v2[0]), x1 = v2[1] + dpp<0xB1>(v2[1]);
    const float outv = b0 ? x1 : x0;
    __builtin_amdgcn_raw_buffer_store_b32(nomark(__float_as_uint(outv)), rws, lane * 4, WS_ST2 + ((mt * 2 + kh) * 16 + nt * 2 + uh) * 256, SC1);
  }
  STAMP(1, 7);
  SB();
#pragma unroll
  for (int tu = 0; tu < 4; ++tu)
#pragma unroll
    for (int k = 0; k < 3; ++k) acc[k][tu] = f32x4{0.f, 0.f, 0.f, 0.f};
  unsigned k3v[6];
  {
    const unsigned k0 = l16 + w * 6144, w3 = (w + 1) >> 2;
    k3v[0] = k0; k3v[1] = k0 + 2048 - w3 * 1024; k3v[2] = k3v[1] + 1024; k3v[3] = k0 + 5120 - w3 * 2048;
    k3v[4] = k0 + 1024 + (w3 << 30); k3v[5] = k3v[4] + 3072;
  }
  v2u x1h[2][2];
  v4u wo[6][4];
#define LOAD_WO(f) wo[(f) >> 2][(f) & 3] = ld128<SC1>(rws, k3v[(f) >> 2], WS_WOT + (nt * 4 + ((f) & 3)) * 22 * 1024)
  f32x4 pt[4];
  float* c1s = st + 768 + w * 96;
#define LOAD_PT(i) asf(ld128<SC1>(rws, l16 + (i) * 1024, WS_ST2 + (mt * 2 + kh) * 4096))
  SB();
#pragma unroll
  for (int i = 0; i < 8; ++i) {
#pragma unroll
    for (int j = 0; j < 12; ++j) {
      const int tu = j / 3, k = j % 3;
      const v4u a = {ah[i & 1][tu][0][0], ah[i & 1][tu][0][1], ah[i & 1][tu][1][0], ah[i & 1][tu][1][1]};
      acc[k][tu] = mfma(wg[k][i], a, acc[k][tu]);
      if (j < 8 && i < 7) ah[(i + 1) & 1][j >> 1][j & 1] = AHALF(j >> 1, i + 1, j & 1);
      if (j < 4 && i == 7) x1h[j >> 1][j & 1] = trread(smem + x1o + (j >> 1) * 4096 + (j & 1) * 512);
      if (i == 0 && j == 0) asm volatile("s_waitcnt vmcnt(24)" ::: "memory");
      if (i < 2 && j >= 4) wq[4 + i][j - 4] = READ_W(i * 8192, j - 4);
      if (i == 3 && j == 0) {
#pragma unroll
        for (int f = 0; f < 16; ++f) PINA(wq[4 + (f >> 3)][f & 7]);
      }
      if (i == 4 && j == 0) { USE(wg[0][7]); USE(wg[1][7]); USE(wg[2][7]); }
      if ((i == 2 || i == 3) && j % 3 == 2) dma_sel(IC<6>{}, slotA, (i - 2) * 4 + j / 3);
      if ((i == 4 || i == 5) && j % 3 == 2) dma_sel(IC<7>{}, slotB, (i - 4) * 4 + j / 3);
      if (j >= 8 && i == 6) pt[j - 8] = LOAD_PT(j - 8);
      if (i == 7 && j >= 8) LOAD_WO(j - 8);
      if (i == 5 && j == 0) {
        asm volatile("s_waitcnt vmcnt(30)" ::: "memory");
        __builtin_amdgcn_s_barrier();
      }
      if (i == 5 && j == 1) st[1408 + lane] = gbv;
      if (i == 5 && j == 9) c1s[lane] = c1v[0];
      if (i == 5 && j == 10) c1s[64 + (lane & 31)] = c1v[1];
      SB();
    }
  }
  STAMP(1, 8);
#pragma unroll
  for (int k = 0; k < 3; ++k)
#pragma unroll
    for (int t = 0; t < 2; ++t) xch[(w * 6 + k * 2 + t) * 64 + lane] = acc[k][2 + t];
  SB();
  {
    unsigned m = 0;
#pragma unroll
    for (int i = 0; i < 4; ++i) m = macc4(m, __builtin_bit_cast(v4u, pt[i]));
    RETRY_BEGIN(m)
#pragma unroll
      for (int i = 0; i < 4; ++i) { pt[i] = LOAD_PT(i); m = macc4(m, __builtin_bit_cast(v4u, pt[i])); }
    RETRY_END(m)
    if (tid < 4) hint_add(p.ws, 64 + (nt * 4 + (mt >> 3)) * 4 + tid);
    const f32x4 sx = (pt[0] + pt[1]) + (pt[2] + pt[3]);
    const float tv = xrow_sum4(sx[0], sx[2], sx[1], sx[3]);
    auto sw = __builtin_amdgcn_permlane32_swap(__float_as_uint(tv), __float_as_uint(tv), false, false);
    const float mm = __uint_as_float(sw[0]) * (1.0f / FH);
    const float r = __builtin_amdgcn_rsqf(fmaxf(__uint_as_float(sw[1]) * (1.0f / FH) - mm * mm, 0.f) + EPS);
    float* f2 = st + 512 + w * 64;
    f2[(q >> 1) * 32 + 2 * c + (q & 1)] = (q >> 1) ? -r * mm : r;
    f32x4 r4[2], m4[2];
#pragma unroll
    for (int t = 0; t < 2; ++t) { r4[t] = *(const f32x4*)(f2 + q * 8 + t * 4); m4[t] = *(const f32x4*)(f2 + 32 + q * 8 + t * 4); }
#pragma unroll
    for (int k = 0; k < 3; ++k) {
      v4u o;
#pragma unroll
      for (int t = 0; t < 2; ++t) {
        o[2 * t] = nomark(pk2(fma2(lo2(z2v[k][t]), lo2(r4[t]), lo2(m4[t]))));
        o[2 * t + 1] = pk2(fma2(hi2(z2v[k][t]), hi2(r4[t]), hi2(m4[t])));
      }
      st128<SC1>(o, rws, z2o[k], 0);
    }
  }
  STAMP(1, 9);
  SB();
  f32x4 z1f[3][2];
  constexpr int RD = 12;
  v4u zf[RD];
#define LOAD_Z(n) ld128<SC1>(rws, zv[((n) >> 1) & 7] + ((n) & 1) * 1024, WS_Z2 + (((n) < 48 ? UT(((n) >> 4) % 3) : UO(((n) >> 4) % 3)) * 64 + w * 16) * 1024 + (((n) >> 4) % 3 == 2 ? oob3 : 0u))
  unsigned zv[8];
#pragma unroll
  for (int i = 0; i < 8; ++i) { zv[i] = l16 + ((CKMAP(i) + kr) & 7) * 2048; PIN(zv[i]); }
  {
    bar();
    STAMP(1, 34);
#pragma unroll
    for (int k = 0; k < 6; ++k) PINA(acc[k >> 1][k & 1]);
    f32x4 oth[3][2], cs1[3], bs1[3];
    float r1[2], m1[2];
#pragma unroll
    for (int t = 0; t < 2; ++t) { r1[t] = fin[t * 16 + c]; m1[t] = fin[32 + t * 16 + c]; }
#pragma unroll
    for (int k = 0; k < 3; ++k) {
      cs1[k] = *(const f32x4*)(c1s + k * 32 + q * 4); bs1[k] = *(const f32x4*)(c1s + k * 32 + 16 + q * 4);
#pragma unroll
      for (int t = 0; t < 2; ++t) oth[k][t] = xch[((w ^ 1) * 6 + k * 2 + t) * 64 + lane];
    }
#pragma unroll
    for (int i = 4; i < WO0; ++i) LOAD_WO(i);
    SB();
    f32x2 z1a[6][2];
#pragma unroll
    for (int i = 0; i < 6; ++i) {
      const int k = i >> 1, t = i & 1;
      const f32x4 a = acc[k][t] + oth[k][t];
      z1a[i][0] = fma2(lo2(a), bc2(r1[t]), fma2(lo2(cs1[k]), bc2(m1[t]), lo2(bs1[k])));
      z1a[i][1] = fma2(hi2(a), bc2(r1[t]), fma2(hi2(cs1[k]), bc2(m1[t]), hi2(bs1[k])));
    }
#pragma unroll
    for (int i = 0; i < GELU_PRE; ++i) {
      const f32x2 lo = gelu2(z1a[i][0]), hi = gelu2(z1a[i][1]);
      z1f[i >> 1][i & 1] = f32x4{lo[0], lo[1], hi[0], hi[1]};
      PIN(z1f[i >> 1][i & 1]);
    }
#pragma unroll
    for (int i = GELU_PRE; i < 6; ++i) { PIN(z1a[i][0]); PIN(z1a[i][1]); }
    SB();
#pragma unroll
    for (int f = 0; f < 16; ++f) wq[6 + (f >> 3)][f & 7] = READ_W((f >> 3) * 8192, f & 7);
    SB();
    STAMP(1, 10);
    hint_wait(rws, 0u, WS_CNT + (64 + (nt * 4 + w) * 4 + (xcd >> 1)) * 128, 8u);
    STAMP(1, 22);
#pragma unroll
    for (int n = 0; n < RD; ++n) zf[n] = LOAD_Z(n);
    SB();
#pragma unroll
    for (int i = GELU_PRE; i < 6; ++i) {
      const f32x2 lo = gelu2(z1a[i][0]), hi = gelu2(z1a[i][1]);
      z1f[i >> 1][i & 1] = f32x4{lo[0], lo[1], hi[0], hi[1]};
      PIN(z1f[i >> 1][i & 1]);
    }
  }
  f32x4* xb = (f32x4*)(smem + LDS_A) + lane;
  f32x4* yb = (f32x4*)(smem + LDS_A + 49152) + lane;
  f32x4 ac2[2][4], ini[4], yo[2];
  const float bs0 = bflo(bspq[0]), bs1_ = bfhi(bspq[0]);
  const f32x4 bq0 = {bs0, bs0, bs0, bs0}, bq1 = {bs1_, bs1_, bs1_, bs1_}, z4 = {0.f, 0.f, 0.f, 0.f};
  unsigned voff[2];
  voff[0] = l16 + WS_V + ((mt * 4 + 2 * kh) * 22 + (uo0 >> 1)) * 1024;
  voff[1] = l16 + WS_V + ((mt * 4 + 2 * kh) * 22 + (uo2 >> 1)) * 1024 + (uo2 & 1) * 8 + oob3;
  PIN(voff[0]); PIN(voff[1]);
  float ev[4];
  unsigned eo0;
  SB();
  STAMP(1, 11);
  auto v_store = [&](int k, int t, unsigned o0, unsigned o1) __attribute__((always_inline)) {
    st64<SC1>(v2u{o0, o1}, rws, voff[k >> 1] + (k & 1) * 8, t * 22 * 1024);
  };
  sfor<6>([&](auto P) __attribute__((always_inline)) {
    constexpr int ps = decltype(P)::value, cur = ps & 1, prv = cur ^ 1;
    unsigned zm = 0;
    sfor<64>([&](auto G) __attribute__((always_inline)) {
      constexpr int g = decltype(G)::value, ks = g >> 2, tu = g & 3, ck = ks >> 1, k2 = ks & 1, n = ps * 16 + ks;
      const f32x4 cin = ks > 0 ? ac2[cur][tu] : ps >= 3 ? ini[tu] : tu == 0 ? bq0 : tu == 1 ? bq1 : z4;
      ac2[cur][tu] = mfma(zf[n % RD], wq[ck][k2 * 4 + tu], cin);
      if constexpr (tu == 1) { zm = macc(zm, zf[n % RD]); PIN(zm); }
      if constexpr (tu == 3) {
        if constexpr (n + RD < 96) zf[n % RD] = LOAD_Z(n + RD);
        else if constexpr (WO0 + n + RD - 96 < 24) LOAD_WO(WO0 + n + RD - 96);
      }
      if constexpr (ps >= 1 && ps <= 3 && g >= 2 && g < 18 && (g & 3) == 2) xb[((w * 3 + ps - 1) * 4 + (g >> 2)) * 64] = ac2[prv][g >> 2];
      if constexpr (ps >= 4 && (g == 2 || g == 6)) yb[((w * 3 + ps - 4) * 2 + (g >> 2)) * 64] = ac2[prv][2 + (g >> 2)];
      if constexpr (ps >= 1 && g == (ps >= 4 ? 12 : 24)) {
        if constexpr (ps == 4) asm volatile("s_waitcnt vmcnt(20)" ::: "memory");
        bar();
      }
      if constexpr (ps >= 4 && (g == 13 || g == 14)) yo[g - 13] = yb[(((w ^ 1) * 3 + ps - 4) * 2 + g - 13) * 64];
      if constexpr (ps >= 4 && g >= 20 && g < 40) {
        constexpr int t = (g - 20) / 10, ph = (g - 20) % 10;
        if constexpr (ph < 4) { ev[ph] = ac2[prv][t][ph] + yo[t][ph]; PIN(ev[ph]); }
        else if constexpr (ph < 8) { ev[ph - 4] *= z1f[ps - 4][t][ph - 4]; PIN(ev[ph - 4]); }
        else if constexpr (ph == 8) { eo0 = nomark(pk2(ev[0], ev[1])); PIN(eo0); }
        else v_store(ps - 4, t, eo0, pk2(ev[2], ev[3]));
      }
      if constexpr (ps >= 2 && ps <= 4 && g >= 28 && g < 44 && (g & 3) == 0) ini[(g - 28) >> 2] = xb[(((w ^ 2) * 3 + ps - 2) * 4 + ((g - 28) >> 2)) * 64];
      if constexpr (ps == 0 && (g == 21 || g == 37 || g == 53)) st128<SC1>(mk, rws, l16, WS_V + ((mt * 4 + w) * 22 + pr0 + (g - 21) / 16) * 1024 + (g == 53 ? oob3 : 0u));
      if constexpr (ps * 64 + g == HINT4_G) { if (tid < 2) hint_add(p.ws, 192 + mt * 2 + tid); }
      if constexpr (ps == 5 && g == POLL4_G) flagv = __builtin_amdgcn_raw_buffer_load_b32(rws, w == 0 ? 0u : OOB, WS_CNT + (192 + mt * 2 + (xcd & 1)) * 128, SC1);
      if constexpr (g == 0 && ps >= 1 && ps < 5) STAMP(1, 55 + ps);
      SB();
    });
    if (__builtin_expect(any_mark(zm), 0)) {
#pragma unroll
      for (int tu = 0; tu < 4; ++tu) ac2[cur][tu] = ps >= 3 ? xb[(((w ^ 2) * 3 + ps - 3) * 4 + tu) * 64] : tu == 0 ? bq0 : tu == 1 ? bq1 : z4;
      sfor<16>([&](auto K) __attribute__((always_inline)) {
        constexpr int ks = decltype(K)::value, ck = ks >> 1, k2 = ks & 1;
        v4u zz;
        unsigned sp = 0;
        do { MEMFENCE(); zz = LOAD_Z(ps * 16 + ks); } while (any_mark(macc(0u, zz)) & (++sp <= SPIN_CAP));
#pragma unroll
        for (int tu = 0; tu < 4; ++tu) ac2[cur][tu] = mfma(zz, wq[ck][k2 * 4 + tu], ac2[cur][tu]);
      });
    }
  });
  STAMP(1, 12);
  {
#pragma unroll
    for (int t = 0; t < 2; ++t) yb[((w * 3 + 2) * 2 + t) * 64] = ac2[1][2 + t];
    PIN(ac2[1][0]); PIN(ac2[1][1]);
    if (w == 0) st[0] = __uint_as_float(flagv < 8u);
    bar();
    late = __float_as_uint(st[0]);
    STAMP(1, 52);
#pragma unroll
    for (int t = 0; t < 2; ++t) yo[t] = yb[(((w ^ 1) * 3 + 2) * 2 + t) * 64];
#pragma unroll
    for (int t = 0; t < 2; ++t) {
      const f32x4 g = (ac2[1][t] + yo[t]) * z1f[2][t];
      v_store(2, t, nomark(pk2(lo2(g))), pk2(hi2(g)));
    }
  }
  STAMP(1, 13);
  SB();
  f32x4 gb4[4];
  f32x2 x1f[2][4];
  STAMP(1, 53);
  if (__builtin_amdgcn_readfirstlane(late)) {
    if (w == 0) hint_wait(rws, 0u, WS_CNT + (192 + mt * 2 + (xcd & 1)) * 128, 8u);
    __builtin_amdgcn_s_barrier();
  }
  MEMFENCE();
  STAMP(1, 23);
  f32x4 a3[4][4];
  {
    unsigned spins = 0;
    for (;;) {
      MEMFENCE();
      v4u vs[6][4];
#define LOAD_V(f) vs[(f) >> 2][(f) & 3] = ld128<SC1>(rws, k3v[(f) >> 2], WS_V + (mt * 4 + (((f) & 3) ^ w)) * 22 * 1024)
#pragma unroll
      for (int f = 0; f < 12; ++f) LOAD_V(f);
      SB();
      unsigned m = 0;
      sfor<96>([&](auto G) __attribute__((always_inline)) {
        constexpr int g = decltype(G)::value, ks = g >> 4, tu = (g >> 2) & 3, e = g & 3;
        a3[e][tu] = mfma(wo[ks][e], vs[ks][tu], ks == 0 ? f32x4{0.f, 0.f, 0.f, 0.f} : a3[e][tu]);
        if constexpr (e == 0 && 12 + (g >> 2) < 24) LOAD_V(12 + (g >> 2));
        if constexpr (e == 1) { m = macc(m, vs[ks][tu]); PIN(m); }
        SB();
      });
      if (!(any_mark(m) & (++spins <= SPIN_CAP))) break;
    }
  }
#pragma unroll
  for (int i = 0; i < 4; ++i) gb4[i] = *(const f32x4*)(st + 1408 + (i >> 1) * 32 + q * 8 + (i & 1) * 4);
#pragma unroll
  for (int i = 0; i < 8; ++i) { x1f[i >> 2][i & 3] = unpk(x1h[i >> 2][(i >> 1) & 1][i & 1]); PIN(x1f[i >> 2][i & 3]); }
  STAMP(1, 14);
  {
    f32x4* red = (f32x4*)(smem + LDS_W);
#pragma unroll
    for (int tu = 1; tu < 4; ++tu)
#pragma unroll
      for (int e = 0; e < 4; ++e) red[((w * 3 + tu - 1) * 4 + e) * 64 + lane] = a3[e][tu];
#pragma unroll
    for (int e = 0; e < 4; ++e) PIN(a3[e][0]);
    bar();
    STAMP(1, 54);
    f32x4 o3[4][3];
#pragma unroll
    for (int e = 0; e < 4; ++e)
#pragma unroll
      for (int rel = 1; rel < 4; ++rel) o3[e][rel - 1] = red[(((w ^ rel) * 3 + rel - 1) * 4 + e) * 64 + lane];
    const rsrc_t rout = mkrs(p.out, L * D * 2);
    SB();
#pragma unroll
    for (int pp = 0; pp < 2; ++pp) {
      v4u o;
#pragma unroll
      for (int h = 0; h < 2; ++h) {
        const int e = pp * 2 + h;
        const f32x4 y = (a3[e][0] + o3[e][0]) + (o3[e][1] + o3[e][2]);
        o[2 * h] = pk2(fma2(lo2(y), lo2(gb4[e]), x1f[pp][2 * h]));
        o[2 * h + 1] = pk2(fma2(hi2(y), hi2(gb4[e]), x1f[pp][2 * h + 1]));
      }
      st128<0>(o, rout, perm(w, c) * 1024 + q * 16 + pp * 64, mt * 64 * 1024 + nt * 128);
    }
  }
  if (b == 0 && tid == 0) *(unsigned*)(p.ws + WS_EPOCH) = tag_s;
  STAMP(1, 15);
  STAMP_FLUSH(1);
  asm volatile("adaln_code_end:");
}

extern "C" void kernel_launch(void* const* d_in, const int* in_sizes, int n_in, void* d_out, int out_size, void* d_ws,
                              size_t ws_size, hipStream_t stream) {
  Params p;
  p.u = (const u16*)d_in[0];
  p.lar = (const u16*)d_in[1];
  p.aim = (const u16*)d_in[2];
  p.cre = (const u16*)d_in[3];
  p.cim = (const u16*)d_in[4];
  p.ldt = (const u16*)d_in[5];
  p.dsk = (const u16*)d_in[6];
  p.win = (const u16*)d_in[8];
  p.wsp = (const u16*)d_in[9];
  p.bsp = (const u16*)d_in[10];
  p.wout = (const u16*)d_in[11];
  p.c = (const u16*)d_in[12];
  p.wmod = (const u16*)d_in[13];
  p.out = (u16*)d_out;
  p.ws = (unsigned char*)d_ws;
  (void)hipFuncSetAttribute((const void*)adaln_mega, hipFuncAttributeMaxDynamicSharedMemorySize, MEGA_LDS);
  adaln_mega<<<256, 256, MEGA_LDS, stream>>>(p);
}
```

```cpp
#define FLAG_AT 59
#define POLL_AT 140
#include <hip/hip_runtime.h>
#include <hip/hip_bf16.h>
#include <stdint.h>
#include <utility>

#define LAS __attribute__((address_space(3)))
typedef unsigned v4u __attribute__((ext_vector_type(4)));
typedef unsigned v2u __attribute__((ext_vector_type(2)));
typedef float f32x4 __attribute__((ext_vector_type(4)));
typedef float f32x2 __attribute__((ext_vector_type(2)));
typedef short bf16x8 __attribute__((ext_vector_type(8)));
typedef _Float16 f16x8 __attribute__((ext_vector_type(8)));
typedef _Float16 f16x2 __attribute__((ext_vector_type(2)));
typedef __bf16 bf16x2_t __attribute__((ext_vector_type(2)));
typedef unsigned short u16;
typedef __amdgpu_buffer_rsrc_t rsrc_t;

#define STAMP_DECL do { } while (0)
#define STAMP(k, i) do { } while (0)
#define STAMP_FLUSH(k) do { } while (0)

#define OPT_A 1
#define OPT_B 1
#define OPT_C 2
#define OPT_D 1
#define OPT_E 2
#define OPT_F 1
constexpr int L = 2048, D = 512, NM = 128, FH = 688, FF = 1376;
constexpr float EPS = 1e-5f;
constexpr int SC1 = 16;
constexpr unsigned OOB = 0x40000000u;
constexpr unsigned SPIN_CAP = 1u << 14;

constexpr unsigned WS_EPOCH = 0;
constexpr unsigned WS_CNT = 4096;
constexpr unsigned WS_SR = WS_CNT + 512 * 128;
constexpr unsigned WS_SB = WS_SR + 2048 * 8;
constexpr unsigned WS_MODG = WS_SB + 2048 * 8;
constexpr unsigned WS_CSB = WS_MODG + 3072 * 32;
constexpr unsigned WS_ST2 = WS_CSB + 88 * 16 * 32;
constexpr unsigned WS_UG = 1u << 20;
static_assert(WS_ST2 + 32 * 2 * 16 * 32 * 8 <= WS_UG, "small buffers");
constexpr unsigned WS_X1T = WS_UG + 256 * 2048 * 8;
constexpr unsigned WS_WP = WS_X1T + 512 * 2048 * 2;
constexpr unsigned WS_WOT = WS_WP + 88 * 16 * 1024;
constexpr unsigned WS_Z2 = WS_WOT + 32 * 22 * 1024;
constexpr unsigned WS_V = WS_Z2 + 44 * 64 * 1024;
constexpr unsigned WS_END = WS_V + 128 * 22 * 1024;

struct Params {
  const u16 *u, *lar, *aim, *cre, *cim, *ldt, *dsk, *win, *wsp, *bsp, *wout, *c, *wmod;
  u16* out;
  unsigned char* ws;
};

#define DI __device__ __forceinline__
template <int V> struct IC { static constexpr int value = V; };
template <class F, int... I> DI void sfor_(F&& f, std::integer_sequence<int, I...>) { int d[] = {(f(IC<I>{}), 0)...}; (void)d; }
template <int N, class F> DI void sfor(F&& f) { sfor_(f, std::make_integer_sequence<int, N>{}); }
DI float bf2f(unsigned b) { return __builtin_bit_cast(float, b << 16); }
DI float bflo(unsigned w) { return __builtin_bit_cast(float, w << 16); }
DI float bfhi(unsigned w) { return __builtin_bit_cast(float, w & 0xFFFF0000u); }
DI f32x2 unpk(unsigned w) { return f32x2{bflo(w), bfhi(w)}; }
DI unsigned pk2(f32x2 v) { return __builtin_bit_cast(unsigned, __builtin_convertvector(v, bf16x2_t)); }
DI unsigned pk2(float lo, float hi) { return pk2(f32x2{lo, hi}); }
DI unsigned nomark(unsigned x) { return x < 0xFFFFFFFEu ? x : 0xFFFFFFFEu; }
DI float rbf(float x) { return bflo(pk2(x, 0.f)); }
DI f32x2 fma2(f32x2 a, f32x2 b, f32x2 c) { return __builtin_elementwise_fma(a, b, c); }
DI f32x2 lo2(f32x4 v) { return f32x2{v[0], v[1]}; }
DI f32x2 hi2(f32x4 v) { return f32x2{v[2], v[3]}; }
DI f32x2 bc2(float x) { return f32x2{x, x}; }
DI f32x4 mfma(v4u a, v4u b, f32x4 c) {
  return __builtin_amdgcn_mfma_f32_16x16x32_bf16(__builtin_bit_cast(bf16x8, a), __builtin_bit_cast(bf16x8, b), c, 0, 0, 0);
}
DI f32x4 mfma_h(v4u a, v4u b, f32x4 c) {
  return __builtin_amdgcn_mfma_f32_16x16x32_f16(__builtin_bit_cast(f16x8, a), __builtin_bit_cast(f16x8, b), c, 0, 0, 0);
}
DI float dot2(unsigned a, unsigned b, float acc) {
  return __builtin_amdgcn_fdot2_f32_bf16(__builtin_bit_cast(bf16x2_t, a), __builtin_bit_cast(bf16x2_t, b), acc, false);
}
constexpr unsigned ONES = 0x3F803F80u;
DI int perm(int u, int c) { return 32 * (u >> 1) + 8 * (c >> 2) + 4 * (u & 1) + (c & 3); }
template <int CTRL> DI float dpp(float x) {
  return __builtin_bit_cast(float, __builtin_amdgcn_mov_dpp(__builtin_bit_cast(int, x), CTRL, 0xf, 0xf, true));
}
DI float row16_sum(float x) {
  x += dpp<0xB1>(x); x += dpp<0x4E>(x); x += dpp<0x141>(x); x += dpp<0x128>(x);
  return x;
}
DI float row16_max(float x) {
  x = fmaxf(x, dpp<0xB1>(x)); x = fmaxf(x, dpp<0x4E>(x)); x = fmaxf(x, dpp<0x141>(x)); x = fmaxf(x, dpp<0x128>(x));
  return x;
}
DI float xrow_sum(float x) {
  auto s = __builtin_amdgcn_permlane16_swap(__float_as_uint(x), __float_as_uint(x), false, false);
  x = __uint_as_float(s[0]) + __uint_as_float(s[1]);
  auto t = __builtin_amdgcn_permlane32_swap(__float_as_uint(x), __float_as_uint(x), false, false);
  return __uint_as_float(t[0]) + __uint_as_float(t[1]);
}
DI float xrow_sum4(float a, float b, float c, float d) {
  auto s = __builtin_amdgcn_permlane16_swap(__float_as_uint(a), __float_as_uint(b), false, false);
  auto t = __builtin_amdgcn_permlane16_swap(__float_as_uint(c), __float_as_uint(d), false, false);
  const float x = __uint_as_float(s[0]) + __uint_as_float(s[1]), y = __uint_as_float(t[0]) + __uint_as_float(t[1]);
  auto z = __builtin_amdgcn_permlane32_swap(__float_as_uint(x), __float_as_uint(y), false, false);
  return __uint_as_float(z[0]) + __uint_as_float(z[1]);
}
DI float xrow_max(float x) {
  auto s = __builtin_amdgcn_permlane16_swap(__float_as_uint(x), __float_as_uint(x), false, false);
  x = fmaxf(__uint_as_float(s[0]), __uint_as_float(s[1]));
  auto t = __builtin_amdgcn_permlane32_swap(__float_as_uint(x), __float_as_uint(x), false, false);
  return fmaxf(__uint_as_float(t[0]), __uint_as_float(t[1]));
}
DI f32x2 gelu2(f32x2 x) {
  const float k0 = -2.0f * 0.7978845608028654f * 1.4426950408889634f, k1 = k0 * 0.044715f;
  const f32x2 a = x * fma2(x * x, bc2(k1), bc2(k0));
  const f32x2 e = f32x2{__builtin_amdgcn_exp2f(a[0]), __builtin_amdgcn_exp2f(a[1])} + bc2(1.0f);
  return x * f32x2{__builtin_amdgcn_rcpf(e[0]), __builtin_amdgcn_rcpf(e[1])};
}
DI rsrc_t mkrs(const void* ptr, unsigned bytes) { return __builtin_amdgcn_make_buffer_rsrc((void*)ptr, 0, (int)bytes, 0x00020000); }
template <int AUX> DI v4u ld128(rsrc_t rs, unsigned v, unsigned s) { return __builtin_amdgcn_raw_buffer_load_b128(rs, (int)v, (int)s, AUX); }
template <int AUX> DI v2u ld64(rsrc_t rs, unsigned v, unsigned s) { return __builtin_amdgcn_raw_buffer_load_b64(rs, (int)v, (int)s, AUX); }
template <int AUX> DI void st128(v4u x, rsrc_t rs, unsigned v, unsigned s) { __builtin_amdgcn_raw_buffer_store_b128(x, rs, (int)(v + s), 0, AUX); }
template <int AUX> DI void st64(v2u x, rsrc_t rs, unsigned v, unsigned s) { __builtin_amdgcn_raw_buffer_store_b64(x, rs, (int)v, (int)s, AUX); }
DI f32x4 asf(v4u x) { return __builtin_bit_cast(f32x4, x); }
DI float sum4(f32x4 x) { return (x[0] + x[1]) + (x[2] + x[3]); }
DI unsigned umax3(unsigned a, unsigned b, unsigned c) { unsigned m = a > b ? a : b; return m > c ? m : c; }
DI unsigned macc4(unsigned m, v4u x) { return umax3(umax3(m, x[0], x[1]), x[2], x[3]); }
DI unsigned macc(unsigned m, v4u x) { return umax3(m, x[0], x[2]); }
DI bool any_mark(unsigned m) { return __builtin_amdgcn_ballot_w64(m == 0xFFFFFFFFu) != 0ull; }
#define SB() __builtin_amdgcn_sched_barrier(0)
#define PIN(x) asm volatile("" : "+v"(x))
#define PINA(x) asm volatile("" : "+a"(x))
#define USE(x) asm volatile("" ::"v"(x))
#define RETRY_BEGIN(m) if (__builtin_expect(any_mark(m), 0)) { unsigned sp_ = 0; do { MEMFENCE(); m = 0;
#define RETRY_END(m) } while (any_mark(m) & (++sp_ <= SPIN_CAP)); }
#define MEMFENCE() asm volatile("" ::: "memory")
DI void bar() { asm volatile("s_waitcnt lgkmcnt(0)" ::: "memory"); __builtin_amdgcn_s_barrier(); asm volatile("" ::: "memory"); }

DI f32x2 cmul(f32x2 a, f32x2 b) { return fma2(bc2(a[1]), f32x2{-b[1], b[0]}, bc2(a[0]) * b); }
DI f32x2 cexp_p(float are2, float rev_hi, float rev_lo, float p) {
  const float mag = __builtin_amdgcn_exp2f(are2 * p);
  const float hi = rev_hi * p;
  const float fr = (hi - __builtin_rintf(hi)) + rev_lo * p;
  return f32x2{mag * __builtin_amdgcn_cosf(fr), mag * __builtin_amdgcn_sinf(fr)};
}
DI float exp_acc(float x) {
  const float hi = x * 1.4426950408889634f;
  const float lo = __builtin_fmaf(x, 1.4426950408889634f, -hi) + x * 1.925963033500011e-8f;
  const float e = __builtin_amdgcn_exp2f(hi);
  return __builtin_fmaf(e, lo * 0.6931471805599453f, e);
}
DI unsigned pkh(float a, float b) {
  f16x2 v = {(_Float16)a, (_Float16)b};
  return __builtin_bit_cast(unsigned, v);
}

constexpr int CV_RK = 4160;
constexpr int CV_H = 2 * CV_RK;
constexpr int CV_CH = CV_H + 4608;
constexpr int LDS_A = 0;
constexpr int LDS_U = 2 * CV_CH;
constexpr int LDS_GS = LDS_U + 8192;
constexpr int LDS_TAB = 36864;
constexpr int KP_CH = 13840;
constexpr int LDS_KP = LDS_TAB + 2 * 64 * 528 + 16;
constexpr int LDS_LN = 0, LDS_MR = 8448;
constexpr int LDS_WI = 145472, LDS_WO = LDS_WI + 10240;
constexpr int LDS_X = 65536;
constexpr int LDS_S = LDS_X + 24576;
constexpr int LDS_W = LDS_S + 6144;
constexpr int MEGA_LDS = LDS_W + 65536;
static_assert(LDS_WO + 4608 <= MEGA_LDS && LDS_KP + 2 * KP_CH <= LDS_WI && LDS_S >= 73728, "lds");
static_assert(LDS_GS + 1280 <= LDS_TAB && LDS_KP + 2 * KP_CH <= MEGA_LDS && MEGA_LDS <= 160 * 1024, "lds");
static_assert(FLAG_AT > 45 && FLAG_AT < 78 && FLAG_AT % 5 != 2, "flag position");
#define WF_EARLY 5
#define WO0 12
#define WSP_P0 4
#define HINT4_KS 15
#define ZDEPTH 3
#define GELU_IN 0
#define EPI_W 0
#define MOD_POS 0
#define POLL3 0
#define GELU_PRE 4
#define HINT4_G 310
static_assert(HINT4_G > 4 * 64 + 12, "behind the marker barrier");
#define POLL4_G 45
#define RELABEL 1
#define CKMAP(ck) (RELABEL ? 2 * ((ck) & 3) + ((ck) >> 2) : (ck))
#define MOD_MID 1
#define LATE_SLEEP 6
#define SMALL_N 3
#define ZRD 12
#define IN_AUX 0
#define WSP_AUX 0

template <class T> DI T ldsr(unsigned a) { return *(const LAS T*)(size_t)a; }
template <class T> DI void ldsw(unsigned a, T v) { *(LAS T*)(size_t)a = v; }
DI v2u trread(const unsigned char* ptr) {
  typedef short v4i16_t __attribute__((ext_vector_type(4)));
  return __builtin_bit_cast(v2u, __builtin_amdgcn_ds_read_tr16_b64_v4i16((LAS v4i16_t*)ptr));
}
DI v2u trread_a(unsigned a) {
  typedef short v4i16_t __attribute__((ext_vector_type(4)));
  return __builtin_bit_cast(v2u, __builtin_amdgcn_ds_read_tr16_b64_v4i16((LAS v4i16_t*)(size_t)a));
}
struct CvOrder { int qq[36], pp[36]; };
constexpr CvOrder cv_order() {
  CvOrder o{};
  int n = 0;
  for (int m = 0; m < 8; ++m) {
    for (int j = 0; j < m; ++j) if (m + j < 8) { o.qq[n] = m; o.pp[n] = j; ++n; }
    for (int j = 0; j <= m; ++j) if (m + j < 8) { o.qq[n] = j; o.pp[n] = m; ++n; }
  }
  return o;
}
constexpr CvOrder CVO = cv_order();

typedef __attribute__((address_space(1))) unsigned gu32;
DI unsigned tx(unsigned m, unsigned x, unsigned tag) { return __builtin_amdgcn_bitop3_b32(m, x, tag, 0xF6); }
DI unsigned tacc(unsigned m, v4u x, unsigned tag) { return tx(tx(m, x[1], tag), x[3], tag); }
DI bool any_nz(unsigned m) { return __builtin_amdgcn_ballot_w64(m != 0u) != 0ull; }
DI void hint_add(unsigned char* ws, int line) { (void)__hip_atomic_fetch_add((gu32*)(ws + WS_CNT + line * 128), 1u, __ATOMIC_RELAXED, __HIP_MEMORY_SCOPE_AGENT); }
DI void hint_wait(rsrc_t rws, unsigned voff, unsigned soff, unsigned want) {
  unsigned sp = 0;
  for (;;) {
    MEMFENCE();
    const unsigned v = __builtin_amdgcn_raw_buffer_load_b32(rws, (int)voff, (int)soff, SC1);
    if (!((__builtin_amdgcn_ballot_w64(v < want) != 0ull) & (++sp <= SPIN_CAP))) break;
    __builtin_amdgcn_s_sleep(OPT_E);
  }
  MEMFENCE();
}

extern "C" __global__ void __launch_bounds__(256, 1) adaln_mega(Params p) {
  extern __shared__ __attribute__((aligned(16))) unsigned char smem[];
  const int tid = threadIdx.x, lane = tid & 63, w = __builtin_amdgcn_readfirstlane(tid >> 6);
  const int b = blockIdx.x, xcd = b & 7, idx = b >> 3;
  const int pb = xcd * 32 + idx;
  const unsigned l16 = lane * 16;
  STAMP_DECL;
  STAMP(1, 0);
  v2u codepf;
  {
    unsigned long long pc;
    int rel_m, rel_e;
    asm volatile("s_getpc_b64 %0\n\ts_mov_b32 %1, adaln_mega@rel32@lo+4\n\ts_mov_b32 %2, adaln_code_end@rel32@lo+12" : "=s"(pc), "=s"(rel_m), "=s"(rel_e));
    const rsrc_t rcode = mkrs((const void*)(pc + (long long)rel_m), rel_e > rel_m ? (unsigned)(rel_e - rel_m) & ~15u : 0u);
    codepf = ld64<0>(rcode, tid * 8, idx * 2048);
  }
  SB();
  const rsrc_t ru = mkrs(p.u, L * D * 2);
  v4u xr[2];
  xr[0] = ld128<IN_AUX>(ru, l16, (pb * 8 + w * 2) * 1024);
  xr[1] = ld128<IN_AUX>(ru, l16 + 1024, (pb * 8 + w * 2) * 1024);
  const int mkg = b & 3, mcg = b >> 2;
  const unsigned cq = p.c[mkg * 128 + (tid & 3) * 32 + (tid >> 3)];
  const unsigned epoch = *(const unsigned*)(p.ws + WS_EPOCH);
  const rsrc_t rws = mkrs(p.ws, WS_END);
  __builtin_amdgcn_raw_buffer_store_b32(0u, rws, tid < 2 ? tid * 32768u : OOB, WS_CNT + b * 128, SC1);
  SB();
  const int cch = w >> 1, eh = w & 1, ns = tid & 127;
  const unsigned q_cre = p.cre[pb * 256 + tid], q_cim = p.cim[pb * 256 + tid], q_lar = p.lar[pb * 256 + tid], q_aim = p.aim[pb * 256 + tid];
  const unsigned q_ldt = p.ldt[pb * 2 + cch], q_dsk = p.dsk[pb * 2 + cch];
  v4u xm[4];
#define LOAD_XM() { \
  const rsrc_t rmod = mkrs(p.wmod, D * 3072 * 2); \
_Pragma("unroll") \
  for (int i = 0; i < 4; ++i) xm[i] = ld128<IN_AUX>(rmod, (tid >> 3) * 6144 + (tid & 7) * 16, (mkg * 128 + i * 32) * 6144 + mcg * 128 + (b < 192 ? 0u : OOB)); }
  SB();
  LOAD_XM();
  SB();
  STAMP(1, 24);
  const int c = lane & 15, q = lane >> 4;
  const unsigned lds0 = (unsigned)(size_t)(LAS unsigned char*)smem;
  unsigned pos4[9];
  {
    const float f4 = (float)(8188 - tid * 32);
#pragma unroll
    for (int j = 0; j < 10; j += 2) {
      const unsigned pp = pk2(bc2(f4) - f32x2{4.f * j, 4.f * j + 4.f});
      const int p0 = (int)bflo(pp), p1 = (int)bfhi(pp);
      pos4[j] = p0 + ((p0 >> 5) << 2) + lds0 + LDS_KP;
      PIN(pos4[j]);
      if (j + 1 < 9) { pos4[j + 1] = p1 + ((p1 >> 5) << 2) + lds0 + LDS_KP; PIN(pos4[j + 1]); }
    }
  }
  unsigned co[2][4];
  {
    const unsigned b0 = (eh ? 1152 + 576 * q : 144 * (q >> 1) + 4 * (q & 1)) * 4 + 36 * c + lds0 + LDS_KP + cch * KP_CH;
    const unsigned b1 = (q < 2 ? 288 + 144 * q : 568 + 4 * q) * 4 + 36 * c + lds0 + LDS_KP + cch * KP_CH;
    const unsigned s0 = eh ? 576 : 4, s1 = q < 2 ? 8 : 576;
#pragma unroll
    for (int r = 0; r < 4; ++r) { co[0][r] = b0 + r * s0; co[1][r] = b1 + r * s1; PIN(co[0][r]); PIN(co[1][r]); }
  }
  *(unsigned*)(smem + LDS_KP - 16 + cch * KP_CH + (lane & 3) * 4) = 0u;
  unsigned a_pad, a_wi, a_wo, a_tab, gm_off, gc_off;
  a_pad = lds0 + cch * CV_CH + (lane < 32 ? CV_H + l16 : ((lane >> 1) & 1) * CV_RK + 4096 + (lane & 1) * 16);
  a_wi = lds0 + LDS_WI + (w * 32 + 8 * q + ((lane >> 2) & 3)) * 80 + (lane & 3) * 16;
  a_wo = lds0 + LDS_WO + (8 * q + ((lane >> 2) & 3)) * 144 + (w >> 1) * 64 + (lane & 3) * 16 + (w & 1) * 8;
  a_tab = lds0 + LDS_TAB + cch * (64 * 528) + c * 528 + q * 16;
  gm_off = ((4 - cch) * 512 + mkg * 128 + ns) * 8;
  gc_off = (((lane & 7) > 5 ? 0 : ((lane & 7) >> 1)) * 512 + pb * 2 + (lane & 1)) * 8;
  PIN(a_pad); PIN(a_wi); PIN(a_wo); PIN(a_tab); PIN(gm_off); PIN(gc_off);
  unsigned ab[4], bb, dv[4], rdo4[4], xoff;
#pragma unroll
  for (int i = 0; i < 4; ++i) {
    ab[i] = lds0 + cch * CV_CH + (1 - (c & 1)) * CV_RK + 2 * (2030 - 2 * (c >> 1) + 8 * q) - eh * 256 - 192 - 512 - 1024 * i;
    dv[i] = (lane >> 3) * 4096 + (((lane & 7) ^ ((lane >> 4) & 1) ^ (i << 1)) * 16) + i * (32768 - 1024);
    rdo4[i] = lds0 + LDS_W + w * 16384 + ((perm((i & 1) * 2 ^ (2 * (w & 1)), c) * 128 + ((q ^ (c >> 1)) * 16)) ^ ((i >> 1) * 64));
    PIN(ab[i]); PIN(dv[i]); PIN(rdo4[i]);
  }
  bb = lds0 + cch * CV_CH + CV_H + 2 * (256 - 16 + 16 * c + 8 * q) - eh * 256 - 192;
  xoff = WS_X1T + (pb * 2 + cch) * 4096 + eh * 2048 + ((c >> 1) * 32 + ((q & 1) * 4 + 2 * (c & 1) + (q >> 1)) * 4) * 2;
  PIN(bb); PIN(xoff);
  SB();
  STAMP(1, 26);
  {
    *(v4u*)(smem + LDS_LN + (w * 2) * 1040 + l16) = xr[0];
    *(v4u*)(smem + LDS_LN + (w * 2 + 1) * 1040 + l16) = xr[1];
    float sm[2], sq[2];
#pragma unroll
    for (int i = 0; i < 2; ++i) {
      sm[i] = dot2(xr[i][0], ONES, dot2(xr[i][1], ONES, dot2(xr[i][2], ONES, dot2(xr[i][3], ONES, 0.f))));
      sq[i] = dot2(xr[i][0], xr[i][0], dot2(xr[i][1], xr[i][1], dot2(xr[i][2], xr[i][2], dot2(xr[i][3], xr[i][3], 0.f))));
    }
    const float tv = row16_sum(xrow_sum4(sm[0], sm[1], sq[0], sq[1]));
    auto sw = __builtin_amdgcn_permlane32_swap(__float_as_uint(tv), __float_as_uint(tv), false, false);
    const float m = __uint_as_float(sw[0]) * (1.0f / D);
    const float r = __builtin_amdgcn_rsqf(fmaxf(__uint_as_float(sw[1]) * (1.0f / D) - m * m, 0.f) + EPS);
    ((float*)(smem + LDS_GS + 1088))[(q >> 1) * 8 + w * 2 + (q & 1)] = (q >> 1) ? -m * r : r;
  }
  const unsigned tag = epoch * 2654435761u + 0x9E3779B9u;
  STAMP(1, 28);
  asm volatile("s_waitcnt vmcnt(%0)" ::"i"(MOD_POS == 0 ? 8 : 4) : "memory");
  bar();
  STAMP(1, 16);
  {
    unsigned ua[4][2];
#pragma unroll
    for (int i = 0; i < 4; ++i) {
      const unsigned char* src = smem + LDS_LN + (tid & 3) * 2080 + (i * 64 + (tid >> 2)) * 4;
      ua[i][0] = *(const unsigned*)src; ua[i][1] = *(const unsigned*)(src + 1040);
    }
    const unsigned lnv = ((const unsigned*)(smem + LDS_GS + 1088))[c];
    SB();
#pragma unroll
    for (int i = 0; i < 4; ++i) st128<SC1>(v4u{ua[i][0], tag, ua[i][1], tag}, rws, (tid >> 2) * 16384 + (tid & 3) * 16, WS_UG + i * 64 * 16384 + pb * 64);
    st64<SC1>(v2u{lnv, tag}, rws, tid < 16 ? (c >> 3) * 16384 + (c & 7) * 8 : OOB, WS_SR + pb * 64);
  }
  STAMP(1, 17);
  SB();
  const unsigned wp_oob = b < 176 ? 0u : OOB;
  const int wpr = b >> 2, wkg = b & 3;
  v4u xi[2], xo;
  {
    const rsrc_t rwin = mkrs(p.win, D * FF * 2), rwout = mkrs(p.wout, FH * D * 2);
    const int hf = wpr >= 22, pp = wpr - 22 * hf;
    const unsigned v = ((tid >> 2) * (FF * 2) + (tid & 3) * 16) | ((pp == 21 ? tid & 2 : 0) << 29);
#pragma unroll
    for (int i = 0; i < 2; ++i) xi[i] = ld128<IN_AUX>(rwin, v, (wkg * 128 + i * 64) * (FF * 2) + (hf * FH + pp * 32) * 2 + wp_oob);
    xo = ld128<IN_AUX>(rwout, (tid >> 3) * 1024 + (tid & 7) * 16, idx * 32 * 1024 + xcd * 128);
  }
  SB();
  const int d0 = pb * 2, dk = d0 + cch;
  const int mt = (xcd >> 1) * 8 + (idx & 7), nt = (xcd & 1) * 4 + (idx >> 3);
  const int big = (nt + 2) >> 3;
  const int np = 3 - big, pr0 = 3 * nt - big * (nt - 6);
  const int kh = w & 1, uh = w >> 1;
  const int uo0 = pr0 * 2 + uh * (4 - 2 * big), ut0 = pr0 * 2 + (uh ^ 1) * (4 - 2 * big), uo2 = pr0 * 2 + 2 + uh, ut2 = pr0 * 2 + 2 + (uh ^ 1);
#define UO(k) ((k) == 2 ? uo2 : uo0 + (k))
#define UT(k) ((k) == 2 ? ut2 : ut0 + (k))
  const unsigned oob3 = (unsigned)big << 30;
  const int kr = 2 * (idx >> 3) + (xcd & 1) + (OPT_B == 0 ? (idx & 7) : OPT_B == 1 ? 4 * (idx & 1) : OPT_B == 2 ? 2 * (idx & 3) : 0);
  const rsrc_t rwsp = mkrs(p.wsp, L * L * 2);
  const unsigned wp_fr = (wpr * 2 * 16 + wkg * 4 + w) * 1024 + wp_oob;
  v4u wr[1][8];
  const unsigned wsp_v = (lane >> 3) * 4096 + (lane & 7) * 16, wsp_s = mt * 64 * 4096 + w * 1024;
#define LOAD_WSP1(ck, i) wr[ck][i] = ld128<WSP_AUX>(rwsp, wsp_v + (((ck) + kr) & 7) * 128, wsp_s + (i) * 8 * 4096)
#define LOAD_WSPN(n) LOAD_WSP1((n) >> 3, (n) & 7)
  const v4u mk = {~0u, ~0u, ~0u, ~0u};
  unsigned bspq[2];
#pragma unroll
  for (int t = 0; t < 2; ++t) bspq[t] = p.bsp[mt * 64 + perm(2 * kh + t, c)];
  const unsigned tag_s = __builtin_amdgcn_readfirstlane(tag);
  {
    const float dt = rbf(exp_acc(bf2f(q_ldt)));
    const float cre = bf2f(q_cre) * dt, cim = bf2f(q_cim) * dt;
    const float are2 = -rbf(exp_acc(bf2f(q_lar))) * dt * 1.4426950408889634f;
    const float aim = bf2f(q_aim) * dt;
    const float rev_hi = aim * 0.15915494309189535f;
    const float rev_lo = __builtin_fmaf(aim, 0.15915494309189535f, -rev_hi) + aim * 6.4206383e-9f;
    int ex = __builtin_amdgcn_frexp_expf(xrow_max(row16_max(fmaxf(fabsf(cre), fabsf(cim))))) + 1;
    ex = ex < -100 ? -100 : ex;
    const float scale = __builtin_amdgcn_ldexpf(1.f, -ex);
    ((float*)(smem + LDS_GS + 1024))[w] = __builtin_amdgcn_ldexpf(2.f, ex);
    const f32x2 c0 = {cre * scale, cim * scale};
    const f32x2 e1 = cexp_p(are2, rev_hi, rev_lo, 1.f), e8 = cexp_p(are2, rev_hi, rev_lo, 8.f), e64 = cexp_p(are2, rev_hi, rev_lo, 64.f);
    unsigned char* tn = smem + LDS_TAB + cch * (64 * 528) + ns * 4;
#define TROW(r, z) *(unsigned*)(tn + (r) * 528) = pkh((z)[0], -(z)[1])
    SB();
    const f32x2 e128 = cmul(e64, e64), e2 = cmul(e1, e1), e256 = cmul(e128, e128), e4 = cmul(e2, e2), e512 = cmul(e256, e256);
    f32x2 f0 = {1.f, 0.f}, f1 = e64;
    f32x2 ca = cmul(c0, e512), cb = cmul(ca, e512);
#pragma unroll
    for (int j = 0; j < 8; ++j) {
      *(unsigned*)(tn + (48 + j) * 528) = pkh(f0[0], f0[1]);
      *(unsigned*)(tn + (56 + j) * 528) = pkh(f1[0], f1[1]);
      TROW(32 + j, cb);
      f0 = j == 0 ? e8 : cmul(f0, e8); f1 = cmul(f1, e8); cb = cmul(cb, e128);
      if (j < 4) {
        const f32x2 z = cmul(ca, e4);
        TROW(24 + j, ca); TROW(28 + j, z);
        ca = cmul(ca, e128);
      }
      if (j == 3 || j == 7) {
        PIN(f0); PIN(f1); PIN(cb);
        if (WSP_P0 >= 4) { LOAD_WSPN(j >> 2 << 1); LOAD_WSPN((j >> 2 << 1) + 1); }
        SB();
      }
    }
    TROW(40, cb);
    SB();
  {
    const float cv = bf2f(cq);
    const float sl = rbf(cv * __builtin_amdgcn_rcpf(1.0f + __builtin_amdgcn_exp2f(cv * -1.4426950408889634f)));
    const float sc[4] = {dpp<0x00>(sl), dpp<0x55>(sl), dpp<0xAA>(sl), dpp<0xFF>(sl)};
    f32x2 a[4];
#pragma unroll
    for (int i = 0; i < 4; ++i)
#pragma unroll
      for (int j = 0; j < 4; ++j) a[j] = i == 0 ? unpk(xm[i][j]) * bc2(sc[i]) : fma2(unpk(xm[i][j]), bc2(sc[i]), a[j]);
    float* red = (float*)(smem + LDS_MR) + (tid >> 3) * 68 + (tid & 7) * 8;
    *(f32x4*)red = f32x4{a[0][0], a[0][1], a[1][0], a[1][1]};
    *(f32x4*)(red + 4) = f32x4{a[2][0], a[2][1], a[3][0], a[3][1]};
  }
    bar();
    float ms8[8];
#pragma unroll
    for (int i = 0; i < 8; ++i) ms8[i] = ((const float*)(smem + LDS_MR))[(q * 8 + i) * 68 + w * 16 + c];
    SB();
    f32x2 t1[8];
    t1[1] = e1; t1[2] = e2; t1[4] = e4;
    t1[3] = cmul(e2, e1); t1[5] = cmul(e4, e1); t1[6] = cmul(e2, e4); t1[7] = cmul(t1[6], e1);
    const f32x2 c1 = cmul(c0, e128), c2 = cmul(c0, e256), c3 = cmul(c2, e128);
    TROW(0, c0); TROW(8, c1); TROW(16, c2); TROW(20, c3);
    {
    const float s = xrow_sum(((ms8[0] + ms8[1]) + (ms8[2] + ms8[3])) + ((ms8[4] + ms8[5]) + (ms8[6] + ms8[7])));
    st64<SC1>(v2u{__float_as_uint(s), tag}, rws, q == 0 ? c * 8 : OOB, WS_MODG + (mkg * 3072 + mcg * 64 + w * 16) * 8 + (b < 192 ? 0u : OOB));
    }
    SB();
#pragma unroll
    for (int v = 1; v < 8; ++v) {
      const f32x2 z0 = cmul(t1[v], c0), z1 = cmul(t1[v], c1);
      TROW(v, z0); TROW(8 + v, z1);
      if ((v & 1) == 0) {
        const f32x2 z2 = cmul(t1[v], c2), z3 = cmul(t1[v], c3);
        TROW(16 + v / 2, z2); TROW(20 + v / 2, z3);
        if (v < 6 && WSP_P0 >= 8) { LOAD_WSPN(v + 2); LOAD_WSPN(v + 3); }
        SB();
      }
    }
  }
  STAMP(1, 27);
#pragma unroll
  for (int i = 0; i < 2; ++i) *(v4u*)(smem + LDS_WI + (i * 64 + (tid >> 2)) * 80 + (tid & 3) * 16) = xi[i];
  *(v4u*)(smem + LDS_WO + (tid >> 3) * 144 + (tid & 7) * 16) = xo;
  STAMP(1, 29);
  bar();
  STAMP(1, 18);
  SB();
  v4u gr[4], gb_[4];
  v2u gm[4], gc[4];
#define LOAD_GS() _Pragma("unroll") for (int i_ = 0; i_ < 4; ++i_) { gr[i_] = ld128<SC1>(rws, tid * 16, WS_SR + i_ * 4096); gb_[i_] = ld128<SC1>(rws, tid * 16, WS_SB + i_ * 4096); }
#define LOAD_GM() _Pragma("unroll") for (int i_ = 0; i_ < 4; ++i_) { gm[i_] = ld64<SC1>(rws, gm_off, WS_MODG + i_ * 24576); gc[i_] = ld64<SC1>(rws, gc_off, WS_MODG + i_ * 24576); }
  v4u ug[4], wt[2];
#define LOAD_UG() _Pragma("unroll") for (int i_ = 0; i_ < 4; ++i_) ug[i_] = ld128<SC1>(rws, tid * 16, WS_UG + pb * 16384 + i_ * 4096)
  {
    v4u fa[2][8], fb[8];
    const unsigned ta = a_tab + eh * (32 * 528);
#pragma unroll
    for (int ks = 0; ks < 3; ++ks) { fb[ks] = ldsr<v4u>(a_tab + 48 * 528 + ks * 64); fa[0][ks] = ldsr<v4u>(ta + ks * 64); fa[1][ks] = ldsr<v4u>(a_tab + 16 * 528 + ks * 64); }
    SB();
    LOAD_UG();
    SB();
    f32x4 kk[2][2];
    f32x2 us;
    v2u wh_[6];
#pragma unroll
    for (int i = 0; i < 4; ++i) kk[i >> 1][i & 1] = f32x4{0.f, 0.f, 0.f, 0.f};
#pragma unroll
    for (int ks = 0; ks < 8; ++ks) {
      if (ks < 5) { fb[3 + ks] = ldsr<v4u>(a_tab + 48 * 528 + (3 + ks) * 64); fa[0][3 + ks] = ldsr<v4u>(ta + (3 + ks) * 64); fa[1][3 + ks] = ldsr<v4u>(a_tab + 16 * 528 + (3 + ks) * 64); }
      if (ks == 5) { us = *(const f32x2*)(smem + LDS_GS + 1024 + cch * 8); wh_[4] = trread_a(a_wo); wh_[5] = trread_a(a_wo + 576); }
      if (ks == 6) { wh_[0] = trread_a(a_wi); wh_[1] = trread_a(a_wi + 320); wh_[2] = trread_a(a_wi + 8); wh_[3] = trread_a(a_wi + 328); }
      kk[0][ks >> 2] = mfma_h(fa[0][ks], fb[ks], kk[0][ks >> 2]); kk[1][ks >> 2] = mfma_h(fa[1][ks], fb[ks], kk[1][ks >> 2]);
      SB();
    }
    STAMP(1, 37);
    wt[0] = v4u{wh_[0][0], wh_[0][1], wh_[1][0], wh_[1][1]};
    wt[1] = v4u{wh_[2][0], wh_[2][1], wh_[3][0], wh_[3][1]};
    st128<SC1>(v4u{wh_[4][0], wh_[4][1], wh_[5][0], wh_[5][1]}, rws, l16, WS_WOT + ((xcd * 4 + w) * 22 + idx) * 1024 + wp_oob);
    LOAD_GS();
    SB();
#pragma unroll
    for (int i = 0; i < 4; ++i) PINA(kk[i >> 1][i & 1]);
    f32x4 k0 = kk[0][0] * us[0] + kk[0][1] * us[1];
    const f32x4 k1 = kk[1][0] * us[0] + kk[1][1] * us[1];
    k0[0] += ns == 0 ? bf2f(q_dsk) : 0.f;
#pragma unroll
    for (int r = 0; r < 4; ++r) { ldsw<float>(co[0][r], k0[r]); ldsw<float>(co[1][r], k1[r]); }
  }
  STAMP(1, 31);
  bar();
  STAMP(1, 19);
  {
    float v[2][9];
#pragma unroll
    for (int j = 0; j < 9; ++j) v[0][j] = ldsr<float>(pos4[j]);
    LOAD_GM();
#pragma unroll
    for (int j = 0; j < 9; ++j) v[1][j] = ldsr<float>(pos4[j] + KP_CH);
    ldsw<v4u>(a_pad, v4u{0u, 0u, 0u, 0u});
    SB();
    STAMP(1, 36);
#pragma unroll
    for (int ch = 0; ch < 2; ++ch) {
      unsigned char* rk = smem + ch * CV_CH + tid * 16;
#pragma unroll
      for (int sft = 0; sft < 2; ++sft)
        *(v4u*)(rk + sft * CV_RK) = v4u{pk2(v[ch][sft], v[ch][sft + 1]), pk2(v[ch][sft + 2], v[ch][sft + 3]), pk2(v[ch][sft + 4], v[ch][sft + 5]), pk2(v[ch][sft + 6], v[ch][sft + 7])};
    }
#pragma unroll
    for (int i = WSP_P0; i < 8; ++i) LOAD_WSPN(i);
    st128<SC1>(mk, rws, l16, WS_X1T + dk * 4096 + eh * 2048);
    st128<SC1>(mk, rws, l16 + 1024, WS_X1T + dk * 4096 + eh * 2048);
    __builtin_amdgcn_raw_buffer_store_b32(~0u, rws, lane * 4, WS_ST2 + ((mt * 2 + kh) * 16 + nt * 2 + uh) * 256, SC1);
    SB();
    STAMP(1, 32);
    unsigned m = 0;
#pragma unroll
    for (int i = 0; i < 4; ++i) { m = tacc(m, ug[i], tag); m = tacc(m, gr[i], tag); m = tacc(m, gb_[i], tag); }
    if (__builtin_expect(any_nz(m), 0)) {
      unsigned sp = 0;
      do {
        MEMFENCE(); LOAD_UG(); LOAD_GS(); m = 0;
#pragma unroll
        for (int i = 0; i < 4; ++i) { m = tacc(m, ug[i], tag); m = tacc(m, gr[i], tag); m = tacc(m, gb_[i], tag); }
      } while (any_nz(m) & (++sp <= SPIN_CAP));
    }
    f32x2 nn[4][2];
#pragma unroll
    for (int i = 0; i < 4; ++i) {
      nn[i][0] = fma2(unpk(ug[i][0]), bc2(__uint_as_float(gr[i][0])), bc2(__uint_as_float(gb_[i][0])));
      nn[i][1] = fma2(unpk(ug[i][2]), bc2(__uint_as_float(gr[i][2])), bc2(__uint_as_float(gb_[i][2])));
      PIN(nn[i][0]); PIN(nn[i][1]);
      *(unsigned*)(smem + LDS_U + i * 1024 + tid * 4) = __builtin_amdgcn_perm(ug[i][2], ug[i][0], 0x05040100u);
      *(unsigned*)(smem + LDS_U + 4096 + i * 1024 + tid * 4) = __builtin_amdgcn_perm(ug[i][2], ug[i][0], 0x07060302u);
    }
    SB();
    STAMP(1, 33);
    m = 0;
#pragma unroll
    for (int i = 0; i < 4; ++i) m = tx(tx(m, gm[i][1], tag), gc[i][1], tag);
    if (__builtin_expect(any_nz(m), 0)) {
      unsigned sp = 0;
      do {
        __builtin_amdgcn_s_sleep(3);
        MEMFENCE(); LOAD_GM(); m = 0;
#pragma unroll
        for (int i = 0; i < 4; ++i) m = tx(tx(m, gm[i][1], tag), gc[i][1], tag);
      } while (any_nz(m) & (++sp <= SPIN_CAP));
    }
    const float mv = rbf((__uint_as_float(gm[0][0]) + __uint_as_float(gm[1][0])) + (__uint_as_float(gm[2][0]) + __uint_as_float(gm[3][0])));
    *(u16*)(smem + LDS_GS + tid * 2) = (u16)pk2(mv + (cch ? 0.f : 1.0f), 0.f);
    const float cv = rbf((__uint_as_float(gc[0][0]) + __uint_as_float(gc[1][0])) + (__uint_as_float(gc[2][0]) + __uint_as_float(gc[3][0])));
    const float gav = dpp<0x104>(cv), gav2 = dpp<0x102>(cv);
    const float hbv = cv * gav, hsv = rbf(1.0f + cv) * gav2;
    const f32x2 hb = {__builtin_bit_cast(float, __builtin_amdgcn_readlane(__builtin_bit_cast(int, hbv), 0)), __builtin_bit_cast(float, __builtin_amdgcn_readlane(__builtin_bit_cast(int, hbv), 1))};
    const f32x2 hs = {__builtin_bit_cast(float, __builtin_amdgcn_readlane(__builtin_bit_cast(int, hsv), 2)), __builtin_bit_cast(float, __builtin_amdgcn_readlane(__builtin_bit_cast(int, hsv), 3))};
    SB();
    bspq[0] |= bspq[1] << 16;
    PIN(bspq[0]);
    STAMP(1, 38);
#pragma unroll
    for (int i = 0; i < 4; ++i) {
      const f32x2 h0 = fma2(nn[i][0], hs, hb), h1 = fma2(nn[i][1], hs, hb);
      *(unsigned*)(smem + CV_H + 512 + i * 1024 + tid * 4) = pk2(h0[0], h1[0]);
      *(unsigned*)(smem + CV_CH + CV_H + 512 + i * 1024 + tid * 4) = pk2(h0[1], h1[1]);
    }
  }
  asm volatile("" ::"v"(codepf));
  STAMP(1, 39);
  bar();
  STAMP(1, 1);
#define CV_A(ee, k) ({ const unsigned a_ = ab[(k) >> 1] + 192 - 64 * (ee) + 512 * (1 - ((k) & 1)); v4u{ldsr<unsigned>(a_), ldsr<unsigned>(a_ + 4), ldsr<unsigned>(a_ + 8), ldsr<unsigned>(a_ + 12)}; })
#define CV_B(ee, k) ldsr<v4u>(bb + 192 - 64 * (ee) + 512 * (k))
  const unsigned slotA = LDS_W + w * 16384, slotB = slotA + 8192;
  v4u wq[8][8];
#define READ_W(dl, f) ldsr<v4u>(rdo4[(((f) >> 1) & 1) + ((f) >> 2) * 2] + (dl) + ((f) & 1) * 512)
  unsigned dso;
#define DMA_W(ck, slot, i) do { \
    if (((i) & 3) == 0) asm volatile("s_add_u32 m0, %1, %2\n\ts_add_u32 %0, %3, %4\n\tbuffer_load_dwordx4 %5, %6, %0 offen lds" \
                 : "=&s"(dso) : "s"(lds0 + (slot)), "i"((i) * 1024), "s"(wsp_s + ((CKMAP(ck) + kr) & 7) * 128), "i"((i) * 32768), "v"(dv[0]), "s"(rwsp) : "scc", "memory"); \
    else asm volatile("buffer_load_dwordx4 %0, %1, %2 offen offset:%3 lds" :: "v"(dv[(i) & 3]), "s"(rwsp), "s"(dso), "i"(((i) & 3) * 1024) : "memory"); } while (0)
  auto dma_sel = [&](auto CK, unsigned slot, int i) __attribute__((always_inline)) {
    constexpr int ck = decltype(CK)::value;
    switch (i) {
      case 0: DMA_W(ck, slot, 0); break; case 1: DMA_W(ck, slot, 1); break; case 2: DMA_W(ck, slot, 2); break; case 3: DMA_W(ck, slot, 3); break;
      case 4: DMA_W(ck, slot, 4); break; case 5: DMA_W(ck, slot, 5); break; case 6: DMA_W(ck, slot, 6); break; default: DMA_W(ck, slot, 7); break;
    }
  };
  v4u cA[2][8], cB[2][8];
  float gs[8];
  v4u gbf, sbf;
  v4u ones4 = {ONES, ONES, ONES, ONES};
  PIN(ones4);
#pragma unroll
  for (int k = 0; k < 2; ++k) { cA[0][k] = CV_A(0, k); cB[0][k] = CV_B(0, k); }
  {
    gbf = *(const v4u*)(smem + LDS_GS + w * 64 + q * 16);
    sbf = *(const v4u*)(smem + LDS_GS + 256 + w * 64 + q * 16);
  }
  SB();
  f32x4 cacc[8];
  v2u ue[4];
  float uf[16];
  unsigned flagv;
  unsigned wro = lds0 + slotA + (lane >> 3) * 128 + (((lane & 7) ^ ((lane >> 4) & 1)) * 16);
  PIN(wro);
  {
    f32x4 cb4[4];
    v4u wo_[2];
    float wl, wh;
    f32x2 cp[4];
#pragma unroll
    for (int t = 0; t < 8; ++t) cacc[t] = f32x4{0.f, 0.f, 0.f, 0.f};
    sfor<144>([&](auto G) __attribute__((always_inline)) {
      constexpr int g = decltype(G)::value, ee = g / 36, j = g % 36;
      if constexpr (ee == 0 && j < 6) { cA[0][2 + j] = CV_A(0, 2 + j); cB[0][2 + j] = CV_B(0, 2 + j); }
      cacc[CVO.pp[j] + CVO.qq[j]] = mfma(cA[ee & 1][CVO.qq[j]], cB[ee & 1][CVO.pp[j]], cacc[CVO.pp[j] + CVO.qq[j]]);
      constexpr int jn = ee == 0 ? j - 20 : (j & 1) ? j >> 1 : -1;
      if constexpr (ee < 3 && jn >= 0 && jn < 16) {
        if constexpr (jn & 1) cB[(ee + 1) & 1][jn >> 1] = CV_B(ee + 1, jn >> 1);
        else cA[(ee + 1) & 1][jn >> 1] = CV_A(ee + 1, jn >> 1);
      }
      if constexpr (ee == 3 && j < 4) ue[j] = *(const v2u*)(smem + LDS_U + cch * 4096 + (eh * 4 + j) * 512 + (c * 16 + q * 4) * 2);
      if constexpr (g >= 6 && g < 30) {
        constexpr int i = (g - 6) / 12, jj = ((g - 6) / 3) & 3, ph = (g - 6) % 3;
        if constexpr (ph == 0) { if constexpr (i == 0) gs[2 * jj] = bflo(gbf[jj]); wl = bflo(wt[i][jj]) * gs[2 * jj]; PIN(wl); }
        if constexpr (ph == 1) { if constexpr (i == 0) gs[2 * jj + 1] = bfhi(gbf[jj]); wh = bfhi(wt[i][jj]) * gs[2 * jj + 1]; PIN(wh); }
        if constexpr (ph == 2) {
          wo_[i][jj] = pk2(wl, wh);
          if constexpr (jj == 3) st128<SC1>(wo_[i], rws, l16 + i * 16384, WS_WP + wp_fr);
          else PIN(wo_[i][jj]);
        }
      }
      if constexpr (g == 4 || g == 5) cb4[2 * (g - 4) + 1] = mfma(sbf, wt[g - 4], f32x4{0.f, 0.f, 0.f, 0.f});
      if constexpr (g == 19 || g == 31) cb4[g == 19 ? 0 : 2] = mfma(ones4, wo_[g == 31], f32x4{0.f, 0.f, 0.f, 0.f});
      if constexpr (g == 33) *(f32x4*)(smem + LDS_S + 4096 + (w * 16 + c) * 16) = f32x4{cb4[0][0], cb4[1][0], cb4[2][0], cb4[3][0]};
      if constexpr (g == 38) bar();
      if constexpr (g == 39) {
#pragma unroll
        for (int k = 0; k < 4; ++k) cp[k] = *(const f32x2*)(smem + LDS_S + 4096 + (k * 16 + c) * 16 + (q & 1) * 8);
      }
      if constexpr (g == 45) {
        float s0 = cp[0][0] + cp[1][0], s1 = cp[2][0] + cp[3][0];
        PIN(s0); PIN(s1);
        s0 += s1;
        PIN(s0);
        float s2 = cp[0][1] + cp[1][1], s3 = cp[2][1] + cp[3][1];
        PIN(s2); PIN(s3);
        st64<SC1>(v2u{__float_as_uint(s0), __float_as_uint(s2 + s3)}, rws, tid < 32 ? q * 512 + c * 8 : OOB, WS_CSB + wpr * 1024 + wkg * 128 + wp_oob);
      }
      if constexpr (g == 36) STAMP(1, 40);
      if constexpr (g == 72) STAMP(1, 41);
      if constexpr (g == FLAG_AT) {
        STAMP(1, 42);
        asm volatile("s_waitcnt vmcnt(%0)" ::"i"((FLAG_AT - 32 + 4) / 5 + 1) : "memory");
        __builtin_amdgcn_s_barrier();
        if (tid < 8) hint_add(p.ws, 256 + tid * 16 + (idx & 15));
        STAMP(1, 43);
      }
      if constexpr (g % 5 == 2 && g < 80) DMA_W(1 + g / 40, g < 40 ? slotA : slotB, (g / 5) & 7);
      if constexpr (g == 88) asm volatile("s_waitcnt vmcnt(8)" ::: "memory");
      if constexpr (g == 124) asm volatile("s_waitcnt vmcnt(0)" ::: "memory");
      if constexpr (g == POLL_AT) flagv = __builtin_amdgcn_raw_buffer_load_b32(rws, w == 0 ? c * 128 : OOB, WS_CNT + (256 + xcd * 16) * 128, SC1);
      if constexpr (g >= 88 && g < 104 && (g & 1) == 0) wq[1][(g - 88) >> 1] = READ_W(0, (g - 88) >> 1);
      if constexpr (g >= 108 && g < 140 && (g & 3) == 0) ldsw<v4u>((wro ^ ((((g - 108) >> 2) & 3) * 32)) + ((g - 108) >> 2) * 1024, wr[0][(g - 108) >> 2]);
      if constexpr (g >= 125 && g < 141 && (g & 1) == 1) wq[2][(g - 125) >> 1] = READ_W(8192, (g - 125) >> 1);
      if constexpr (g >= 128 && g < 144) { uf[g - 128] = ((g & 1) ? bfhi : bflo)(ue[(g - 128) >> 2][((g - 128) >> 1) & 1]); PIN(uf[g - 128]); }
      SB();
    });
  }
  STAMP(1, 2);
  unsigned late;
  {
    f32x4* red = (f32x4*)(smem + LDS_X) + w * 256 + lane;
    f32x4 keep[4];
    if (eh) {
#pragma unroll
      for (int t = 0; t < 4; ++t) red[t * 64] = cacc[t];
      SB();
#pragma unroll
      for (int t = 0; t < 4; ++t) { PINA(cacc[4 + t]); keep[t] = cacc[4 + t]; PIN(keep[t]); }
    } else {
#pragma unroll
      for (int t = 0; t < 4; ++t) red[t * 64] = cacc[4 + t];
      SB();
#pragma unroll
      for (int t = 0; t < 4; ++t) { PINA(cacc[t]); keep[t] = cacc[t]; PIN(keep[t]); }
    }
    f32x2 pre[4][2];
#pragma unroll
    for (int t = 0; t < 4; ++t) {
      pre[t][0] = f32x2{uf[4 * t], uf[4 * t + 1]} + lo2(keep[t]);
      pre[t][1] = f32x2{uf[4 * t + 2], uf[4 * t + 3]} + hi2(keep[t]);
      PIN(pre[t][0]); PIN(pre[t][1]);
    }
    if (w == 0) *(unsigned*)(smem + LDS_S) = __builtin_amdgcn_ballot_w64(flagv < 16u) != 0ull;
    bar();
    late = *(const unsigned*)(smem + LDS_S);
    const f32x4* rd = (const f32x4*)(smem + LDS_X) + (w ^ 1) * 256 + lane;
    f32x4 oth[4];
#pragma unroll
    for (int t = 0; t < 4; ++t) oth[t] = rd[t * 64];
#pragma unroll
    for (int t = 0; t < 4; ++t) {
      v2u o;
      o[0] = nomark(pk2(pre[t][0] + lo2(oth[t])));
      o[1] = pk2(pre[t][1] + hi2(oth[t]));
      st64<SC1>(o, rws, xoff + t * 512, 0);
    }
  }
  STAMP(1, 3);
  SB();
  v4u wf[3][8];
  const unsigned rot = uh * 4;
#define LOAD_WP(half, k, i) ld128<SC1>(rws, l16 + ((i) & 3) * 1024, WS_WP + (((half) * 44 + UO(k)) * 16 + kh * 8 + (((i) & 4) ^ rot)) * 1024 + ((k) == 2 ? oob3 : 0u))
#define LOAD_XS(i) ld128<SC1>(rws, wsp_v, WS_X1T + ((i) * 32 + w * 8) * 4096 + mt * 128)
  v4u xs[16];
  unsigned tb[2][4], xdst;
  SB();
  int x1o;
  unsigned z2o[3];
  v2u cz[2][3];
  f32x2 csb[2][3];
  float gbv, c1v[2];
  unsigned gq[4];
#define LOAD_CZ(hf, kc) ld64<SC1>(rws, lane * 8, WS_CSB + ((hf) * 44 + UO(kc)) * 512 + ((kc) == 2 ? oob3 : 0u))
  {
    STAMP(1, 20);
    if (__builtin_amdgcn_readfirstlane(late)) {
      if (w == 0) { hint_wait(rws, c * 128, WS_CNT + (256 + xcd * 16) * 128, 16u); __builtin_amdgcn_s_sleep(LATE_SLEEP); }
      __builtin_amdgcn_s_barrier();
    }
    MEMFENCE();
    STAMP(1, 21);
#pragma unroll
    for (int i = 0; i < 3; ++i)
#pragma unroll
      for (int k = 0; k < 3; ++k) wf[k][i] = LOAD_WP(1, k, i);
    SB();
#pragma unroll
  for (int f = 0; f < 8; ++f) wq[0][f] = READ_W(0, f);
#pragma unroll
  for (int f = 0; f < 24; ++f) PINA(wq[f >> 3][f & 7]);
    int lz = lane;
    PIN(lz);
  {
    const int row = 8 * (lz >> 4) + ((lz >> 2) & 3);
    const int sg = ((row >> 1) & 1) | (((row >> 3) & 1) << 1);
#pragma unroll
    for (int tu = 0; tu < 4; ++tu) {
      const unsigned t0 = lds0 + LDS_A + kh * 32768 + row * 128 + ((tu ^ (2 * kh) ^ sg) * 32) + (lz & 3) * 8;
      tb[0][tu] = t0 + uh * 16384;
      tb[1][tu] = t0 + (uh ^ 1) * 16384;
    }
    const int rl = w * 8 + (lz >> 3), chn = lz & 7;
    const int sg2 = ((rl >> 1) & 1) | (((rl >> 3) & 1) << 1);
    xdst = lds0 + LDS_A + rl * 128 + (((chn >> 1) ^ sg2) * 32) + (chn & 1) * 16;
  }
  {
    const int row = 8 * (lz >> 4) + ((lz >> 2) & 3);
    const int sg = ((row >> 1) & 1) | (((row >> 3) & 1) << 1);
    x1o = LDS_A + nt * 8192 + row * 128 + ((w ^ sg) * 32) + (lz & 3) * 8;
  }
#pragma unroll
  for (int k = 0; k < 3; ++k) { z2o[k] = lz * 16 + WS_Z2 + (UO(k) * 64 + mt * 2 + kh) * 1024 + (k == 2 ? oob3 : 0u); }
#pragma unroll
    for (int tu = 0; tu < 8; ++tu) PIN(tb[tu >> 2][tu & 3]);
    PIN(xdst); PIN(x1o);
#pragma unroll
    for (int k = 0; k < 3; ++k) PIN(z2o[k]);
    SB();
#pragma unroll
    for (int i = 3; i < WF_EARLY; ++i)
#pragma unroll
      for (int k = 0; k < 3; ++k) wf[k][i] = LOAD_WP(1, k, i);
    SB();
#pragma unroll
    for (int i = 0; i < 16; ++i) xs[i] = LOAD_XS(i);
    SB();
    STAMP(1, 44);
    unsigned m = 0;
#pragma unroll
    for (int i = 0; i < 16; ++i) {
      m = macc(m, xs[i]); ldsw<v4u>(xdst + i * 4096, xs[i]);
      if (WF_EARLY * 3 + i < 24) wf[(WF_EARLY * 3 + i) % 3][(WF_EARLY * 3 + i) / 3] = LOAD_WP(1, (WF_EARLY * 3 + i) % 3, (WF_EARLY * 3 + i) / 3);
      SB();
    }
    PIN(m);
    STAMP(1, 45);
    RETRY_BEGIN(m)
#pragma unroll
      for (int i = 0; i < 16; ++i) xs[i] = LOAD_XS(i);
      SB();
#pragma unroll
      for (int i = 0; i < 16; ++i) { m = macc(m, xs[i]); ldsw<v4u>(xdst + i * 4096, xs[i]); }
    RETRY_END(m)
  }
  STAMP(1, 4);
  bar();
  STAMP(1, 5);
#define AHALF(tu, i, h) trread_a(tb[(i) >> 2][tu] + ((i) & 3) * 4096 + (h) * 512)
  f32x4 acc[3][4];
  v4u wg[3][8];
  v2u ah[2][4][2];
  float* st = (float*)(smem + LDS_S);
  float* fin = st + 256 + w * 64;
  f32x4* xch = (f32x4*)(smem + LDS_X);
  f32x4 r4[2], tm[3][2];
  {
    f32x4 s1[4], s2[4], m4[2];
    v2u ar[3][2][2];
    float pa[4], pb[4];
    constexpr int ORD[20] = {0, 1, 2, 3, 4, 5, 6, 7, 16, 17, 18, 19, 8, 9, 10, 11, 12, 13, 14, 15};
    int dgo[2], rdo[2];
    dgo[0] = w * 64 + kh * 32 + c; dgo[1] = w * 64 + (kh ^ 1) * 32 + c;
    rdo[0] = (kh * 32 + (lane & 31)) * 4; rdo[1] = rdo[0] * 4 + (lane & 3) * 4;
    PIN(dgo[0]); PIN(dgo[1]); PIN(rdo[0]); PIN(rdo[1]);
    const bool diag = (c >> 2) == q;
#define GFRAG(gi, t, h) AHALF(((gi) < 8 ? 2 : 0) + (t), (gi) < 16 ? (gi) & 7 : (gi) - 16, h)
#pragma unroll
    for (int n = 0; n < 2; ++n)
#pragma unroll
      for (int e = 0; e < 4; ++e) ar[n][e >> 1][e & 1] = GFRAG(ORD[n], e >> 1, e & 1);
    SB();
    sfor<20 * 6>([&](auto G) __attribute__((always_inline)) {
      constexpr int n = decltype(G)::value / 6, j = decltype(G)::value % 6, gi = ORD[n], t = j / 3, k = j % 3;
      constexpr int tu = (gi < 8 ? 2 : 0) + t, i = gi < 16 ? gi & 7 : gi - 16;
      const v4u a = {ar[n % 3][t][0][0], ar[n % 3][t][0][1], ar[n % 3][t][1][0], ar[n % 3][t][1][1]};
      const f32x4 z4 = {0.f, 0.f, 0.f, 0.f};
      if constexpr (gi < 16) acc[k][tu] = mfma(a, wf[k][i], gi == 0 ? z4 : acc[k][tu]);
      if constexpr ((gi >= 4 && gi < 8) || gi >= 16) {
        if constexpr (k == 0) s1[tu] = mfma(ones4, a, (gi == 4 || gi == 16) ? z4 : s1[tu]);
        if constexpr (k == 1) s2[tu] = mfma(a, a, (gi == 4 || gi == 16) ? z4 : s2[tu]);
      }
      if constexpr (j < 4 && n + 2 < 20) ar[(n + 2) % 3][j >> 1][j & 1] = GFRAG(ORD[n + 2], j >> 1, j & 1);
      if constexpr (n >= 18 && j < 4) ah[0][(n - 18) * 2 + (j >> 1)][j & 1] = AHALF((n - 18) * 2 + (j >> 1), 0, j & 1);
      if constexpr (n < 4 && (j == 1 || j == 4)) DMA_W(3, slotB, n * 2 + (j == 4));
      if constexpr (n == SMALL_N) cz[j / 3][j % 3] = LOAD_CZ(j / 3, j % 3);
      if constexpr (n == 4 && j < 4) gq[j] = __builtin_amdgcn_raw_buffer_load_b32(rws, lane * 8, WS_MODG + (j * 3072 + 5 * 512 + nt * 64) * 8, SC1);
      if constexpr (n == 8 && j == 1) USE(gq[3]);
      if constexpr (n >= 8 && n < 12 && (j == 2 || j == 5)) DMA_W(4, slotA, (n - 8) * 2 + (j == 5));
      if constexpr (n >= 12 && j == 4) DMA_W(5, slotB, n - 12);
      if constexpr ((n == 8 || n == 9) && (j & 1) == 0) {
        constexpr int x = (n - 8) * 3 + (j >> 1);
        xch[(w * 6 + (x % 3) * 2 + x / 3) * 64 + lane] = acc[x % 3][2 + x / 3];
      }
      if constexpr (n == 10 && j == 0) {
#pragma unroll
    for (int k = 0; k < 3; ++k) {
      const float y = xrow_sum4(__uint_as_float(cz[1][k][0]), __uint_as_float(cz[1][k][1]), __uint_as_float(cz[1][k][0]), __uint_as_float(cz[1][k][1]));
      auto sw = __builtin_amdgcn_permlane16_swap(__float_as_uint(y), __float_as_uint(y), false, false);
      csb[1][k] = f32x2{__uint_as_float(sw[0]), __uint_as_float(sw[1])};
      PIN(csb[1][k]);
    }
    c1v[0] = xrow_sum4(__uint_as_float(cz[0][0][0]), __uint_as_float(cz[0][0][1]), __uint_as_float(cz[0][1][0]), __uint_as_float(cz[0][1][1]));
    c1v[1] = xrow_sum4(__uint_as_float(cz[0][2][0]), __uint_as_float(cz[0][2][1]), __uint_as_float(cz[0][2][0]), __uint_as_float(cz[0][2][1]));
    PIN(c1v[0]); PIN(c1v[1]);
    gbv = rbf((__uint_as_float(gq[0]) + __uint_as_float(gq[1])) + (__uint_as_float(gq[2]) + __uint_as_float(gq[3])));
    PIN(gbv);
      }
      if constexpr ((n == 8 || n == 9) && j >= 2) wq[3][(n - 8) * 4 + j - 2] = READ_W(8192, (n - 8) * 4 + j - 2);
      if constexpr (n == 10 && j == 5) {
#pragma unroll
        for (int f = 0; f < 8; ++f) PINA(wq[3][f]);
      }
      if constexpr (n == 10 && j == 4) bar();
      if constexpr (n == 11) acc[j % 3][j / 3] = xch[((w ^ 1) * 6 + (j % 3) * 2 + j / 3) * 64 + lane];
      if constexpr (n == 12 && j == 3) {
        float s1v[4];
#pragma unroll
        for (int x = 0; x < 4; ++x) { s1v[x] = s1[x][0]; PIN(s1v[x]); }
        if (diag) {
#pragma unroll
          for (int x = 0; x < 4; ++x) {
            *(float*)(smem + LDS_S + dgo[x >> 1] * 4 + (x & 1) * 64) = s1v[x];
            *(f32x4*)(smem + LDS_S + 2048 + dgo[x >> 1] * 16 + (x & 1) * 256) = s2[x];
          }
        }
      }
      if constexpr (n == 13 && j == 5) bar();
      if constexpr (n == 14 && j < 4) {
        pa[j] = *(const float*)(smem + LDS_S + rdo[0] + j * 256); pb[j] = *(const float*)(smem + LDS_S + 2048 + rdo[1] + j * 1024);
      }
      if constexpr (n == 15 && j == 1) { pa[0] += pa[1]; pa[2] += pa[3]; PIN(pa[0]); PIN(pa[2]); }
      if constexpr (n == 15 && j == 2) { pb[0] += pb[1]; pb[2] += pb[3]; PIN(pb[0]); PIN(pb[2]); }
      if constexpr (n == 15 && j == 3) { pa[0] = (pa[0] + pa[2]) * (1.0f / D); PIN(pa[0]); }
      if constexpr (n == 15 && j == 4) { pb[0] = (pb[0] + pb[2]) * (1.0f / D); PIN(pb[0]); }
      if constexpr (n == 15 && j == 5) { pb[0] = fmaxf(__builtin_fmaf(-pa[0], pa[0], pb[0]), 0.f); PIN(pb[0]); }
      if constexpr (n == 16 && j == 0) { pb[0] = __builtin_amdgcn_rsqf(pb[0] + EPS); PIN(pb[0]); }
      if constexpr (n == 16 && j == 1) fin[lane] = lane < 32 ? pb[0] : -pb[0] * pa[0];
      if constexpr (n == 16 && (j == 2 || j == 3)) { m4[j - 2] = *(const f32x4*)(fin + 32 + (j - 2) * 16 + q * 4); r4[j - 2] = *(const f32x4*)(fin + (j - 2) * 16 + q * 4); }
      if constexpr (n >= 18) {
        constexpr int x = (n - 18) * 6 + j, kk = x >> 2, tt = (x >> 1) & 1, h = (x & 1) * 2;
        float t0 = __builtin_fmaf(m4[tt][h], csb[1][kk][0], csb[1][kk][1]);
        PIN(t0);
        float t1 = __builtin_fmaf(m4[tt][h + 1], csb[1][kk][0], csb[1][kk][1]);
        PIN(t1);
        tm[kk][tt][h] = t0; tm[kk][tt][h + 1] = t1;
      }
      if constexpr (n >= 12 && n < 15 && j == 5) st128<SC1>(mk, rws, z2o[n - 12], 0);
      if constexpr (n == 7 && j == 5) STAMP(1, 46);
      if constexpr (n == 11 && j == 5) STAMP(1, 47);
      SB();
    });
  }
#pragma unroll
  for (int k = 0; k < 6; ++k) PINA(acc[k >> 1][k & 1]);
  STAMP(1, 6);
  f32x4 z2v[3][2];
  {
    f32x4 rs[2], rq[2];
#pragma unroll
    for (int t = 0; t < 2; ++t) {
#pragma unroll
      for (int k = 0; k < 3; ++k) {
        const f32x4 a = acc[k][t];
        const f32x2 zl = gelu2(fma2(lo2(a), lo2(r4[t]), lo2(tm[k][t])));
        const f32x2 zh = gelu2(fma2(hi2(a), hi2(r4[t]), hi2(tm[k][t])));
        z2v[k][t] = f32x4{zl[0], zl[1], zh[0], zh[1]};
        if (k == 0) { rs[t] = z2v[k][t]; rq[t] = z2v[k][t] * z2v[k][t]; }
        else { rs[t] += z2v[k][t]; rq[t] += z2v[k][t] * z2v[k][t]; }
        if (((t * 3 + k) & 1) == 0) {
#pragma unroll
          for (int f = (t * 3 + k) * 4; f < (t * 3 + k) * 4 + 8; ++f) wg[f % 3][f / 3] = LOAD_WP(0, f % 3, f / 3);
        } else {
          PIN(z2v[k][t]);
          SB();
        }
      }
    }
    STAMP(1, 50);
    float v8[8], v4[4], v2[2];
    const bool b3 = c & 8, b2 = c & 4, b1 = c & 2, b0 = c & 1;
#pragma unroll
    for (int r_ = 0; r_ < 4; ++r_) {
      const float a0 = rs[0][r_] + dpp<0x128>(rs[0][r_]), a1 = rs[1][r_] + dpp<0x128>(rs[1][r_]);
      const float q0 = rq[0][r_] + dpp<0x128>(rq[0][r_]), q1 = rq[1][r_] + dpp<0x128>(rq[1][r_]);
      v8[2 * r_] = b3 ? a1 : a0; v8[2 * r_ + 1] = b3 ? q1 : q0;
    }
#pragma unroll
    for (int j = 0; j < 4; ++j) {
      const float x0 = v8[j] + dpp<0x141>(v8[j]), x1 = v8[4 + j] + dpp<0x141>(v8[4 + j]);
      v4[j] = b2 ? x1 : x0;
    }
#pragma unroll
    for (int j = 0; j < 2; ++j) {
      const float x0 = v4[j] + dpp<0x4E>(v4[j]), x1 = v4[2 + j] + dpp<0x4E>(v4[2 + j]);
      v2[j] = b1 ? x1 : x0;
    }
    const float x0 = v2[0] + dpp<0xB1>(v2[0]), x1 = v2[1] + dpp<0xB1>(v2[1]);
    const float outv = b0 ? x1 : x0;
    __builtin_amdgcn_raw_buffer_store_b32(nomark(__float_as_uint(outv)), rws, lane * 4, WS_ST2 + ((mt * 2 + kh) * 16 + nt * 2 + uh) * 256, SC1);
  }
  STAMP(1, 7);
  SB();
#pragma unroll
  for (int tu = 0; tu < 4; ++tu)
#pragma unroll
    for (int k = 0; k < 3; ++k) acc[k][tu] = f32x4{0.f, 0.f, 0.f, 0.f};
  unsigned k3v[6];
  {
    const unsigned k0 = l16 + w * 6144, w3 = (w + 1) >> 2;
    k3v[0] = k0; k3v[1] = k0 + 2048 - w3 * 1024; k3v[2] = k3v[1] + 1024; k3v[3] = k0 + 5120 - w3 * 2048;
    k3v[4] = k0 + 1024 + (w3 << 30); k3v[5] = k3v[4] + 3072;
  }
  v2u x1h[2][2];
  v4u wo[6][4];
#define LOAD_WO(f) wo[(f) >> 2][(f) & 3] = ld128<SC1>(rws, k3v[(f) >> 2], WS_WOT + (nt * 4 + ((f) & 3)) * 22 * 1024)
  f32x4 pt[4];
  float* c1s = st + 768 + w * 96;
#define LOAD_PT(i) asf(ld128<SC1>(rws, l16 + (i) * 1024, WS_ST2 + (mt * 2 + kh) * 4096))
  SB();
#pragma unroll
  for (int i = 0; i < 8; ++i) {
#pragma unroll
    for (int j = 0; j < 12; ++j) {
      const int tu = j / 3, k = j % 3;
      const v4u a = {ah[i & 1][tu][0][0], ah[i & 1][tu][0][1], ah[i & 1][tu][1][0], ah[i & 1][tu][1][1]};
      acc[k][tu] = mfma(wg[k][i], a, acc[k][tu]);
      if (j < 8 && i < 7) ah[(i + 1) & 1][j >> 1][j & 1] = AHALF(j >> 1, i + 1, j & 1);
      if (j < 4 && i == 7) x1h[j >> 1][j & 1] = trread(smem + x1o + (j >> 1) * 4096 + (j & 1) * 512);
      if (i == 0 && j == 0) asm volatile("s_waitcnt vmcnt(24)" ::: "memory");
      if (i < 2 && j >= 4) wq[4 + i][j - 4] = READ_W(i * 8192, j - 4);
      if (i == 3 && j == 0) {
#pragma unroll
        for (int f = 0; f < 16; ++f) PINA(wq[4 + (f >> 3)][f & 7]);
      }
      if (i == 4 && j == 0) { USE(wg[0][7]); USE(wg[1][7]); USE(wg[2][7]); }
      if ((i == 2 || i == 3) && j % 3 == 2) dma_sel(IC<6>{}, slotA, (i - 2) * 4 + j / 3);
      if ((i == 4 || i == 5) && j % 3 == 2) dma_sel(IC<7>{}, slotB, (i - 4) * 4 + j / 3);
      if (j >= 8 && i == 6) pt[j - 8] = LOAD_PT(j - 8);
      if (i == 7 && j >= 8) LOAD_WO(j - 8);
      if (i == 5 && j == 0) {
        asm volatile("s_waitcnt vmcnt(30)" ::: "memory");
        __builtin_amdgcn_s_barrier();
      }
      if (i == 5 && j == 1) st[1408 + lane] = gbv;
      if (i == 5 && j == 9) c1s[lane] = c1v[0];
      if (i == 5 && j == 10) c1s[64 + (lane & 31)] = c1v[1];
      SB();
    }
  }
  STAMP(1, 8);
#pragma unroll
  for (int k = 0; k < 3; ++k)
#pragma unroll
    for (int t = 0; t < 2; ++t) xch[(w * 6 + k * 2 + t) * 64 + lane] = acc[k][2 + t];
  SB();
  {
    unsigned m = 0;
#pragma unroll
    for (int i = 0; i < 4; ++i) m = macc4(m, __builtin_bit_cast(v4u, pt[i]));
    RETRY_BEGIN(m)
#pragma unroll
      for (int i = 0; i < 4; ++i) { pt[i] = LOAD_PT(i); m = macc4(m, __builtin_bit_cast(v4u, pt[i])); }
    RETRY_END(m)
    if (tid < 4) hint_add(p.ws, 64 + (nt * 4 + (mt >> 3)) * 4 + tid);
    const f32x4 sx = (pt[0] + pt[1]) + (pt[2] + pt[3]);
    const float tv = xrow_sum4(sx[0], sx[2], sx[1], sx[3]);
    auto sw = __builtin_amdgcn_permlane32_swap(__float_as_uint(tv), __float_as_uint(tv), false, false);
    const float mm = __uint_as_float(sw[0]) * (1.0f / FH);
    const float r = __builtin_amdgcn_rsqf(fmaxf(__uint_as_float(sw[1]) * (1.0f / FH) - mm * mm, 0.f) + EPS);
    float* f2 = st + 512 + w * 64;
    f2[(q >> 1) * 32 + 2 * c + (q & 1)] = (q >> 1) ? -r * mm : r;
    f32x4 r4[2], m4[2];
#pragma unroll
    for (int t = 0; t < 2; ++t) { r4[t] = *(const f32x4*)(f2 + q * 8 + t * 4); m4[t] = *(const f32x4*)(f2 + 32 + q * 8 + t * 4); }
#pragma unroll
    for (int k = 0; k < 3; ++k) {
      v4u o;
#pragma unroll
      for (int t = 0; t < 2; ++t) {
        o[2 * t] = nomark(pk2(fma2(lo2(z2v[k][t]), lo2(r4[t]), lo2(m4[t]))));
        o[2 * t + 1] = pk2(fma2(hi2(z2v[k][t]), hi2(r4[t]), hi2(m4[t])));
      }
      st128<SC1>(o, rws, z2o[k], 0);
    }
  }
  STAMP(1, 9);
  SB();
  f32x4 z1f[3][2];
  constexpr int RD = ZRD;
  v4u zf[RD];
#define LOAD_Z(n) ld128<SC1>(rws, zv[((n) >> 1) & 7] + ((n) & 1) * 1024, WS_Z2 + (((n) < 48 ? UT(((n) >> 4) % 3) : UO(((n) >> 4) % 3)) * 64 + w * 16) * 1024 + (((n) >> 4) % 3 == 2 ? oob3 : 0u))
  unsigned zv[8];
#pragma unroll
  for (int i = 0; i < 8; ++i) { zv[i] = l16 + ((CKMAP(i) + kr) & 7) * 2048; PIN(zv[i]); }
  {
    bar();
    STAMP(1, 34);
#pragma unroll
    for (int k = 0; k < 6; ++k) PINA(acc[k >> 1][k & 1]);
    f32x4 oth[3][2], cs1[3], bs1[3];
    float r1[2], m1[2];
#pragma unroll
    for (int t = 0; t < 2; ++t) { r1[t] = fin[t * 16 + c]; m1[t] = fin[32 + t * 16 + c]; }
#pragma unroll
    for (int k = 0; k < 3; ++k) {
      cs1[k] = *(const f32x4*)(c1s + k * 32 + q * 4); bs1[k] = *(const f32x4*)(c1s + k * 32 + 16 + q * 4);
#pragma unroll
      for (int t = 0; t < 2; ++t) oth[k][t] = xch[((w ^ 1) * 6 + k * 2 + t) * 64 + lane];
    }
#pragma unroll
    for (int i = 4; i < WO0; ++i) LOAD_WO(i);
    SB();
    f32x2 z1a[6][2];
#pragma unroll
    for (int i = 0; i < 6; ++i) {
      const int k = i >> 1, t = i & 1;
      const f32x4 a = acc[k][t] + oth[k][t];
      z1a[i][0] = fma2(lo2(a), bc2(r1[t]), fma2(lo2(cs1[k]), bc2(m1[t]), lo2(bs1[k])));
      z1a[i][1] = fma2(hi2(a), bc2(r1[t]), fma2(hi2(cs1[k]), bc2(m1[t]), hi2(bs1[k])));
    }
#pragma unroll
    for (int i = 0; i < GELU_PRE; ++i) {
      const f32x2 lo = gelu2(z1a[i][0]), hi = gelu2(z1a[i][1]);
      z1f[i >> 1][i & 1] = f32x4{lo[0], lo[1], hi[0], hi[1]};
      PIN(z1f[i >> 1][i & 1]);
    }
#pragma unroll
    for (int i = GELU_PRE; i < 6; ++i) { PIN(z1a[i][0]); PIN(z1a[i][1]); }
    SB();
#pragma unroll
    for (int f = 0; f < 16; ++f) wq[6 + (f >> 3)][f & 7] = READ_W((f >> 3) * 8192, f & 7);
    SB();
    STAMP(1, 10);
    hint_wait(rws, 0u, WS_CNT + (64 + (nt * 4 + w) * 4 + (xcd >> 1)) * 128, 8u);
    STAMP(1, 22);
#pragma unroll
    for (int n = 0; n < RD; ++n) zf[n] = LOAD_Z(n);
    SB();
#pragma unroll
    for (int i = GELU_PRE; i < 6; ++i) {
      const f32x2 lo = gelu2(z1a[i][0]), hi = gelu2(z1a[i][1]);
      z1f[i >> 1][i & 1] = f32x4{lo[0], lo[1], hi[0], hi[1]};
      PIN(z1f[i >> 1][i & 1]);
    }
  }
  f32x4* xb = (f32x4*)(smem + LDS_A) + lane;
  f32x4* yb = (f32x4*)(smem + LDS_A + 49152) + lane;
  f32x4 ac2[2][4], ini[4], yo[2];
  const float bs0 = bflo(bspq[0]), bs1_ = bfhi(bspq[0]);
  const f32x4 bq0 = {bs0, bs0, bs0, bs0}, bq1 = {bs1_, bs1_, bs1_, bs1_}, z4 = {0.f, 0.f, 0.f, 0.f};
  unsigned voff[2];
  voff[0] = l16 + WS_V + ((mt * 4 + 2 * kh) * 22 + (uo0 >> 1)) * 1024;
  voff[1] = l16 + WS_V + ((mt * 4 + 2 * kh) * 22 + (uo2 >> 1)) * 1024 + (uo2 & 1) * 8 + oob3;
  PIN(voff[0]); PIN(voff[1]);
  float ev[4];
  unsigned eo0;
  SB();
  STAMP(1, 11);
  auto v_store = [&](int k, int t, unsigned o0, unsigned o1) __attribute__((always_inline)) {
    st64<SC1>(v2u{o0, o1}, rws, voff[k >> 1] + (k & 1) * 8, t * 22 * 1024);
  };
  sfor<6>([&](auto P) __attribute__((always_inline)) {
    constexpr int ps = decltype(P)::value, cur = ps & 1, prv = cur ^ 1;
    unsigned zm = 0;
    sfor<64>([&](auto G) __attribute__((always_inline)) {
      constexpr int g = decltype(G)::value, ks = g >> 2, tu = g & 3, ck = ks >> 1, k2 = ks & 1, n = ps * 16 + ks;
      const f32x4 cin = ks > 0 ? ac2[cur][tu] : ps >= 3 ? ini[tu] : tu == 0 ? bq0 : tu == 1 ? bq1 : z4;
      ac2[cur][tu] = mfma(zf[n % RD], wq[ck][k2 * 4 + tu], cin);
      if constexpr (tu == 1) { zm = macc(zm, zf[n % RD]); PIN(zm); }
      if constexpr (tu == 3) {
        if constexpr (n + RD < 96) zf[n % RD] = LOAD_Z(n + RD);
        else if constexpr (WO0 + n + RD - 96 < 24) LOAD_WO(WO0 + n + RD - 96);
      }
      if constexpr (ps >= 1 && ps <= 3 && g >= 2 && g < 18 && (g & 3) == 2) xb[((w * 3 + ps - 1) * 4 + (g >> 2)) * 64] = ac2[prv][g >> 2];
      if constexpr (ps >= 4 && (g == 2 || g == 6)) yb[((w * 3 + ps - 4) * 2 + (g >> 2)) * 64] = ac2[prv][2 + (g >> 2)];
      if constexpr (ps >= 1 && g == (ps >= 4 ? 12 : 24)) {
        if constexpr (ps == 4) asm volatile("s_waitcnt vmcnt(20)" ::: "memory");
        bar();
      }
      if constexpr (ps >= 4 && (g == 13 || g == 14)) yo[g - 13] = yb[(((w ^ 1) * 3 + ps - 4) * 2 + g - 13) * 64];
      if constexpr (ps >= 4 && g >= 20 && g < 40) {
        constexpr int t = (g - 20) / 10, ph = (g - 20) % 10;
        if constexpr (ph < 4) { ev[ph] = ac2[prv][t][ph] + yo[t][ph]; PIN(ev[ph]); }
        else if constexpr (ph < 8) { ev[ph - 4] *= z1f[ps - 4][t][ph - 4]; PIN(ev[ph - 4]); }
        else if constexpr (ph == 8) { eo0 = nomark(pk2(ev[0], ev[1])); PIN(eo0); }
        else v_store(ps - 4, t, eo0, pk2(ev[2], ev[3]));
      }
      if constexpr (ps >= 2 && ps <= 4 && g >= 28 && g < 44 && (g & 3) == 0) ini[(g - 28) >> 2] = xb[(((w ^ 2) * 3 + ps - 2) * 4 + ((g - 28) >> 2)) * 64];
      if constexpr (ps == 0 && (g == 21 || g == 37 || g == 53)) st128<SC1>(mk, rws, l16, WS_V + ((mt * 4 + w) * 22 + pr0 + (g - 21) / 16) * 1024 + (g == 53 ? oob3 : 0u));
      if constexpr (ps * 64 + g == HINT4_G) { if (tid < 2) hint_add(p.ws, 192 + mt * 2 + tid); }
      if constexpr (ps == 5 && g == POLL4_G) flagv = __builtin_amdgcn_raw_buffer_load_b32(rws, w == 0 ? 0u : OOB, WS_CNT + (192 + mt * 2 + (xcd & 1)) * 128, SC1);
      if constexpr (g == 0 && ps >= 1 && ps < 5) STAMP(1, 55 + ps);
      SB();
    });
    if (__builtin_expect(any_mark(zm), 0)) {
#pragma unroll
      for (int tu = 0; tu < 4; ++tu) ac2[cur][tu] = ps >= 3 ? xb[(((w ^ 2) * 3 + ps - 3) * 4 + tu) * 64] : tu == 0 ? bq0 : tu == 1 ? bq1 : z4;
      sfor<16>([&](auto K) __attribute__((always_inline)) {
        constexpr int ks = decltype(K)::value, ck = ks >> 1, k2 = ks & 1;
        v4u zz;
        unsigned sp = 0;
        do { MEMFENCE(); zz = LOAD_Z(ps * 16 + ks); } while (any_mark(macc(0u, zz)) & (++sp <= SPIN_CAP));
#pragma unroll
        for (int tu = 0; tu < 4; ++tu) ac2[cur][tu] = mfma(zz, wq[ck][k2 * 4 + tu], ac2[cur][tu]);
      });
    }
  });
  STAMP(1, 12);
  {
#pragma unroll
    for (int t = 0; t < 2; ++t) yb[((w * 3 + 2) * 2 + t) * 64] = ac2[1][2 + t];
    PIN(ac2[1][0]); PIN(ac2[1][1]);
    if (w == 0) st[0] = __uint_as_float(flagv < 8u);
    bar();
    late = __float_as_uint(st[0]);
    STAMP(1, 52);
#pragma unroll
    for (int t = 0; t < 2; ++t) yo[t] = yb[(((w ^ 1) * 3 + 2) * 2 + t) * 64];
#pragma unroll
    for (int t = 0; t < 2; ++t) {
      const f32x4 g = (ac2[1][t] + yo[t]) * z1f[2][t];
      v_store(2, t, nomark(pk2(lo2(g))), pk2(hi2(g)));
    }
  }
  STAMP(1, 13);
  SB();
  f32x4 gb4[4];
  f32x2 x1f[2][4];
  STAMP(1, 53);
  if (__builtin_amdgcn_readfirstlane(late)) {
    if (w == 0) hint_wait(rws, 0u, WS_CNT + (192 + mt * 2 + (xcd & 1)) * 128, 8u);
    __builtin_amdgcn_s_barrier();
  }
  MEMFENCE();
  STAMP(1, 23);
  f32x4 a3[4][4];
  {
    unsigned spins = 0;
    for (;;) {
      MEMFENCE();
      v4u vs[6][4];
#define LOAD_V(f) vs[(f) >> 2][(f) & 3] = ld128<SC1>(rws, k3v[(f) >> 2], WS_V + (mt * 4 + (((f) & 3) ^ w)) * 22 * 1024)
#pragma unroll
      for (int f = 0; f < 12; ++f) LOAD_V(f);
      SB();
      unsigned m = 0;
      sfor<96>([&](auto G) __attribute__((always_inline)) {
        constexpr int g = decltype(G)::value, ks = g >> 4, tu = (g >> 2) & 3, e = g & 3;
        a3[e][tu] = mfma(wo[ks][e], vs[ks][tu], ks == 0 ? f32x4{0.f, 0.f, 0.f, 0.f} : a3[e][tu]);
        if constexpr (e == 0 && 12 + (g >> 2) < 24) LOAD_V(12 + (g >> 2));
        if constexpr (e == 1) { m = macc(m, vs[ks][tu]); PIN(m); }
        SB();
      });
      if (!(any_mark(m) & (++spins <= SPIN_CAP))) break;
    }
  }
#pragma unroll
  for (int i = 0; i < 4; ++i) gb4[i] = *(const f32x4*)(st + 1408 + (i >> 1) * 32 + q * 8 + (i & 1) * 4);
#pragma unroll
  for (int i = 0; i < 8; ++i) { x1f[i >> 2][i & 3] = unpk(x1h[i >> 2][(i >> 1) & 1][i & 1]); PIN(x1f[i >> 2][i & 3]); }
  STAMP(1, 14);
  {
    f32x4* red = (f32x4*)(smem + LDS_W);
#pragma unroll
    for (int tu = 1; tu < 4; ++tu)
#pragma unroll
      for (int e = 0; e < 4; ++e) red[((w * 3 + tu - 1) * 4 + e) * 64 + lane] = a3[e][tu];
#pragma unroll
    for (int e = 0; e < 4; ++e) PIN(a3[e][0]);
    bar();
    STAMP(1, 54);
    f32x4 o3[4][3];
#pragma unroll
    for (int e = 0; e < 4; ++e)
#pragma unroll
      for (int rel = 1; rel < 4; ++rel) o3[e][rel - 1] = red[(((w ^ rel) * 3 + rel - 1) * 4 + e) * 64 + lane];
    const rsrc_t rout = mkrs(p.out, L * D * 2);
    SB();
#pragma unroll
    for (int pp = 0; pp < 2; ++pp) {
      v4u o;
#pragma unroll
      for (int h = 0; h < 2; ++h) {
        const int e = pp * 2 + h;
        const f32x4 y = (a3[e][0] + o3[e][0]) + (o3[e][1] + o3[e][2]);
        o[2 * h] = pk2(fma2(lo2(y), lo2(gb4[e]), x1f[pp][2 * h]));
        o[2 * h + 1] = pk2(fma2(hi2(y), hi2(gb4[e]), x1f[pp][2 * h + 1]));
      }
      st128<0>(o, rout, perm(w, c) * 1024 + q * 16 + pp * 64, mt * 64 * 1024 + nt * 128);
    }
  }
  if (b == 0 && tid == 0) *(unsigned*)(p.ws + WS_EPOCH) = tag_s;
  STAMP(1, 15);
  STAMP_FLUSH(1);
  asm volatile("adaln_code_end:");
}

extern "C" void kernel_launch(void* const* d_in, const int* in_sizes, int n_in, void* d_out, int out_size, void* d_ws,
                              size_t ws_size, hipStream_t stream) {
  Params p;
  p.u = (const u16*)d_in[0];
  p.lar = (const u16*)d_in[1];
  p.aim = (const u16*)d_in[2];
  p.cre = (const u16*)d_in[3];
  p.cim = (const u16*)d_in[4];
  p.ldt = (const u16*)d_in[5];
  p.dsk = (const u16*)d_in[6];
  p.win = (const u16*)d_in[8];
  p.wsp = (const u16*)d_in[9];
  p.bsp = (const u16*)d_in[10];
  p.wout = (const u16*)d_in[11];
  p.c = (const u16*)d_in[12];
  p.wmod = (const u16*)d_in[13];
  p.out = (u16*)d_out;
  p.ws = (unsigned char*)d_ws;
  (void)hipFuncSetAttribute((const void*)adaln_mega, hipFuncAttributeMaxDynamicSharedMemorySize, MEGA_LDS);
  adaln_mega<<<256, 256, MEGA_LDS, stream>>>(p);
}
```

```cpp
#define TLBW 10
#define CODE_END "adaln_code_end"
#include <hip/hip_runtime.h>
#include <hip/hip_bf16.h>
#include <stdint.h>
#include <utility>

#define LAS __attribute__((address_space(3)))
typedef unsigned v4u __attribute__((ext_vector_type(4)));
typedef unsigned v2u __attribute__((ext_vector_type(2)));
typedef float f32x4 __attribute__((ext_vector_type(4)));
typedef float f32x2 __attribute__((ext_vector_type(2)));
typedef short bf16x8 __attribute__((ext_vector_type(8)));
typedef _Float16 f16x8 __attribute__((ext_vector_type(8)));
typedef _Float16 f16x2 __attribute__((ext_vector_type(2)));
typedef __bf16 bf16x2_t __attribute__((ext_vector_type(2)));
typedef unsigned short u16;
typedef __amdgpu_buffer_rsrc_t rsrc_t;

#define STAMP_DECL do { } while (0)
#define STAMP(k, i) do { } while (0)
#define STAMP_FLUSH(k) do { } while (0)

#define OPT_A 1
#define OPT_B 1
#define OPT_C 2
#define OPT_D 1
#define OPT_E 2
#define OPT_F 1
constexpr int L = 2048, D = 512, NM = 128, FH = 688, FF = 1376;
constexpr float EPS = 1e-5f;
constexpr int SC1 = 16;
constexpr unsigned OOB = 0x40000000u;
constexpr unsigned SPIN_CAP = 1u << 14;

constexpr unsigned WS_EPOCH = 0;
constexpr unsigned WS_CNT = 4096;
constexpr unsigned WS_SR = WS_CNT + 512 * 128;
constexpr unsigned WS_SB = WS_SR + 2048 * 8;
constexpr unsigned WS_MODG = WS_SB + 2048 * 8;
constexpr unsigned WS_CSB = WS_MODG + 3072 * 32;
constexpr unsigned WS_ST2 = WS_CSB + 88 * 16 * 32;
constexpr unsigned WS_UG = 1u << 20;
static_assert(WS_ST2 + 32 * 2 * 16 * 32 * 8 <= WS_UG, "small buffers");
constexpr unsigned WS_X1T = WS_UG + 256 * 2048 * 8;
constexpr unsigned WS_WP = WS_X1T + 512 * 2048 * 2;
constexpr unsigned WS_WOT = WS_WP + 88 * 16 * 1024;
constexpr unsigned WS_Z2 = WS_WOT + 32 * 22 * 1024;
constexpr unsigned WS_V = WS_Z2 + 44 * 64 * 1024;
constexpr unsigned WS_END = WS_V + 128 * 22 * 1024;

struct Params {
  const u16 *u, *lar, *aim, *cre, *cim, *ldt, *dsk, *win, *wsp, *bsp, *wout, *c, *wmod;
  u16* out;
  unsigned char* ws;
};

#define DI __device__ __forceinline__
template <int V> struct IC { static constexpr int value = V; };
template <class F, int... I> DI void sfor_(F&& f, std::integer_sequence<int, I...>) { int d[] = {(f(IC<I>{}), 0)...}; (void)d; }
template <int N, class F> DI void sfor(F&& f) { sfor_(f, std::make_integer_sequence<int, N>{}); }
DI float bf2f(unsigned b) { return __builtin_bit_cast(float, b << 16); }
DI float bflo(unsigned w) { return __builtin_bit_cast(float, w << 16); }
DI float bfhi(unsigned w) { return __builtin_bit_cast(float, w & 0xFFFF0000u); }
DI f32x2 unpk(unsigned w) { return f32x2{bflo(w), bfhi(w)}; }
DI unsigned pk2(f32x2 v) { return __builtin_bit_cast(unsigned, __builtin_convertvector(v, bf16x2_t)); }
DI unsigned pk2(float lo, float hi) { return pk2(f32x2{lo, hi}); }
DI unsigned nomark(unsigned x) { return x < 0xFFFFFFFEu ? x : 0xFFFFFFFEu; }
DI float rbf(float x) { return bflo(pk2(x, 0.f)); }
DI f32x2 fma2(f32x2 a, f32x2 b, f32x2 c) { return __builtin_elementwise_fma(a, b, c); }
DI f32x2 lo2(f32x4 v) { return f32x2{v[0], v[1]}; }
DI f32x2 hi2(f32x4 v) { return f32x2{v[2], v[3]}; }
DI f32x2 bc2(float x) { return f32x2{x, x}; }
DI f32x4 mfma(v4u a, v4u b, f32x4 c) {
  return __builtin_amdgcn_mfma_f32_16x16x32_bf16(__builtin_bit_cast(bf16x8, a), __builtin_bit_cast(bf16x8, b), c, 0, 0, 0);
}
DI f32x4 mfma_h(v4u a, v4u b, f32x4 c) {
  return __builtin_amdgcn_mfma_f32_16x16x32_f16(__builtin_bit_cast(f16x8, a), __builtin_bit_cast(f16x8, b), c, 0, 0, 0);
}
DI float dot2(unsigned a, unsigned b, float acc) {
  return __builtin_amdgcn_fdot2_f32_bf16(__builtin_bit_cast(bf16x2_t, a), __builtin_bit_cast(bf16x2_t, b), acc, false);
}
constexpr unsigned ONES = 0x3F803F80u;
DI int perm(int u, int c) { return 32 * (u >> 1) + 8 * (c >> 2) + 4 * (u & 1) + (c & 3); }
template <int CTRL> DI float dpp(float x) {
  return __builtin_bit_cast(float, __builtin_amdgcn_mov_dpp(__builtin_bit_cast(int, x), CTRL, 0xf, 0xf, true));
}
DI float row16_sum(float x) {
  x += dpp<0xB1>(x); x += dpp<0x4E>(x); x += dpp<0x141>(x); x += dpp<0x128>(x);
  return x;
}
DI float row16_max(float x) {
  x = fmaxf(x, dpp<0xB1>(x)); x = fmaxf(x, dpp<0x4E>(x)); x = fmaxf(x, dpp<0x141>(x)); x = fmaxf(x, dpp<0x128>(x));
  return x;
}
DI float xrow_sum(float x) {
  auto s = __builtin_amdgcn_permlane16_swap(__float_as_uint(x), __float_as_uint(x), false, false);
  x = __uint_as_float(s[0]) + __uint_as_float(s[1]);
  auto t = __builtin_amdgcn_permlane32_swap(__float_as_uint(x), __float_as_uint(x), false, false);
  return __uint_as_float(t[0]) + __uint_as_float(t[1]);
}
DI float xrow_sum4(float a, float b, float c, float d) {
  auto s = __builtin_amdgcn_permlane16_swap(__float_as_uint(a), __float_as_uint(b), false, false);
  auto t = __builtin_amdgcn_permlane16_swap(__float_as_uint(c), __float_as_uint(d), false, false);
  const float x = __uint_as_float(s[0]) + __uint_as_float(s[1]), y = __uint_as_float(t[0]) + __uint_as_float(t[1]);
  auto z = __builtin_amdgcn_permlane32_swap(__float_as_uint(x), __float_as_uint(y), false, false);
  return __uint_as_float(z[0]) + __uint_as_float(z[1]);
}
DI float xrow_max(float x) {
  auto s = __builtin_amdgcn_permlane16_swap(__float_as_uint(x), __float_as_uint(x), false, false);
  x = fmaxf(__uint_as_float(s[0]), __uint_as_float(s[1]));
  auto t = __builtin_amdgcn_permlane32_swap(__float_as_uint(x), __float_as_uint(x), false, false);
  return fmaxf(__uint_as_float(t[0]), __uint_as_float(t[1]));
}
DI f32x2 gelu2(f32x2 x) {
  const float k0 = -2.0f * 0.7978845608028654f * 1.4426950408889634f, k1 = k0 * 0.044715f;
  const f32x2 a = x * fma2(x * x, bc2(k1), bc2(k0));
  const f32x2 e = f32x2{__builtin_amdgcn_exp2f(a[0]), __builtin_amdgcn_exp2f(a[1])} + bc2(1.0f);
  return x * f32x2{__builtin_amdgcn_rcpf(e[0]), __builtin_amdgcn_rcpf(e[1])};
}
DI rsrc_t mkrs(const void* ptr, unsigned bytes) { return __builtin_amdgcn_make_buffer_rsrc((void*)ptr, 0, (int)bytes, 0x00020000); }
template <int AUX> DI v4u ld128(rsrc_t rs, unsigned v, unsigned s) { return __builtin_amdgcn_raw_buffer_load_b128(rs, (int)v, (int)s, AUX); }
template <int AUX> DI v2u ld64(rsrc_t rs, unsigned v, unsigned s) { return __builtin_amdgcn_raw_buffer_load_b64(rs, (int)v, (int)s, AUX); }
template <int AUX> DI void st128(v4u x, rsrc_t rs, unsigned v, unsigned s) { __builtin_amdgcn_raw_buffer_store_b128(x, rs, (int)(v + s), 0, AUX); }
template <int AUX> DI void st64(v2u x, rsrc_t rs, unsigned v, unsigned s) { __builtin_amdgcn_raw_buffer_store_b64(x, rs, (int)v, (int)s, AUX); }
DI f32x4 asf(v4u x) { return __builtin_bit_cast(f32x4, x); }
DI float sum4(f32x4 x) { return (x[0] + x[1]) + (x[2] + x[3]); }
DI unsigned umax3(unsigned a, unsigned b, unsigned c) { unsigned m = a > b ? a : b; return m > c ? m : c; }
DI unsigned macc4(unsigned m, v4u x) { return umax3(umax3(m, x[0], x[1]), x[2], x[3]); }
DI unsigned macc(unsigned m, v4u x) { return umax3(m, x[0], x[2]); }
DI bool any_mark(unsigned m) { return __builtin_amdgcn_ballot_w64(m == 0xFFFFFFFFu) != 0ull; }
#define SB() __builtin_amdgcn_sched_barrier(0)
#define PIN(x) asm volatile("" : "+v"(x))
#define PINA(x) asm volatile("" : "+a"(x))
#define USE(x) asm volatile("" ::"v"(x))
#define RETRY_BEGIN(m) if (__builtin_expect(any_mark(m), 0)) { unsigned sp_ = 0; do { MEMFENCE(); m = 0;
#define RETRY_END(m) } while (any_mark(m) & (++sp_ <= SPIN_CAP)); }
#define MEMFENCE() asm volatile("" ::: "memory")
DI void bar() { asm volatile("s_waitcnt lgkmcnt(0)" ::: "memory"); __builtin_amdgcn_s_barrier(); asm volatile("" ::: "memory"); }

DI f32x2 cmul(f32x2 a, f32x2 b) { return fma2(bc2(a[1]), f32x2{-b[1], b[0]}, bc2(a[0]) * b); }
DI f32x2 cexp_p(float are2, float rev_hi, float rev_lo, float p) {
  const float mag = __builtin_amdgcn_exp2f(are2 * p);
  const float hi = rev_hi * p;
  const float fr = (hi - __builtin_rintf(hi)) + rev_lo * p;
  return f32x2{mag * __builtin_amdgcn_cosf(fr), mag * __builtin_amdgcn_sinf(fr)};
}
DI float exp_acc(float x) {
  const float hi = x * 1.4426950408889634f;
  const float lo = __builtin_fmaf(x, 1.4426950408889634f, -hi) + x * 1.925963033500011e-8f;
  const float e = __builtin_amdgcn_exp2f(hi);
  return __builtin_fmaf(e, lo * 0.6931471805599453f, e);
}
DI unsigned pkh(float a, float b) {
  f16x2 v = {(_Float16)a, (_Float16)b};
  return __builtin_bit_cast(unsigned, v);
}

constexpr int CV_RK = 4160;
constexpr int CV_H = 2 * CV_RK;
constexpr int CV_CH = CV_H + 4608;
constexpr int LDS_A = 0;
constexpr int LDS_U = 2 * CV_CH;
constexpr int LDS_GS = LDS_U + 8192;
constexpr int LDS_TAB = 36864;
constexpr int KP_CH = 13840;
constexpr int LDS_KP = LDS_TAB + 2 * 64 * 528 + 16;
constexpr int LDS_LN = 0, LDS_MR = 8448;
constexpr int LDS_WI = 145472, LDS_WO = LDS_WI + 10240;
constexpr int LDS_X = 65536;
constexpr int LDS_S = LDS_X + 24576;
constexpr int LDS_W = LDS_S + 6144;
constexpr int MEGA_LDS = LDS_W + 65536;
static_assert(LDS_WO + 4608 <= MEGA_LDS && LDS_KP + 2 * KP_CH <= LDS_WI && LDS_S >= 73728, "lds");
static_assert(LDS_GS + 1280 <= LDS_TAB && LDS_KP + 2 * KP_CH <= MEGA_LDS && MEGA_LDS <= 160 * 1024, "lds");
#define FLAG_AT 64
static_assert(FLAG_AT > 45 && FLAG_AT < 78 && FLAG_AT % 5 != 2, "flag position");
#define WF_EARLY 5
#define WO0 12
#define WSP_P0 4
#define HINT4_KS 15
#define ZDEPTH 3
#define GELU_IN 0
#define EPI_W 0
#define MOD_POS 0
#define POLL3 0
#define GELU_PRE 4
#define HINT4_G 310
static_assert(HINT4_G > 4 * 64 + 12, "behind the marker barrier");
#define POLL4_G 45
#define RELABEL 1
#define CKMAP(ck) (RELABEL ? 2 * ((ck) & 3) + ((ck) >> 2) : (ck))
#define POLL_AT 134
#define MOD_MID 1
#define LATE_SLEEP 6
#define SMALL_N 3
#define ZRD 12
#define IN_AUX 0
#define WSP_AUX 0

template <class T> DI T ldsr(unsigned a) { return *(const LAS T*)(size_t)a; }
template <class T> DI void ldsw(unsigned a, T v) { *(LAS T*)(size_t)a = v; }
DI v2u trread(const unsigned char* ptr) {
  typedef short v4i16_t __attribute__((ext_vector_type(4)));
  return __builtin_bit_cast(v2u, __builtin_amdgcn_ds_read_tr16_b64_v4i16((LAS v4i16_t*)ptr));
}
DI v2u trread_a(unsigned a) {
  typedef short v4i16_t __attribute__((ext_vector_type(4)));
  return __builtin_bit_cast(v2u, __builtin_amdgcn_ds_read_tr16_b64_v4i16((LAS v4i16_t*)(size_t)a));
}
struct CvOrder { int qq[36], pp[36]; };
constexpr CvOrder cv_order() {
  CvOrder o{};
  int n = 0;
  for (int m = 0; m < 8; ++m) {
    for (int j = 0; j < m; ++j) if (m + j < 8) { o.qq[n] = m; o.pp[n] = j; ++n; }
    for (int j = 0; j <= m; ++j) if (m + j < 8) { o.qq[n] = j; o.pp[n] = m; ++n; }
  }
  return o;
}
constexpr CvOrder CVO = cv_order();

typedef __attribute__((address_space(1))) unsigned gu32;
DI unsigned tx(unsigned m, unsigned x, unsigned tag) { return __builtin_amdgcn_bitop3_b32(m, x, tag, 0xF6); }
DI unsigned tacc(unsigned m, v4u x, unsigned tag) { return tx(tx(m, x[1], tag), x[3], tag); }
DI bool any_nz(unsigned m) { return __builtin_amdgcn_ballot_w64(m != 0u) != 0ull; }
DI void hint_add(unsigned char* ws, int line) { (void)__hip_atomic_fetch_add((gu32*)(ws + WS_CNT + line * 128), 1u, __ATOMIC_RELAXED, __HIP_MEMORY_SCOPE_AGENT); }
DI void hint_wait(rsrc_t rws, unsigned voff, unsigned soff, unsigned want) {
  unsigned sp = 0;
  for (;;) {
    MEMFENCE();
    const unsigned v = __builtin_amdgcn_raw_buffer_load_b32(rws, (int)voff, (int)soff, SC1);
    if (!((__builtin_amdgcn_ballot_w64(v < want) != 0ull) & (++sp <= SPIN_CAP))) break;
    __builtin_amdgcn_s_sleep(OPT_E);
  }
  MEMFENCE();
}

extern "C" __global__ void __launch_bounds__(256, 1) adaln_mega(Params p) {
  extern __shared__ __attribute__((aligned(16))) unsigned char smem[];
  const int tid = threadIdx.x, lane = tid & 63, w = __builtin_amdgcn_readfirstlane(tid >> 6);
  const int b = blockIdx.x, xcd = b & 7, idx = b >> 3;
  const int pb = xcd * 32 + idx;
  const unsigned l16 = lane * 16;
  STAMP_DECL;
  STAMP(1, 0);
  v2u codepf;
  unsigned long long cbase;
  {
    unsigned long long pc;
    int rel_m, rel_e;
    asm volatile("s_getpc_b64 %0\n\ts_mov_b32 %1, adaln_mega@rel32@lo+4\n\ts_mov_b32 %2, " CODE_END "@rel32@lo+12" : "=s"(pc), "=s"(rel_m), "=s"(rel_e));
    const rsrc_t rcode = mkrs((const void*)(pc + (long long)rel_m), rel_e > rel_m ? (unsigned)(rel_e - rel_m) & ~15u : 0u);
    codepf = ld64<0>(rcode, tid * 8, idx * 2048);
    cbase = pc + (long long)rel_m;
  }
  SB();
  const rsrc_t ru = mkrs(p.u, L * D * 2);
  v4u xr[2];
  xr[0] = ld128<IN_AUX>(ru, l16, (pb * 8 + w * 2) * 1024);
  xr[1] = ld128<IN_AUX>(ru, l16 + 1024, (pb * 8 + w * 2) * 1024);
  const int mkg = b & 3, mcg = b >> 2;
  const unsigned cq = p.c[mkg * 128 + (tid & 3) * 32 + (tid >> 3)];
  const unsigned epoch = *(const unsigned*)(p.ws + WS_EPOCH);
  const rsrc_t rws = mkrs(p.ws, WS_END);
  __builtin_amdgcn_raw_buffer_store_b32(0u, rws, tid < 2 ? tid * 32768u : OOB, WS_CNT + b * 128, SC1);
  SB();
  const int cch = w >> 1, eh = w & 1, ns = tid & 127;
  const unsigned q_cre = p.cre[pb * 256 + tid], q_cim = p.cim[pb * 256 + tid], q_lar = p.lar[pb * 256 + tid], q_aim = p.aim[pb * 256 + tid];
  const unsigned q_ldt = p.ldt[pb * 2 + cch], q_dsk = p.dsk[pb * 2 + cch];
  v4u xm[4];
#define LOAD_XM() { \
  const rsrc_t rmod = mkrs(p.wmod, D * 3072 * 2); \
_Pragma("unroll") \
  for (int i = 0; i < 4; ++i) xm[i] = ld128<IN_AUX>(rmod, (tid >> 3) * 6144 + (tid & 7) * 16, (mkg * 128 + i * 32) * 6144 + mcg * 128 + (b < 192 ? 0u : OOB)); }
  SB();
  LOAD_XM();
  unsigned tw[TLBW];
#pragma unroll
  for (int i = 0; i < TLBW; ++i) asm volatile("s_load_dword %0, %1, %2" : "=s"(tw[i]) : "s"(cbase), "i"((i + 1) * 4096 - 2048 * (TLBW > 20) * (i + 1)));
  SB();
  STAMP(1, 24);
  const int c = lane & 15, q = lane >> 4;
  const unsigned lds0 = (unsigned)(size_t)(LAS unsigned char*)smem;
  unsigned pos4[9];
  {
    const float f4 = (float)(8188 - tid * 32);
#pragma unroll
    for (int j = 0; j < 10; j += 2) {
      const unsigned pp = pk2(bc2(f4) - f32x2{4.f * j, 4.f * j + 4.f});
      const int p0 = (int)bflo(pp), p1 = (int)bfhi(pp);
      pos4[j] = p0 + ((p0 >> 5) << 2) + lds0 + LDS_KP;
      PIN(pos4[j]);
      if (j + 1 < 9) { pos4[j + 1] = p1 + ((p1 >> 5) << 2) + lds0 + LDS_KP; PIN(pos4[j + 1]); }
    }
  }
  unsigned co[2][4];
  {
    const unsigned b0 = (eh ? 1152 + 576 * q : 144 * (q >> 1) + 4 * (q & 1)) * 4 + 36 * c + lds0 + LDS_KP + cch * KP_CH;
    const unsigned b1 = (q < 2 ? 288 + 144 * q : 568 + 4 * q) * 4 + 36 * c + lds0 + LDS_KP + cch * KP_CH;
    const unsigned s0 = eh ? 576 : 4, s1 = q < 2 ? 8 : 576;
#pragma unroll
    for (int r = 0; r < 4; ++r) { co[0][r] = b0 + r * s0; co[1][r] = b1 + r * s1; PIN(co[0][r]); PIN(co[1][r]); }
  }
  *(unsigned*)(smem + LDS_KP - 16 + cch * KP_CH + (lane & 3) * 4) = 0u;
  unsigned a_pad, a_wi, a_wo, a_tab, gm_off, gc_off;
  a_pad = lds0 + cch * CV_CH + (lane < 32 ? CV_H + l16 : ((lane >> 1) & 1) * CV_RK + 4096 + (lane & 1) * 16);
  a_wi = lds0 + LDS_WI + (w * 32 + 8 * q + ((lane >> 2) & 3)) * 80 + (lane & 3) * 16;
  a_wo = lds0 + LDS_WO + (8 * q + ((lane >> 2) & 3)) * 144 + (w >> 1) * 64 + (lane & 3) * 16 + (w & 1) * 8;
  a_tab = lds0 + LDS_TAB + cch * (64 * 528) + c * 528 + q * 16;
  gm_off = ((4 - cch) * 512 + mkg * 128 + ns) * 8;
  gc_off = (((lane & 7) > 5 ? 0 : ((lane & 7) >> 1)) * 512 + pb * 2 + (lane & 1)) * 8;
  PIN(a_pad); PIN(a_wi); PIN(a_wo); PIN(a_tab); PIN(gm_off); PIN(gc_off);
  unsigned ab[4], bb, dv[4], rdo4[4], xoff;
#pragma unroll
  for (int i = 0; i < 4; ++i) {
    ab[i] = lds0 + cch * CV_CH + (1 - (c & 1)) * CV_RK + 2 * (2030 - 2 * (c >> 1) + 8 * q) - eh * 256 - 192 - 512 - 1024 * i;
    dv[i] = (lane >> 3) * 4096 + (((lane & 7) ^ ((lane >> 4) & 1) ^ (i << 1)) * 16) + i * (32768 - 1024);
    rdo4[i] = lds0 + LDS_W + w * 16384 + ((perm((i & 1) * 2 ^ (2 * (w & 1)), c) * 128 + ((q ^ (c >> 1)) * 16)) ^ ((i >> 1) * 64));
    PIN(ab[i]); PIN(dv[i]); PIN(rdo4[i]);
  }
  bb = lds0 + cch * CV_CH + CV_H + 2 * (256 - 16 + 16 * c + 8 * q) - eh * 256 - 192;
  xoff = WS_X1T + (pb * 2 + cch) * 4096 + eh * 2048 + ((c >> 1) * 32 + ((q & 1) * 4 + 2 * (c & 1) + (q >> 1)) * 4) * 2;
  PIN(bb); PIN(xoff);
  SB();
  STAMP(1, 26);
  {
    *(v4u*)(smem + LDS_LN + (w * 2) * 1040 + l16) = xr[0];
    *(v4u*)(smem + LDS_LN + (w * 2 + 1) * 1040 + l16) = xr[1];
    float sm[2], sq[2];
#pragma unroll
    for (int i = 0; i < 2; ++i) {
      sm[i] = dot2(xr[i][0], ONES, dot2(xr[i][1], ONES, dot2(xr[i][2], ONES, dot2(xr[i][3], ONES, 0.f))));
      sq[i] = dot2(xr[i][0], xr[i][0], dot2(xr[i][1], xr[i][1], dot2(xr[i][2], xr[i][2], dot2(xr[i][3], xr[i][3], 0.f))));
    }
    const float tv = row16_sum(xrow_sum4(sm[0], sm[1], sq[0], sq[1]));
    auto sw = __builtin_amdgcn_permlane32_swap(__float_as_uint(tv), __float_as_uint(tv), false, false);
    const float m = __uint_as_float(sw[0]) * (1.0f / D);
    const float r = __builtin_amdgcn_rsqf(fmaxf(__uint_as_float(sw[1]) * (1.0f / D) - m * m, 0.f) + EPS);
    ((float*)(smem + LDS_GS + 1088))[(q >> 1) * 8 + w * 2 + (q & 1)] = (q >> 1) ? -m * r : r;
  }
  const unsigned tag = epoch * 2654435761u + 0x9E3779B9u;
  STAMP(1, 28);
  asm volatile("s_waitcnt vmcnt(%0)" ::"i"(MOD_POS == 0 ? 8 : 4) : "memory");
  bar();
#pragma unroll
  for (int i = 0; i < TLBW; ++i) asm volatile("" ::"s"(tw[i]));
  STAMP(1, 16);
  {
    unsigned ua[4][2];
#pragma unroll
    for (int i = 0; i < 4; ++i) {
      const unsigned char* src = smem + LDS_LN + (tid & 3) * 2080 + (i * 64 + (tid >> 2)) * 4;
      ua[i][0] = *(const unsigned*)src; ua[i][1] = *(const unsigned*)(src + 1040);
    }
    const unsigned lnv = ((const unsigned*)(smem + LDS_GS + 1088))[c];
    SB();
#pragma unroll
    for (int i = 0; i < 4; ++i) st128<SC1>(v4u{ua[i][0], tag, ua[i][1], tag}, rws, (tid >> 2) * 16384 + (tid & 3) * 16, WS_UG + i * 64 * 16384 + pb * 64);
    st64<SC1>(v2u{lnv, tag}, rws, tid < 16 ? (c >> 3) * 16384 + (c & 7) * 8 : OOB, WS_SR + pb * 64);
  }
  STAMP(1, 17);
  SB();
  const unsigned wp_oob = b < 176 ? 0u : OOB;
  const int wpr = b >> 2, wkg = b & 3;
  v4u xi[2], xo;
  {
    const rsrc_t rwin = mkrs(p.win, D * FF * 2), rwout = mkrs(p.wout, FH * D * 2);
    const int hf = wpr >= 22, pp = wpr - 22 * hf;
    const unsigned v = ((tid >> 2) * (FF * 2) + (tid & 3) * 16) | ((pp == 21 ? tid & 2 : 0) << 29);
#pragma unroll
    for (int i = 0; i < 2; ++i) xi[i] = ld128<IN_AUX>(rwin, v, (wkg * 128 + i * 64) * (FF * 2) + (hf * FH + pp * 32) * 2 + wp_oob);
    xo = ld128<IN_AUX>(rwout, (tid >> 3) * 1024 + (tid & 7) * 16, idx * 32 * 1024 + xcd * 128);
  }
  SB();
  const int d0 = pb * 2, dk = d0 + cch;
  const int mt = (xcd >> 1) * 8 + (idx & 7), nt = (xcd & 1) * 4 + (idx >> 3);
  const int big = (nt + 2) >> 3;
  const int np = 3 - big, pr0 = 3 * nt - big * (nt - 6);
  const int kh = w & 1, uh = w >> 1;
  const int uo0 = pr0 * 2 + uh * (4 - 2 * big), ut0 = pr0 * 2 + (uh ^ 1) * (4 - 2 * big), uo2 = pr0 * 2 + 2 + uh, ut2 = pr0 * 2 + 2 + (uh ^ 1);
#define UO(k) ((k) == 2 ? uo2 : uo0 + (k))
#define UT(k) ((k) == 2 ? ut2 : ut0 + (k))
  const unsigned oob3 = (unsigned)big << 30;
  const int kr = 2 * (idx >> 3) + (xcd & 1) + (OPT_B == 0 ? (idx & 7) : OPT_B == 1 ? 4 * (idx & 1) : OPT_B == 2 ? 2 * (idx & 3) : 0);
  const rsrc_t rwsp = mkrs(p.wsp, L * L * 2);
  const unsigned wp_fr = (wpr * 2 * 16 + wkg * 4 + w) * 1024 + wp_oob;
  v4u wr[1][8];
  const unsigned wsp_v = (lane >> 3) * 4096 + (lane & 7) * 16, wsp_s = mt * 64 * 4096 + w * 1024;
#define LOAD_WSP1(ck, i) wr[ck][i] = ld128<WSP_AUX>(rwsp, wsp_v + (((ck) + kr) & 7) * 128, wsp_s + (i) * 8 * 4096)
#define LOAD_WSPN(n) LOAD_WSP1((n) >> 3, (n) & 7)
  const v4u mk = {~0u, ~0u, ~0u, ~0u};
  unsigned bspq[2];
#pragma unroll
  for (int t = 0; t < 2; ++t) bspq[t] = p.bsp[mt * 64 + perm(2 * kh + t, c)];
  const unsigned tag_s = __builtin_amdgcn_readfirstlane(tag);
  {
    const float dt = rbf(exp_acc(bf2f(q_ldt)));
    const float cre = bf2f(q_cre) * dt, cim = bf2f(q_cim) * dt;
    const float are2 = -rbf(exp_acc(bf2f(q_lar))) * dt * 1.4426950408889634f;
    const float aim = bf2f(q_aim) * dt;
    const float rev_hi = aim * 0.15915494309189535f;
    const float rev_lo = __builtin_fmaf(aim, 0.15915494309189535f, -rev_hi) + aim * 6.4206383e-9f;
    int ex = __builtin_amdgcn_frexp_expf(xrow_max(row16_max(fmaxf(fabsf(cre), fabsf(cim))))) + 1;
    ex = ex < -100 ? -100 : ex;
    const float scale = __builtin_amdgcn_ldexpf(1.f, -ex);
    ((float*)(smem + LDS_GS + 1024))[w] = __builtin_amdgcn_ldexpf(2.f, ex);
    const f32x2 c0 = {cre * scale, cim * scale};
    const f32x2 e1 = cexp_p(are2, rev_hi, rev_lo, 1.f), e8 = cexp_p(are2, rev_hi, rev_lo, 8.f), e64 = cexp_p(are2, rev_hi, rev_lo, 64.f);
    unsigned char* tn = smem + LDS_TAB + cch * (64 * 528) + ns * 4;
#define TROW(r, z) *(unsigned*)(tn + (r) * 528) = pkh((z)[0], -(z)[1])
    SB();
    const f32x2 e128 = cmul(e64, e64), e2 = cmul(e1, e1), e256 = cmul(e128, e128), e4 = cmul(e2, e2), e512 = cmul(e256, e256);
    f32x2 f0 = {1.f, 0.f}, f1 = e64;
    f32x2 ca = cmul(c0, e512), cb = cmul(ca, e512);
#pragma unroll
    for (int j = 0; j < 8; ++j) {
      *(unsigned*)(tn + (48 + j) * 528) = pkh(f0[0], f0[1]);
      *(unsigned*)(tn + (56 + j) * 528) = pkh(f1[0], f1[1]);
      TROW(32 + j, cb);
      f0 = j == 0 ? e8 : cmul(f0, e8); f1 = cmul(f1, e8); cb = cmul(cb, e128);
      if (j < 4) {
        const f32x2 z = cmul(ca, e4);
        TROW(24 + j, ca); TROW(28 + j, z);
        ca = cmul(ca, e128);
      }
      if (j == 3 || j == 7) {
        PIN(f0); PIN(f1); PIN(cb);
        if (WSP_P0 >= 4) { LOAD_WSPN(j >> 2 << 1); LOAD_WSPN((j >> 2 << 1) + 1); }
        SB();
      }
    }
    TROW(40, cb);
    SB();
  {
    const float cv = bf2f(cq);
    const float sl = rbf(cv * __builtin_amdgcn_rcpf(1.0f + __builtin_amdgcn_exp2f(cv * -1.4426950408889634f)));
    const float sc[4] = {dpp<0x00>(sl), dpp<0x55>(sl), dpp<0xAA>(sl), dpp<0xFF>(sl)};
    f32x2 a[4];
#pragma unroll
    for (int i = 0; i < 4; ++i)
#pragma unroll
      for (int j = 0; j < 4; ++j) a[j] = i == 0 ? unpk(xm[i][j]) * bc2(sc[i]) : fma2(unpk(xm[i][j]), bc2(sc[i]), a[j]);
    float* red = (float*)(smem + LDS_MR) + (tid >> 3) * 68 + (tid & 7) * 8;
    *(f32x4*)red = f32x4{a[0][0], a[0][1], a[1][0], a[1][1]};
    *(f32x4*)(red + 4) = f32x4{a[2][0], a[2][1], a[3][0], a[3][1]};
  }
    bar();
    float ms8[8];
#pragma unroll
    for (int i = 0; i < 8; ++i) ms8[i] = ((const float*)(smem + LDS_MR))[(q * 8 + i) * 68 + w * 16 + c];
    SB();
    f32x2 t1[8];
    t1[1] = e1; t1[2] = e2; t1[4] = e4;
    t1[3] = cmul(e2, e1); t1[5] = cmul(e4, e1); t1[6] = cmul(e2, e4); t1[7] = cmul(t1[6], e1);
    const f32x2 c1 = cmul(c0, e128), c2 = cmul(c0, e256), c3 = cmul(c2, e128);
    TROW(0, c0); TROW(8, c1); TROW(16, c2); TROW(20, c3);
    {
    const float s = xrow_sum(((ms8[0] + ms8[1]) + (ms8[2] + ms8[3])) + ((ms8[4] + ms8[5]) + (ms8[6] + ms8[7])));
    st64<SC1>(v2u{__float_as_uint(s), tag}, rws, q == 0 ? c * 8 : OOB, WS_MODG + (mkg * 3072 + mcg * 64 + w * 16) * 8 + (b < 192 ? 0u : OOB));
    }
    SB();
#pragma unroll
    for (int v = 1; v < 8; ++v) {
      const f32x2 z0 = cmul(t1[v], c0), z1 = cmul(t1[v], c1);
      TROW(v, z0); TROW(8 + v, z1);
      if ((v & 1) == 0) {
        const f32x2 z2 = cmul(t1[v], c2), z3 = cmul(t1[v], c3);
        TROW(16 + v / 2, z2); TROW(20 + v / 2, z3);
        if (v < 6 && WSP_P0 >= 8) { LOAD_WSPN(v + 2); LOAD_WSPN(v + 3); }
        SB();
      }
    }
  }
  STAMP(1, 27);
#pragma unroll
  for (int i = 0; i < 2; ++i) *(v4u*)(smem + LDS_WI + (i * 64 + (tid >> 2)) * 80 + (tid & 3) * 16) = xi[i];
  *(v4u*)(smem + LDS_WO + (tid >> 3) * 144 + (tid & 7) * 16) = xo;
  STAMP(1, 29);
  bar();
  STAMP(1, 18);
  SB();
  v4u gr[4], gb_[4];
  v2u gm[4], gc[4];
#define LOAD_GS() _Pragma("unroll") for (int i_ = 0; i_ < 4; ++i_) { gr[i_] = ld128<SC1>(rws, tid * 16, WS_SR + i_ * 4096); gb_[i_] = ld128<SC1>(rws, tid * 16, WS_SB + i_ * 4096); }
#define LOAD_GM() _Pragma("unroll") for (int i_ = 0; i_ < 4; ++i_) { gm[i_] = ld64<SC1>(rws, gm_off, WS_MODG + i_ * 24576); gc[i_] = ld64<SC1>(rws, gc_off, WS_MODG + i_ * 24576); }
  v4u ug[4], wt[2];
#define LOAD_UG() _Pragma("unroll") for (int i_ = 0; i_ < 4; ++i_) ug[i_] = ld128<SC1>(rws, tid * 16, WS_UG + pb * 16384 + i_ * 4096)
  {
    v4u fa[2][8], fb[8];
    const unsigned ta = a_tab + eh * (32 * 528);
#pragma unroll
    for (int ks = 0; ks < 3; ++ks) { fb[ks] = ldsr<v4u>(a_tab + 48 * 528 + ks * 64); fa[0][ks] = ldsr<v4u>(ta + ks * 64); fa[1][ks] = ldsr<v4u>(a_tab + 16 * 528 + ks * 64); }
    SB();
    LOAD_UG();
    SB();
    f32x4 kk[2][2];
    f32x2 us;
    v2u wh_[6];
#pragma unroll
    for (int i = 0; i < 4; ++i) kk[i >> 1][i & 1] = f32x4{0.f, 0.f, 0.f, 0.f};
#pragma unroll
    for (int ks = 0; ks < 8; ++ks) {
      if (ks < 5) { fb[3 + ks] = ldsr<v4u>(a_tab + 48 * 528 + (3 + ks) * 64); fa[0][3 + ks] = ldsr<v4u>(ta + (3 + ks) * 64); fa[1][3 + ks] = ldsr<v4u>(a_tab + 16 * 528 + (3 + ks) * 64); }
      if (ks == 5) { us = *(const f32x2*)(smem + LDS_GS + 1024 + cch * 8); wh_[4] = trread_a(a_wo); wh_[5] = trread_a(a_wo + 576); }
      if (ks == 6) { wh_[0] = trread_a(a_wi); wh_[1] = trread_a(a_wi + 320); wh_[2] = trread_a(a_wi + 8); wh_[3] = trread_a(a_wi + 328); }
      kk[0][ks >> 2] = mfma_h(fa[0][ks], fb[ks], kk[0][ks >> 2]); kk[1][ks >> 2] = mfma_h(fa[1][ks], fb[ks], kk[1][ks >> 2]);
      SB();
    }
    STAMP(1, 37);
    wt[0] = v4u{wh_[0][0], wh_[0][1], wh_[1][0], wh_[1][1]};
    wt[1] = v4u{wh_[2][0], wh_[2][1], wh_[3][0], wh_[3][1]};
    st128<SC1>(v4u{wh_[4][0], wh_[4][1], wh_[5][0], wh_[5][1]}, rws, l16, WS_WOT + ((xcd * 4 + w) * 22 + idx) * 1024 + wp_oob);
    LOAD_GS();
    SB();
#pragma unroll
    for (int i = 0; i < 4; ++i) PINA(kk[i >> 1][i & 1]);
    f32x4 k0 = kk[0][0] * us[0] + kk[0][1] * us[1];
    const f32x4 k1 = kk[1][0] * us[0] + kk[1][1] * us[1];
    k0[0] += ns == 0 ? bf2f(q_dsk) : 0.f;
#pragma unroll
    for (int r = 0; r < 4; ++r) { ldsw<float>(co[0][r], k0[r]); ldsw<float>(co[1][r], k1[r]); }
  }
  STAMP(1, 31);
  bar();
  STAMP(1, 19);
  {
    float v[2][9];
#pragma unroll
    for (int j = 0; j < 9; ++j) v[0][j] = ldsr<float>(pos4[j]);
    LOAD_GM();
#pragma unroll
    for (int j = 0; j < 9; ++j) v[1][j] = ldsr<float>(pos4[j] + KP_CH);
    ldsw<v4u>(a_pad, v4u{0u, 0u, 0u, 0u});
    SB();
    STAMP(1, 36);
#pragma unroll
    for (int ch = 0; ch < 2; ++ch) {
      unsigned char* rk = smem + ch * CV_CH + tid * 16;
#pragma unroll
      for (int sft = 0; sft < 2; ++sft)
        *(v4u*)(rk + sft * CV_RK) = v4u{pk2(v[ch][sft], v[ch][sft + 1]), pk2(v[ch][sft + 2], v[ch][sft + 3]), pk2(v[ch][sft + 4], v[ch][sft + 5]), pk2(v[ch][sft + 6], v[ch][sft + 7])};
    }
#pragma unroll
    for (int i = WSP_P0; i < 8; ++i) LOAD_WSPN(i);
    st128<SC1>(mk, rws, l16, WS_X1T + dk * 4096 + eh * 2048);
    st128<SC1>(mk, rws, l16 + 1024, WS_X1T + dk * 4096 + eh * 2048);
    __builtin_amdgcn_raw_buffer_store_b32(~0u, rws, lane * 4, WS_ST2 + ((mt * 2 + kh) * 16 + nt * 2 + uh) * 256, SC1);
    SB();
    STAMP(1, 32);
    unsigned m = 0;
#pragma unroll
    for (int i = 0; i < 4; ++i) { m = tacc(m, ug[i], tag); m = tacc(m, gr[i], tag); m = tacc(m, gb_[i], tag); }
    if (__builtin_expect(any_nz(m), 0)) {
      unsigned sp = 0;
      do {
        MEMFENCE(); LOAD_UG(); LOAD_GS(); m = 0;
#pragma unroll
        for (int i = 0; i < 4; ++i) { m = tacc(m, ug[i], tag); m = tacc(m, gr[i], tag); m = tacc(m, gb_[i], tag); }
      } while (any_nz(m) & (++sp <= SPIN_CAP));
    }
    f32x2 nn[4][2];
#pragma unroll
    for (int i = 0; i < 4; ++i) {
      nn[i][0] = fma2(unpk(ug[i][0]), bc2(__uint_as_float(gr[i][0])), bc2(__uint_as_float(gb_[i][0])));
      nn[i][1] = fma2(unpk(ug[i][2]), bc2(__uint_as_float(gr[i][2])), bc2(__uint_as_float(gb_[i][2])));
      PIN(nn[i][0]); PIN(nn[i][1]);
      *(unsigned*)(smem + LDS_U + i * 1024 + tid * 4) = __builtin_amdgcn_perm(ug[i][2], ug[i][0], 0x05040100u);
      *(unsigned*)(smem + LDS_U + 4096 + i * 1024 + tid * 4) = __builtin_amdgcn_perm(ug[i][2], ug[i][0], 0x07060302u);
    }
    SB();
    STAMP(1, 33);
    m = 0;
#pragma unroll
    for (int i = 0; i < 4; ++i) m = tx(tx(m, gm[i][1], tag), gc[i][1], tag);
    if (__builtin_expect(any_nz(m), 0)) {
      unsigned sp = 0;
      do {
        __builtin_amdgcn_s_sleep(3);
        MEMFENCE(); LOAD_GM(); m = 0;
#pragma unroll
        for (int i = 0; i < 4; ++i) m = tx(tx(m, gm[i][1], tag), gc[i][1], tag);
      } while (any_nz(m) & (++sp <= SPIN_CAP));
    }
    const float mv = rbf((__uint_as_float(gm[0][0]) + __uint_as_float(gm[1][0])) + (__uint_as_float(gm[2][0]) + __uint_as_float(gm[3][0])));
    *(u16*)(smem + LDS_GS + tid * 2) = (u16)pk2(mv + (cch ? 0.f : 1.0f), 0.f);
    const float cv = rbf((__uint_as_float(gc[0][0]) + __uint_as_float(gc[1][0])) + (__uint_as_float(gc[2][0]) + __uint_as_float(gc[3][0])));
    const float gav = dpp<0x104>(cv), gav2 = dpp<0x102>(cv);
    const float hbv = cv * gav, hsv = rbf(1.0f + cv) * gav2;
    const f32x2 hb = {__builtin_bit_cast(float, __builtin_amdgcn_readlane(__builtin_bit_cast(int, hbv), 0)), __builtin_bit_cast(float, __builtin_amdgcn_readlane(__builtin_bit_cast(int, hbv), 1))};
    const f32x2 hs = {__builtin_bit_cast(float, __builtin_amdgcn_readlane(__builtin_bit_cast(int, hsv), 2)), __builtin_bit_cast(float, __builtin_amdgcn_readlane(__builtin_bit_cast(int, hsv), 3))};
    SB();
    bspq[0] |= bspq[1] << 16;
    PIN(bspq[0]);
    STAMP(1, 38);
#pragma unroll
    for (int i = 0; i < 4; ++i) {
      const f32x2 h0 = fma2(nn[i][0], hs, hb), h1 = fma2(nn[i][1], hs, hb);
      *(unsigned*)(smem + CV_H + 512 + i * 1024 + tid * 4) = pk2(h0[0], h1[0]);
      *(unsigned*)(smem + CV_CH + CV_H + 512 + i * 1024 + tid * 4) = pk2(h0[1], h1[1]);
    }
  }
  asm volatile("" ::"v"(codepf));
  STAMP(1, 39);
  bar();
  STAMP(1, 1);
#define CV_A(ee, k) ({ const unsigned a_ = ab[(k) >> 1] + 192 - 64 * (ee) + 512 * (1 - ((k) & 1)); v4u{ldsr<unsigned>(a_), ldsr<unsigned>(a_ + 4), ldsr<unsigned>(a_ + 8), ldsr<unsigned>(a_ + 12)}; })
#define CV_B(ee, k) ldsr<v4u>(bb + 192 - 64 * (ee) + 512 * (k))
  const unsigned slotA = LDS_W + w * 16384, slotB = slotA + 8192;
  v4u wq[8][8];
#define READ_W(dl, f) ldsr<v4u>(rdo4[(((f) >> 1) & 1) + ((f) >> 2) * 2] + (dl) + ((f) & 1) * 512)
  unsigned dso;
#define DMA_W(ck, slot, i) do { \
    if (((i) & 3) == 0) asm volatile("s_add_u32 m0, %1, %2\n\ts_add_u32 %0, %3, %4\n\tbuffer_load_dwordx4 %5, %6, %0 offen lds" \
                 : "=&s"(dso) : "s"(lds0 + (slot)), "i"((i) * 1024), "s"(wsp_s + ((CKMAP(ck) + kr) & 7) * 128), "i"((i) * 32768), "v"(dv[0]), "s"(rwsp) : "scc", "memory"); \
    else asm volatile("buffer_load_dwordx4 %0, %1, %2 offen offset:%3 lds" :: "v"(dv[(i) & 3]), "s"(rwsp), "s"(dso), "i"(((i) & 3) * 1024) : "memory"); } while (0)
  auto dma_sel = [&](auto CK, unsigned slot, int i) __attribute__((always_inline)) {
    constexpr int ck = decltype(CK)::value;
    switch (i) {
      case 0: DMA_W(ck, slot, 0); break; case 1: DMA_W(ck, slot, 1); break; case 2: DMA_W(ck, slot, 2); break; case 3: DMA_W(ck, slot, 3); break;
      case 4: DMA_W(ck, slot, 4); break; case 5: DMA_W(ck, slot, 5); break; case 6: DMA_W(ck, slot, 6); break; default: DMA_W(ck, slot, 7); break;
    }
  };
  v4u cA[2][8], cB[2][8];
  float gs[8];
  v4u gbf, sbf;
  v4u ones4 = {ONES, ONES, ONES, ONES};
  PIN(ones4);
#pragma unroll
  for (int k = 0; k < 2; ++k) { cA[0][k] = CV_A(0, k); cB[0][k] = CV_B(0, k); }
  {
    gbf = *(const v4u*)(smem + LDS_GS + w * 64 + q * 16);
    sbf = *(const v4u*)(smem + LDS_GS + 256 + w * 64 + q * 16);
  }
  SB();
  f32x4 cacc[8];
  v2u ue[4];
  float uf[16];
  unsigned flagv;
  unsigned wro = lds0 + slotA + (lane >> 3) * 128 + (((lane & 7) ^ ((lane >> 4) & 1)) * 16);
  PIN(wro);
  {
    f32x4 cb4[4];
    v4u wo_[2];
    float wl, wh;
    f32x2 cp[4];
#pragma unroll
    for (int t = 0; t < 8; ++t) cacc[t] = f32x4{0.f, 0.f, 0.f, 0.f};
    sfor<144>([&](auto G) __attribute__((always_inline)) {
      constexpr int g = decltype(G)::value, ee = g / 36, j = g % 36;
      if constexpr (ee == 0 && j < 6) { cA[0][2 + j] = CV_A(0, 2 + j); cB[0][2 + j] = CV_B(0, 2 + j); }
      cacc[CVO.pp[j] + CVO.qq[j]] = mfma(cA[ee & 1][CVO.qq[j]], cB[ee & 1][CVO.pp[j]], cacc[CVO.pp[j] + CVO.qq[j]]);
      constexpr int jn = ee == 0 ? j - 20 : (j & 1) ? j >> 1 : -1;
      if constexpr (ee < 3 && jn >= 0 && jn < 16) {
        if constexpr (jn & 1) cB[(ee + 1) & 1][jn >> 1] = CV_B(ee + 1, jn >> 1);
        else cA[(ee + 1) & 1][jn >> 1] = CV_A(ee + 1, jn >> 1);
      }
      if constexpr (ee == 3 && j < 4) ue[j] = *(const v2u*)(smem + LDS_U + cch * 4096 + (eh * 4 + j) * 512 + (c * 16 + q * 4) * 2);
      if constexpr (g >= 6 && g < 30) {
        constexpr int i = (g - 6) / 12, jj = ((g - 6) / 3) & 3, ph = (g - 6) % 3;
        if constexpr (ph == 0) { if constexpr (i == 0) gs[2 * jj] = bflo(gbf[jj]); wl = bflo(wt[i][jj]) * gs[2 * jj]; PIN(wl); }
        if constexpr (ph == 1) { if constexpr (i == 0) gs[2 * jj + 1] = bfhi(gbf[jj]); wh = bfhi(wt[i][jj]) * gs[2 * jj + 1]; PIN(wh); }
        if constexpr (ph == 2) {
          wo_[i][jj] = pk2(wl, wh);
          if constexpr (jj == 3) st128<SC1>(wo_[i], rws, l16 + i * 16384, WS_WP + wp_fr);
          else PIN(wo_[i][jj]);
        }
      }
      if constexpr (g == 4 || g == 5) cb4[2 * (g - 4) + 1] = mfma(sbf, wt[g - 4], f32x4{0.f, 0.f, 0.f, 0.f});
      if constexpr (g == 19 || g == 31) cb4[g == 19 ? 0 : 2] = mfma(ones4, wo_[g == 31], f32x4{0.f, 0.f, 0.f, 0.f});
      if constexpr (g == 33) *(f32x4*)(smem + LDS_S + 4096 + (w * 16 + c) * 16) = f32x4{cb4[0][0], cb4[1][0], cb4[2][0], cb4[3][0]};
      if constexpr (g == 38) bar();
      if constexpr (g == 39) {
#pragma unroll
        for (int k = 0; k < 4; ++k) cp[k] = *(const f32x2*)(smem + LDS_S + 4096 + (k * 16 + c) * 16 + (q & 1) * 8);
      }
      if constexpr (g == 45) {
        float s0 = cp[0][0] + cp[1][0], s1 = cp[2][0] + cp[3][0];
        PIN(s0); PIN(s1);
        s0 += s1;
        PIN(s0);
        float s2 = cp[0][1] + cp[1][1], s3 = cp[2][1] + cp[3][1];
        PIN(s2); PIN(s3);
        st64<SC1>(v2u{__float_as_uint(s0), __float_as_uint(s2 + s3)}, rws, tid < 32 ? q * 512 + c * 8 : OOB, WS_CSB + wpr * 1024 + wkg * 128 + wp_oob);
      }
      if constexpr (g == 36) STAMP(1, 40);
      if constexpr (g == 72) STAMP(1, 41);
      if constexpr (g == FLAG_AT) {
        STAMP(1, 42);
        asm volatile("s_waitcnt vmcnt(%0)" ::"i"((FLAG_AT - 32 + 4) / 5 + 1) : "memory");
        __builtin_amdgcn_s_barrier();
        if (tid < 8) hint_add(p.ws, 256 + tid * 16 + (idx & 15));
        STAMP(1, 43);
      }
      if constexpr (g % 5 == 2 && g < 80) DMA_W(1 + g / 40, g < 40 ? slotA : slotB, (g / 5) & 7);
      if constexpr (g == 88) asm volatile("s_waitcnt vmcnt(8)" ::: "memory");
      if constexpr (g == 124) asm volatile("s_waitcnt vmcnt(0)" ::: "memory");
      if constexpr (g == POLL_AT) flagv = __builtin_amdgcn_raw_buffer_load_b32(rws, w == 0 ? c * 128 : OOB, WS_CNT + (256 + xcd * 16) * 128, SC1);
      if constexpr (g >= 88 && g < 104 && (g & 1) == 0) wq[1][(g - 88) >> 1] = READ_W(0, (g - 88) >> 1);
      if constexpr (g >= 108 && g < 140 && (g & 3) == 0) ldsw<v4u>((wro ^ ((((g - 108) >> 2) & 3) * 32)) + ((g - 108) >> 2) * 1024, wr[0][(g - 108) >> 2]);
      if constexpr (g >= 125 && g < 141 && (g & 1) == 1) wq[2][(g - 125) >> 1] = READ_W(8192, (g - 125) >> 1);
      if constexpr (g >= 128 && g < 144) { uf[g - 128] = ((g & 1) ? bfhi : bflo)(ue[(g - 128) >> 2][((g - 128) >> 1) & 1]); PIN(uf[g - 128]); }
      SB();
    });
  }
  STAMP(1, 2);
  unsigned late;
  {
    f32x4* red = (f32x4*)(smem + LDS_X) + w * 256 + lane;
    f32x4 keep[4];
    if (eh) {
#pragma unroll
      for (int t = 0; t < 4; ++t) red[t * 64] = cacc[t];
      SB();
#pragma unroll
      for (int t = 0; t < 4; ++t) { PINA(cacc[4 + t]); keep[t] = cacc[4 + t]; PIN(keep[t]); }
    } else {
#pragma unroll
      for (int t = 0; t < 4; ++t) red[t * 64] = cacc[4 + t];
      SB();
#pragma unroll
      for (int t = 0; t < 4; ++t) { PINA(cacc[t]); keep[t] = cacc[t]; PIN(keep[t]); }
    }
    f32x2 pre[4][2];
#pragma unroll
    for (int t = 0; t < 4; ++t) {
      pre[t][0] = f32x2{uf[4 * t], uf[4 * t + 1]} + lo2(keep[t]);
      pre[t][1] = f32x2{uf[4 * t + 2], uf[4 * t + 3]} + hi2(keep[t]);
      PIN(pre[t][0]); PIN(pre[t][1]);
    }
    if (w == 0) *(unsigned*)(smem + LDS_S) = __builtin_amdgcn_ballot_w64(flagv < 16u) != 0ull;
    bar();
    late = *(const unsigned*)(smem + LDS_S);
    const f32x4* rd = (const f32x4*)(smem + LDS_X) + (w ^ 1) * 256 + lane;
    f32x4 oth[4];
#pragma unroll
    for (int t = 0; t < 4; ++t) oth[t] = rd[t * 64];
#pragma unroll
    for (int t = 0; t < 4; ++t) {
      v2u o;
      o[0] = nomark(pk2(pre[t][0] + lo2(oth[t])));
      o[1] = pk2(pre[t][1] + hi2(oth[t]));
      st64<SC1>(o, rws, xoff + t * 512, 0);
    }
  }
  STAMP(1, 3);
  SB();
  v4u wf[3][8];
  const unsigned rot = uh * 4;
#define LOAD_WP(half, k, i) ld128<SC1>(rws, l16 + ((i) & 3) * 1024, WS_WP + (((half) * 44 + UO(k)) * 16 + kh * 8 + (((i) & 4) ^ rot)) * 1024 + ((k) == 2 ? oob3 : 0u))
#define LOAD_XS(i) ld128<SC1>(rws, wsp_v, WS_X1T + ((i) * 32 + w * 8) * 4096 + mt * 128)
  v4u xs[16];
  unsigned tb[2][4], xdst;
  SB();
  int x1o;
  unsigned z2o[3];
  v2u cz[2][3];
  f32x2 csb[2][3];
  float gbv, c1v[2];
  unsigned gq[4];
#define LOAD_CZ(hf, kc) ld64<SC1>(rws, lane * 8, WS_CSB + ((hf) * 44 + UO(kc)) * 512 + ((kc) == 2 ? oob3 : 0u))
  {
    STAMP(1, 20);
    if (__builtin_amdgcn_readfirstlane(late)) {
      if (w == 0) { hint_wait(rws, c * 128, WS_CNT + (256 + xcd * 16) * 128, 16u); __builtin_amdgcn_s_sleep(LATE_SLEEP); }
      __builtin_amdgcn_s_barrier();
    }
    MEMFENCE();
    STAMP(1, 21);
#pragma unroll
    for (int i = 0; i < 3; ++i)
#pragma unroll
      for (int k = 0; k < 3; ++k) wf[k][i] = LOAD_WP(1, k, i);
    SB();
#pragma unroll
  for (int f = 0; f < 8; ++f) wq[0][f] = READ_W(0, f);
#pragma unroll
  for (int f = 0; f < 24; ++f) PINA(wq[f >> 3][f & 7]);
    int lz = lane;
    PIN(lz);
  {
    const int row = 8 * (lz >> 4) + ((lz >> 2) & 3);
    const int sg = ((row >> 1) & 1) | (((row >> 3) & 1) << 1);
#pragma unroll
    for (int tu = 0; tu < 4; ++tu) {
      const unsigned t0 = lds0 + LDS_A + kh * 32768 + row * 128 + ((tu ^ (2 * kh) ^ sg) * 32) + (lz & 3) * 8;
      tb[0][tu] = t0 + uh * 16384;
      tb[1][tu] = t0 + (uh ^ 1) * 16384;
    }
    const int rl = w * 8 + (lz >> 3), chn = lz & 7;
    const int sg2 = ((rl >> 1) & 1) | (((rl >> 3) & 1) << 1);
    xdst = lds0 + LDS_A + rl * 128 + (((chn >> 1) ^ sg2) * 32) + (chn & 1) * 16;
  }
  {
    const int row = 8 * (lz >> 4) + ((lz >> 2) & 3);
    const int sg = ((row >> 1) & 1) | (((row >> 3) & 1) << 1);
    x1o = LDS_A + nt * 8192 + row * 128 + ((w ^ sg) * 32) + (lz & 3) * 8;
  }
#pragma unroll
  for (int k = 0; k < 3; ++k) { z2o[k] = lz * 16 + WS_Z2 + (UO(k) * 64 + mt * 2 + kh) * 1024 + (k == 2 ? oob3 : 0u); }
#pragma unroll
    for (int tu = 0; tu < 8; ++tu) PIN(tb[tu >> 2][tu & 3]);
    PIN(xdst); PIN(x1o);
#pragma unroll
    for (int k = 0; k < 3; ++k) PIN(z2o[k]);
    SB();
#pragma unroll
    for (int i = 3; i < WF_EARLY; ++i)
#pragma unroll
      for (int k = 0; k < 3; ++k) wf[k][i] = LOAD_WP(1, k, i);
    SB();
#pragma unroll
    for (int i = 0; i < 16; ++i) xs[i] = LOAD_XS(i);
    SB();
    STAMP(1, 44);
    unsigned m = 0;
#pragma unroll
    for (int i = 0; i < 16; ++i) {
      m = macc(m, xs[i]); ldsw<v4u>(xdst + i * 4096, xs[i]);
      if (WF_EARLY * 3 + i < 24) wf[(WF_EARLY * 3 + i) % 3][(WF_EARLY * 3 + i) / 3] = LOAD_WP(1, (WF_EARLY * 3 + i) % 3, (WF_EARLY * 3 + i) / 3);
      SB();
    }
    PIN(m);
    STAMP(1, 45);
    RETRY_BEGIN(m)
#pragma unroll
      for (int i = 0; i < 16; ++i) xs[i] = LOAD_XS(i);
      SB();
#pragma unroll
      for (int i = 0; i < 16; ++i) { m = macc(m, xs[i]); ldsw<v4u>(xdst + i * 4096, xs[i]); }
    RETRY_END(m)
  }
  STAMP(1, 4);
  bar();
  STAMP(1, 5);
#define AHALF(tu, i, h) trread_a(tb[(i) >> 2][tu] + ((i) & 3) * 4096 + (h) * 512)
  f32x4 acc[3][4];
  v4u wg[3][8];
  v2u ah[2][4][2];
  float* st = (float*)(smem + LDS_S);
  float* fin = st + 256 + w * 64;
  f32x4* xch = (f32x4*)(smem + LDS_X);
  f32x4 r4[2], tm[3][2];
  {
    f32x4 s1[4], s2[4], m4[2];
    v2u ar[3][2][2];
    float pa[4], pb[4];
    constexpr int ORD[20] = {0, 1, 2, 3, 4, 5, 6, 7, 16, 17, 18, 19, 8, 9, 10, 11, 12, 13, 14, 15};
    int dgo[2], rdo[2];
    dgo[0] = w * 64 + kh * 32 + c; dgo[1] = w * 64 + (kh ^ 1) * 32 + c;
    rdo[0] = (kh * 32 + (lane & 31)) * 4; rdo[1] = rdo[0] * 4 + (lane & 3) * 4;
    PIN(dgo[0]); PIN(dgo[1]); PIN(rdo[0]); PIN(rdo[1]);
    const bool diag = (c >> 2) == q;
#define GFRAG(gi, t, h) AHALF(((gi) < 8 ? 2 : 0) + (t), (gi) < 16 ? (gi) & 7 : (gi) - 16, h)
#pragma unroll
    for (int n = 0; n < 2; ++n)
#pragma unroll
      for (int e = 0; e < 4; ++e) ar[n][e >> 1][e & 1] = GFRAG(ORD[n], e >> 1, e & 1);
    SB();
    sfor<20 * 6>([&](auto G) __attribute__((always_inline)) {
      constexpr int n = decltype(G)::value / 6, j = decltype(G)::value % 6, gi = ORD[n], t = j / 3, k = j % 3;
      constexpr int tu = (gi < 8 ? 2 : 0) + t, i = gi < 16 ? gi & 7 : gi - 16;
      const v4u a = {ar[n % 3][t][0][0], ar[n % 3][t][0][1], ar[n % 3][t][1][0], ar[n % 3][t][1][1]};
      const f32x4 z4 = {0.f, 0.f, 0.f, 0.f};
      if constexpr (gi < 16) acc[k][tu] = mfma(a, wf[k][i], gi == 0 ? z4 : acc[k][tu]);
      if constexpr ((gi >= 4 && gi < 8) || gi >= 16) {
        if constexpr (k == 0) s1[tu] = mfma(ones4, a, (gi == 4 || gi == 16) ? z4 : s1[tu]);
        if constexpr (k == 1) s2[tu] = mfma(a, a, (gi == 4 || gi == 16) ? z4 : s2[tu]);
      }
      if constexpr (j < 4 && n + 2 < 20) ar[(n + 2) % 3][j >> 1][j & 1] = GFRAG(ORD[n + 2], j >> 1, j & 1);
      if constexpr (n >= 18 && j < 4) ah[0][(n - 18) * 2 + (j >> 1)][j & 1] = AHALF((n - 18) * 2 + (j >> 1), 0, j & 1);
      if constexpr (n < 4 && (j == 1 || j == 4)) DMA_W(3, slotB, n * 2 + (j == 4));
      if constexpr (n == SMALL_N) cz[j / 3][j % 3] = LOAD_CZ(j / 3, j % 3);
      if constexpr (n == 4 && j < 4) gq[j] = __builtin_amdgcn_raw_buffer_load_b32(rws, lane * 8, WS_MODG + (j * 3072 + 5 * 512 + nt * 64) * 8, SC1);
      if constexpr (n == 8 && j == 1) USE(gq[3]);
      if constexpr (n >= 8 && n < 12 && (j == 2 || j == 5)) DMA_W(4, slotA, (n - 8) * 2 + (j == 5));
      if constexpr (n >= 12 && j == 4) DMA_W(5, slotB, n - 12);
      if constexpr ((n == 8 || n == 9) && (j & 1) == 0) {
        constexpr int x = (n - 8) * 3 + (j >> 1);
        xch[(w * 6 + (x % 3) * 2 + x / 3) * 64 + lane] = acc[x % 3][2 + x / 3];
      }
      if constexpr (n == 10 && j == 0) {
#pragma unroll
    for (int k = 0; k < 3; ++k) {
      const float y = xrow_sum4(__uint_as_float(cz[1][k][0]), __uint_as_float(cz[1][k][1]), __uint_as_float(cz[1][k][0]), __uint_as_float(cz[1][k][1]));
      auto sw = __builtin_amdgcn_permlane16_swap(__float_as_uint(y), __float_as_uint(y), false, false);
      csb[1][k] = f32x2{__uint_as_float(sw[0]), __uint_as_float(sw[1])};
      PIN(csb[1][k]);
    }
    c1v[0] = xrow_sum4(__uint_as_float(cz[0][0][0]), __uint_as_float(cz[0][0][1]), __uint_as_float(cz[0][1][0]), __uint_as_float(cz[0][1][1]));
    c1v[1] = xrow_sum4(__uint_as_float(cz[0][2][0]), __uint_as_float(cz[0][2][1]), __uint_as_float(cz[0][2][0]), __uint_as_float(cz[0][2][1]));
    PIN(c1v[0]); PIN(c1v[1]);
    gbv = rbf((__uint_as_float(gq[0]) + __uint_as_float(gq[1])) + (__uint_as_float(gq[2]) + __uint_as_float(gq[3])));
    PIN(gbv);
      }
      if constexpr ((n == 8 || n == 9) && j >= 2) wq[3][(n - 8) * 4 + j - 2] = READ_W(8192, (n - 8) * 4 + j - 2);
      if constexpr (n == 10 && j == 5) {
#pragma unroll
        for (int f = 0; f < 8; ++f) PINA(wq[3][f]);
      }
      if constexpr (n == 10 && j == 4) bar();
      if constexpr (n == 11) acc[j % 3][j / 3] = xch[((w ^ 1) * 6 + (j % 3) * 2 + j / 3) * 64 + lane];
      if constexpr (n == 12 && j == 3) {
        float s1v[4];
#pragma unroll
        for (int x = 0; x < 4; ++x) { s1v[x] = s1[x][0]; PIN(s1v[x]); }
        if (diag) {
#pragma unroll
          for (int x = 0; x < 4; ++x) {
            *(float*)(smem + LDS_S + dgo[x >> 1] * 4 + (x & 1) * 64) = s1v[x];
            *(f32x4*)(smem + LDS_S + 2048 + dgo[x >> 1] * 16 + (x & 1) * 256) = s2[x];
          }
        }
      }
      if constexpr (n == 13 && j == 5) bar();
      if constexpr (n == 14 && j < 4) {
        pa[j] = *(const float*)(smem + LDS_S + rdo[0] + j * 256); pb[j] = *(const float*)(smem + LDS_S + 2048 + rdo[1] + j * 1024);
      }
      if constexpr (n == 15 && j == 1) { pa[0] += pa[1]; pa[2] += pa[3]; PIN(pa[0]); PIN(pa[2]); }
      if constexpr (n == 15 && j == 2) { pb[0] += pb[1]; pb[2] += pb[3]; PIN(pb[0]); PIN(pb[2]); }
      if constexpr (n == 15 && j == 3) { pa[0] = (pa[0] + pa[2]) * (1.0f / D); PIN(pa[0]); }
      if constexpr (n == 15 && j == 4) { pb[0] = (pb[0] + pb[2]) * (1.0f / D); PIN(pb[0]); }
      if constexpr (n == 15 && j == 5) { pb[0] = fmaxf(__builtin_fmaf(-pa[0], pa[0], pb[0]), 0.f); PIN(pb[0]); }
      if constexpr (n == 16 && j == 0) { pb[0] = __builtin_amdgcn_rsqf(pb[0] + EPS); PIN(pb[0]); }
      if constexpr (n == 16 && j == 1) fin[lane] = lane < 32 ? pb[0] : -pb[0] * pa[0];
      if constexpr (n == 16 && (j == 2 || j == 3)) { m4[j - 2] = *(const f32x4*)(fin + 32 + (j - 2) * 16 + q * 4); r4[j - 2] = *(const f32x4*)(fin + (j - 2) * 16 + q * 4); }
      if constexpr (n >= 18) {
        constexpr int x = (n - 18) * 6 + j, kk = x >> 2, tt = (x >> 1) & 1, h = (x & 1) * 2;
        float t0 = __builtin_fmaf(m4[tt][h], csb[1][kk][0], csb[1][kk][1]);
        PIN(t0);
        float t1 = __builtin_fmaf(m4[tt][h + 1], csb[1][kk][0], csb[1][kk][1]);
        PIN(t1);
        tm[kk][tt][h] = t0; tm[kk][tt][h + 1] = t1;
      }
      if constexpr (n >= 12 && n < 15 && j == 5) st128<SC1>(mk, rws, z2o[n - 12], 0);
      if constexpr (n == 7 && j == 5) STAMP(1, 46);
      if constexpr (n == 11 && j == 5) STAMP(1, 47);
      SB();
    });
  }
#pragma unroll
  for (int k = 0; k < 6; ++k) PINA(acc[k >> 1][k & 1]);
  STAMP(1, 6);
  f32x4 z2v[3][2];
  {
    f32x4 rs[2], rq[2];
#pragma unroll
    for (int t = 0; t < 2; ++t) {
#pragma unroll
      for (int k = 0; k < 3; ++k) {
        const f32x4 a = acc[k][t];
        const f32x2 zl = gelu2(fma2(lo2(a), lo2(r4[t]), lo2(tm[k][t])));
        const f32x2 zh = gelu2(fma2(hi2(a), hi2(r4[t]), hi2(tm[k][t])));
        z2v[k][t] = f32x4{zl[0], zl[1], zh[0], zh[1]};
        if (k == 0) { rs[t] = z2v[k][t]; rq[t] = z2v[k][t] * z2v[k][t]; }
        else { rs[t] += z2v[k][t]; rq[t] += z2v[k][t] * z2v[k][t]; }
        if (((t * 3 + k) & 1) == 0) {
#pragma unroll
          for (int f = (t * 3 + k) * 4; f < (t * 3 + k) * 4 + 8; ++f) wg[f % 3][f / 3] = LOAD_WP(0, f % 3, f / 3);
        } else {
          PIN(z2v[k][t]);
          SB();
        }
      }
    }
    STAMP(1, 50);
    float v8[8], v4[4], v2[2];
    const bool b3 = c & 8, b2 = c & 4, b1 = c & 2, b0 = c & 1;
#pragma unroll
    for (int r_ = 0; r_ < 4; ++r_) {
      const float a0 = rs[0][r_] + dpp<0x128>(rs[0][r_]), a1 = rs[1][r_] + dpp<0x128>(rs[1][r_]);
      const float q0 = rq[0][r_] + dpp<0x128>(rq[0][r_]), q1 = rq[1][r_] + dpp<0x128>(rq[1][r_]);
      v8[2 * r_] = b3 ? a1 : a0; v8[2 * r_ + 1] = b3 ? q1 : q0;
    }
#pragma unroll
    for (int j = 0; j < 4; ++j) {
      const float x0 = v8[j] + dpp<0x141>(v8[j]), x1 = v8[4 + j] + dpp<0x141>(v8[4 + j]);
      v4[j] = b2 ? x1 : x0;
    }
#pragma unroll
    for (int j = 0; j < 2; ++j) {
      const float x0 = v4[j] + dpp<0x4E>(v4[j]), x1 = v4[2 + j] + dpp<0x4E>(v4[2 + j]);
      v2[j] = b1 ? x1 : x0;
    }
    const float x0 = v2[0] + dpp<0xB1>(v2[0]), x1 = v2[1] + dpp<0xB1>(v2[1]);
    const float outv = b0 ? x1 : x0;
    __builtin_amdgcn_raw_buffer_store_b32(nomark(__float_as_uint(outv)), rws, lane * 4, WS_ST2 + ((mt * 2 + kh) * 16 + nt * 2 + uh) * 256, SC1);
  }
  STAMP(1, 7);
  SB();
#pragma unroll
  for (int tu = 0; tu < 4; ++tu)
#pragma unroll
    for (int k = 0; k < 3; ++k) acc[k][tu] = f32x4{0.f, 0.f, 0.f, 0.f};
  unsigned k3v[6];
  {
    const unsigned k0 = l16 + w * 6144, w3 = (w + 1) >> 2;
    k3v[0] = k0; k3v[1] = k0 + 2048 - w3 * 1024; k3v[2] = k3v[1] + 1024; k3v[3] = k0 + 5120 - w3 * 2048;
    k3v[4] = k0 + 1024 + (w3 << 30); k3v[5] = k3v[4] + 3072;
  }
  v2u x1h[2][2];
  v4u wo[6][4];
#define LOAD_WO(f) wo[(f) >> 2][(f) & 3] = ld128<SC1>(rws, k3v[(f) >> 2], WS_WOT + (nt * 4 + ((f) & 3)) * 22 * 1024)
  f32x4 pt[4];
  float* c1s = st + 768 + w * 96;
#define LOAD_PT(i) asf(ld128<SC1>(rws, l16 + (i) * 1024, WS_ST2 + (mt * 2 + kh) * 4096))
  SB();
#pragma unroll
  for (int i = 0; i < 8; ++i) {
#pragma unroll
    for (int j = 0; j < 12; ++j) {
      const int tu = j / 3, k = j % 3;
      const v4u a = {ah[i & 1][tu][0][0], ah[i & 1][tu][0][1], ah[i & 1][tu][1][0], ah[i & 1][tu][1][1]};
      acc[k][tu] = mfma(wg[k][i], a, acc[k][tu]);
      if (j < 8 && i < 7) ah[(i + 1) & 1][j >> 1][j & 1] = AHALF(j >> 1, i + 1, j & 1);
      if (j < 4 && i == 7) x1h[j >> 1][j & 1] = trread(smem + x1o + (j >> 1) * 4096 + (j & 1) * 512);
      if (i == 0 && j == 0) asm volatile("s_waitcnt vmcnt(24)" ::: "memory");
      if (i < 2 && j >= 4) wq[4 + i][j - 4] = READ_W(i * 8192, j - 4);
      if (i == 3 && j == 0) {
#pragma unroll
        for (int f = 0; f < 16; ++f) PINA(wq[4 + (f >> 3)][f & 7]);
      }
      if (i == 4 && j == 0) { USE(wg[0][7]); USE(wg[1][7]); USE(wg[2][7]); }
      if ((i == 2 || i == 3) && j % 3 == 2) dma_sel(IC<6>{}, slotA, (i - 2) * 4 + j / 3);
      if ((i == 4 || i == 5) && j % 3 == 2) dma_sel(IC<7>{}, slotB, (i - 4) * 4 + j / 3);
      if (j >= 8 && i == 6) pt[j - 8] = LOAD_PT(j - 8);
      if (i == 7 && j >= 8) LOAD_WO(j - 8);
      if (i == 5 && j == 0) {
        asm volatile("s_waitcnt vmcnt(30)" ::: "memory");
        __builtin_amdgcn_s_barrier();
      }
      if (i == 5 && j == 1) st[1408 + lane] = gbv;
      if (i == 5 && j == 9) c1s[lane] = c1v[0];
      if (i == 5 && j == 10) c1s[64 + (lane & 31)] = c1v[1];
      SB();
    }
  }
  STAMP(1, 8);
#pragma unroll
  for (int k = 0; k < 3; ++k)
#pragma unroll
    for (int t = 0; t < 2; ++t) xch[(w * 6 + k * 2 + t) * 64 + lane] = acc[k][2 + t];
  SB();
  {
    unsigned m = 0;
#pragma unroll
    for (int i = 0; i < 4; ++i) m = macc4(m, __builtin_bit_cast(v4u, pt[i]));
    RETRY_BEGIN(m)
#pragma unroll
      for (int i = 0; i < 4; ++i) { pt[i] = LOAD_PT(i); m = macc4(m, __builtin_bit_cast(v4u, pt[i])); }
    RETRY_END(m)
    if (tid < 4) hint_add(p.ws, 64 + (nt * 4 + (mt >> 3)) * 4 + tid);
    const f32x4 sx = (pt[0] + pt[1]) + (pt[2] + pt[3]);
    const float tv = xrow_sum4(sx[0], sx[2], sx[1], sx[3]);
    auto sw = __builtin_amdgcn_permlane32_swap(__float_as_uint(tv), __float_as_uint(tv), false, false);
    const float mm = __uint_as_float(sw[0]) * (1.0f / FH);
    const float r = __builtin_amdgcn_rsqf(fmaxf(__uint_as_float(sw[1]) * (1.0f / FH) - mm * mm, 0.f) + EPS);
    float* f2 = st + 512 + w * 64;
    f2[(q >> 1) * 32 + 2 * c + (q & 1)] = (q >> 1) ? -r * mm : r;
    f32x4 r4[2], m4[2];
#pragma unroll
    for (int t = 0; t < 2; ++t) { r4[t] = *(const f32x4*)(f2 + q * 8 + t * 4); m4[t] = *(const f32x4*)(f2 + 32 + q * 8 + t * 4); }
#pragma unroll
    for (int k = 0; k < 3; ++k) {
      v4u o;
#pragma unroll
      for (int t = 0; t < 2; ++t) {
        o[2 * t] = nomark(pk2(fma2(lo2(z2v[k][t]), lo2(r4[t]), lo2(m4[t]))));
        o[2 * t + 1] = pk2(fma2(hi2(z2v[k][t]), hi2(r4[t]), hi2(m4[t])));
      }
      st128<SC1>(o, rws, z2o[k], 0);
    }
  }
  STAMP(1, 9);
  SB();
  f32x4 z1f[3][2];
  constexpr int RD = ZRD;
  v4u zf[RD];
#define LOAD_Z(n) ld128<SC1>(rws, zv[((n) >> 1) & 7] + ((n) & 1) * 1024, WS_Z2 + (((n) < 48 ? UT(((n) >> 4) % 3) : UO(((n) >> 4) % 3)) * 64 + w * 16) * 1024 + (((n) >> 4) % 3 == 2 ? oob3 : 0u))
  unsigned zv[8];
#pragma unroll
  for (int i = 0; i < 8; ++i) { zv[i] = l16 + ((CKMAP(i) + kr) & 7) * 2048; PIN(zv[i]); }
  {
    bar();
    STAMP(1, 34);
#pragma unroll
    for (int k = 0; k < 6; ++k) PINA(acc[k >> 1][k & 1]);
    f32x4 oth[3][2], cs1[3], bs1[3];
    float r1[2], m1[2];
#pragma unroll
    for (int t = 0; t < 2; ++t) { r1[t] = fin[t * 16 + c]; m1[t] = fin[32 + t * 16 + c]; }
#pragma unroll
    for (int k = 0; k < 3; ++k) {
      cs1[k] = *(const f32x4*)(c1s + k * 32 + q * 4); bs1[k] = *(const f32x4*)(c1s + k * 32 + 16 + q * 4);
#pragma unroll
      for (int t = 0; t < 2; ++t) oth[k][t] = xch[((w ^ 1) * 6 + k * 2 + t) * 64 + lane];
    }
#pragma unroll
    for (int i = 4; i < WO0; ++i) LOAD_WO(i);
    SB();
    f32x2 z1a[6][2];
#pragma unroll
    for (int i = 0; i < 6; ++i) {
      const int k = i >> 1, t = i & 1;
      const f32x4 a = acc[k][t] + oth[k][t];
      z1a[i][0] = fma2(lo2(a), bc2(r1[t]), fma2(lo2(cs1[k]), bc2(m1[t]), lo2(bs1[k])));
      z1a[i][1] = fma2(hi2(a), bc2(r1[t]), fma2(hi2(cs1[k]), bc2(m1[t]), hi2(bs1[k])));
    }
#pragma unroll
    for (int i = 0; i < GELU_PRE; ++i) {
      const f32x2 lo = gelu2(z1a[i][0]), hi = gelu2(z1a[i][1]);
      z1f[i >> 1][i & 1] = f32x4{lo[0], lo[1], hi[0], hi[1]};
      PIN(z1f[i >> 1][i & 1]);
    }
#pragma unroll
    for (int i = GELU_PRE; i < 6; ++i) { PIN(z1a[i][0]); PIN(z1a[i][1]); }
    SB();
#pragma unroll
    for (int f = 0; f < 16; ++f) wq[6 + (f >> 3)][f & 7] = READ_W((f >> 3) * 8192, f & 7);
    SB();
    STAMP(1, 10);
    hint_wait(rws, 0u, WS_CNT + (64 + (nt * 4 + w) * 4 + (xcd >> 1)) * 128, 8u);
    STAMP(1, 22);
#pragma unroll
    for (int n = 0; n < RD; ++n) zf[n] = LOAD_Z(n);
    SB();
#pragma unroll
    for (int i = GELU_PRE; i < 6; ++i) {
      const f32x2 lo = gelu2(z1a[i][0]), hi = gelu2(z1a[i][1]);
      z1f[i >> 1][i & 1] = f32x4{lo[0], lo[1], hi[0], hi[1]};
      PIN(z1f[i >> 1][i & 1]);
    }
  }
  f32x4* xb = (f32x4*)(smem + LDS_A) + lane;
  f32x4* yb = (f32x4*)(smem + LDS_A + 49152) + lane;
  f32x4 ac2[2][4], ini[4], yo[2];
  const float bs0 = bflo(bspq[0]), bs1_ = bfhi(bspq[0]);
  const f32x4 bq0 = {bs0, bs0, bs0, bs0}, bq1 = {bs1_, bs1_, bs1_, bs1_}, z4 = {0.f, 0.f, 0.f, 0.f};
  unsigned voff[2];
  voff[0] = l16 + WS_V + ((mt * 4 + 2 * kh) * 22 + (uo0 >> 1)) * 1024;
  voff[1] = l16 + WS_V + ((mt * 4 + 2 * kh) * 22 + (uo2 >> 1)) * 1024 + (uo2 & 1) * 8 + oob3;
  PIN(voff[0]); PIN(voff[1]);
  float ev[4];
  unsigned eo0;
  SB();
  STAMP(1, 11);
  auto v_store = [&](int k, int t, unsigned o0, unsigned o1) __attribute__((always_inline)) {
    st64<SC1>(v2u{o0, o1}, rws, voff[k >> 1] + (k & 1) * 8, t * 22 * 1024);
  };
  sfor<6>([&](auto P) __attribute__((always_inline)) {
    constexpr int ps = decltype(P)::value, cur = ps & 1, prv = cur ^ 1;
    unsigned zm = 0;
    sfor<64>([&](auto G) __attribute__((always_inline)) {
      constexpr int g = decltype(G)::value, ks = g >> 2, tu = g & 3, ck = ks >> 1, k2 = ks & 1, n = ps * 16 + ks;
      const f32x4 cin = ks > 0 ? ac2[cur][tu] : ps >= 3 ? ini[tu] : tu == 0 ? bq0 : tu == 1 ? bq1 : z4;
      ac2[cur][tu] = mfma(zf[n % RD], wq[ck][k2 * 4 + tu], cin);
      if constexpr (tu == 1) { zm = macc(zm, zf[n % RD]); PIN(zm); }
      if constexpr (tu == 3) {
        if constexpr (n + RD < 96) zf[n % RD] = LOAD_Z(n + RD);
        else if constexpr (WO0 + n + RD - 96 < 24) LOAD_WO(WO0 + n + RD - 96);
      }
      if constexpr (ps >= 1 && ps <= 3 && g >= 2 && g < 18 && (g & 3) == 2) xb[((w * 3 + ps - 1) * 4 + (g >> 2)) * 64] = ac2[prv][g >> 2];
      if constexpr (ps >= 4 && (g == 2 || g == 6)) yb[((w * 3 + ps - 4) * 2 + (g >> 2)) * 64] = ac2[prv][2 + (g >> 2)];
      if constexpr (ps >= 1 && g == (ps >= 4 ? 12 : 24)) {
        if constexpr (ps == 4) asm volatile("s_waitcnt vmcnt(20)" ::: "memory");
        bar();
      }
      if constexpr (ps >= 4 && (g == 13 || g == 14)) yo[g - 13] = yb[(((w ^ 1) * 3 + ps - 4) * 2 + g - 13) * 64];
      if constexpr (ps >= 4 && g >= 20 && g < 40) {
        constexpr int t = (g - 20) / 10, ph = (g - 20) % 10;
        if constexpr (ph < 4) { ev[ph] = ac2[prv][t][ph] + yo[t][ph]; PIN(ev[ph]); }
        else if constexpr (ph < 8) { ev[ph - 4] *= z1f[ps - 4][t][ph - 4]; PIN(ev[ph - 4]); }
        else if constexpr (ph == 8) { eo0 = nomark(pk2(ev[0], ev[1])); PIN(eo0); }
        else v_store(ps - 4, t, eo0, pk2(ev[2], ev[3]));
      }
      if constexpr (ps >= 2 && ps <= 4 && g >= 28 && g < 44 && (g & 3) == 0) ini[(g - 28) >> 2] = xb[(((w ^ 2) * 3 + ps - 2) * 4 + ((g - 28) >> 2)) * 64];
      if constexpr (ps == 0 && (g == 21 || g == 37 || g == 53)) st128<SC1>(mk, rws, l16, WS_V + ((mt * 4 + w) * 22 + pr0 + (g - 21) / 16) * 1024 + (g == 53 ? oob3 : 0u));
      if constexpr (ps * 64 + g == HINT4_G) { if (tid < 2) hint_add(p.ws, 192 + mt * 2 + tid); }
      if constexpr (ps == 5 && g == POLL4_G) flagv = __builtin_amdgcn_raw_buffer_load_b32(rws, w == 0 ? 0u : OOB, WS_CNT + (192 + mt * 2 + (xcd & 1)) * 128, SC1);
      if constexpr (g == 0 && ps >= 1 && ps < 5) STAMP(1, 55 + ps);
      SB();
    });
    if (__builtin_expect(any_mark(zm), 0)) {
#pragma unroll
      for (int tu = 0; tu < 4; ++tu) ac2[cur][tu] = ps >= 3 ? xb[(((w ^ 2) * 3 + ps - 3) * 4 + tu) * 64] : tu == 0 ? bq0 : tu == 1 ? bq1 : z4;
      sfor<16>([&](auto K) __attribute__((always_inline)) {
        constexpr int ks = decltype(K)::value, ck = ks >> 1, k2 = ks & 1;
        v4u zz;
        unsigned sp = 0;
        do { MEMFENCE(); zz = LOAD_Z(ps * 16 + ks); } while (any_mark(macc(0u, zz)) & (++sp <= SPIN_CAP));
#pragma unroll
        for (int tu = 0; tu < 4; ++tu) ac2[cur][tu] = mfma(zz, wq[ck][k2 * 4 + tu], ac2[cur][tu]);
      });
    }
  });
  STAMP(1, 12);
  {
#pragma unroll
    for (int t = 0; t < 2; ++t) yb[((w * 3 + 2) * 2 + t) * 64] = ac2[1][2 + t];
    PIN(ac2[1][0]); PIN(ac2[1][1]);
    if (w == 0) st[0] = __uint_as_float(flagv < 8u);
    bar();
    late = __float_as_uint(st[0]);
    STAMP(1, 52);
#pragma unroll
    for (int t = 0; t < 2; ++t) yo[t] = yb[(((w ^ 1) * 3 + 2) * 2 + t) * 64];
#pragma unroll
    for (int t = 0; t < 2; ++t) {
      const f32x4 g = (ac2[1][t] + yo[t]) * z1f[2][t];
      v_store(2, t, nomark(pk2(lo2(g))), pk2(hi2(g)));
    }
  }
  STAMP(1, 13);
  SB();
  f32x4 gb4[4];
  f32x2 x1f[2][4];
  STAMP(1, 53);
  if (__builtin_amdgcn_readfirstlane(late)) {
    if (w == 0) hint_wait(rws, 0u, WS_CNT + (192 + mt * 2 + (xcd & 1)) * 128, 8u);
    __builtin_amdgcn_s_barrier();
  }
  MEMFENCE();
  STAMP(1, 23);
  f32x4 a3[4][4];
  {
    unsigned spins = 0;
    for (;;) {
      MEMFENCE();
      v4u vs[6][4];
#define LOAD_V(f) vs[(f) >> 2][(f) & 3] = ld128<SC1>(rws, k3v[(f) >> 2], WS_V + (mt * 4 + (((f) & 3) ^ w)) * 22 * 1024)
#pragma unroll
      for (int f = 0; f < 12; ++f) LOAD_V(f);
      SB();
      unsigned m = 0;
      sfor<96>([&](auto G) __attribute__((always_inline)) {
        constexpr int g = decltype(G)::value, ks = g >> 4, tu = (g >> 2) & 3, e = g & 3;
        a3[e][tu] = mfma(wo[ks][e], vs[ks][tu], ks == 0 ? f32x4{0.f, 0.f, 0.f, 0.f} : a3[e][tu]);
        if constexpr (e == 0 && 12 + (g >> 2) < 24) LOAD_V(12 + (g >> 2));
        if constexpr (e == 1) { m = macc(m, vs[ks][tu]); PIN(m); }
        SB();
      });
      if (!(any_mark(m) & (++spins <= SPIN_CAP))) break;
    }
  }
#pragma unroll
  for (int i = 0; i < 4; ++i) gb4[i] = *(const f32x4*)(st + 1408 + (i >> 1) * 32 + q * 8 + (i & 1) * 4);
#pragma unroll
  for (int i = 0; i < 8; ++i) { x1f[i >> 2][i & 3] = unpk(x1h[i >> 2][(i >> 1) & 1][i & 1]); PIN(x1f[i >> 2][i & 3]); }
  STAMP(1, 14);
  {
    f32x4* red = (f32x4*)(smem + LDS_W);
#pragma unroll
    for (int tu = 1; tu < 4; ++tu)
#pragma unroll
      for (int e = 0; e < 4; ++e) red[((w * 3 + tu - 1) * 4 + e) * 64 + lane] = a3[e][tu];
#pragma unroll
    for (int e = 0; e < 4; ++e) PIN(a3[e][0]);
    bar();
    STAMP(1, 54);
    f32x4 o3[4][3];
#pragma unroll
    for (int e = 0; e < 4; ++e)
#pragma unroll
      for (int rel = 1; rel < 4; ++rel) o3[e][rel - 1] = red[(((w ^ rel) * 3 + rel - 1) * 4 + e) * 64 + lane];
    const rsrc_t rout = mkrs(p.out, L * D * 2);
    SB();
#pragma unroll
    for (int pp = 0; pp < 2; ++pp) {
      v4u o;
#pragma unroll
      for (int h = 0; h < 2; ++h) {
        const int e = pp * 2 + h;
        const f32x4 y = (a3[e][0] + o3[e][0]) + (o3[e][1] + o3[e][2]);
        o[2 * h] = pk2(fma2(lo2(y), lo2(gb4[e]), x1f[pp][2 * h]));
        o[2 * h + 1] = pk2(fma2(hi2(y), hi2(gb4[e]), x1f[pp][2 * h + 1]));
      }
      st128<0>(o, rout, perm(w, c) * 1024 + q * 16 + pp * 64, mt * 64 * 1024 + nt * 128);
    }
  }
  if (b == 0 && tid == 0) *(unsigned*)(p.ws + WS_EPOCH) = tag_s;
  STAMP(1, 15);
  STAMP_FLUSH(1);
  asm volatile("adaln_code_end:");
}

extern "C" __device__ __attribute__((used, noinline)) void adaln_tail() {}

extern "C" void kernel_launch(void* const* d_in, const int* in_sizes, int n_in, void* d_out, int out_size, void* d_ws,
                              size_t ws_size, hipStream_t stream) {
  Params p;
  p.u = (const u16*)d_in[0];
  p.lar = (const u16*)d_in[1];
  p.aim = (const u16*)d_in[2];
  p.cre = (const u16*)d_in[3];
  p.cim = (const u16*)d_in[4];
  p.ldt = (const u16*)d_in[5];
  p.dsk = (const u16*)d_in[6];
  p.win = (const u16*)d_in[8];
  p.wsp = (const u16*)d_in[9];
  p.bsp = (const u16*)d_in[10];
  p.wout = (const u16*)d_in[11];
  p.c = (const u16*)d_in[12];
  p.wmod = (const u16*)d_in[13];
  p.out = (u16*)d_out;
  p.ws = (unsigned char*)d_ws;
  (void)hipFuncSetAttribute((const void*)adaln_mega, hipFuncAttributeMaxDynamicSharedMemorySize, MEGA_LDS);
  adaln_mega<<<256, 256, MEGA_LDS, stream>>>(p);
}
```

```cpp
#include <hip/hip_runtime.h>
#include <hip/hip_bf16.h>
#include <stdint.h>
#include <utility>

#define LAS __attribute__((address_space(3)))
typedef unsigned v4u __attribute__((ext_vector_type(4)));
typedef unsigned v2u __attribute__((ext_vector_type(2)));
typedef float f32x4 __attribute__((ext_vector_type(4)));
typedef float f32x2 __attribute__((ext_vector_type(2)));
typedef short bf16x8 __attribute__((ext_vector_type(8)));
typedef _Float16 f16x8 __attribute__((ext_vector_type(8)));
typedef _Float16 f16x2 __attribute__((ext_vector_type(2)));
typedef __bf16 bf16x2_t __attribute__((ext_vector_type(2)));
typedef unsigned short u16;
typedef __amdgpu_buffer_rsrc_t rsrc_t;

#define STAMP_DECL do { } while (0)
#define STAMP(k, i) do { } while (0)
#define STAMP_FLUSH(k) do { } while (0)

#define OPT_A 1
#define OPT_B 1
#define OPT_C 2
#define OPT_D 1
#define OPT_E 2
#define OPT_F 1
constexpr int L = 2048, D = 512, NM = 128, FH = 688, FF = 1376;
constexpr float EPS = 1e-5f;
constexpr int SC1 = 16;
constexpr unsigned OOB = 0x40000000u;
constexpr unsigned SPIN_CAP = 1u << 14;

constexpr unsigned WS_EPOCH = 0;
constexpr unsigned WS_CNT = 4096;
constexpr unsigned WS_SR = WS_CNT + 512 * 128;
constexpr unsigned WS_SB = WS_SR + 2048 * 8;
constexpr unsigned WS_MODG = WS_SB + 2048 * 8;
constexpr unsigned WS_CSB = WS_MODG + 3072 * 32;
constexpr unsigned WS_ST2 = WS_CSB + 88 * 16 * 32;
constexpr unsigned WS_UG = 1u << 20;
static_assert(WS_ST2 + 32 * 2 * 16 * 32 * 8 <= WS_UG, "small buffers");
constexpr unsigned WS_X1T = WS_UG + 256 * 2048 * 8;
constexpr unsigned WS_WP = WS_X1T + 512 * 2048 * 2;
constexpr unsigned WS_WOT = WS_WP + 88 * 16 * 1024;
constexpr unsigned WS_Z2 = WS_WOT + 32 * 22 * 1024;
constexpr unsigned WS_V = WS_Z2 + 44 * 64 * 1024;
constexpr unsigned WS_END = WS_V + 128 * 22 * 1024;

struct Params {
  const u16 *u, *lar, *aim, *cre, *cim, *ldt, *dsk, *win, *wsp, *bsp, *wout, *c, *wmod;
  u16* out;
  unsigned char* ws;
};

#define DI __device__ __forceinline__
template <int V> struct IC { static constexpr int value = V; };
template <class F, int... I> DI void sfor_(F&& f, std::integer_sequence<int, I...>) { int d[] = {(f(IC<I>{}), 0)...}; (void)d; }
template <int N, class F> DI void sfor(F&& f) { sfor_(f, std::make_integer_sequence<int, N>{}); }
DI float bf2f(unsigned b) { return __builtin_bit_cast(float, b << 16); }
DI float bflo(unsigned w) { return __builtin_bit_cast(float, w << 16); }
DI float bfhi(unsigned w) { return __builtin_bit_cast(float, w & 0xFFFF0000u); }
DI f32x2 unpk(unsigned w) { return f32x2{bflo(w), bfhi(w)}; }
DI unsigned pk2(f32x2 v) { return __builtin_bit_cast(unsigned, __builtin_convertvector(v, bf16x2_t)); }
DI unsigned pk2(float lo, float hi) { return pk2(f32x2{lo, hi}); }
DI unsigned nomark(unsigned x) { return x < 0xFFFFFFFEu ? x : 0xFFFFFFFEu; }
DI float rbf(float x) { return bflo(pk2(x, 0.f)); }
DI f32x2 fma2(f32x2 a, f32x2 b, f32x2 c) { return __builtin_elementwise_fma(a, b, c); }
DI f32x2 lo2(f32x4 v) { return f32x2{v[0], v[1]}; }
DI f32x2 hi2(f32x4 v) { return f32x2{v[2], v[3]}; }
DI f32x2 bc2(float x) { return f32x2{x, x}; }
DI f32x4 mfma(v4u a, v4u b, f32x4 c) {
  return __builtin_amdgcn_mfma_f32_16x16x32_bf16(__builtin_bit_cast(bf16x8, a), __builtin_bit_cast(bf16x8, b), c, 0, 0, 0);
}
DI f32x4 mfma_h(v4u a, v4u b, f32x4 c) {
  return __builtin_amdgcn_mfma_f32_16x16x32_f16(__builtin_bit_cast(f16x8, a), __builtin_bit_cast(f16x8, b), c, 0, 0, 0);
}
DI float dot2(unsigned a, unsigned b, float acc) {
  return __builtin_amdgcn_fdot2_f32_bf16(__builtin_bit_cast(bf16x2_t, a), __builtin_bit_cast(bf16x2_t, b), acc, false);
}
constexpr unsigned ONES = 0x3F803F80u;
DI int perm(int u, int c) { return 32 * (u >> 1) + 8 * (c >> 2) + 4 * (u & 1) + (c & 3); }
template <int CTRL> DI float dpp(float x) {
  return __builtin_bit_cast(float, __builtin_amdgcn_mov_dpp(__builtin_bit_cast(int, x), CTRL, 0xf, 0xf, true));
}
DI float row16_sum(float x) {
  x += dpp<0xB1>(x); x += dpp<0x4E>(x); x += dpp<0x141>(x); x += dpp<0x128>(x);
  return x;
}
DI float row16_max(float x) {
  x = fmaxf(x, dpp<0xB1>(x)); x = fmaxf(x, dpp<0x4E>(x)); x = fmaxf(x, dpp<0x141>(x)); x = fmaxf(x, dpp<0x128>(x));
  return x;
}
DI float xrow_sum(float x) {
  auto s = __builtin_amdgcn_permlane16_swap(__float_as_uint(x), __float_as_uint(x), false, false);
  x = __uint_as_float(s[0]) + __uint_as_float(s[1]);
  auto t = __builtin_amdgcn_permlane32_swap(__float_as_uint(x), __float_as_uint(x), false, false);
  return __uint_as_float(t[0]) + __uint_as_float(t[1]);
}
DI float xrow_sum4(float a, float b, float c, float d) {
  auto s = __builtin_amdgcn_permlane16_swap(__float_as_uint(a), __float_as_uint(b), false, false);
  auto t = __builtin_amdgcn_permlane16_swap(__float_as_uint(c), __float_as_uint(d), false, false);
  const float x = __uint_as_float(s[0]) + __uint_as_float(s[1]), y = __uint_as_float(t[0]) + __uint_as_float(t[1]);
  auto z = __builtin_amdgcn_permlane32_swap(__float_as_uint(x), __float_as_uint(y), false, false);
  return __uint_as_float(z[0]) + __uint_as_float(z[1]);
}
DI float xrow_max(float x) {
  auto s = __builtin_amdgcn_permlane16_swap(__float_as_uint(x), __float_as_uint(x), false, false);
  x = fmaxf(__uint_as_float(s[0]), __uint_as_float(s[1]));
  auto t = __builtin_amdgcn_permlane32_swap(__float_as_uint(x), __float_as_uint(x), false, false);
  return fmaxf(__uint_as_float(t[0]), __uint_as_float(t[1]));
}
DI f32x2 gelu2(f32x2 x) {
  const float k0 = -2.0f * 0.7978845608028654f * 1.4426950408889634f, k1 = k0 * 0.044715f;
  const f32x2 a = x * fma2(x * x, bc2(k1), bc2(k0));
  const f32x2 e = f32x2{__builtin_amdgcn_exp2f(a[0]), __builtin_amdgcn_exp2f(a[1])} + bc2(1.0f);
  return x * f32x2{__builtin_amdgcn_rcpf(e[0]), __builtin_amdgcn_rcpf(e[1])};
}
DI rsrc_t mkrs(const void* ptr, unsigned bytes) { return __builtin_amdgcn_make_buffer_rsrc((void*)ptr, 0, (int)bytes, 0x00020000); }
template <int AUX> DI v4u ld128(rsrc_t rs, unsigned v, unsigned s) { return __builtin_amdgcn_raw_buffer_load_b128(rs, (int)v, (int)s, AUX); }
template <int AUX> DI v2u ld64(rsrc_t rs, unsigned v, unsigned s) { return __builtin_amdgcn_raw_buffer_load_b64(rs, (int)v, (int)s, AUX); }
template <int AUX> DI void st128(v4u x, rsrc_t rs, unsigned v, unsigned s) { __builtin_amdgcn_raw_buffer_store_b128(x, rs, (int)(v + s), 0, AUX); }
template <int AUX> DI void st64(v2u x, rsrc_t rs, unsigned v, unsigned s) { __builtin_amdgcn_raw_buffer_store_b64(x, rs, (int)v, (int)s, AUX); }
DI f32x4 asf(v4u x) { return __builtin_bit_cast(f32x4, x); }
DI float sum4(f32x4 x) { return (x[0] + x[1]) + (x[2] + x[3]); }
DI unsigned umax3(unsigned a, unsigned b, unsigned c) { unsigned m = a > b ? a : b; return m > c ? m : c; }
DI unsigned macc4(unsigned m, v4u x) { return umax3(umax3(m, x[0], x[1]), x[2], x[3]); }
DI unsigned macc(unsigned m, v4u x) { return umax3(m, x[0], x[2]); }
DI bool any_mark(unsigned m) { return __builtin_amdgcn_ballot_w64(m == 0xFFFFFFFFu) != 0ull; }
#define SB() __builtin_amdgcn_sched_barrier(0)
#define PIN(x) asm volatile("" : "+v"(x))
#define PINA(x) asm volatile("" : "+a"(x))
#define USE(x) asm volatile("" ::"v"(x))
#define RETRY_BEGIN(m) if (__builtin_expect(any_mark(m), 0)) { unsigned sp_ = 0; do { MEMFENCE(); m = 0;
#define RETRY_END(m) } while (any_mark(m) & (++sp_ <= SPIN_CAP)); }
#define MEMFENCE() asm volatile("" ::: "memory")
DI void bar() { asm volatile("s_waitcnt lgkmcnt(0)" ::: "memory"); __builtin_amdgcn_s_barrier(); asm volatile("" ::: "memory"); }

DI f32x2 cmul(f32x2 a, f32x2 b) { return fma2(bc2(a[1]), f32x2{-b[1], b[0]}, bc2(a[0]) * b); }
DI f32x2 cexp_p(float are2, float rev_hi, float rev_lo, float p) {
  const float mag = __builtin_amdgcn_exp2f(are2 * p);
  const float hi = rev_hi * p;
  const float fr = (hi - __builtin_rintf(hi)) + rev_lo * p;
  return f32x2{mag * __builtin_amdgcn_cosf(fr), mag * __builtin_amdgcn_sinf(fr)};
}
DI float exp_acc(float x) {
  const float hi = x * 1.4426950408889634f;
  const float lo = __builtin_fmaf(x, 1.4426950408889634f, -hi) + x * 1.925963033500011e-8f;
  const float e = __builtin_amdgcn_exp2f(hi);
  return __builtin_fmaf(e, lo * 0.6931471805599453f, e);
}
DI unsigned pkh(float a, float b) {
  f16x2 v = {(_Float16)a, (_Float16)b};
  return __builtin_bit_cast(unsigned, v);
}

constexpr int CV_RK = 4160;
constexpr int CV_H = 2 * CV_RK;
constexpr int CV_CH = CV_H + 4608;
constexpr int LDS_A = 0;
constexpr int LDS_U = 2 * CV_CH;
constexpr int LDS_GS = LDS_U + 8192;
constexpr int LDS_TAB = 36864;
constexpr int KP_CH = 13840;
constexpr int LDS_KP = LDS_TAB + 2 * 64 * 528 + 16;
constexpr int LDS_LN = 0, LDS_MR = 8448;
constexpr int LDS_WI = 145472, LDS_WO = LDS_WI + 10240;
constexpr int LDS_X = 65536;
constexpr int LDS_S = LDS_X + 24576;
constexpr int LDS_W = LDS_S + 6144;
constexpr int MEGA_LDS = LDS_W + 65536 + 2048;
static_assert(LDS_WO + 4608 <= MEGA_LDS && LDS_KP + 2 * KP_CH <= LDS_WI && LDS_S >= 73728, "lds");
static_assert(LDS_GS + 1280 <= LDS_TAB && LDS_KP + 2 * KP_CH <= MEGA_LDS && MEGA_LDS <= 160 * 1024, "lds");
#define FLAG_AT 48
static_assert(FLAG_AT >= 36 && FLAG_AT <= 56 && FLAG_AT % 4 == 0, "flag position");
#define WF_EARLY 5
#define WO0 12
#define WSP_P0 4
#define HINT4_KS 15
#define ZDEPTH 3
#define GELU_IN 0
#define EPI_W 0
#define MOD_POS 0
#define POLL3 0
#define GELU_PRE 4
#define HINT4_G 314
static_assert(HINT4_G > 4 * 64 + 12, "behind the marker barrier");
#define POLL4_G 48
#define RELABEL 1
#define CKMAP(ck) (RELABEL ? 2 * ((ck) & 3) + ((ck) >> 2) : (ck))
#define POLL_AT 130
#define MOD_MID 1
#define LATE_SLEEP 6
#define SMALL_N 3
#define ZRD 12
#define ZLA 4
#define IN_AUX 0
#define WSP_AUX 0

template <class T> DI T ldsr(unsigned a) { return *(const LAS T*)(size_t)a; }
template <class T> DI void ldsw(unsigned a, T v) { *(LAS T*)(size_t)a = v; }
DI v2u trread(const unsigned char* ptr) {
  typedef short v4i16_t __attribute__((ext_vector_type(4)));
  return __builtin_bit_cast(v2u, __builtin_amdgcn_ds_read_tr16_b64_v4i16((LAS v4i16_t*)ptr));
}
DI v2u trread_a(unsigned a) {
  typedef short v4i16_t __attribute__((ext_vector_type(4)));
  return __builtin_bit_cast(v2u, __builtin_amdgcn_ds_read_tr16_b64_v4i16((LAS v4i16_t*)(size_t)a));
}
struct CvOrder { int qq[36], pp[36]; bool first[36]; };
constexpr CvOrder cv_order() {
  CvOrder o{};
  int n = 0;
  for (int m = 0; m < 8; ++m) {
    for (int j = 0; j < m; ++j) if (m + j < 8) { o.qq[n] = m; o.pp[n] = j; ++n; }
    for (int j = 0; j <= m; ++j) if (m + j < 8) { o.qq[n] = j; o.pp[n] = m; ++n; }
  }
  for (int i = 0; i < 36; ++i) {
    o.first[i] = true;
    for (int j = 0; j < i; ++j) if (o.qq[j] + o.pp[j] == o.qq[i] + o.pp[i]) o.first[i] = false;
  }
  return o;
}
constexpr CvOrder CVO = cv_order();
struct Z2Order { int u[128], kind[128], pos[128]; };
constexpr Z2Order z2_order() {
  Z2Order o{};
  int n = 0;
  for (int u = 0; u < 40; ++u) {
    int pos = 0;
    const bool st = u < 24 && (u < 8 || (u & 7) < 4);
    for (int k = 0; k < 5; ++k)
      if (k < 3 ? u >= 8 : st) { o.u[n] = u; o.kind[n] = k; o.pos[n] = pos++; ++n; }
  }
  return o;
}
constexpr Z2Order Z2O = z2_order();

typedef __attribute__((address_space(1))) unsigned gu32;
DI unsigned tx(unsigned m, unsigned x, unsigned tag) { return __builtin_amdgcn_bitop3_b32(m, x, tag, 0xF6); }
DI unsigned tacc(unsigned m, v4u x, unsigned tag) { return tx(tx(m, x[1], tag), x[3], tag); }
DI bool any_nz(unsigned m) { return __builtin_amdgcn_ballot_w64(m != 0u) != 0ull; }
DI void hint_add(unsigned char* ws, int line) { (void)__hip_atomic_fetch_add((gu32*)(ws + WS_CNT + line * 128), 1u, __ATOMIC_RELAXED, __HIP_MEMORY_SCOPE_AGENT); }
DI void hint_wait(rsrc_t rws, unsigned voff, unsigned soff, unsigned want) {
  unsigned sp = 0;
  for (;;) {
    MEMFENCE();
    const unsigned v = __builtin_amdgcn_raw_buffer_load_b32(rws, (int)voff, (int)soff, SC1);
    if (!((__builtin_amdgcn_ballot_w64(v < want) != 0ull) & (++sp <= SPIN_CAP))) break;
    __builtin_amdgcn_s_sleep(OPT_E);
  }
  MEMFENCE();
}

extern "C" __global__ void __launch_bounds__(256, 1) adaln_mega(Params p) {
  extern __shared__ __attribute__((aligned(16))) unsigned char smem[];
  const int tid = threadIdx.x, lane = tid & 63, w = __builtin_amdgcn_readfirstlane(tid >> 6);
  const int b = blockIdx.x, xcd = b & 7, idx = b >> 3;
  const int pb = xcd * 32 + idx;
  const unsigned l16 = lane * 16;
  STAMP_DECL;
  STAMP(1, 0);
  v2u codepf;
  {
    unsigned long long pc;
    int rel_m, rel_e;
    asm volatile("s_getpc_b64 %0\n\ts_mov_b32 %1, adaln_mega@rel32@lo+4\n\ts_mov_b32 %2, adaln_code_end@rel32@lo+12" : "=s"(pc), "=s"(rel_m), "=s"(rel_e));
    const rsrc_t rcode = mkrs((const void*)(pc + (long long)rel_m), rel_e > rel_m ? (unsigned)(rel_e - rel_m) & ~15u : 0u);
    codepf = ld64<0>(rcode, tid * 8, idx * 2048);
  }
  SB();
  const rsrc_t ru = mkrs(p.u, L * D * 2);
  v4u xr[2];
  xr[0] = ld128<IN_AUX>(ru, l16, (pb * 8 + w * 2) * 1024);
  xr[1] = ld128<IN_AUX>(ru, l16 + 1024, (pb * 8 + w * 2) * 1024);
  const int mkg = b & 3, mcg = b >> 2;
  const unsigned cq = p.c[mkg * 128 + (tid & 3) * 32 + (tid >> 3)];
  const unsigned epoch = *(const unsigned*)(p.ws + WS_EPOCH);
  const rsrc_t rws = mkrs(p.ws, WS_END);
  __builtin_amdgcn_raw_buffer_store_b32(0u, rws, tid < 2 ? tid * 32768u : OOB, WS_CNT + b * 128, SC1);
  SB();
  const int cch = w >> 1, eh = w & 1, ns = tid & 127;
  const unsigned q_cre = p.cre[pb * 256 + tid], q_cim = p.cim[pb * 256 + tid], q_lar = p.lar[pb * 256 + tid], q_aim = p.aim[pb * 256 + tid];
  const unsigned q_ldt = p.ldt[pb * 2 + cch], q_dsk = p.dsk[pb * 2 + cch];
  v4u xm[4];
#define LOAD_XM() { \
  const rsrc_t rmod = mkrs(p.wmod, D * 3072 * 2); \
_Pragma("unroll") \
  for (int i = 0; i < 4; ++i) xm[i] = ld128<IN_AUX>(rmod, (tid >> 3) * 6144 + (tid & 7) * 16, (mkg * 128 + i * 32) * 6144 + mcg * 128 + (b < 192 ? 0u : OOB)); }
  SB();
  LOAD_XM();
  SB();
  STAMP(1, 24);
  const int c = lane & 15, q = lane >> 4;
  const unsigned lds0 = (unsigned)(size_t)(LAS unsigned char*)smem;
  unsigned pos4[9];
  {
    const float f4 = (float)(8188 - tid * 32);
#pragma unroll
    for (int j = 0; j < 10; j += 2) {
      const unsigned pp = pk2(bc2(f4) - f32x2{4.f * j, 4.f * j + 4.f});
      const int p0 = (int)bflo(pp), p1 = (int)bfhi(pp);
      pos4[j] = p0 + ((p0 >> 5) << 2) + lds0 + LDS_KP;
      PIN(pos4[j]);
      if (j + 1 < 9) { pos4[j + 1] = p1 + ((p1 >> 5) << 2) + lds0 + LDS_KP; PIN(pos4[j + 1]); }
    }
  }
  unsigned co[2][4];
  {
    const unsigned b0 = (eh ? 1152 + 576 * q : 144 * (q >> 1) + 4 * (q & 1)) * 4 + 36 * c + lds0 + LDS_KP + cch * KP_CH;
    const unsigned b1 = (q < 2 ? 288 + 144 * q : 568 + 4 * q) * 4 + 36 * c + lds0 + LDS_KP + cch * KP_CH;
    const unsigned s0 = eh ? 576 : 4, s1 = q < 2 ? 8 : 576;
#pragma unroll
    for (int r = 0; r < 4; ++r) { co[0][r] = b0 + r * s0; co[1][r] = b1 + r * s1; PIN(co[0][r]); PIN(co[1][r]); }
  }
  *(unsigned*)(smem + LDS_KP - 16 + cch * KP_CH + (lane & 3) * 4) = 0u;
  unsigned a_pad, a_wi, a_wo, a_tab, gm_off, gc_off;
  a_pad = lds0 + cch * CV_CH + (lane < 32 ? CV_H + l16 : ((lane >> 1) & 1) * CV_RK + 4096 + (lane & 1) * 16);
  a_wi = lds0 + LDS_WI + (w * 32 + 8 * q + ((lane >> 2) & 3)) * 80 + (lane & 3) * 16;
  a_wo = lds0 + LDS_WO + (8 * q + ((lane >> 2) & 3)) * 144 + (w >> 1) * 64 + (lane & 3) * 16 + (w & 1) * 8;
  a_tab = lds0 + LDS_TAB + cch * (64 * 528) + c * 528 + q * 16;
  gm_off = ((4 - cch) * 512 + mkg * 128 + ns) * 8;
  gc_off = (((lane & 7) > 5 ? 0 : ((lane & 7) >> 1)) * 512 + pb * 2 + (lane & 1)) * 8;
  PIN(a_pad); PIN(a_wi); PIN(a_wo); PIN(a_tab); PIN(gm_off); PIN(gc_off);
  unsigned ab[4], bb, dv[4], rdo4[4], xoff;
#pragma unroll
  for (int i = 0; i < 4; ++i) {
    ab[i] = lds0 + cch * CV_CH + (1 - (c & 1)) * CV_RK + 2 * (2030 - 2 * (c >> 1) + 8 * q) - eh * 256 - 192 - 512 - 1024 * i;
    dv[i] = (lane >> 3) * 4096 + (((lane & 7) ^ ((lane >> 4) & 1) ^ (i << 1)) * 16) + i * (32768 - 1024);
    rdo4[i] = lds0 + LDS_W + w * 16384 + ((perm((i & 1) * 2 ^ (2 * (w & 1)), c) * 128 + ((q ^ (c >> 1)) * 16)) ^ ((i >> 1) * 64));
    PIN(ab[i]); PIN(dv[i]); PIN(rdo4[i]);
  }
  bb = lds0 + cch * CV_CH + CV_H + 2 * (256 - 16 + 16 * c + 8 * q) - eh * 256 - 192;
  xoff = WS_X1T + (pb * 2 + cch) * 4096 + eh * 2048 + ((c >> 1) * 32 + ((q & 1) * 4 + 2 * (c & 1) + (q >> 1)) * 4) * 2;
  PIN(bb); PIN(xoff);
  SB();
  STAMP(1, 26);
  {
    *(v4u*)(smem + LDS_LN + (w * 2) * 1040 + l16) = xr[0];
    *(v4u*)(smem + LDS_LN + (w * 2 + 1) * 1040 + l16) = xr[1];
    float sm[2], sq[2];
#pragma unroll
    for (int i = 0; i < 2; ++i) {
      sm[i] = dot2(xr[i][0], ONES, dot2(xr[i][1], ONES, dot2(xr[i][2], ONES, dot2(xr[i][3], ONES, 0.f))));
      sq[i] = dot2(xr[i][0], xr[i][0], dot2(xr[i][1], xr[i][1], dot2(xr[i][2], xr[i][2], dot2(xr[i][3], xr[i][3], 0.f))));
    }
    const float tv = row16_sum(xrow_sum4(sm[0], sm[1], sq[0], sq[1]));
    auto sw = __builtin_amdgcn_permlane32_swap(__float_as_uint(tv), __float_as_uint(tv), false, false);
    const float m = __uint_as_float(sw[0]) * (1.0f / D);
    const float r = __builtin_amdgcn_rsqf(fmaxf(__uint_as_float(sw[1]) * (1.0f / D) - m * m, 0.f) + EPS);
    ((float*)(smem + LDS_GS + 1088))[(q >> 1) * 8 + w * 2 + (q & 1)] = (q >> 1) ? -m * r : r;
  }
  const unsigned tag = epoch * 2654435761u + 0x9E3779B9u;
  STAMP(1, 28);
  asm volatile("s_waitcnt vmcnt(%0)" ::"i"(MOD_POS == 0 ? 8 : 4) : "memory");
  bar();
  STAMP(1, 16);
  {
    unsigned ua[4][2];
#pragma unroll
    for (int i = 0; i < 4; ++i) {
      const unsigned char* src = smem + LDS_LN + (tid & 3) * 2080 + (i * 64 + (tid >> 2)) * 4;
      ua[i][0] = *(const unsigned*)src; ua[i][1] = *(const unsigned*)(src + 1040);
    }
    const unsigned lnv = ((const unsigned*)(smem + LDS_GS + 1088))[c];
    SB();
#pragma unroll
    for (int i = 0; i < 4; ++i) st128<SC1>(v4u{ua[i][0], tag, ua[i][1], tag}, rws, (tid >> 2) * 16384 + (tid & 3) * 16, WS_UG + i * 64 * 16384 + pb * 64);
    st64<SC1>(v2u{lnv, tag}, rws, tid < 16 ? (c >> 3) * 16384 + (c & 7) * 8 : OOB, WS_SR + pb * 64);
  }
  STAMP(1, 17);
  SB();
  const unsigned wp_oob = b < 176 ? 0u : OOB;
  const int wpr = b >> 2, wkg = b & 3;
  v4u xi[2], xo;
  {
    const rsrc_t rwin = mkrs(p.win, D * FF * 2), rwout = mkrs(p.wout, FH * D * 2);
    const int hf = wpr >= 22, pp = wpr - 22 * hf;
    const unsigned v = ((tid >> 2) * (FF * 2) + (tid & 3) * 16) | ((pp == 21 ? tid & 2 : 0) << 29);
#pragma unroll
    for (int i = 0; i < 2; ++i) xi[i] = ld128<IN_AUX>(rwin, v, (wkg * 128 + i * 64) * (FF * 2) + (hf * FH + pp * 32) * 2 + wp_oob);
    xo = ld128<IN_AUX>(rwout, (tid >> 3) * 1024 + (tid & 7) * 16, idx * 32 * 1024 + xcd * 128);
  }
  SB();
  const int d0 = pb * 2, dk = d0 + cch;
  const int mt = (xcd >> 1) * 8 + (idx & 7), nt = (xcd & 1) * 4 + (idx >> 3);
  const int big = (nt + 2) >> 3;
  const int np = 3 - big, pr0 = 3 * nt - big * (nt - 6);
  const int kh = w & 1, uh = w >> 1;
  const int uo0 = pr0 * 2 + uh * (4 - 2 * big), ut0 = pr0 * 2 + (uh ^ 1) * (4 - 2 * big), uo2 = pr0 * 2 + 2 + uh, ut2 = pr0 * 2 + 2 + (uh ^ 1);
#define UO(k) ((k) == 2 ? uo2 : uo0 + (k))
#define UT(k) ((k) == 2 ? ut2 : ut0 + (k))
  const unsigned oob3 = (unsigned)big << 30;
  const int kr = 2 * (idx >> 3) + (xcd & 1) + (OPT_B == 0 ? (idx & 7) : OPT_B == 1 ? 4 * (idx & 1) : OPT_B == 2 ? 2 * (idx & 3) : 0);
  const rsrc_t rwsp = mkrs(p.wsp, L * L * 2);
  const unsigned wp_fr = (wpr * 2 * 16 + wkg * 4 + w) * 1024 + wp_oob;
  v4u wr[1][8];
  const unsigned wsp_v = (lane >> 3) * 4096 + (lane & 7) * 16, wsp_s = mt * 64 * 4096 + w * 1024;
#define LOAD_WSP1(ck, i) wr[ck][i] = ld128<WSP_AUX>(rwsp, wsp_v + (((ck) + kr) & 7) * 128, wsp_s + (i) * 8 * 4096)
#define LOAD_WSPN(n) LOAD_WSP1((n) >> 3, (n) & 7)
  const v4u mk = {~0u, ~0u, ~0u, ~0u};
  unsigned bspq[2];
#pragma unroll
  for (int t = 0; t < 2; ++t) bspq[t] = p.bsp[mt * 64 + perm(2 * kh + t, c)];
  const unsigned tag_s = __builtin_amdgcn_readfirstlane(tag);
  {
    const float dt = rbf(exp_acc(bf2f(q_ldt)));
    const float cre = bf2f(q_cre) * dt, cim = bf2f(q_cim) * dt;
    const float are2 = -rbf(exp_acc(bf2f(q_lar))) * dt * 1.4426950408889634f;
    const float aim = bf2f(q_aim) * dt;
    const float rev_hi = aim * 0.15915494309189535f;
    const float rev_lo = __builtin_fmaf(aim, 0.15915494309189535f, -rev_hi) + aim * 6.4206383e-9f;
    int ex = __builtin_amdgcn_frexp_expf(xrow_max(row16_max(fmaxf(fabsf(cre), fabsf(cim))))) + 1;
    ex = ex < -100 ? -100 : ex;
    const float scale = __builtin_amdgcn_ldexpf(1.f, -ex);
    ((float*)(smem + LDS_GS + 1024))[w] = __builtin_amdgcn_ldexpf(2.f, ex);
    const f32x2 c0 = {cre * scale, cim * scale};
    const f32x2 e1 = cexp_p(are2, rev_hi, rev_lo, 1.f), e8 = cexp_p(are2, rev_hi, rev_lo, 8.f), e64 = cexp_p(are2, rev_hi, rev_lo, 64.f);
    unsigned char* tn = smem + LDS_TAB + cch * (64 * 528) + ns * 4;
#define TROW(r, z) *(unsigned*)(tn + (r) * 528) = pkh((z)[0], -(z)[1])
    SB();
    const f32x2 e128 = cmul(e64, e64), e2 = cmul(e1, e1), e256 = cmul(e128, e128), e4 = cmul(e2, e2), e512 = cmul(e256, e256);
    f32x2 f0 = {1.f, 0.f}, f1 = e64;
    f32x2 ca = cmul(c0, e512), cb = cmul(ca, e512);
#pragma unroll
    for (int j = 0; j < 8; ++j) {
      *(unsigned*)(tn + (48 + j) * 528) = pkh(f0[0], f0[1]);
      *(unsigned*)(tn + (56 + j) * 528) = pkh(f1[0], f1[1]);
      TROW(32 + j, cb);
      f0 = j == 0 ? e8 : cmul(f0, e8); f1 = cmul(f1, e8); cb = cmul(cb, e128);
      if (j < 4) {
        const f32x2 z = cmul(ca, e4);
        TROW(24 + j, ca); TROW(28 + j, z);
        ca = cmul(ca, e128);
      }
      if (j == 3 || j == 7) {
        PIN(f0); PIN(f1); PIN(cb);
        if (WSP_P0 >= 4) { LOAD_WSPN(j >> 2 << 1); LOAD_WSPN((j >> 2 << 1) + 1); }
        SB();
      }
    }
    TROW(40, cb);
    SB();
  {
    const float cv = bf2f(cq);
    const float sl = rbf(cv * __builtin_amdgcn_rcpf(1.0f + __builtin_amdgcn_exp2f(cv * -1.4426950408889634f)));
    const float sc[4] = {dpp<0x00>(sl), dpp<0x55>(sl), dpp<0xAA>(sl), dpp<0xFF>(sl)};
    f32x2 a[4];
#pragma unroll
    for (int i = 0; i < 4; ++i)
#pragma unroll
      for (int j = 0; j < 4; ++j) a[j] = i == 0 ? unpk(xm[i][j]) * bc2(sc[i]) : fma2(unpk(xm[i][j]), bc2(sc[i]), a[j]);
    float* red = (float*)(smem + LDS_MR) + (tid >> 3) * 68 + (tid & 7) * 8;
    *(f32x4*)red = f32x4{a[0][0], a[0][1], a[1][0], a[1][1]};
    *(f32x4*)(red + 4) = f32x4{a[2][0], a[2][1], a[3][0], a[3][1]};
  }
    bar();
    float ms8[8];
#pragma unroll
    for (int i = 0; i < 8; ++i) ms8[i] = ((const float*)(smem + LDS_MR))[(q * 8 + i) * 68 + w * 16 + c];
    SB();
    f32x2 t1[8];
    t1[1] = e1; t1[2] = e2; t1[4] = e4;
    t1[3] = cmul(e2, e1); t1[5] = cmul(e4, e1); t1[6] = cmul(e2, e4); t1[7] = cmul(t1[6], e1);
    const f32x2 c1 = cmul(c0, e128), c2 = cmul(c0, e256), c3 = cmul(c2, e128);
    TROW(0, c0); TROW(8, c1); TROW(16, c2); TROW(20, c3);
    {
    const float s = xrow_sum(((ms8[0] + ms8[1]) + (ms8[2] + ms8[3])) + ((ms8[4] + ms8[5]) + (ms8[6] + ms8[7])));
    st64<SC1>(v2u{__float_as_uint(s), tag}, rws, q == 0 ? c * 8 : OOB, WS_MODG + (mkg * 3072 + mcg * 64 + w * 16) * 8 + (b < 192 ? 0u : OOB));
    }
    SB();
#pragma unroll
    for (int v = 1; v < 8; ++v) {
      const f32x2 z0 = cmul(t1[v], c0), z1 = cmul(t1[v], c1);
      TROW(v, z0); TROW(8 + v, z1);
      if ((v & 1) == 0) {
        const f32x2 z2 = cmul(t1[v], c2), z3 = cmul(t1[v], c3);
        TROW(16 + v / 2, z2); TROW(20 + v / 2, z3);
        if (v < 6 && WSP_P0 >= 8) { LOAD_WSPN(v + 2); LOAD_WSPN(v + 3); }
        SB();
      }
    }
  }
  STAMP(1, 27);
#pragma unroll
  for (int i = 0; i < 2; ++i) *(v4u*)(smem + LDS_WI + (i * 64 + (tid >> 2)) * 80 + (tid & 3) * 16) = xi[i];
  *(v4u*)(smem + LDS_WO + (tid >> 3) * 144 + (tid & 7) * 16) = xo;
  STAMP(1, 29);
  bar();
  STAMP(1, 18);
  SB();
  v4u gr[4], gb_[4];
  v2u gm[4], gc[4];
#define LOAD_GS() _Pragma("unroll") for (int i_ = 0; i_ < 4; ++i_) { gr[i_] = ld128<SC1>(rws, tid * 16, WS_SR + i_ * 4096); gb_[i_] = ld128<SC1>(rws, tid * 16, WS_SB + i_ * 4096); }
#define LOAD_GM() _Pragma("unroll") for (int i_ = 0; i_ < 4; ++i_) { gm[i_] = ld64<SC1>(rws, gm_off, WS_MODG + i_ * 24576); gc[i_] = ld64<SC1>(rws, gc_off, WS_MODG + i_ * 24576); }
  v4u ug[4], wt[2];
#define LOAD_UG() _Pragma("unroll") for (int i_ = 0; i_ < 4; ++i_) ug[i_] = ld128<SC1>(rws, tid * 16, WS_UG + pb * 16384 + i_ * 4096)
  {
    v4u fa[2][8], fb[8];
    const unsigned ta = a_tab + eh * (32 * 528);
#pragma unroll
    for (int ks = 0; ks < 3; ++ks) { fb[ks] = ldsr<v4u>(a_tab + 48 * 528 + ks * 64); fa[0][ks] = ldsr<v4u>(ta + ks * 64); fa[1][ks] = ldsr<v4u>(a_tab + 16 * 528 + ks * 64); }
    SB();
    LOAD_UG();
    SB();
    f32x4 kk[2][2];
    f32x2 us;
    v2u wh_[6];
#pragma unroll
    for (int i = 0; i < 4; ++i) kk[i >> 1][i & 1] = f32x4{0.f, 0.f, 0.f, 0.f};
#pragma unroll
    for (int ks = 0; ks < 8; ++ks) {
      if (ks < 5) { fb[3 + ks] = ldsr<v4u>(a_tab + 48 * 528 + (3 + ks) * 64); fa[0][3 + ks] = ldsr<v4u>(ta + (3 + ks) * 64); fa[1][3 + ks] = ldsr<v4u>(a_tab + 16 * 528 + (3 + ks) * 64); }
      if (ks == 5) { us = *(const f32x2*)(smem + LDS_GS + 1024 + cch * 8); wh_[4] = trread_a(a_wo); wh_[5] = trread_a(a_wo + 576); }
      if (ks == 6) { wh_[0] = trread_a(a_wi); wh_[1] = trread_a(a_wi + 320); wh_[2] = trread_a(a_wi + 8); wh_[3] = trread_a(a_wi + 328); }
      kk[0][ks >> 2] = mfma_h(fa[0][ks], fb[ks], kk[0][ks >> 2]); kk[1][ks >> 2] = mfma_h(fa[1][ks], fb[ks], kk[1][ks >> 2]);
      SB();
    }
    STAMP(1, 37);
    wt[0] = v4u{wh_[0][0], wh_[0][1], wh_[1][0], wh_[1][1]};
    wt[1] = v4u{wh_[2][0], wh_[2][1], wh_[3][0], wh_[3][1]};
    st128<SC1>(v4u{wh_[4][0], wh_[4][1], wh_[5][0], wh_[5][1]}, rws, l16, WS_WOT + ((xcd * 4 + w) * 22 + idx) * 1024 + wp_oob);
    LOAD_GS();
    SB();
#pragma unroll
    for (int i = 0; i < 4; ++i) PINA(kk[i >> 1][i & 1]);
    f32x4 k0 = kk[0][0] * us[0] + kk[0][1] * us[1];
    const f32x4 k1 = kk[1][0] * us[0] + kk[1][1] * us[1];
    k0[0] += ns == 0 ? bf2f(q_dsk) : 0.f;
#pragma unroll
    for (int r = 0; r < 4; ++r) { ldsw<float>(co[0][r], k0[r]); ldsw<float>(co[1][r], k1[r]); }
  }
  STAMP(1, 31);
  bar();
  STAMP(1, 19);
  {
    float v[2][9];
#pragma unroll
    for (int j = 0; j < 9; ++j) v[0][j] = ldsr<float>(pos4[j]);
    LOAD_GM();
#pragma unroll
    for (int j = 0; j < 9; ++j) v[1][j] = ldsr<float>(pos4[j] + KP_CH);
    ldsw<v4u>(a_pad, v4u{0u, 0u, 0u, 0u});
    SB();
    STAMP(1, 36);
#pragma unroll
    for (int ch = 0; ch < 2; ++ch) {
      unsigned char* rk = smem + ch * CV_CH + tid * 16;
#pragma unroll
      for (int sft = 0; sft < 2; ++sft)
        *(v4u*)(rk + sft * CV_RK) = v4u{pk2(v[ch][sft], v[ch][sft + 1]), pk2(v[ch][sft + 2], v[ch][sft + 3]), pk2(v[ch][sft + 4], v[ch][sft + 5]), pk2(v[ch][sft + 6], v[ch][sft + 7])};
    }
#pragma unroll
    for (int i = WSP_P0; i < 8; ++i) LOAD_WSPN(i);
    st128<SC1>(mk, rws, l16, WS_X1T + dk * 4096 + eh * 2048);
    st128<SC1>(mk, rws, l16 + 1024, WS_X1T + dk * 4096 + eh * 2048);
    __builtin_amdgcn_raw_buffer_store_b32(~0u, rws, lane * 4, WS_ST2 + ((mt * 2 + kh) * 16 + nt * 2 + uh) * 256, SC1);
    SB();
    STAMP(1, 32);
    unsigned m = 0;
#pragma unroll
    for (int i = 0; i < 4; ++i) { m = tacc(m, ug[i], tag); m = tacc(m, gr[i], tag); m = tacc(m, gb_[i], tag); }
    if (__builtin_expect(any_nz(m), 0)) {
      unsigned sp = 0;
      do {
        MEMFENCE(); LOAD_UG(); LOAD_GS(); m = 0;
#pragma unroll
        for (int i = 0; i < 4; ++i) { m = tacc(m, ug[i], tag); m = tacc(m, gr[i], tag); m = tacc(m, gb_[i], tag); }
      } while (any_nz(m) & (++sp <= SPIN_CAP));
    }
    f32x2 nn[4][2];
#pragma unroll
    for (int i = 0; i < 4; ++i) {
      nn[i][0] = fma2(unpk(ug[i][0]), bc2(__uint_as_float(gr[i][0])), bc2(__uint_as_float(gb_[i][0])));
      nn[i][1] = fma2(unpk(ug[i][2]), bc2(__uint_as_float(gr[i][2])), bc2(__uint_as_float(gb_[i][2])));
      PIN(nn[i][0]); PIN(nn[i][1]);
      *(unsigned*)(smem + LDS_U + i * 1024 + tid * 4) = __builtin_amdgcn_perm(ug[i][2], ug[i][0], 0x05040100u);
      *(unsigned*)(smem + LDS_U + 4096 + i * 1024 + tid * 4) = __builtin_amdgcn_perm(ug[i][2], ug[i][0], 0x07060302u);
    }
    SB();
    STAMP(1, 33);
    m = 0;
#pragma unroll
    for (int i = 0; i < 4; ++i) m = tx(tx(m, gm[i][1], tag), gc[i][1], tag);
    if (__builtin_expect(any_nz(m), 0)) {
      unsigned sp = 0;
      do {
        __builtin_amdgcn_s_sleep(3);
        MEMFENCE(); LOAD_GM(); m = 0;
#pragma unroll
        for (int i = 0; i < 4; ++i) m = tx(tx(m, gm[i][1], tag), gc[i][1], tag);
      } while (any_nz(m) & (++sp <= SPIN_CAP));
    }
    const float mv = rbf((__uint_as_float(gm[0][0]) + __uint_as_float(gm[1][0])) + (__uint_as_float(gm[2][0]) + __uint_as_float(gm[3][0])));
    *(u16*)(smem + LDS_GS + tid * 2) = (u16)pk2(mv + (cch ? 0.f : 1.0f), 0.f);
    const float cv = rbf((__uint_as_float(gc[0][0]) + __uint_as_float(gc[1][0])) + (__uint_as_float(gc[2][0]) + __uint_as_float(gc[3][0])));
    const float gav = dpp<0x104>(cv), gav2 = dpp<0x102>(cv);
    const float hbv = cv * gav, hsv = rbf(1.0f + cv) * gav2;
    const f32x2 hb = {__builtin_bit_cast(float, __builtin_amdgcn_readlane(__builtin_bit_cast(int, hbv), 0)), __builtin_bit_cast(float, __builtin_amdgcn_readlane(__builtin_bit_cast(int, hbv), 1))};
    const f32x2 hs = {__builtin_bit_cast(float, __builtin_amdgcn_readlane(__builtin_bit_cast(int, hsv), 2)), __builtin_bit_cast(float, __builtin_amdgcn_readlane(__builtin_bit_cast(int, hsv), 3))};
    SB();
    bspq[0] |= bspq[1] << 16;
    PIN(bspq[0]);
    STAMP(1, 38);
#pragma unroll
    for (int i = 0; i < 4; ++i) {
      const f32x2 h0 = fma2(nn[i][0], hs, hb), h1 = fma2(nn[i][1], hs, hb);
      *(unsigned*)(smem + CV_H + 512 + i * 1024 + tid * 4) = pk2(h0[0], h1[0]);
      *(unsigned*)(smem + CV_CH + CV_H + 512 + i * 1024 + tid * 4) = pk2(h0[1], h1[1]);
    }
  }
  asm volatile("" ::"v"(codepf));
  STAMP(1, 39);
  bar();
  STAMP(1, 1);
#define CV_A(ee, k) ({ const unsigned a_ = ab[(k) >> 1] + 192 - 64 * (ee) + 512 * (1 - ((k) & 1)); v4u{ldsr<unsigned>(a_), ldsr<unsigned>(a_ + 4), ldsr<unsigned>(a_ + 8), ldsr<unsigned>(a_ + 12)}; })
#define CV_B(ee, k) ldsr<v4u>(bb + 192 - 64 * (ee) + 512 * (k))
  const unsigned slotA = LDS_W + w * 16384, slotB = slotA + 8192;
  v4u wq[8][8];
#define READ_W(dl, f) ldsr<v4u>(rdo4[(((f) >> 1) & 1) + ((f) >> 2) * 2] + (dl) + ((f) & 1) * 512)
  unsigned dso;
#define DMA_W(ck, slot, i) do { \
    if (((i) & 3) == 0) asm volatile("s_add_u32 m0, %1, %2\n\ts_add_u32 %0, %3, %4\n\tbuffer_load_dwordx4 %5, %6, %0 offen lds" \
                 : "=&s"(dso) : "s"(lds0 + (slot)), "i"((i) * 1024), "s"(wsp_s + ((CKMAP(ck) + kr) & 7) * 128), "i"((i) * 32768), "v"(dv[0]), "s"(rwsp) : "scc", "memory"); \
    else asm volatile("buffer_load_dwordx4 %0, %1, %2 offen offset:%3 lds" :: "v"(dv[(i) & 3]), "s"(rwsp), "s"(dso), "i"(((i) & 3) * 1024) : "memory"); } while (0)
  auto dma_sel = [&](auto CK, unsigned slot, int i) __attribute__((always_inline)) {
    constexpr int ck = decltype(CK)::value;
    switch (i) {
      case 0: DMA_W(ck, slot, 0); break; case 1: DMA_W(ck, slot, 1); break; case 2: DMA_W(ck, slot, 2); break; case 3: DMA_W(ck, slot, 3); break;
      case 4: DMA_W(ck, slot, 4); break; case 5: DMA_W(ck, slot, 5); break; case 6: DMA_W(ck, slot, 6); break; default: DMA_W(ck, slot, 7); break;
    }
  };
  v4u cA[2][8], cB[2][8];
  v4u ones4 = {ONES, ONES, ONES, ONES};
  PIN(ones4);
  f32x4 cb4[4];
  {
    const v4u gbf = *(const v4u*)(smem + LDS_GS + w * 64 + q * 16), sbf = *(const v4u*)(smem + LDS_GS + 256 + w * 64 + q * 16);
#pragma unroll
    for (int k = 0; k < 4; ++k) { cA[0][k] = CV_A(0, k); cB[0][k] = CV_B(0, k); }
    SB();
    const f32x4 z4 = {0.f, 0.f, 0.f, 0.f};
#pragma unroll
    for (int e = 0; e < 2; ++e) {
      v4u o;
#pragma unroll
      for (int jj = 0; jj < 4; ++jj) o[jj] = pk2(unpk(wt[e][jj]) * unpk(gbf[jj]));
      st128<SC1>(o, rws, l16 + e * 16384, WS_WP + wp_fr);
      cb4[2 * e + 1] = mfma(sbf, wt[e], z4);
      cb4[2 * e] = mfma(ones4, o, z4);
    }
  }
  SB();
  f32x4 cacc[8];
  v2u ue[4];
  float uf[16];
  unsigned flagv;
  unsigned wro[4], a_cp, a_ue, o_csb, o_flag;
  gu32* p_flag = (gu32*)(p.ws + WS_CNT + (256 + (tid & 7) * 16 + (idx & 15)) * 128);
  {
    f32x2 cp[4];
    float s0, s1, s2, s3;
    sfor<144>([&](auto G) __attribute__((always_inline)) {
      constexpr int g = decltype(G)::value, ee = g / 36, j = g % 36, t = CVO.pp[j] + CVO.qq[j];
      constexpr bool first = ee == 0 && CVO.first[j];
      cacc[t] = mfma(cA[ee & 1][CVO.qq[j]], cB[ee & 1][CVO.pp[j]], first ? f32x4{0.f, 0.f, 0.f, 0.f} : cacc[t]);
      SB();
      if constexpr (g < 8) {
        if constexpr (g & 1) cB[0][4 + (g >> 1)] = CV_B(0, 4 + (g >> 1));
        else cA[0][4 + (g >> 1)] = CV_A(0, 4 + (g >> 1));
      }
      constexpr int jn = ee == 0 ? (j >= 12 && (j - 12) % 3 != 2 ? (j - 12) / 3 * 2 + (j - 12) % 3 : -1) : (j & 1) ? j >> 1 : -1;
      if constexpr (ee < 3 && jn >= 0 && jn < 16) {
        if constexpr (jn & 1) cB[(ee + 1) & 1][jn >> 1] = CV_B(ee + 1, jn >> 1);
        else cA[(ee + 1) & 1][jn >> 1] = CV_A(ee + 1, jn >> 1);
      }
      if constexpr (g == 8) { a_cp = lds0 + LDS_S + 4096 + c * 16; PIN(a_cp); }
      if constexpr (g == 9) { cb4[0][1] = cb4[1][0]; cb4[0][2] = cb4[2][0]; PINA(cb4[0]); }
      if constexpr (g == 10) { cb4[0][3] = cb4[3][0]; PINA(cb4[0]); }
      if constexpr (g == 11) ldsw<f32x4>(a_cp + w * 256, cb4[0]);
      if constexpr (g == 17) { a_cp += (q & 1) * 8; PIN(a_cp); }
      if constexpr (g == 20) { o_csb = q * 512 + c * 8; PIN(o_csb); }
      if constexpr (g == 23) { o_csb = tid < 32 ? o_csb : OOB; PIN(o_csb); }
      if constexpr (g == 26) { PIN(p_flag); }
      if constexpr (g == FLAG_AT) {
        STAMP(1, 42);
        asm volatile("s_waitcnt vmcnt(%0) lgkmcnt(8)" ::"i"(FLAG_AT > 38 ? 4 + (FLAG_AT - 38 + 3) / 4 : 4) : "memory");
        __builtin_amdgcn_s_barrier();
        if (tid < 8) (void)__hip_atomic_fetch_add(p_flag, 1u, __ATOMIC_RELAXED, __HIP_MEMORY_SCOPE_AGENT);
        STAMP(1, 43);
      }
      if constexpr (g == FLAG_AT + 4 || g == FLAG_AT + 8) {
        cp[(g - FLAG_AT - 4) / 2] = ldsr<f32x2>(a_cp + (g - FLAG_AT - 4) * 128);
        cp[(g - FLAG_AT - 4) / 2 + 1] = ldsr<f32x2>(a_cp + (g - FLAG_AT - 4) * 128 + 256);
      }
      if constexpr (g == FLAG_AT + 16) { s0 = cp[0][0] + cp[1][0]; PIN(s0); s2 = cp[0][1] + cp[1][1]; PIN(s2); }
      if constexpr (g == FLAG_AT + 20) { s1 = cp[2][0] + cp[3][0]; PIN(s1); s3 = cp[2][1] + cp[3][1]; PIN(s3); }
      if constexpr (g == FLAG_AT + 24) { s0 += s1; PIN(s0); s2 += s3; PIN(s2); }
      if constexpr (g == FLAG_AT + 28) st64<SC1>(v2u{__float_as_uint(s0), __float_as_uint(s2)}, rws, o_csb, WS_CSB + wpr * 1024 + wkg * 128 + wp_oob);
      if constexpr (g == 36) STAMP(1, 40);
      if constexpr (g == 72) STAMP(1, 41);
      if constexpr (g == 14 || g == 29 || g == 32 || g == 35) DMA_W(1, slotA, g == 14 ? 0 : (g - 26) / 3);
      if constexpr (g >= 38 && g < 86 && (g & 3) == 2) DMA_W(1 + (g + 10) / 64, g < 54 ? slotA : slotB, (4 + (g - 38) / 4) & 7);
      if constexpr (g == 88) asm volatile("s_waitcnt vmcnt(8)" ::: "memory");
      if constexpr (g >= 90 && g < 106 && (g & 1) == 0) wq[1][(g - 90) >> 1] = READ_W(0, (g - 90) >> 1);
      if constexpr (g >= 68 && g < 72) { wro[g - 68] = (lds0 + slotA + (lane >> 3) * 128 + (((lane & 7) ^ ((lane >> 4) & 1)) * 16)) ^ ((g - 68) * 32); PIN(wro[g - 68]); }
      if constexpr (g == 105) { a_ue = lds0 + LDS_U + cch * 4096 + eh * 2048 + c * 32 + q * 8; PIN(a_ue); }
      if constexpr (g == 106 || g == 107) { ue[(g - 106) * 2] = ldsr<v2u>(a_ue + (g - 106) * 1024); ue[(g - 106) * 2 + 1] = ldsr<v2u>(a_ue + (g - 106) * 1024 + 512); }
      if constexpr (g >= 108 && g < 140 && (g & 3) == 0) ldsw<v4u>(wro[((g - 108) >> 2) & 3] + ((g - 108) >> 2) * 1024, wr[0][(g - 108) >> 2]);
      if constexpr (g >= 109 && g < 125 && (g & 1) == 1) {
        constexpr int x = (g - 109) >> 1;
        if constexpr (x >= 2) { uf[2 * (x - 2)] = bflo(ue[(x - 2) >> 1][x & 1]); PIN(uf[2 * (x - 2)]); uf[2 * (x - 2) + 1] = bfhi(ue[(x - 2) >> 1][x & 1]); PIN(uf[2 * (x - 2) + 1]); }
      }
      if constexpr (g == 118 || g == 122) { constexpr int x = 6 + (g == 122); uf[2 * x] = bflo(ue[3][x & 1]); PIN(uf[2 * x]); uf[2 * x + 1] = bfhi(ue[3][x & 1]); PIN(uf[2 * x + 1]); }
      if constexpr (g == 110) { o_flag = w == 0 ? c * 128 : OOB; PIN(o_flag); }
      if constexpr (g == 126) asm volatile("s_waitcnt vmcnt(0)" ::: "memory");
      if constexpr (g >= 127 && g < 143 && (g & 1) == 1) wq[2][(g - 127) >> 1] = READ_W(8192, (g - 127) >> 1);
      if constexpr (g == POLL_AT) flagv = __builtin_amdgcn_raw_buffer_load_b32(rws, o_flag, WS_CNT + (256 + xcd * 16) * 128, SC1);
      SB();
    });
  }
  STAMP(1, 2);
  unsigned late;
  {
    f32x4* red = (f32x4*)(smem + LDS_X) + w * 256 + lane;
    f32x4 keep[4];
    if (eh) {
#pragma unroll
      for (int t = 0; t < 4; ++t) red[t * 64] = cacc[t];
      SB();
#pragma unroll
      for (int t = 0; t < 4; ++t) { PINA(cacc[4 + t]); keep[t] = cacc[4 + t]; PIN(keep[t]); }
    } else {
#pragma unroll
      for (int t = 0; t < 4; ++t) red[t * 64] = cacc[4 + t];
      SB();
#pragma unroll
      for (int t = 0; t < 4; ++t) { PINA(cacc[t]); keep[t] = cacc[t]; PIN(keep[t]); }
    }
    f32x2 pre[4][2];
#pragma unroll
    for (int t = 0; t < 4; ++t) {
      pre[t][0] = f32x2{uf[4 * t], uf[4 * t + 1]} + lo2(keep[t]);
      pre[t][1] = f32x2{uf[4 * t + 2], uf[4 * t + 3]} + hi2(keep[t]);
      PIN(pre[t][0]); PIN(pre[t][1]);
    }
    if (w == 0) *(unsigned*)(smem + LDS_S) = __builtin_amdgcn_ballot_w64(flagv < 16u) != 0ull;
    bar();
    late = *(const unsigned*)(smem + LDS_S);
    const f32x4* rd = (const f32x4*)(smem + LDS_X) + (w ^ 1) * 256 + lane;
    f32x4 oth[4];
#pragma unroll
    for (int t = 0; t < 4; ++t) oth[t] = rd[t * 64];
#pragma unroll
    for (int t = 0; t < 4; ++t) {
      v2u o;
      o[0] = nomark(pk2(pre[t][0] + lo2(oth[t])));
      o[1] = pk2(pre[t][1] + hi2(oth[t]));
      st64<SC1>(o, rws, xoff + t * 512, 0);
    }
  }
  STAMP(1, 3);
  SB();
  v4u wf[3][8];
  const unsigned rot = uh * 4;
#define LOAD_WP(half, k, i) ld128<SC1>(rws, l16 + ((i) & 3) * 1024, WS_WP + (((half) * 44 + UO(k)) * 16 + kh * 8 + (((i) & 4) ^ rot)) * 1024 + ((k) == 2 ? oob3 : 0u))
#define LOAD_XS(i) ld128<SC1>(rws, wsp_v, WS_X1T + ((i) * 32 + w * 8) * 4096 + mt * 128)
  v4u xs[16];
  unsigned tb[2][4], xdst;
  SB();
  int x1o;
  unsigned z2o[3], dgo[4], rdo[2], xw, xrd, a_fin, a_fr;
  v4u mkv = mk;
  v2u cz[2][3];
  f32x2 csb[2][3];
  unsigned gq[4];
#define LOAD_CZ(hf, kc) ld64<SC1>(rws, lane * 8, WS_CSB + ((hf) * 44 + UO(kc)) * 512 + ((kc) == 2 ? oob3 : 0u))
  {
    STAMP(1, 20);
    if (__builtin_amdgcn_readfirstlane(late)) {
      if (w == 0) { hint_wait(rws, c * 128, WS_CNT + (256 + xcd * 16) * 128, 16u); __builtin_amdgcn_s_sleep(LATE_SLEEP); }
      __builtin_amdgcn_s_barrier();
    }
    MEMFENCE();
    STAMP(1, 21);
#pragma unroll
    for (int i = 0; i < 3; ++i)
#pragma unroll
      for (int k = 0; k < 3; ++k) wf[k][i] = LOAD_WP(1, k, i);
    SB();
#pragma unroll
  for (int f = 0; f < 8; ++f) wq[0][f] = READ_W(0, f);
#pragma unroll
  for (int f = 0; f < 24; ++f) PINA(wq[f >> 3][f & 7]);
    int lz = lane;
    PIN(lz);
  {
    const int row = 8 * (lz >> 4) + ((lz >> 2) & 3);
    const int sg = ((row >> 1) & 1) | (((row >> 3) & 1) << 1);
#pragma unroll
    for (int tu = 0; tu < 4; ++tu) {
      const unsigned t0 = lds0 + LDS_A + kh * 32768 + row * 128 + ((tu ^ (2 * kh) ^ sg) * 32) + (lz & 3) * 8;
      tb[0][tu] = t0 + uh * 16384;
      tb[1][tu] = t0 + (uh ^ 1) * 16384;
    }
    const int rl = w * 8 + (lz >> 3), chn = lz & 7;
    const int sg2 = ((rl >> 1) & 1) | (((rl >> 3) & 1) << 1);
    xdst = lds0 + LDS_A + rl * 128 + (((chn >> 1) ^ sg2) * 32) + (chn & 1) * 16;
  }
  {
    const int row = 8 * (lz >> 4) + ((lz >> 2) & 3);
    const int sg = ((row >> 1) & 1) | (((row >> 3) & 1) << 1);
    x1o = LDS_A + nt * 8192 + row * 128 + ((w ^ sg) * 32) + (lz & 3) * 8;
  }
#pragma unroll
  for (int k = 0; k < 3; ++k) { z2o[k] = lz * 16 + WS_Z2 + (UO(k) * 64 + mt * 2 + kh) * 1024 + (k == 2 ? oob3 : 0u); }
#pragma unroll
    for (int tu = 0; tu < 8; ++tu) PIN(tb[tu >> 2][tu & 3]);
    PIN(xdst); PIN(x1o);
    {
      const int c2 = lz & 15;
      const bool dg = (c2 >> 2) == (lz >> 4);
      const unsigned dump = lds0 + LDS_W + 65536 + lz * 16;
      dgo[0] = dg ? lds0 + LDS_S + (w * 64 + kh * 32 + c2) * 4 : dump; dgo[1] = dg ? lds0 + LDS_S + (w * 64 + (kh ^ 1) * 32 + c2) * 4 : dump;
      dgo[2] = dg ? lds0 + LDS_S + 2048 + (w * 64 + kh * 32 + c2) * 16 : dump; dgo[3] = dg ? lds0 + LDS_S + 2048 + (w * 64 + (kh ^ 1) * 32 + c2) * 16 : dump;
      rdo[0] = lds0 + LDS_S + (kh * 32 + (lz & 31)) * 4; rdo[1] = lds0 + LDS_S + 2048 + (kh * 32 + (lz & 31)) * 16 + (lz & 3) * 4;
      xw = lds0 + LDS_X + w * 6144 + lz * 16; xrd = lds0 + LDS_X + (w ^ 1) * 6144 + lz * 16;
      a_fin = lds0 + LDS_S + 1024 + w * 256 + lz * 4; a_fr = lds0 + LDS_S + 1024 + w * 256 + (lz >> 4) * 16;
      PIN(dgo[0]); PIN(dgo[1]); PIN(dgo[2]); PIN(dgo[3]); PIN(rdo[0]); PIN(rdo[1]); PIN(xw); PIN(xrd); PIN(a_fin); PIN(a_fr); PIN(mkv);
    }
#pragma unroll
    for (int k = 0; k < 3; ++k) PIN(z2o[k]);
    SB();
#pragma unroll
    for (int i = 3; i < WF_EARLY; ++i)
#pragma unroll
      for (int k = 0; k < 3; ++k) wf[k][i] = LOAD_WP(1, k, i);
    SB();
#pragma unroll
    for (int i = 0; i < 16; ++i) xs[i] = LOAD_XS(i);
    SB();
    STAMP(1, 44);
    unsigned m = 0;
#pragma unroll
    for (int i = 0; i < 16; ++i) {
      m = macc(m, xs[i]); ldsw<v4u>(xdst + i * 4096, xs[i]);
      if (WF_EARLY * 3 + i < 24) wf[(WF_EARLY * 3 + i) % 3][(WF_EARLY * 3 + i) / 3] = LOAD_WP(1, (WF_EARLY * 3 + i) % 3, (WF_EARLY * 3 + i) / 3);
      SB();
    }
    PIN(m);
    STAMP(1, 45);
    RETRY_BEGIN(m)
#pragma unroll
      for (int i = 0; i < 16; ++i) xs[i] = LOAD_XS(i);
      SB();
#pragma unroll
      for (int i = 0; i < 16; ++i) { m = macc(m, xs[i]); ldsw<v4u>(xdst + i * 4096, xs[i]); }
    RETRY_END(m)
  }
  STAMP(1, 4);
  bar();
  STAMP(1, 5);
#define AHALF(tu, i, h) trread_a(tb[(i) >> 2][tu] + ((i) & 3) * 4096 + (h) * 512)
  f32x4 acc[3][4];
  v4u wg[3][8];
  v2u ah[2][4][2];
  float* st = (float*)(smem + LDS_S);
  float* fin = st + 256 + w * 64;
  f32x4* xch = (f32x4*)(smem + LDS_X);
  f32x4 r4[2], m4[2];
  {
    f32x4 s1[4], s2[4];
    constexpr int LA = ZLA;
    v2u ar[LA + 1][2];
    float pa[4], pb[4];
#define UTILE(u) ((u) < 8 ? (u) >> 2 : (((u) >> 3) + 1) & 3)
#define UKS(u) ((u) < 8 ? (u) & 3 : (u) & 7)
#pragma unroll
    for (int u = 0; u < LA; ++u) { ar[u][0] = AHALF(UTILE(u), UKS(u), 0); ar[u][1] = AHALF(UTILE(u), UKS(u), 1); }
#pragma unroll
    for (int i = 0; i < 6; ++i) cz[i / 3][i % 3] = LOAD_CZ(i / 3, i % 3);
#pragma unroll
    for (int i = 0; i < 4; ++i) gq[i] = __builtin_amdgcn_raw_buffer_load_b32(rws, lane * 8, WS_MODG + (i * 3072 + 5 * 512 + nt * 64) * 8, SC1);
    DMA_W(3, slotB, 0);
    SB();
    sfor<128>([&](auto G) __attribute__((always_inline)) {
      constexpr int m = decltype(G)::value, u = Z2O.u[m], k = Z2O.kind[m], pos = Z2O.pos[m], tu = UTILE(u), i = UKS(u);
      const v4u a = {ar[u % (LA + 1)][0][0], ar[u % (LA + 1)][0][1], ar[u % (LA + 1)][1][0], ar[u % (LA + 1)][1][1]};
      const f32x4 z4 = {0.f, 0.f, 0.f, 0.f};
      if constexpr (k < 3) acc[k][tu] = mfma(a, wf[k][i], (i == 0 && tu >= 2) ? z4 : acc[k][tu]);
      if constexpr (k == 3) s1[tu] = mfma(ones4, a, i == 0 ? z4 : s1[tu]);
      if constexpr (k == 4) s2[tu] = mfma(a, a, i == 0 ? z4 : s2[tu]);
      SB();
      if constexpr (pos < 2 && u + LA < 40) ar[(u + LA) % (LA + 1)][pos] = AHALF(UTILE(u + LA), UKS(u + LA), pos);
      if constexpr (pos < 2 && u >= 36) ah[0][u - 36][pos] = AHALF(u - 36, 0, pos);
      if constexpr (u < 8 && pos == 0 && u != 4) { if constexpr (u > 0) DMA_W(3, slotB, u); }
      if constexpr (u == 4 && pos == 0) DMA_W(3, slotB, 4);
      if constexpr (u >= 12 && u < 15 && pos == 0) st128<SC1>(mkv, rws, z2o[u - 12], 0);
      if constexpr (m == 23 || m == 28 || m == 55 || m == 70) {
        constexpr int x = m == 23 ? 0 : m == 28 ? 1 : m == 55 ? 2 : 3;
        PINA(s1[x]);
        ldsw<float>(dgo[x >> 1] + (x & 1) * 64, s1[x][0]);
        if constexpr (x < 3) ldsw<f32x4>(dgo[2 + (x >> 1)] + (x & 1) * 256, s2[x]);
      }
      if constexpr (m == 71) ldsw<f32x4>(dgo[3] + 256, s2[3]);
      if constexpr (m == 35) __builtin_amdgcn_s_waitcnt(0x0F70);
      if constexpr (m == 46 || m == 51 || m == 56 || m == 61) {
        constexpr int x = (m - 46) / 5 * 2;
        wq[3][x] = READ_W(8192, x); wq[3][x + 1] = READ_W(8192, x + 1);
      }
      if constexpr (m == 50 || m == 60 || m == 65) ldsw<f32x4>(xw + (m == 50 ? 0 : m == 60 ? 1 : 2) * 2048, acc[m == 50 ? 0 : m == 60 ? 1 : 2][2]);
      if constexpr (m == 38 || m == 41 || m == 44 || m == 47) DMA_W(4, slotA, (m - 38) / 3);
      if constexpr (m == 66) DMA_W(4, slotA, 4);
      if constexpr (m == 68 || m == 72 || m == 78) DMA_W(4, slotA, m == 68 ? 5 : m == 72 ? 6 : 7);
      if constexpr (m == 73 || m == 94) bar();
      if constexpr (m >= 74 && m < 77) acc[m - 74][0] = ldsr<f32x4>(xrd + (m - 74) * 2048);
      if constexpr (m == 82 || m == 85 || m == 88) ldsw<f32x4>(xw + (m - 82) / 3 * 2048 + 1024, acc[(m - 82) / 3][3]);
      if constexpr (m >= 95 && m < 98) acc[m - 95][1] = ldsr<f32x4>(xrd + (m - 95) * 2048 + 1024);
      if constexpr (m == 77) {
#pragma unroll
        for (int f = 0; f < 8; ++f) PINA(wq[3][f]);
      }
      if constexpr (m >= 83 && m < 93 && (m - 83) % 3 != 2) {
        constexpr int x = (m - 83) / 3 * 2 + (m - 83) % 3;
        if constexpr (x < 4) pa[x] = ldsr<float>(rdo[0] + x * 256);
        else pb[x - 4] = ldsr<float>(rdo[1] + (x - 4) * 1024);
      }
      if constexpr (m == 93) pb[3] = ldsr<float>(rdo[1] + 3072);
      if constexpr (m == 98) { pa[0] += pa[1]; PIN(pa[0]); }
      if constexpr (m == 99) { pa[2] += pa[3]; PIN(pa[2]); }
      if constexpr (m == 101) { pb[0] += pb[1]; PIN(pb[0]); }
      if constexpr (m == 102) { pb[2] += pb[3]; PIN(pb[2]); }
      if constexpr (m == 104) { pa[0] += pa[2]; PIN(pa[0]); }
      if constexpr (m == 105) { pb[0] += pb[2]; PIN(pb[0]); }
      if constexpr (m == 107) { pa[0] *= (1.0f / D); PIN(pa[0]); }
      if constexpr (m == 108) { pa[1] = pa[0] * pa[0]; PIN(pa[1]); }
      if constexpr (m == 110) { pb[0] = __builtin_fmaf(pb[0], 1.0f / D, -pa[1]); PIN(pb[0]); }
      if constexpr (m == 111) { pb[0] = __builtin_amdgcn_fmed3f(pb[0], 0.f, 3.0e38f); PIN(pb[0]); }
      if constexpr (m == 113) { pb[0] += EPS; PIN(pb[0]); }
      if constexpr (m == 114) { pb[0] = __builtin_amdgcn_rsqf(pb[0]); PIN(pb[0]); }
      if constexpr (m == 116) { pa[0] = lane < 32 ? 1.0f : -pa[0]; PIN(pa[0]); }
      if constexpr (m == 117) { pb[0] *= pa[0]; PIN(pb[0]); }
      if constexpr (m == 119) ldsw<float>(a_fin, pb[0]);
      if constexpr (m == 120 || m == 122 || m == 123 || m == 125) {
        constexpr int x = m == 120 ? 0 : m == 122 ? 1 : m == 123 ? 2 : 3;
        if constexpr (x & 1) r4[x >> 1] = ldsr<f32x4>(a_fr + (x >> 1) * 64);
        else m4[x >> 1] = ldsr<f32x4>(a_fr + 128 + (x >> 1) * 64);
      }
      if constexpr (m >= 100 && m < 124 && (m - 100) % 3 == 0) DMA_W(5, slotB, (m - 100) / 3);
      if constexpr (m == 47) STAMP(1, 46);
      if constexpr (m == 79) STAMP(1, 47);
      SB();
    });
  }
#pragma unroll
  for (int x = 0; x < 3; ++x) {
    const float y = xrow_sum4(__uint_as_float(cz[1][x][0]), __uint_as_float(cz[1][x][1]), __uint_as_float(cz[1][x][0]), __uint_as_float(cz[1][x][1]));
    auto sw = __builtin_amdgcn_permlane16_swap(__float_as_uint(y), __float_as_uint(y), false, false);
    csb[1][x] = f32x2{__uint_as_float(sw[0]), __uint_as_float(sw[1])};
  }
#pragma unroll
  for (int k = 0; k < 6; ++k) PINA(acc[k >> 1][k & 1]);
  STAMP(1, 6);
  f32x4 z2v[3][2];
  {
    f32x4 rs[2], rq[2];
#pragma unroll
    for (int t = 0; t < 2; ++t) {
#pragma unroll
      for (int k = 0; k < 3; ++k) {
        const f32x4 a = acc[k][t];
        const f32x2 zl = gelu2(fma2(lo2(a), lo2(r4[t]), fma2(lo2(m4[t]), bc2(csb[1][k][0]), bc2(csb[1][k][1]))));
        const f32x2 zh = gelu2(fma2(hi2(a), hi2(r4[t]), fma2(hi2(m4[t]), bc2(csb[1][k][0]), bc2(csb[1][k][1]))));
        z2v[k][t] = f32x4{zl[0], zl[1], zh[0], zh[1]};
        if (k == 0) { rs[t] = z2v[k][t]; rq[t] = z2v[k][t] * z2v[k][t]; }
        else { rs[t] += z2v[k][t]; rq[t] += z2v[k][t] * z2v[k][t]; }
        if (((t * 3 + k) & 1) == 0) {
#pragma unroll
          for (int f = (t * 3 + k) * 4; f < (t * 3 + k) * 4 + 8; ++f) wg[f % 3][f / 3] = LOAD_WP(0, f % 3, f / 3);
        } else {
          PIN(z2v[k][t]);
          SB();
        }
      }
    }
    STAMP(1, 50);
    float v8[8], v4[4], v2[2];
    const bool b3 = c & 8, b2 = c & 4, b1 = c & 2, b0 = c & 1;
#pragma unroll
    for (int r_ = 0; r_ < 4; ++r_) {
      const float a0 = rs[0][r_] + dpp<0x128>(rs[0][r_]), a1 = rs[1][r_] + dpp<0x128>(rs[1][r_]);
      const float q0 = rq[0][r_] + dpp<0x128>(rq[0][r_]), q1 = rq[1][r_] + dpp<0x128>(rq[1][r_]);
      v8[2 * r_] = b3 ? a1 : a0; v8[2 * r_ + 1] = b3 ? q1 : q0;
    }
#pragma unroll
    for (int j = 0; j < 4; ++j) {
      const float x0 = v8[j] + dpp<0x141>(v8[j]), x1 = v8[4 + j] + dpp<0x141>(v8[4 + j]);
      v4[j] = b2 ? x1 : x0;
    }
#pragma unroll
    for (int j = 0; j < 2; ++j) {
      const float x0 = v4[j] + dpp<0x4E>(v4[j]), x1 = v4[2 + j] + dpp<0x4E>(v4[2 + j]);
      v2[j] = b1 ? x1 : x0;
    }
    const float x0 = v2[0] + dpp<0xB1>(v2[0]), x1 = v2[1] + dpp<0xB1>(v2[1]);
    const float outv = b0 ? x1 : x0;
    __builtin_amdgcn_raw_buffer_store_b32(nomark(__float_as_uint(outv)), rws, lane * 4, WS_ST2 + ((mt * 2 + kh) * 16 + nt * 2 + uh) * 256, SC1);
  }
  STAMP(1, 7);
  SB();
#pragma unroll
  for (int tu = 0; tu < 4; ++tu)
#pragma unroll
    for (int k = 0; k < 3; ++k) acc[k][tu] = f32x4{0.f, 0.f, 0.f, 0.f};
  unsigned k3v[6];
  {
    const unsigned k0 = l16 + w * 6144, w3 = (w + 1) >> 2;
    k3v[0] = k0; k3v[1] = k0 + 2048 - w3 * 1024; k3v[2] = k3v[1] + 1024; k3v[3] = k0 + 5120 - w3 * 2048;
    k3v[4] = k0 + 1024 + (w3 << 30); k3v[5] = k3v[4] + 3072;
  }
  v2u x1h[2][2];
  v4u wo[6][4];
#define LOAD_WO(f) wo[(f) >> 2][(f) & 3] = ld128<SC1>(rws, k3v[(f) >> 2], WS_WOT + (nt * 4 + ((f) & 3)) * 22 * 1024)
  auto dma_sel2 = [&](int i, int x) __attribute__((always_inline)) {
    if (i < 4) dma_sel(IC<6>{}, slotA, (i - 2) * 4 + x); else dma_sel(IC<7>{}, slotB, (i - 4) * 4 + x);
  };
  f32x4 pt[4];
  float* c1s = st + 768 + w * 96;
#define LOAD_PT(i) asf(ld128<SC1>(rws, l16 + (i) * 1024, WS_ST2 + (mt * 2 + kh) * 4096))
  SB();
#pragma unroll
  for (int i = 0; i < 8; ++i) {
#pragma unroll
    for (int j = 0; j < 12; ++j) {
      const int tu = j / 3, k = j % 3;
      const v4u a = {ah[i & 1][tu][0][0], ah[i & 1][tu][0][1], ah[i & 1][tu][1][0], ah[i & 1][tu][1][1]};
      acc[k][tu] = mfma(wg[k][i], a, acc[k][tu]);
      SB();
      if (j < 8 && i < 7) ah[(i + 1) & 1][j >> 1][j & 1] = AHALF(j >> 1, i + 1, j & 1);
      if (j < 4 && i == 7) x1h[j >> 1][j & 1] = trread(smem + x1o + (j >> 1) * 4096 + (j & 1) * 512);
      if (i == 0 && j == 0) asm volatile("s_waitcnt vmcnt(24)" ::: "memory");
      if (i < 2 && j >= 4) wq[4 + i][j - 4] = READ_W(i * 8192, j - 4);
      if (i == 3 && j == 0) {
#pragma unroll
        for (int f = 0; f < 16; ++f) PINA(wq[4 + (f >> 3)][f & 7]);
      }
      if (i == 2 && j == 8) USE(wg[2][7]);
      if (i >= 2 && i < 6 && j == 9) dma_sel2(i, 0);
      if (i >= 3 && i < 7 && (j == 1 || j == 4 || j == 7)) dma_sel2(i - 1, (j + 2) / 3);
      if (j >= 8 && i == 6) pt[j - 8] = LOAD_PT(j - 8);
      if (i == 7 && j >= 8) LOAD_WO(j - 8);
      if (i == 5 && j == 10) {
        asm volatile("s_waitcnt vmcnt(30)" ::: "memory");
        __builtin_amdgcn_s_barrier();
      }
      SB();
    }
  }
  STAMP(1, 8);
#pragma unroll
  for (int k = 0; k < 3; ++k)
#pragma unroll
    for (int t = 0; t < 2; ++t) xch[(w * 6 + k * 2 + t) * 64 + lane] = acc[k][2 + t];
  SB();
  {
    unsigned m = 0;
#pragma unroll
    for (int i = 0; i < 4; ++i) m = macc4(m, __builtin_bit_cast(v4u, pt[i]));
    RETRY_BEGIN(m)
#pragma unroll
      for (int i = 0; i < 4; ++i) { pt[i] = LOAD_PT(i); m = macc4(m, __builtin_bit_cast(v4u, pt[i])); }
    RETRY_END(m)
    if (tid < 4) hint_add(p.ws, 64 + (nt * 4 + (mt >> 3)) * 4 + tid);
    const f32x4 sx = (pt[0] + pt[1]) + (pt[2] + pt[3]);
    const float tv = xrow_sum4(sx[0], sx[2], sx[1], sx[3]);
    auto sw = __builtin_amdgcn_permlane32_swap(__float_as_uint(tv), __float_as_uint(tv), false, false);
    const float mm = __uint_as_float(sw[0]) * (1.0f / FH);
    const float r = __builtin_amdgcn_rsqf(fmaxf(__uint_as_float(sw[1]) * (1.0f / FH) - mm * mm, 0.f) + EPS);
    float* f2 = st + 512 + w * 64;
    f2[(q >> 1) * 32 + 2 * c + (q & 1)] = (q >> 1) ? -r * mm : r;
#pragma unroll
    for (int t = 0; t < 2; ++t) { r4[t] = *(const f32x4*)(f2 + q * 8 + t * 4); m4[t] = *(const f32x4*)(f2 + 32 + q * 8 + t * 4); }
#pragma unroll
    for (int k = 0; k < 3; ++k) {
      v4u o;
#pragma unroll
      for (int t = 0; t < 2; ++t) {
        o[2 * t] = nomark(pk2(fma2(lo2(z2v[k][t]), lo2(r4[t]), lo2(m4[t]))));
        o[2 * t + 1] = pk2(fma2(hi2(z2v[k][t]), hi2(r4[t]), hi2(m4[t])));
      }
      st128<SC1>(o, rws, z2o[k], 0);
    }
  }
  STAMP(1, 9);
  c1s[lane] = xrow_sum4(__uint_as_float(cz[0][0][0]), __uint_as_float(cz[0][0][1]), __uint_as_float(cz[0][1][0]), __uint_as_float(cz[0][1][1]));
  c1s[64 + (lane & 31)] = xrow_sum4(__uint_as_float(cz[0][2][0]), __uint_as_float(cz[0][2][1]), __uint_as_float(cz[0][2][0]), __uint_as_float(cz[0][2][1]));
  st[1408 + lane] = rbf((__uint_as_float(gq[0]) + __uint_as_float(gq[1])) + (__uint_as_float(gq[2]) + __uint_as_float(gq[3])));
  SB();
  f32x4 z1f[3][2];
  constexpr int RD = ZRD;
  v4u zf[RD];
#define LOAD_Z(n) ld128<SC1>(rws, zv[((n) >> 1) & 7] + ((n) & 1) * 1024, WS_Z2 + (((n) < 48 ? UT(((n) >> 4) % 3) : UO(((n) >> 4) % 3)) * 64 + w * 16) * 1024 + (((n) >> 4) % 3 == 2 ? oob3 : 0u))
  unsigned zv[8];
#pragma unroll
  for (int i = 0; i < 8; ++i) { zv[i] = l16 + ((CKMAP(i) + kr) & 7) * 2048; PIN(zv[i]); }
  {
    bar();
    STAMP(1, 34);
#pragma unroll
    for (int k = 0; k < 6; ++k) PINA(acc[k >> 1][k & 1]);
    f32x4 oth[3][2], cs1[3], bs1[3];
    float r1[2], m1[2];
#pragma unroll
    for (int t = 0; t < 2; ++t) { r1[t] = fin[t * 16 + c]; m1[t] = fin[32 + t * 16 + c]; }
#pragma unroll
    for (int k = 0; k < 3; ++k) {
      cs1[k] = *(const f32x4*)(c1s + k * 32 + q * 4); bs1[k] = *(const f32x4*)(c1s + k * 32 + 16 + q * 4);
#pragma unroll
      for (int t = 0; t < 2; ++t) oth[k][t] = xch[((w ^ 1) * 6 + k * 2 + t) * 64 + lane];
    }
#pragma unroll
    for (int i = 4; i < WO0; ++i) LOAD_WO(i);
    SB();
    f32x2 z1a[6][2];
#pragma unroll
    for (int i = 0; i < 6; ++i) {
      const int k = i >> 1, t = i & 1;
      const f32x4 a = acc[k][t] + oth[k][t];
      z1a[i][0] = fma2(lo2(a), bc2(r1[t]), fma2(lo2(cs1[k]), bc2(m1[t]), lo2(bs1[k])));
      z1a[i][1] = fma2(hi2(a), bc2(r1[t]), fma2(hi2(cs1[k]), bc2(m1[t]), hi2(bs1[k])));
    }
#pragma unroll
    for (int i = 0; i < GELU_PRE; ++i) {
      const f32x2 lo = gelu2(z1a[i][0]), hi = gelu2(z1a[i][1]);
      z1f[i >> 1][i & 1] = f32x4{lo[0], lo[1], hi[0], hi[1]};
      PIN(z1f[i >> 1][i & 1]);
    }
#pragma unroll
    for (int i = GELU_PRE; i < 6; ++i) { PIN(z1a[i][0]); PIN(z1a[i][1]); }
    SB();
#pragma unroll
    for (int f = 0; f < 16; ++f) wq[6 + (f >> 3)][f & 7] = READ_W((f >> 3) * 8192, f & 7);
    SB();
    STAMP(1, 10);
    hint_wait(rws, 0u, WS_CNT + (64 + (nt * 4 + w) * 4 + (xcd >> 1)) * 128, 8u);
    STAMP(1, 22);
#pragma unroll
    for (int n = 0; n < RD; ++n) zf[n] = LOAD_Z(n);
    SB();
#pragma unroll
    for (int i = GELU_PRE; i < 6; ++i) {
      const f32x2 lo = gelu2(z1a[i][0]), hi = gelu2(z1a[i][1]);
      z1f[i >> 1][i & 1] = f32x4{lo[0], lo[1], hi[0], hi[1]};
      PIN(z1f[i >> 1][i & 1]);
    }
  }
  f32x4* xb = (f32x4*)(smem + LDS_A) + lane;
  f32x4* yb = (f32x4*)(smem + LDS_A + 49152) + lane;
  f32x4 ac2[2][4], ini[4], yo[2];
  const float bs0 = bflo(bspq[0]), bs1_ = bfhi(bspq[0]);
  f32x4 bq0 = {bs0, bs0, bs0, bs0}, bq1 = {bs1_, bs1_, bs1_, bs1_};
  const f32x4 z4 = {0.f, 0.f, 0.f, 0.f};
  PINA(bq0); PINA(bq1);
  unsigned vmk = l16 + WS_V + ((mt * 4 + w) * 22 + pr0) * 1024, o_fl4 = w == 0 ? 0u : OOB;
  gu32* p_fl4 = (gu32*)(p.ws + WS_CNT + (192 + mt * 2 + (tid & 1)) * 128);
  PIN(vmk); PIN(mkv); PIN(o_fl4);
  unsigned voff[2];
  voff[0] = l16 + WS_V + ((mt * 4 + 2 * kh) * 22 + (uo0 >> 1)) * 1024;
  voff[1] = l16 + WS_V + ((mt * 4 + 2 * kh) * 22 + (uo2 >> 1)) * 1024 + (uo2 & 1) * 8 + oob3;
  PIN(voff[0]); PIN(voff[1]);
  float ev[4];
  unsigned eo0, eos, eo1[2];
  float evs[2];
  SB();
  STAMP(1, 11);
  auto v_store = [&](int k, int t, unsigned o0, unsigned o1) __attribute__((always_inline)) {
    st64<SC1>(v2u{o0, o1}, rws, voff[k >> 1] + (k & 1) * 8, t * 22 * 1024);
  };
  sfor<6>([&](auto P) __attribute__((always_inline)) {
    constexpr int ps = decltype(P)::value, cur = ps & 1, prv = cur ^ 1;
    unsigned zm;
    sfor<64>([&](auto G) __attribute__((always_inline)) {
      constexpr int g = decltype(G)::value, ks = g >> 2, tu = g & 3, ck = ks >> 1, k2 = ks & 1, n = ps * 16 + ks;
      const f32x4 cin = ks > 0 ? ac2[cur][tu] : ps >= 3 ? ini[tu] : tu == 0 ? bq0 : tu == 1 ? bq1 : z4;
      ac2[cur][tu] = mfma(zf[n % RD], wq[ck][k2 * 4 + tu], cin);
      SB();
      if constexpr (tu == 1) { zm = ks == 0 ? (zf[n % RD][0] > zf[n % RD][2] ? zf[n % RD][0] : zf[n % RD][2]) : macc(zm, zf[n % RD]); PIN(zm); }
      if constexpr (tu == 3) {
        if constexpr (n + RD < 96) zf[n % RD] = LOAD_Z(n + RD);
        else if constexpr (WO0 + n + RD - 96 < 24) LOAD_WO(WO0 + n + RD - 96);
      }
      if constexpr (ps >= 1 && ps <= 3 && g >= 2 && g < 18 && (g & 3) == 2) xb[((w * 3 + ps - 1) * 4 + (g >> 2)) * 64] = ac2[prv][g >> 2];
      if constexpr (ps >= 4 && (g == 2 || g == 6)) yb[((w * 3 + ps - 4) * 2 + (g >> 2)) * 64] = ac2[prv][2 + (g >> 2)];
      if constexpr (ps >= 1 && g == (ps >= 4 ? 12 : 24)) {
        if constexpr (ps == 4) asm volatile("s_waitcnt vmcnt(20)" ::: "memory");
        bar();
      }
      if constexpr (ps >= 4 && (g == 13 || g == 14)) yo[g - 13] = yb[(((w ^ 1) * 3 + ps - 4) * 2 + g - 13) * 64];
      if constexpr (ps >= 4 && g >= 18 && g < 46 && (g & 1) == 0) {
        constexpr int t = (g - 18) / 14, ph = ((g - 18) % 14) >> 1;
        if constexpr (ph == 0) PINA(ac2[prv][t]);
        if constexpr (ph < 4) { ev[ph] = ac2[prv][t][ph] + yo[t][ph]; PIN(ev[ph]); }
        else if constexpr (ph < 6) { ev[2 * ph - 8] *= z1f[ps - 4][t][2 * ph - 8]; PIN(ev[2 * ph - 8]); ev[2 * ph - 7] *= z1f[ps - 4][t][2 * ph - 7]; PIN(ev[2 * ph - 7]); }
        else { eo0 = nomark(pk2(ev[0], ev[1])); PIN(eo0); }
      }
      if constexpr (ps >= 4 && (g == 29 + 4 || g == 43 + 2)) { eo1[g > 40] = pk2(g > 40 ? ev[2] : evs[0], g > 40 ? ev[3] : evs[1]); PIN(eo1[g > 40]); }
      if constexpr (ps >= 4 && g == 30) { evs[0] = ev[2]; evs[1] = ev[3]; eos = eo0; }
      if constexpr (ps >= 4 && g == 37) v_store(ps - 4, 0, eos, eo1[0]);
      if constexpr (ps >= 4 && g == 46) v_store(ps - 4, 1, eo0, eo1[1]);
      if constexpr (ps >= 2 && ps <= 4 && g >= 48 && g < 56 && (g & 1) == 0) ini[(g - 48) >> 1] = xb[(((w ^ 2) * 3 + ps - 2) * 4 + ((g - 48) >> 1)) * 64];
      if constexpr (ps == 0 && (g == 22 || g == 38 || g == 54)) st128<SC1>(mkv, rws, vmk + (g - 22) / 16 * 1024, g == 54 ? oob3 : 0u);
      if constexpr (ps * 64 + g == HINT4_G) { if (tid < 2) (void)__hip_atomic_fetch_add(p_fl4, 1u, __ATOMIC_RELAXED, __HIP_MEMORY_SCOPE_AGENT); }
      if constexpr (ps == 3 && g == 40) PIN(p_fl4);
      if constexpr (ps == 5 && g == POLL4_G) flagv = __builtin_amdgcn_raw_buffer_load_b32(rws, o_fl4, WS_CNT + (192 + mt * 2 + (xcd & 1)) * 128, SC1);
      if constexpr (g == 0 && ps >= 1 && ps < 5) STAMP(1, 55 + ps);
      SB();
    });
    if (__builtin_expect(any_mark(zm), 0)) {
#pragma unroll
      for (int tu = 0; tu < 4; ++tu) ac2[cur][tu] = ps >= 3 ? xb[(((w ^ 2) * 3 + ps - 3) * 4 + tu) * 64] : tu == 0 ? bq0 : tu == 1 ? bq1 : z4;
      sfor<16>([&](auto K) __attribute__((always_inline)) {
        constexpr int ks = decltype(K)::value, ck = ks >> 1, k2 = ks & 1;
        v4u zz;
        unsigned sp = 0;
        do { MEMFENCE(); zz = LOAD_Z(ps * 16 + ks); } while (any_mark(macc(0u, zz)) & (++sp <= SPIN_CAP));
#pragma unroll
        for (int tu = 0; tu < 4; ++tu) ac2[cur][tu] = mfma(zz, wq[ck][k2 * 4 + tu], ac2[cur][tu]);
      });
    }
  });
  STAMP(1, 12);
  {
#pragma unroll
    for (int t = 0; t < 2; ++t) yb[((w * 3 + 2) * 2 + t) * 64] = ac2[1][2 + t];
    PIN(ac2[1][0]); PIN(ac2[1][1]);
    if (w == 0) st[0] = __uint_as_float(flagv < 8u);
    bar();
    late = __float_as_uint(st[0]);
    STAMP(1, 52);
#pragma unroll
    for (int t = 0; t < 2; ++t) yo[t] = yb[(((w ^ 1) * 3 + 2) * 2 + t) * 64];
#pragma unroll
    for (int t = 0; t < 2; ++t) {
      const f32x4 g = (ac2[1][t] + yo[t]) * z1f[2][t];
      v_store(2, t, nomark(pk2(lo2(g))), pk2(hi2(g)));
    }
  }
  STAMP(1, 13);
  SB();
  f32x4 gb4[4];
  f32x2 x1f[2][4];
  STAMP(1, 53);
  if (__builtin_amdgcn_readfirstlane(late)) {
    if (w == 0) hint_wait(rws, 0u, WS_CNT + (192 + mt * 2 + (xcd & 1)) * 128, 8u);
    __builtin_amdgcn_s_barrier();
  }
  MEMFENCE();
  STAMP(1, 23);
  f32x4 a3[4][4];
  {
    unsigned spins = 0;
    for (;;) {
      MEMFENCE();
      v4u vs[6][4];
#define LOAD_V(f) vs[(f) >> 2][(f) & 3] = ld128<SC1>(rws, k3v[(f) >> 2], WS_V + (mt * 4 + (((f) & 3) ^ w)) * 22 * 1024)
#pragma unroll
      for (int f = 0; f < 12; ++f) LOAD_V(f);
      SB();
      unsigned m = 0;
      sfor<96>([&](auto G) __attribute__((always_inline)) {
        constexpr int g = decltype(G)::value, ks = g >> 4, tu = (g >> 2) & 3, e = g & 3;
        a3[e][tu] = mfma(wo[ks][e], vs[ks][tu], ks == 0 ? f32x4{0.f, 0.f, 0.f, 0.f} : a3[e][tu]);
        if constexpr (e == 0 && 12 + (g >> 2) < 24) LOAD_V(12 + (g >> 2));
        if constexpr (e == 1) { m = macc(m, vs[ks][tu]); PIN(m); }
        SB();
      });
      if (!(any_mark(m) & (++spins <= SPIN_CAP))) break;
    }
  }
#pragma unroll
  for (int i = 0; i < 4; ++i) gb4[i] = *(const f32x4*)(st + 1408 + (i >> 1) * 32 + q * 8 + (i & 1) * 4);
#pragma unroll
  for (int i = 0; i < 8; ++i) { x1f[i >> 2][i & 3] = unpk(x1h[i >> 2][(i >> 1) & 1][i & 1]); PIN(x1f[i >> 2][i & 3]); }
  STAMP(1, 14);
  {
    f32x4* red = (f32x4*)(smem + LDS_W);
#pragma unroll
    for (int tu = 1; tu < 4; ++tu)
#pragma unroll
      for (int e = 0; e < 4; ++e) red[((w * 3 + tu - 1) * 4 + e) * 64 + lane] = a3[e][tu];
#pragma unroll
    for (int e = 0; e < 4; ++e) PIN(a3[e][0]);
    bar();
    STAMP(1, 54);
    f32x4 o3[4][3];
#pragma unroll
    for (int e = 0; e < 4; ++e)
#pragma unroll
      for (int rel = 1; rel < 4; ++rel) o3[e][rel - 1] = red[(((w ^ rel) * 3 + rel - 1) * 4 + e) * 64 + lane];
    const rsrc_t rout = mkrs(p.out, L * D * 2);
    SB();
#pragma unroll
    for (int pp = 0; pp < 2; ++pp) {
      v4u o;
#pragma unroll
      for (int h = 0; h < 2; ++h) {
        const int e = pp * 2 + h;
        const f32x4 y = (a3[e][0] + o3[e][0]) + (o3[e][1] + o3[e][2]);
        o[2 * h] = pk2(fma2(lo2(y), lo2(gb4[e]), x1f[pp][2 * h]));
        o[2 * h + 1] = pk2(fma2(hi2(y), hi2(gb4[e]), x1f[pp][2 * h + 1]));
      }
      st128<0>(o, rout, perm(w, c) * 1024 + q * 16 + pp * 64, mt * 64 * 1024 + nt * 128);
    }
  }
  if (b == 0 && tid == 0) *(unsigned*)(p.ws + WS_EPOCH) = tag_s;
  STAMP(1, 15);
  STAMP_FLUSH(1);
  asm volatile("adaln_code_end:");
}

extern "C" void kernel_launch(void* const* d_in, const int* in_sizes, int n_in, void* d_out, int out_size, void* d_ws,
                              size_t ws_size, hipStream_t stream) {
  Params p;
  p.u = (const u16*)d_in[0];
  p.lar = (const u16*)d_in[1];
  p.aim = (const u16*)d_in[2];
  p.cre = (const u16*)d_in[3];
  p.cim = (const u16*)d_in[4];
  p.ldt = (const u16*)d_in[5];
  p.dsk = (const u16*)d_in[6];
  p.win = (const u16*)d_in[8];
  p.wsp = (const u16*)d_in[9];
  p.bsp = (const u16*)d_in[10];
  p.wout = (const u16*)d_in[11];
  p.c = (const u16*)d_in[12];
  p.wmod = (const u16*)d_in[13];
  p.out = (u16*)d_out;
  p.ws = (unsigned char*)d_ws;
  (void)hipFuncSetAttribute((const void*)adaln_mega, hipFuncAttributeMaxDynamicSharedMemorySize, MEGA_LDS);
  adaln_mega<<<256, 256, MEGA_LDS, stream>>>(p);
}
```

```cpp
#include <hip/hip_runtime.h>
#include <hip/hip_bf16.h>
#include <stdint.h>
#include <utility>

#define LAS __attribute__((address_space(3)))
typedef unsigned v4u __attribute__((ext_vector_type(4)));
typedef unsigned v2u __attribute__((ext_vector_type(2)));
typedef float f32x4 __attribute__((ext_vector_type(4)));
typedef float f32x2 __attribute__((ext_vector_type(2)));
typedef short bf16x8 __attribute__((ext_vector_type(8)));
typedef _Float16 f16x8 __attribute__((ext_vector_type(8)));
typedef _Float16 f16x2 __attribute__((ext_vector_type(2)));
typedef __bf16 bf16x2_t __attribute__((ext_vector_type(2)));
typedef unsigned short u16;
typedef __amdgpu_buffer_rsrc_t rsrc_t;

#define STAMP_DECL do { } while (0)
#define STAMP(k, i) do { } while (0)
#define STAMP_FLUSH(k) do { } while (0)

#define OPT_A 1
#define OPT_B 1
#define OPT_C 2
#define OPT_D 1
#define OPT_E 2
#define OPT_F 1
constexpr int L = 2048, D = 512, NM = 128, FH = 688, FF = 1376;
constexpr float EPS = 1e-5f;
constexpr int SC1 = 16;
constexpr unsigned OOB = 0x40000000u;
constexpr unsigned SPIN_CAP = 1u << 14;

constexpr unsigned WS_EPOCH = 0;
constexpr unsigned WS_CNT = 4096;
constexpr unsigned WS_SR = WS_CNT + 512 * 128;
constexpr unsigned WS_SB = WS_SR + 2048 * 8;
constexpr unsigned WS_MODG = WS_SB + 2048 * 8;
constexpr unsigned WS_CSB = WS_MODG + 3072 * 32;
constexpr unsigned WS_ST2 = WS_CSB + 88 * 16 * 32;
constexpr unsigned WS_UG = 1u << 20;
static_assert(WS_ST2 + 32 * 2 * 16 * 32 * 8 <= WS_UG, "small buffers");
constexpr unsigned WS_X1T = WS_UG + 256 * 2048 * 8;
constexpr unsigned WS_WP = WS_X1T + 512 * 2048 * 2;
constexpr unsigned WS_WOT = WS_WP + 88 * 16 * 1024;
constexpr unsigned WS_Z2 = WS_WOT + 32 * 22 * 1024;
constexpr unsigned WS_V = WS_Z2 + 44 * 64 * 1024;
constexpr unsigned WS_END = WS_V + 128 * 22 * 1024;

struct Params {
  const u16 *u, *lar, *aim, *cre, *cim, *ldt, *dsk, *win, *wsp, *bsp, *wout, *c, *wmod;
  u16* out;
  unsigned char* ws;
};

#define DI __device__ __forceinline__
template <int V> struct IC { static constexpr int value = V; };
template <class F, int... I> DI void sfor_(F&& f, std::integer_sequence<int, I...>) { int d[] = {(f(IC<I>{}), 0)...}; (void)d; }
template <int N, class F> DI void sfor(F&& f) { sfor_(f, std::make_integer_sequence<int, N>{}); }
DI float bf2f(unsigned b) { return __builtin_bit_cast(float, b << 16); }
DI float bflo(unsigned w) { return __builtin_bit_cast(float, w << 16); }
DI float bfhi(unsigned w) { return __builtin_bit_cast(float, w & 0xFFFF0000u); }
DI f32x2 unpk(unsigned w) { return f32x2{bflo(w), bfhi(w)}; }
DI unsigned pk2(f32x2 v) { return __builtin_bit_cast(unsigned, __builtin_convertvector(v, bf16x2_t)); }
DI unsigned pk2(float lo, float hi) { return pk2(f32x2{lo, hi}); }
DI unsigned nomark(unsigned x) { return x < 0xFFFFFFFEu ? x : 0xFFFFFFFEu; }
DI float rbf(float x) { return bflo(pk2(x, 0.f)); }
DI f32x2 fma2(f32x2 a, f32x2 b, f32x2 c) { return __builtin_elementwise_fma(a, b, c); }
DI f32x2 lo2(f32x4 v) { return f32x2{v[0], v[1]}; }
DI f32x2 hi2(f32x4 v) { return f32x2{v[2], v[3]}; }
DI f32x2 bc2(float x) { return f32x2{x, x}; }
DI f32x4 mfma(v4u a, v4u b, f32x4 c) {
  return __builtin_amdgcn_mfma_f32_16x16x32_bf16(__builtin_bit_cast(bf16x8, a), __builtin_bit_cast(bf16x8, b), c, 0, 0, 0);
}
DI f32x4 mfma_h(v4u a, v4u b, f32x4 c) {
  return __builtin_amdgcn_mfma_f32_16x16x32_f16(__builtin_bit_cast(f16x8, a), __builtin_bit_cast(f16x8, b), c, 0, 0, 0);
}
DI float dot2(unsigned a, unsigned b, float acc) {
  return __builtin_amdgcn_fdot2_f32_bf16(__builtin_bit_cast(bf16x2_t, a), __builtin_bit_cast(bf16x2_t, b), acc, false);
}
constexpr unsigned ONES = 0x3F803F80u;
DI int perm(int u, int c) { return 32 * (u >> 1) + 8 * (c >> 2) + 4 * (u & 1) + (c & 3); }
template <int CTRL> DI float dpp(float x) {
  return __builtin_bit_cast(float, __builtin_amdgcn_mov_dpp(__builtin_bit_cast(int, x), CTRL, 0xf, 0xf, true));
}
DI float row16_sum(float x) {
  x += dpp<0xB1>(x); x += dpp<0x4E>(x); x += dpp<0x141>(x); x += dpp<0x128>(x);
  return x;
}
DI float row16_max(float x) {
  x = fmaxf(x, dpp<0xB1>(x)); x = fmaxf(x, dpp<0x4E>(x)); x = fmaxf(x, dpp<0x141>(x)); x = fmaxf(x, dpp<0x128>(x));
  return x;
}
DI float xrow_sum(float x) {
  auto s = __builtin_amdgcn_permlane16_swap(__float_as_uint(x), __float_as_uint(x), false, false);
  x = __uint_as_float(s[0]) + __uint_as_float(s[1]);
  auto t = __builtin_amdgcn_permlane32_swap(__float_as_uint(x), __float_as_uint(x), false, false);
  return __uint_as_float(t[0]) + __uint_as_float(t[1]);
}
DI float xrow_sum4(float a, float b, float c, float d) {
  auto s = __builtin_amdgcn_permlane16_swap(__float_as_uint(a), __float_as_uint(b), false, false);
  auto t = __builtin_amdgcn_permlane16_swap(__float_as_uint(c), __float_as_uint(d), false, false);
  const float x = __uint_as_float(s[0]) + __uint_as_float(s[1]), y = __uint_as_float(t[0]) + __uint_as_float(t[1]);
  auto z = __builtin_amdgcn_permlane32_swap(__float_as_uint(x), __float_as_uint(y), false, false);
  return __uint_as_float(z[0]) + __uint_as_float(z[1]);
}
DI float xrow_max(float x) {
  auto s = __builtin_amdgcn_permlane16_swap(__float_as_uint(x), __float_as_uint(x), false, false);
  x = fmaxf(__uint_as_float(s[0]), __uint_as_float(s[1]));
  auto t = __builtin_amdgcn_permlane32_swap(__float_as_uint(x), __float_as_uint(x), false, false);
  return fmaxf(__uint_as_float(t[0]), __uint_as_float(t[1]));
}
DI f32x2 gelu2(f32x2 x) {
  const float k0 = -2.0f * 0.7978845608028654f * 1.4426950408889634f, k1 = k0 * 0.044715f;
  const f32x2 a = x * fma2(x * x, bc2(k1), bc2(k0));
  const f32x2 e = f32x2{__builtin_amdgcn_exp2f(a[0]), __builtin_amdgcn_exp2f(a[1])} + bc2(1.0f);
  return x * f32x2{__builtin_amdgcn_rcpf(e[0]), __builtin_amdgcn_rcpf(e[1])};
}
DI rsrc_t mkrs(const void* ptr, unsigned bytes) { return __builtin_amdgcn_make_buffer_rsrc((void*)ptr, 0, (int)bytes, 0x00020000); }
template <int AUX> DI v4u ld128(rsrc_t rs, unsigned v, unsigned s) { return __builtin_amdgcn_raw_buffer_load_b128(rs, (int)v, (int)s, AUX); }
template <int AUX> DI v2u ld64(rsrc_t rs, unsigned v, unsigned s) { return __builtin_amdgcn_raw_buffer_load_b64(rs, (int)v, (int)s, AUX); }
template <int AUX> DI void st128(v4u x, rsrc_t rs, unsigned v, unsigned s) { __builtin_amdgcn_raw_buffer_store_b128(x, rs, (int)(v + s), 0, AUX); }
template <int AUX> DI void st64(v2u x, rsrc_t rs, unsigned v, unsigned s) { __builtin_amdgcn_raw_buffer_store_b64(x, rs, (int)v, (int)s, AUX); }
DI f32x4 asf(v4u x) { return __builtin_bit_cast(f32x4, x); }
DI float sum4(f32x4 x) { return (x[0] + x[1]) + (x[2] + x[3]); }
DI unsigned umax3(unsigned a, unsigned b, unsigned c) { unsigned m = a > b ? a : b; return m > c ? m : c; }
DI unsigned macc4(unsigned m, v4u x) { return umax3(umax3(m, x[0], x[1]), x[2], x[3]); }
DI unsigned macc(unsigned m, v4u x) { return umax3(m, x[0], x[2]); }
DI bool any_mark(unsigned m) { return __builtin_amdgcn_ballot_w64(m == 0xFFFFFFFFu) != 0ull; }
#define SB() __builtin_amdgcn_sched_barrier(0)
#define PIN(x) asm volatile("" : "+v"(x))
#define PINA(x) asm volatile("" : "+a"(x))
#define USE(x) asm volatile("" ::"v"(x))
#define RETRY_BEGIN(m) if (__builtin_expect(any_mark(m), 0)) { unsigned sp_ = 0; do { MEMFENCE(); m = 0;
#define RETRY_END(m) } while (any_mark(m) & (++sp_ <= SPIN_CAP)); }
#define MEMFENCE() asm volatile("" ::: "memory")
DI void bar() { asm volatile("s_waitcnt lgkmcnt(0)" ::: "memory"); __builtin_amdgcn_s_barrier(); asm volatile("" ::: "memory"); }

DI f32x2 cmul(f32x2 a, f32x2 b) { return fma2(bc2(a[1]), f32x2{-b[1], b[0]}, bc2(a[0]) * b); }
DI f32x2 cexp_p(float are2, float rev_hi, float rev_lo, float p) {
  const float mag = __builtin_amdgcn_exp2f(are2 * p);
  const float hi = rev_hi * p;
  const float fr = (hi - __builtin_rintf(hi)) + rev_lo * p;
  return f32x2{mag * __builtin_amdgcn_cosf(fr), mag * __builtin_amdgcn_sinf(fr)};
}
DI float exp_acc(float x) {
  const float hi = x * 1.4426950408889634f;
  const float lo = __builtin_fmaf(x, 1.4426950408889634f, -hi) + x * 1.925963033500011e-8f;
  const float e = __builtin_amdgcn_exp2f(hi);
  return __builtin_fmaf(e, lo * 0.6931471805599453f, e);
}
DI unsigned pkh(float a, float b) {
  f16x2 v = {(_Float16)a, (_Float16)b};
  return __builtin_bit_cast(unsigned, v);
}

constexpr int CV_RK = 4160;
constexpr int CV_H = 2 * CV_RK;
constexpr int CV_CH = CV_H + 4608;
constexpr int LDS_A = 0;
constexpr int LDS_U = 2 * CV_CH;
constexpr int LDS_GS = LDS_U + 8192;
constexpr int LDS_TAB = 36864;
constexpr int KP_CH = 13840;
constexpr int LDS_KP = LDS_TAB + 2 * 64 * 528 + 16;
constexpr int LDS_LN = 0, LDS_MR = 8448;
constexpr int LDS_WI = 145472, LDS_WO = LDS_WI + 10240;
constexpr int LDS_X = 65536;
constexpr int LDS_S = LDS_X + 24576;
constexpr int LDS_W = LDS_S + 6144;
constexpr int MEGA_LDS = LDS_W + 65536 + 2048;
static_assert(LDS_WO + 4608 <= MEGA_LDS && LDS_KP + 2 * KP_CH <= LDS_WI && LDS_S >= 73728, "lds");
static_assert(LDS_GS + 1280 <= LDS_TAB && LDS_KP + 2 * KP_CH <= MEGA_LDS && MEGA_LDS <= 160 * 1024, "lds");
#define FLAG_AT 48
static_assert(FLAG_AT >= 36 && FLAG_AT <= 56 && FLAG_AT % 4 == 0, "flag position");
#define WF_EARLY 5
#define WO0 12
#define WSP_P0 4
#define HINT4_KS 15
#define ZDEPTH 3
#define GELU_IN 0
#define EPI_W 0
#define MOD_POS 0
#define POLL3 0
#define GELU_PRE 4
#define HINT4_G 314
static_assert(HINT4_G > 4 * 64 + 12, "behind the marker barrier");
#define POLL4_G 48
#define RELABEL 1
#define CKMAP(ck) (RELABEL ? 2 * ((ck) & 3) + ((ck) >> 2) : (ck))
#define POLL_AT 130
#define MOD_MID 1
#define LATE_SLEEP 6
#define SMALL_N 3
#define ZRD 12
#define ZLA 4
#define IN_AUX 0
#define WSP_AUX 0

template <class T> DI T ldsr(unsigned a) { return *(const LAS T*)(size_t)a; }
template <class T> DI void ldsw(unsigned a, T v) { *(LAS T*)(size_t)a = v; }
DI v2u trread(const unsigned char* ptr) {
  typedef short v4i16_t __attribute__((ext_vector_type(4)));
  return __builtin_bit_cast(v2u, __builtin_amdgcn_ds_read_tr16_b64_v4i16((LAS v4i16_t*)ptr));
}
DI v2u trread_a(unsigned a) {
  typedef short v4i16_t __attribute__((ext_vector_type(4)));
  return __builtin_bit_cast(v2u, __builtin_amdgcn_ds_read_tr16_b64_v4i16((LAS v4i16_t*)(size_t)a));
}
struct CvOrder { int qq[36], pp[36]; bool first[36]; };
constexpr CvOrder cv_order() {
  CvOrder o{};
  int n = 0;
  for (int m = 0; m < 8; ++m) {
    for (int j = 0; j < m; ++j) if (m + j < 8) { o.qq[n] = m; o.pp[n] = j; ++n; }
    for (int j = 0; j <= m; ++j) if (m + j < 8) { o.qq[n] = j; o.pp[n] = m; ++n; }
  }
  for (int i = 0; i < 36; ++i) {
    o.first[i] = true;
    for (int j = 0; j < i; ++j) if (o.qq[j] + o.pp[j] == o.qq[i] + o.pp[i]) o.first[i] = false;
  }
  return o;
}
constexpr CvOrder CVO = cv_order();
struct Z2Order { int u[128], kind[128], pos[128]; };
constexpr Z2Order z2_order() {
  Z2Order o{};
  int n = 0;
  for (int u = 0; u < 40; ++u) {
    int pos = 0;
    const bool st = u < 24 && (u < 8 || (u & 7) < 4);
    for (int k = 0; k < 5; ++k)
      if (k < 3 ? u >= 8 : st) { o.u[n] = u; o.kind[n] = k; o.pos[n] = pos++; ++n; }
  }
  return o;
}
constexpr Z2Order Z2O = z2_order();

typedef __attribute__((address_space(1))) unsigned gu32;
DI unsigned tx(unsigned m, unsigned x, unsigned tag) { return __builtin_amdgcn_bitop3_b32(m, x, tag, 0xF6); }
DI unsigned tacc(unsigned m, v4u x, unsigned tag) { return tx(tx(m, x[1], tag), x[3], tag); }
DI bool any_nz(unsigned m) { return __builtin_amdgcn_ballot_w64(m != 0u) != 0ull; }
DI void hint_add(unsigned char* ws, int line) { (void)__hip_atomic_fetch_add((gu32*)(ws + WS_CNT + line * 128), 1u, __ATOMIC_RELAXED, __HIP_MEMORY_SCOPE_AGENT); }
DI void hint_wait(rsrc_t rws, unsigned voff, unsigned soff, unsigned want) {
  unsigned sp = 0;
  for (;;) {
    MEMFENCE();
    const unsigned v = __builtin_amdgcn_raw_buffer_load_b32(rws, (int)voff, (int)soff, SC1);
    if (!((__builtin_amdgcn_ballot_w64(v < want) != 0ull) & (++sp <= SPIN_CAP))) break;
    __builtin_amdgcn_s_sleep(OPT_E);
  }
  MEMFENCE();
}

extern "C" __global__ void __launch_bounds__(256, 1) adaln_mega(Params p) {
  extern __shared__ __attribute__((aligned(16))) unsigned char smem[];
  const int tid = threadIdx.x, lane = tid & 63, w = __builtin_amdgcn_readfirstlane(tid >> 6);
  const int b = blockIdx.x, xcd = b & 7, idx = b >> 3;
  const int pb = xcd * 32 + idx;
  const unsigned l16 = lane * 16;
  STAMP_DECL;
  STAMP(1, 0);
  v2u codepf;
  {
    unsigned long long pc;
    int rel_m, rel_e;
    asm volatile("s_getpc_b64 %0\n\ts_mov_b32 %1, adaln_mega@rel32@lo+4\n\ts_mov_b32 %2, adaln_code_end@rel32@lo+12" : "=s"(pc), "=s"(rel_m), "=s"(rel_e));
    const rsrc_t rcode = mkrs((const void*)(pc + (long long)rel_m), rel_e > rel_m ? (unsigned)(rel_e - rel_m) & ~15u : 0u);
    codepf = ld64<0>(rcode, tid * 8, idx * 2048);
  }
  SB();
  const rsrc_t ru = mkrs(p.u, L * D * 2);
  v4u xr[2];
  xr[0] = ld128<IN_AUX>(ru, l16, (pb * 8 + w * 2) * 1024);
  xr[1] = ld128<IN_AUX>(ru, l16 + 1024, (pb * 8 + w * 2) * 1024);
  const int mkg = b & 3, mcg = b >> 2;
  const unsigned cq = p.c[mkg * 128 + (tid & 3) * 32 + (tid >> 3)];
  const unsigned epoch = *(const unsigned*)(p.ws + WS_EPOCH);
  const rsrc_t rws = mkrs(p.ws, WS_END);
  __builtin_amdgcn_raw_buffer_store_b32(0u, rws, tid < 2 ? tid * 32768u : OOB, WS_CNT + b * 128, SC1);
  SB();
  const int cch = w >> 1, eh = w & 1, ns = tid & 127;
  const unsigned q_cre = p.cre[pb * 256 + tid], q_cim = p.cim[pb * 256 + tid], q_lar = p.lar[pb * 256 + tid], q_aim = p.aim[pb * 256 + tid];
  const unsigned q_ldt = p.ldt[pb * 2 + cch], q_dsk = p.dsk[pb * 2 + cch];
  v4u xm[4];
#define LOAD_XM() { \
  const rsrc_t rmod = mkrs(p.wmod, D * 3072 * 2); \
_Pragma("unroll") \
  for (int i = 0; i < 4; ++i) xm[i] = ld128<IN_AUX>(rmod, (tid >> 3) * 6144 + (tid & 7) * 16, (mkg * 128 + i * 32) * 6144 + mcg * 128 + (b < 192 ? 0u : OOB)); }
  SB();
  LOAD_XM();
  SB();
  STAMP(1, 24);
  const int c = lane & 15, q = lane >> 4;
  const unsigned lds0 = (unsigned)(size_t)(LAS unsigned char*)smem;
  unsigned pos4[9];
  {
    const float f4 = (float)(8188 - tid * 32);
#pragma unroll
    for (int j = 0; j < 10; j += 2) {
      const unsigned pp = pk2(bc2(f4) - f32x2{4.f * j, 4.f * j + 4.f});
      const int p0 = (int)bflo(pp), p1 = (int)bfhi(pp);
      pos4[j] = p0 + ((p0 >> 5) << 2) + lds0 + LDS_KP;
      PIN(pos4[j]);
      if (j + 1 < 9) { pos4[j + 1] = p1 + ((p1 >> 5) << 2) + lds0 + LDS_KP; PIN(pos4[j + 1]); }
    }
  }
  unsigned co[2][4];
  {
    const unsigned b0 = (eh ? 1152 + 576 * q : 144 * (q >> 1) + 4 * (q & 1)) * 4 + 36 * c + lds0 + LDS_KP + cch * KP_CH;
    const unsigned b1 = (q < 2 ? 288 + 144 * q : 568 + 4 * q) * 4 + 36 * c + lds0 + LDS_KP + cch * KP_CH;
    const unsigned s0 = eh ? 576 : 4, s1 = q < 2 ? 8 : 576;
#pragma unroll
    for (int r = 0; r < 4; ++r) { co[0][r] = b0 + r * s0; co[1][r] = b1 + r * s1; PIN(co[0][r]); PIN(co[1][r]); }
  }
  *(unsigned*)(smem + LDS_KP - 16 + cch * KP_CH + (lane & 3) * 4) = 0u;
  unsigned a_pad, a_wi, a_wo, a_tab, gm_off, gc_off;
  a_pad = lds0 + cch * CV_CH + (lane < 32 ? CV_H + l16 : ((lane >> 1) & 1) * CV_RK + 4096 + (lane & 1) * 16);
  a_wi = lds0 + LDS_WI + (w * 32 + 8 * q + ((lane >> 2) & 3)) * 80 + (lane & 3) * 16;
  a_wo = lds0 + LDS_WO + (8 * q + ((lane >> 2) & 3)) * 144 + (w >> 1) * 64 + (lane & 3) * 16 + (w & 1) * 8;
  a_tab = lds0 + LDS_TAB + cch * (64 * 528) + c * 528 + q * 16;
  gm_off = ((4 - cch) * 512 + mkg * 128 + ns) * 8;
  gc_off = (((lane & 7) > 5 ? 0 : ((lane & 7) >> 1)) * 512 + pb * 2 + (lane & 1)) * 8;
  PIN(a_pad); PIN(a_wi); PIN(a_wo); PIN(a_tab); PIN(gm_off); PIN(gc_off);
  unsigned ab[4], bb, dv[4], rdo4[4], xoff;
#pragma unroll
  for (int i = 0; i < 4; ++i) {
    ab[i] = lds0 + cch * CV_CH + (1 - (c & 1)) * CV_RK + 2 * (2030 - 2 * (c >> 1) + 8 * q) - eh * 256 - 192 - 512 - 1024 * i;
    dv[i] = (lane >> 3) * 4096 + (((lane & 7) ^ ((lane >> 4) & 1) ^ (i << 1)) * 16) + i * (32768 - 1024);
    rdo4[i] = lds0 + LDS_W + w * 16384 + ((perm((i & 1) * 2 ^ (2 * (w & 1)), c) * 128 + ((q ^ (c >> 1)) * 16)) ^ ((i >> 1) * 64));
    PIN(ab[i]); PIN(dv[i]); PIN(rdo4[i]);
  }
  bb = lds0 + cch * CV_CH + CV_H + 2 * (256 - 16 + 16 * c + 8 * q) - eh * 256 - 192;
  xoff = WS_X1T + (pb * 2 + cch) * 4096 + eh * 2048 + ((c >> 1) * 32 + ((q & 1) * 4 + 2 * (c & 1) + (q >> 1)) * 4) * 2;
  PIN(bb); PIN(xoff);
  SB();
  STAMP(1, 26);
  {
    *(v4u*)(smem + LDS_LN + (w * 2) * 1040 + l16) = xr[0];
    *(v4u*)(smem + LDS_LN + (w * 2 + 1) * 1040 + l16) = xr[1];
    float sm[2], sq[2];
#pragma unroll
    for (int i = 0; i < 2; ++i) {
      sm[i] = dot2(xr[i][0], ONES, dot2(xr[i][1], ONES, dot2(xr[i][2], ONES, dot2(xr[i][3], ONES, 0.f))));
      sq[i] = dot2(xr[i][0], xr[i][0], dot2(xr[i][1], xr[i][1], dot2(xr[i][2], xr[i][2], dot2(xr[i][3], xr[i][3], 0.f))));
    }
    const float tv = row16_sum(xrow_sum4(sm[0], sm[1], sq[0], sq[1]));
    auto sw = __builtin_amdgcn_permlane32_swap(__float_as_uint(tv), __float_as_uint(tv), false, false);
    const float m = __uint_as_float(sw[0]) * (1.0f / D);
    const float r = __builtin_amdgcn_rsqf(fmaxf(__uint_as_float(sw[1]) * (1.0f / D) - m * m, 0.f) + EPS);
    ((float*)(smem + LDS_GS + 1088))[(q >> 1) * 8 + w * 2 + (q & 1)] = (q >> 1) ? -m * r : r;
  }
  const unsigned tag = epoch * 2654435761u + 0x9E3779B9u;
  STAMP(1, 28);
  asm volatile("s_waitcnt vmcnt(%0)" ::"i"(MOD_POS == 0 ? 8 : 4) : "memory");
  bar();
  STAMP(1, 16);
  {
    unsigned ua[4][2];
#pragma unroll
    for (int i = 0; i < 4; ++i) {
      const unsigned char* src = smem + LDS_LN + (tid & 3) * 2080 + (i * 64 + (tid >> 2)) * 4;
      ua[i][0] = *(const unsigned*)src; ua[i][1] = *(const unsigned*)(src + 1040);
    }
    const unsigned lnv = ((const unsigned*)(smem + LDS_GS + 1088))[c];
    SB();
#pragma unroll
    for (int i = 0; i < 4; ++i) st128<SC1>(v4u{ua[i][0], tag, ua[i][1], tag}, rws, (tid >> 2) * 16384 + (tid & 3) * 16, WS_UG + i * 64 * 16384 + pb * 64);
    st64<SC1>(v2u{lnv, tag}, rws, tid < 16 ? (c >> 3) * 16384 + (c & 7) * 8 : OOB, WS_SR + pb * 64);
  }
  STAMP(1, 17);
  SB();
  const unsigned wp_oob = b < 176 ? 0u : OOB;
  const int wpr = b >> 2, wkg = b & 3;
  v4u xi[2], xo;
  {
    const rsrc_t rwin = mkrs(p.win, D * FF * 2), rwout = mkrs(p.wout, FH * D * 2);
    const int hf = wpr >= 22, pp = wpr - 22 * hf;
    const unsigned v = ((tid >> 2) * (FF * 2) + (tid & 3) * 16) | ((pp == 21 ? tid & 2 : 0) << 29);
#pragma unroll
    for (int i = 0; i < 2; ++i) xi[i] = ld128<IN_AUX>(rwin, v, (wkg * 128 + i * 64) * (FF * 2) + (hf * FH + pp * 32) * 2 + wp_oob);
    xo = ld128<IN_AUX>(rwout, (tid >> 3) * 1024 + (tid & 7) * 16, idx * 32 * 1024 + xcd * 128);
  }
  SB();
  const int d0 = pb * 2, dk = d0 + cch;
  const int mt = (xcd >> 1) * 8 + (idx & 7), nt = (xcd & 1) * 4 + (idx >> 3);
  const int big = (nt + 2) >> 3;
  const int np = 3 - big, pr0 = 3 * nt - big * (nt - 6);
  const int kh = w & 1, uh = w >> 1;
  const int uo0 = pr0 * 2 + uh * (4 - 2 * big), ut0 = pr0 * 2 + (uh ^ 1) * (4 - 2 * big), uo2 = pr0 * 2 + 2 + uh, ut2 = pr0 * 2 + 2 + (uh ^ 1);
#define UO(k) ((k) == 2 ? uo2 : uo0 + (k))
#define UT(k) ((k) == 2 ? ut2 : ut0 + (k))
  const unsigned oob3 = (unsigned)big << 30;
  const int kr = 2 * (idx >> 3) + (xcd & 1) + (OPT_B == 0 ? (idx & 7) : OPT_B == 1 ? 4 * (idx & 1) : OPT_B == 2 ? 2 * (idx & 3) : 0);
  const rsrc_t rwsp = mkrs(p.wsp, L * L * 2);
  const unsigned wp_fr = (wpr * 2 * 16 + wkg * 4 + w) * 1024 + wp_oob;
  v4u wr[1][8];
  const unsigned wsp_v = (lane >> 3) * 4096 + (lane & 7) * 16, wsp_s = mt * 64 * 4096 + w * 1024;
#define LOAD_WSP1(ck, i) wr[ck][i] = ld128<WSP_AUX>(rwsp, wsp_v + (((ck) + kr) & 7) * 128, wsp_s + (i) * 8 * 4096)
#define LOAD_WSPN(n) LOAD_WSP1((n) >> 3, (n) & 7)
  const v4u mk = {~0u, ~0u, ~0u, ~0u};
  unsigned bspq[2];
#pragma unroll
  for (int t = 0; t < 2; ++t) bspq[t] = p.bsp[mt * 64 + perm(2 * kh + t, c)];
  const unsigned tag_s = __builtin_amdgcn_readfirstlane(tag);
  {
    const float dt = rbf(exp_acc(bf2f(q_ldt)));
    const float cre = bf2f(q_cre) * dt, cim = bf2f(q_cim) * dt;
    const float are2 = -rbf(exp_acc(bf2f(q_lar))) * dt * 1.4426950408889634f;
    const float aim = bf2f(q_aim) * dt;
    const float rev_hi = aim * 0.15915494309189535f;
    const float rev_lo = __builtin_fmaf(aim, 0.15915494309189535f, -rev_hi) + aim * 6.4206383e-9f;
    int ex = __builtin_amdgcn_frexp_expf(xrow_max(row16_max(fmaxf(fabsf(cre), fabsf(cim))))) + 1;
    ex = ex < -100 ? -100 : ex;
    const float scale = __builtin_amdgcn_ldexpf(1.f, -ex);
    ((float*)(smem + LDS_GS + 1024))[w] = __builtin_amdgcn_ldexpf(2.f, ex);
    const f32x2 c0 = {cre * scale, cim * scale};
    const f32x2 e1 = cexp_p(are2, rev_hi, rev_lo, 1.f), e8 = cexp_p(are2, rev_hi, rev_lo, 8.f), e64 = cexp_p(are2, rev_hi, rev_lo, 64.f);
    unsigned char* tn = smem + LDS_TAB + cch * (64 * 528) + ns * 4;
#define TROW(r, z) *(unsigned*)(tn + (r) * 528) = pkh((z)[0], -(z)[1])
    SB();
    const f32x2 e128 = cmul(e64, e64), e2 = cmul(e1, e1), e256 = cmul(e128, e128), e4 = cmul(e2, e2), e512 = cmul(e256, e256);
    f32x2 f0 = {1.f, 0.f}, f1 = e64;
    f32x2 ca = cmul(c0, e512), cb = cmul(ca, e512);
#pragma unroll
    for (int j = 0; j < 8; ++j) {
      *(unsigned*)(tn + (48 + j) * 528) = pkh(f0[0], f0[1]);
      *(unsigned*)(tn + (56 + j) * 528) = pkh(f1[0], f1[1]);
      TROW(32 + j, cb);
      f0 = j == 0 ? e8 : cmul(f0, e8); f1 = cmul(f1, e8); cb = cmul(cb, e128);
      if (j < 4) {
        const f32x2 z = cmul(ca, e4);
        TROW(24 + j, ca); TROW(28 + j, z);
        ca = cmul(ca, e128);
      }
      if (j == 3 || j == 7) {
        PIN(f0); PIN(f1); PIN(cb);
        if (WSP_P0 >= 4) { LOAD_WSPN(j >> 2 << 1); LOAD_WSPN((j >> 2 << 1) + 1); }
        SB();
      }
    }
    TROW(40, cb);
    SB();
  {
    const float cv = bf2f(cq);
    const float sl = rbf(cv * __builtin_amdgcn_rcpf(1.0f + __builtin_amdgcn_exp2f(cv * -1.4426950408889634f)));
    const float sc[4] = {dpp<0x00>(sl), dpp<0x55>(sl), dpp<0xAA>(sl), dpp<0xFF>(sl)};
    f32x2 a[4];
#pragma unroll
    for (int i = 0; i < 4; ++i)
#pragma unroll
      for (int j = 0; j < 4; ++j) a[j] = i == 0 ? unpk(xm[i][j]) * bc2(sc[i]) : fma2(unpk(xm[i][j]), bc2(sc[i]), a[j]);
    float* red = (float*)(smem + LDS_MR) + (tid >> 3) * 68 + (tid & 7) * 8;
    *(f32x4*)red = f32x4{a[0][0], a[0][1], a[1][0], a[1][1]};
    *(f32x4*)(red + 4) = f32x4{a[2][0], a[2][1], a[3][0], a[3][1]};
  }
    bar();
    float ms8[8];
#pragma unroll
    for (int i = 0; i < 8; ++i) ms8[i] = ((const float*)(smem + LDS_MR))[(q * 8 + i) * 68 + w * 16 + c];
    SB();
    f32x2 t1[8];
    t1[1] = e1; t1[2] = e2; t1[4] = e4;
    t1[3] = cmul(e2, e1); t1[5] = cmul(e4, e1); t1[6] = cmul(e2, e4); t1[7] = cmul(t1[6], e1);
    const f32x2 c1 = cmul(c0, e128), c2 = cmul(c0, e256), c3 = cmul(c2, e128);
    TROW(0, c0); TROW(8, c1); TROW(16, c2); TROW(20, c3);
    {
    const float s = xrow_sum(((ms8[0] + ms8[1]) + (ms8[2] + ms8[3])) + ((ms8[4] + ms8[5]) + (ms8[6] + ms8[7])));
    st64<SC1>(v2u{__float_as_uint(s), tag}, rws, q == 0 ? c * 8 : OOB, WS_MODG + (mkg * 3072 + mcg * 64 + w * 16) * 8 + (b < 192 ? 0u : OOB));
    }
    SB();
#pragma unroll
    for (int v = 1; v < 8; ++v) {
      const f32x2 z0 = cmul(t1[v], c0), z1 = cmul(t1[v], c1);
      TROW(v, z0); TROW(8 + v, z1);
      if ((v & 1) == 0) {
        const f32x2 z2 = cmul(t1[v], c2), z3 = cmul(t1[v], c3);
        TROW(16 + v / 2, z2); TROW(20 + v / 2, z3);
        if (v < 6 && WSP_P0 >= 8) { LOAD_WSPN(v + 2); LOAD_WSPN(v + 3); }
        SB();
      }
    }
  }
  STAMP(1, 27);
#pragma unroll
  for (int i = 0; i < 2; ++i) *(v4u*)(smem + LDS_WI + (i * 64 + (tid >> 2)) * 80 + (tid & 3) * 16) = xi[i];
  *(v4u*)(smem + LDS_WO + (tid >> 3) * 144 + (tid & 7) * 16) = xo;
  STAMP(1, 29);
  bar();
  STAMP(1, 18);
  SB();
  v4u gr[4], gb_[4];
  v2u gm[4], gc[4];
#define LOAD_GS() _Pragma("unroll") for (int i_ = 0; i_ < 4; ++i_) { gr[i_] = ld128<SC1>(rws, tid * 16, WS_SR + i_ * 4096); gb_[i_] = ld128<SC1>(rws, tid * 16, WS_SB + i_ * 4096); }
#define LOAD_GM() _Pragma("unroll") for (int i_ = 0; i_ < 4; ++i_) { gm[i_] = ld64<SC1>(rws, gm_off, WS_MODG + i_ * 24576); gc[i_] = ld64<SC1>(rws, gc_off, WS_MODG + i_ * 24576); }
  v4u ug[4], wt[2];
#define LOAD_UG() _Pragma("unroll") for (int i_ = 0; i_ < 4; ++i_) ug[i_] = ld128<SC1>(rws, tid * 16, WS_UG + pb * 16384 + i_ * 4096)
  {
    v4u fa[2][8], fb[8];
    const unsigned ta = a_tab + eh * (32 * 528);
#pragma unroll
    for (int ks = 0; ks < 3; ++ks) { fb[ks] = ldsr<v4u>(a_tab + 48 * 528 + ks * 64); fa[0][ks] = ldsr<v4u>(ta + ks * 64); fa[1][ks] = ldsr<v4u>(a_tab + 16 * 528 + ks * 64); }
    SB();
    LOAD_UG();
    SB();
    f32x4 kk[2][2];
    f32x2 us;
    v2u wh_[6];
#pragma unroll
    for (int i = 0; i < 4; ++i) kk[i >> 1][i & 1] = f32x4{0.f, 0.f, 0.f, 0.f};
#pragma unroll
    for (int ks = 0; ks < 8; ++ks) {
      if (ks < 5) { fb[3 + ks] = ldsr<v4u>(a_tab + 48 * 528 + (3 + ks) * 64); fa[0][3 + ks] = ldsr<v4u>(ta + (3 + ks) * 64); fa[1][3 + ks] = ldsr<v4u>(a_tab + 16 * 528 + (3 + ks) * 64); }
      if (ks == 5) { us = *(const f32x2*)(smem + LDS_GS + 1024 + cch * 8); wh_[4] = trread_a(a_wo); wh_[5] = trread_a(a_wo + 576); }
      if (ks == 6) { wh_[0] = trread_a(a_wi); wh_[1] = trread_a(a_wi + 320); wh_[2] = trread_a(a_wi + 8); wh_[3] = trread_a(a_wi + 328); }
      kk[0][ks >> 2] = mfma_h(fa[0][ks], fb[ks], kk[0][ks >> 2]); kk[1][ks >> 2] = mfma_h(fa[1][ks], fb[ks], kk[1][ks >> 2]);
      SB();
    }
    STAMP(1, 37);
    wt[0] = v4u{wh_[0][0], wh_[0][1], wh_[1][0], wh_[1][1]};
    wt[1] = v4u{wh_[2][0], wh_[2][1], wh_[3][0], wh_[3][1]};
    st128<SC1>(v4u{wh_[4][0], wh_[4][1], wh_[5][0], wh_[5][1]}, rws, l16, WS_WOT + ((xcd * 4 + w) * 22 + idx) * 1024 + wp_oob);
    LOAD_GS();
    SB();
#pragma unroll
    for (int i = 0; i < 4; ++i) PINA(kk[i >> 1][i & 1]);
    f32x4 k0 = kk[0][0] * us[0] + kk[0][1] * us[1];
    const f32x4 k1 = kk[1][0] * us[0] + kk[1][1] * us[1];
    k0[0] += ns == 0 ? bf2f(q_dsk) : 0.f;
#pragma unroll
    for (int r = 0; r < 4; ++r) { ldsw<float>(co[0][r], k0[r]); ldsw<float>(co[1][r], k1[r]); }
  }
  STAMP(1, 31);
  bar();
  STAMP(1, 19);
  asm volatile("s_branch adaln_pad0\n\t.rept 64\n\ts_nop 0\n\t.endr\nadaln_pad0:");
  {
    float v[2][9];
#pragma unroll
    for (int j = 0; j < 9; ++j) v[0][j] = ldsr<float>(pos4[j]);
    LOAD_GM();
#pragma unroll
    for (int j = 0; j < 9; ++j) v[1][j] = ldsr<float>(pos4[j] + KP_CH);
    ldsw<v4u>(a_pad, v4u{0u, 0u, 0u, 0u});
    SB();
    STAMP(1, 36);
#pragma unroll
    for (int ch = 0; ch < 2; ++ch) {
      unsigned char* rk = smem + ch * CV_CH + tid * 16;
#pragma unroll
      for (int sft = 0; sft < 2; ++sft)
        *(v4u*)(rk + sft * CV_RK) = v4u{pk2(v[ch][sft], v[ch][sft + 1]), pk2(v[ch][sft + 2], v[ch][sft + 3]), pk2(v[ch][sft + 4], v[ch][sft + 5]), pk2(v[ch][sft + 6], v[ch][sft + 7])};
    }
#pragma unroll
    for (int i = WSP_P0; i < 8; ++i) LOAD_WSPN(i);
    st128<SC1>(mk, rws, l16, WS_X1T + dk * 4096 + eh * 2048);
    st128<SC1>(mk, rws, l16 + 1024, WS_X1T + dk * 4096 + eh * 2048);
    __builtin_amdgcn_raw_buffer_store_b32(~0u, rws, lane * 4, WS_ST2 + ((mt * 2 + kh) * 16 + nt * 2 + uh) * 256, SC1);
    SB();
    STAMP(1, 32);
    unsigned m = 0;
#pragma unroll
    for (int i = 0; i < 4; ++i) { m = tacc(m, ug[i], tag); m = tacc(m, gr[i], tag); m = tacc(m, gb_[i], tag); }
    if (__builtin_expect(any_nz(m), 0)) {
      unsigned sp = 0;
      do {
        MEMFENCE(); LOAD_UG(); LOAD_GS(); m = 0;
#pragma unroll
        for (int i = 0; i < 4; ++i) { m = tacc(m, ug[i], tag); m = tacc(m, gr[i], tag); m = tacc(m, gb_[i], tag); }
      } while (any_nz(m) & (++sp <= SPIN_CAP));
    }
    f32x2 nn[4][2];
#pragma unroll
    for (int i = 0; i < 4; ++i) {
      nn[i][0] = fma2(unpk(ug[i][0]), bc2(__uint_as_float(gr[i][0])), bc2(__uint_as_float(gb_[i][0])));
      nn[i][1] = fma2(unpk(ug[i][2]), bc2(__uint_as_float(gr[i][2])), bc2(__uint_as_float(gb_[i][2])));
      PIN(nn[i][0]); PIN(nn[i][1]);
      *(unsigned*)(smem + LDS_U + i * 1024 + tid * 4) = __builtin_amdgcn_perm(ug[i][2], ug[i][0], 0x05040100u);
      *(unsigned*)(smem + LDS_U + 4096 + i * 1024 + tid * 4) = __builtin_amdgcn_perm(ug[i][2], ug[i][0], 0x07060302u);
    }
    SB();
    STAMP(1, 33);
    m = 0;
#pragma unroll
    for (int i = 0; i < 4; ++i) m = tx(tx(m, gm[i][1], tag), gc[i][1], tag);
    if (__builtin_expect(any_nz(m), 0)) {
      unsigned sp = 0;
      do {
        __builtin_amdgcn_s_sleep(3);
        MEMFENCE(); LOAD_GM(); m = 0;
#pragma unroll
        for (int i = 0; i < 4; ++i) m = tx(tx(m, gm[i][1], tag), gc[i][1], tag);
      } while (any_nz(m) & (++sp <= SPIN_CAP));
    }
    const float mv = rbf((__uint_as_float(gm[0][0]) + __uint_as_float(gm[1][0])) + (__uint_as_float(gm[2][0]) + __uint_as_float(gm[3][0])));
    *(u16*)(smem + LDS_GS + tid * 2) = (u16)pk2(mv + (cch ? 0.f : 1.0f), 0.f);
    const float cv = rbf((__uint_as_float(gc[0][0]) + __uint_as_float(gc[1][0])) + (__uint_as_float(gc[2][0]) + __uint_as_float(gc[3][0])));
    const float gav = dpp<0x104>(cv), gav2 = dpp<0x102>(cv);
    const float hbv = cv * gav, hsv = rbf(1.0f + cv) * gav2;
    const f32x2 hb = {__builtin_bit_cast(float, __builtin_amdgcn_readlane(__builtin_bit_cast(int, hbv), 0)), __builtin_bit_cast(float, __builtin_amdgcn_readlane(__builtin_bit_cast(int, hbv), 1))};
    const f32x2 hs = {__builtin_bit_cast(float, __builtin_amdgcn_readlane(__builtin_bit_cast(int, hsv), 2)), __builtin_bit_cast(float, __builtin_amdgcn_readlane(__builtin_bit_cast(int, hsv), 3))};
    SB();
    bspq[0] |= bspq[1] << 16;
    PIN(bspq[0]);
    STAMP(1, 38);
#pragma unroll
    for (int i = 0; i < 4; ++i) {
      const f32x2 h0 = fma2(nn[i][0], hs, hb), h1 = fma2(nn[i][1], hs, hb);
      *(unsigned*)(smem + CV_H + 512 + i * 1024 + tid * 4) = pk2(h0[0], h1[0]);
      *(unsigned*)(smem + CV_CH + CV_H + 512 + i * 1024 + tid * 4) = pk2(h0[1], h1[1]);
    }
  }
  asm volatile("" ::"v"(codepf));
  STAMP(1, 39);
  bar();
  STAMP(1, 1);
#define CV_A(ee, k) ({ const unsigned a_ = ab[(k) >> 1] + 192 - 64 * (ee) + 512 * (1 - ((k) & 1)); v4u{ldsr<unsigned>(a_), ldsr<unsigned>(a_ + 4), ldsr<unsigned>(a_ + 8), ldsr<unsigned>(a_ + 12)}; })
#define CV_B(ee, k) ldsr<v4u>(bb + 192 - 64 * (ee) + 512 * (k))
  const unsigned slotA = LDS_W + w * 16384, slotB = slotA + 8192;
  v4u wq[8][8];
#define READ_W(dl, f) ldsr<v4u>(rdo4[(((f) >> 1) & 1) + ((f) >> 2) * 2] + (dl) + ((f) & 1) * 512)
  unsigned dso;
#define DMA_W(ck, slot, i) do { \
    if (((i) & 3) == 0) asm volatile("s_add_u32 m0, %1, %2\n\ts_add_u32 %0, %3, %4\n\tbuffer_load_dwordx4 %5, %6, %0 offen lds" \
                 : "=&s"(dso) : "s"(lds0 + (slot)), "i"((i) * 1024), "s"(wsp_s + ((CKMAP(ck) + kr) & 7) * 128), "i"((i) * 32768), "v"(dv[0]), "s"(rwsp) : "scc", "memory"); \
    else asm volatile("buffer_load_dwordx4 %0, %1, %2 offen offset:%3 lds" :: "v"(dv[(i) & 3]), "s"(rwsp), "s"(dso), "i"(((i) & 3) * 1024) : "memory"); } while (0)
  auto dma_sel = [&](auto CK, unsigned slot, int i) __attribute__((always_inline)) {
    constexpr int ck = decltype(CK)::value;
    switch (i) {
      case 0: DMA_W(ck, slot, 0); break; case 1: DMA_W(ck, slot, 1); break; case 2: DMA_W(ck, slot, 2); break; case 3: DMA_W(ck, slot, 3); break;
      case 4: DMA_W(ck, slot, 4); break; case 5: DMA_W(ck, slot, 5); break; case 6: DMA_W(ck, slot, 6); break; default: DMA_W(ck, slot, 7); break;
    }
  };
  v4u cA[2][8], cB[2][8];
  v4u ones4 = {ONES, ONES, ONES, ONES};
  PIN(ones4);
  f32x4 cb4[4];
  {
    const v4u gbf = *(const v4u*)(smem + LDS_GS + w * 64 + q * 16), sbf = *(const v4u*)(smem + LDS_GS + 256 + w * 64 + q * 16);
#pragma unroll
    for (int k = 0; k < 4; ++k) { cA[0][k] = CV_A(0, k); cB[0][k] = CV_B(0, k); }
    SB();
    const f32x4 z4 = {0.f, 0.f, 0.f, 0.f};
#pragma unroll
    for (int e = 0; e < 2; ++e) {
      v4u o;
#pragma unroll
      for (int jj = 0; jj < 4; ++jj) o[jj] = pk2(unpk(wt[e][jj]) * unpk(gbf[jj]));
      st128<SC1>(o, rws, l16 + e * 16384, WS_WP + wp_fr);
      cb4[2 * e + 1] = mfma(sbf, wt[e], z4);
      cb4[2 * e] = mfma(ones4, o, z4);
    }
  }
  SB();
  f32x4 cacc[8];
  v2u ue[4];
  float uf[16];
  unsigned flagv;
  unsigned wro[4], a_cp, a_ue, o_csb, o_flag;
  gu32* p_flag = (gu32*)(p.ws + WS_CNT + (256 + (tid & 7) * 16 + (idx & 15)) * 128);
  {
    f32x2 cp[4];
    float s0, s1, s2, s3;
    sfor<144>([&](auto G) __attribute__((always_inline)) {
      constexpr int g = decltype(G)::value, ee = g / 36, j = g % 36, t = CVO.pp[j] + CVO.qq[j];
      constexpr bool first = ee == 0 && CVO.first[j];
      cacc[t] = mfma(cA[ee & 1][CVO.qq[j]], cB[ee & 1][CVO.pp[j]], first ? f32x4{0.f, 0.f, 0.f, 0.f} : cacc[t]);
      SB();
      if constexpr (g < 8) {
        if constexpr (g & 1) cB[0][4 + (g >> 1)] = CV_B(0, 4 + (g >> 1));
        else cA[0][4 + (g >> 1)] = CV_A(0, 4 + (g >> 1));
      }
      constexpr int jn = ee == 0 ? (j >= 12 && (j - 12) % 3 != 2 ? (j - 12) / 3 * 2 + (j - 12) % 3 : -1) : (j & 1) ? j >> 1 : -1;
      if constexpr (ee < 3 && jn >= 0 && jn < 16) {
        if constexpr (jn & 1) cB[(ee + 1) & 1][jn >> 1] = CV_B(ee + 1, jn >> 1);
        else cA[(ee + 1) & 1][jn >> 1] = CV_A(ee + 1, jn >> 1);
      }
      if constexpr (g == 8) { a_cp = lds0 + LDS_S + 4096 + c * 16; PIN(a_cp); }
      if constexpr (g == 9) { cb4[0][1] = cb4[1][0]; cb4[0][2] = cb4[2][0]; PINA(cb4[0]); }
      if constexpr (g == 10) { cb4[0][3] = cb4[3][0]; PINA(cb4[0]); }
      if constexpr (g == 11) ldsw<f32x4>(a_cp + w * 256, cb4[0]);
      if constexpr (g == 17) { a_cp += (q & 1) * 8; PIN(a_cp); }
      if constexpr (g == 20) { o_csb = q * 512 + c * 8; PIN(o_csb); }
      if constexpr (g == 23) { o_csb = tid < 32 ? o_csb : OOB; PIN(o_csb); }
      if constexpr (g == 26) { PIN(p_flag); }
      if constexpr (g == FLAG_AT) {
        STAMP(1, 42);
        asm volatile("s_waitcnt vmcnt(%0) lgkmcnt(8)" ::"i"(FLAG_AT > 38 ? 4 + (FLAG_AT - 38 + 3) / 4 : 4) : "memory");
        __builtin_amdgcn_s_barrier();
        if (tid < 8) (void)__hip_atomic_fetch_add(p_flag, 1u, __ATOMIC_RELAXED, __HIP_MEMORY_SCOPE_AGENT);
        STAMP(1, 43);
      }
      if constexpr (g == FLAG_AT + 4 || g == FLAG_AT + 8) {
        cp[(g - FLAG_AT - 4) / 2] = ldsr<f32x2>(a_cp + (g - FLAG_AT - 4) * 128);
        cp[(g - FLAG_AT - 4) / 2 + 1] = ldsr<f32x2>(a_cp + (g - FLAG_AT - 4) * 128 + 256);
      }
      if constexpr (g == FLAG_AT + 16) { s0 = cp[0][0] + cp[1][0]; PIN(s0); s2 = cp[0][1] + cp[1][1]; PIN(s2); }
      if constexpr (g == FLAG_AT + 20) { s1 = cp[2][0] + cp[3][0]; PIN(s1); s3 = cp[2][1] + cp[3][1]; PIN(s3); }
      if constexpr (g == FLAG_AT + 24) { s0 += s1; PIN(s0); s2 += s3; PIN(s2); }
      if constexpr (g == FLAG_AT + 28) st64<SC1>(v2u{__float_as_uint(s0), __float_as_uint(s2)}, rws, o_csb, WS_CSB + wpr * 1024 + wkg * 128 + wp_oob);
      if constexpr (g == 36) STAMP(1, 40);
      if constexpr (g == 72) STAMP(1, 41);
      if constexpr (g == 14 || g == 29 || g == 32 || g == 35) DMA_W(1, slotA, g == 14 ? 0 : (g - 26) / 3);
      if constexpr (g >= 38 && g < 86 && (g & 3) == 2) DMA_W(1 + (g + 10) / 64, g < 54 ? slotA : slotB, (4 + (g - 38) / 4) & 7);
      if constexpr (g == 88) asm volatile("s_waitcnt vmcnt(8)" ::: "memory");
      if constexpr (g >= 90 && g < 106 && (g & 1) == 0) wq[1][(g - 90) >> 1] = READ_W(0, (g - 90) >> 1);
      if constexpr (g >= 68 && g < 72) { wro[g - 68] = (lds0 + slotA + (lane >> 3) * 128 + (((lane & 7) ^ ((lane >> 4) & 1)) * 16)) ^ ((g - 68) * 32); PIN(wro[g - 68]); }
      if constexpr (g == 105) { a_ue = lds0 + LDS_U + cch * 4096 + eh * 2048 + c * 32 + q * 8; PIN(a_ue); }
      if constexpr (g == 106 || g == 107) { ue[(g - 106) * 2] = ldsr<v2u>(a_ue + (g - 106) * 1024); ue[(g - 106) * 2 + 1] = ldsr<v2u>(a_ue + (g - 106) * 1024 + 512); }
      if constexpr (g >= 108 && g < 140 && (g & 3) == 0) ldsw<v4u>(wro[((g - 108) >> 2) & 3] + ((g - 108) >> 2) * 1024, wr[0][(g - 108) >> 2]);
      if constexpr (g >= 109 && g < 125 && (g & 1) == 1) {
        constexpr int x = (g - 109) >> 1;
        if constexpr (x >= 2) { uf[2 * (x - 2)] = bflo(ue[(x - 2) >> 1][x & 1]); PIN(uf[2 * (x - 2)]); uf[2 * (x - 2) + 1] = bfhi(ue[(x - 2) >> 1][x & 1]); PIN(uf[2 * (x - 2) + 1]); }
      }
      if constexpr (g == 118 || g == 122) { constexpr int x = 6 + (g == 122); uf[2 * x] = bflo(ue[3][x & 1]); PIN(uf[2 * x]); uf[2 * x + 1] = bfhi(ue[3][x & 1]); PIN(uf[2 * x + 1]); }
      if constexpr (g == 110) { o_flag = w == 0 ? c * 128 : OOB; PIN(o_flag); }
      if constexpr (g == 126) asm volatile("s_waitcnt vmcnt(0)" ::: "memory");
      if constexpr (g >= 127 && g < 143 && (g & 1) == 1) wq[2][(g - 127) >> 1] = READ_W(8192, (g - 127) >> 1);
      if constexpr (g == POLL_AT) flagv = __builtin_amdgcn_raw_buffer_load_b32(rws, o_flag, WS_CNT + (256 + xcd * 16) * 128, SC1);
      SB();
    });
  }
  STAMP(1, 2);
  asm volatile("s_branch adaln_pad1\n\t.rept 64\n\ts_nop 0\n\t.endr\nadaln_pad1:");
  unsigned late;
  {
    f32x4* red = (f32x4*)(smem + LDS_X) + w * 256 + lane;
    f32x4 keep[4];
    if (eh) {
#pragma unroll
      for (int t = 0; t < 4; ++t) red[t * 64] = cacc[t];
      SB();
#pragma unroll
      for (int t = 0; t < 4; ++t) { PINA(cacc[4 + t]); keep[t] = cacc[4 + t]; PIN(keep[t]); }
    } else {
#pragma unroll
      for (int t = 0; t < 4; ++t) red[t * 64] = cacc[4 + t];
      SB();
#pragma unroll
      for (int t = 0; t < 4; ++t) { PINA(cacc[t]); keep[t] = cacc[t]; PIN(keep[t]); }
    }
    f32x2 pre[4][2];
#pragma unroll
    for (int t = 0; t < 4; ++t) {
      pre[t][0] = f32x2{uf[4 * t], uf[4 * t + 1]} + lo2(keep[t]);
      pre[t][1] = f32x2{uf[4 * t + 2], uf[4 * t + 3]} + hi2(keep[t]);
      PIN(pre[t][0]); PIN(pre[t][1]);
    }
    if (w == 0) *(unsigned*)(smem + LDS_S) = __builtin_amdgcn_ballot_w64(flagv < 16u) != 0ull;
    bar();
    late = *(const unsigned*)(smem + LDS_S);
    const f32x4* rd = (const f32x4*)(smem + LDS_X) + (w ^ 1) * 256 + lane;
    f32x4 oth[4];
#pragma unroll
    for (int t = 0; t < 4; ++t) oth[t] = rd[t * 64];
#pragma unroll
    for (int t = 0; t < 4; ++t) {
      v2u o;
      o[0] = nomark(pk2(pre[t][0] + lo2(oth[t])));
      o[1] = pk2(pre[t][1] + hi2(oth[t]));
      st64<SC1>(o, rws, xoff + t * 512, 0);
    }
  }
  STAMP(1, 3);
  SB();
  v4u wf[3][8];
  const unsigned rot = uh * 4;
#define LOAD_WP(half, k, i) ld128<SC1>(rws, l16 + ((i) & 3) * 1024, WS_WP + (((half) * 44 + UO(k)) * 16 + kh * 8 + (((i) & 4) ^ rot)) * 1024 + ((k) == 2 ? oob3 : 0u))
#define LOAD_XS(i) ld128<SC1>(rws, wsp_v, WS_X1T + ((i) * 32 + w * 8) * 4096 + mt * 128)
  v4u xs[16];
  unsigned tb[2][4], xdst;
  SB();
  int x1o;
  unsigned z2o[3], dgo[4], rdo[2], xw, xrd, a_fin, a_fr;
  v4u mkv = mk;
  v2u cz[2][3];
  f32x2 csb[2][3];
  unsigned gq[4];
#define LOAD_CZ(hf, kc) ld64<SC1>(rws, lane * 8, WS_CSB + ((hf) * 44 + UO(kc)) * 512 + ((kc) == 2 ? oob3 : 0u))
  {
    STAMP(1, 20);
    if (__builtin_amdgcn_readfirstlane(late)) {
      if (w == 0) { hint_wait(rws, c * 128, WS_CNT + (256 + xcd * 16) * 128, 16u); __builtin_amdgcn_s_sleep(LATE_SLEEP); }
      __builtin_amdgcn_s_barrier();
    }
    MEMFENCE();
    STAMP(1, 21);
#pragma unroll
    for (int i = 0; i < 3; ++i)
#pragma unroll
      for (int k = 0; k < 3; ++k) wf[k][i] = LOAD_WP(1, k, i);
    SB();
#pragma unroll
  for (int f = 0; f < 8; ++f) wq[0][f] = READ_W(0, f);
#pragma unroll
  for (int f = 0; f < 24; ++f) PINA(wq[f >> 3][f & 7]);
    int lz = lane;
    PIN(lz);
  {
    const int row = 8 * (lz >> 4) + ((lz >> 2) & 3);
    const int sg = ((row >> 1) & 1) | (((row >> 3) & 1) << 1);
#pragma unroll
    for (int tu = 0; tu < 4; ++tu) {
      const unsigned t0 = lds0 + LDS_A + kh * 32768 + row * 128 + ((tu ^ (2 * kh) ^ sg) * 32) + (lz & 3) * 8;
      tb[0][tu] = t0 + uh * 16384;
      tb[1][tu] = t0 + (uh ^ 1) * 16384;
    }
    const int rl = w * 8 + (lz >> 3), chn = lz & 7;
    const int sg2 = ((rl >> 1) & 1) | (((rl >> 3) & 1) << 1);
    xdst = lds0 + LDS_A + rl * 128 + (((chn >> 1) ^ sg2) * 32) + (chn & 1) * 16;
  }
  {
    const int row = 8 * (lz >> 4) + ((lz >> 2) & 3);
    const int sg = ((row >> 1) & 1) | (((row >> 3) & 1) << 1);
    x1o = LDS_A + nt * 8192 + row * 128 + ((w ^ sg) * 32) + (lz & 3) * 8;
  }
#pragma unroll
  for (int k = 0; k < 3; ++k) { z2o[k] = lz * 16 + WS_Z2 + (UO(k) * 64 + mt * 2 + kh) * 1024 + (k == 2 ? oob3 : 0u); }
#pragma unroll
    for (int tu = 0; tu < 8; ++tu) PIN(tb[tu >> 2][tu & 3]);
    PIN(xdst); PIN(x1o);
    {
      const int c2 = lz & 15;
      const bool dg = (c2 >> 2) == (lz >> 4);
      const unsigned dump = lds0 + LDS_W + 65536 + lz * 16;
      dgo[0] = dg ? lds0 + LDS_S + (w * 64 + kh * 32 + c2) * 4 : dump; dgo[1] = dg ? lds0 + LDS_S + (w * 64 + (kh ^ 1) * 32 + c2) * 4 : dump;
      dgo[2] = dg ? lds0 + LDS_S + 2048 + (w * 64 + kh * 32 + c2) * 16 : dump; dgo[3] = dg ? lds0 + LDS_S + 2048 + (w * 64 + (kh ^ 1) * 32 + c2) * 16 : dump;
      rdo[0] = lds0 + LDS_S + (kh * 32 + (lz & 31)) * 4; rdo[1] = lds0 + LDS_S + 2048 + (kh * 32 + (lz & 31)) * 16 + (lz & 3) * 4;
      xw = lds0 + LDS_X + w * 6144 + lz * 16; xrd = lds0 + LDS_X + (w ^ 1) * 6144 + lz * 16;
      a_fin = lds0 + LDS_S + 1024 + w * 256 + lz * 4; a_fr = lds0 + LDS_S + 1024 + w * 256 + (lz >> 4) * 16;
      PIN(dgo[0]); PIN(dgo[1]); PIN(dgo[2]); PIN(dgo[3]); PIN(rdo[0]); PIN(rdo[1]); PIN(xw); PIN(xrd); PIN(a_fin); PIN(a_fr); PIN(mkv);
    }
#pragma unroll
    for (int k = 0; k < 3; ++k) PIN(z2o[k]);
    SB();
#pragma unroll
    for (int i = 3; i < WF_EARLY; ++i)
#pragma unroll
      for (int k = 0; k < 3; ++k) wf[k][i] = LOAD_WP(1, k, i);
    SB();
#pragma unroll
    for (int i = 0; i < 16; ++i) xs[i] = LOAD_XS(i);
    SB();
    STAMP(1, 44);
    unsigned m = 0;
#pragma unroll
    for (int i = 0; i < 16; ++i) {
      m = macc(m, xs[i]); ldsw<v4u>(xdst + i * 4096, xs[i]);
      if (WF_EARLY * 3 + i < 24) wf[(WF_EARLY * 3 + i) % 3][(WF_EARLY * 3 + i) / 3] = LOAD_WP(1, (WF_EARLY * 3 + i) % 3, (WF_EARLY * 3 + i) / 3);
      SB();
    }
    PIN(m);
    STAMP(1, 45);
    RETRY_BEGIN(m)
#pragma unroll
      for (int i = 0; i < 16; ++i) xs[i] = LOAD_XS(i);
      SB();
#pragma unroll
      for (int i = 0; i < 16; ++i) { m = macc(m, xs[i]); ldsw<v4u>(xdst + i * 4096, xs[i]); }
    RETRY_END(m)
  }
  STAMP(1, 4);
  bar();
  STAMP(1, 5);
#define AHALF(tu, i, h) trread_a(tb[(i) >> 2][tu] + ((i) & 3) * 4096 + (h) * 512)
  f32x4 acc[3][4];
  v4u wg[3][8];
  v2u ah[2][4][2];
  float* st = (float*)(smem + LDS_S);
  float* fin = st + 256 + w * 64;
  f32x4* xch = (f32x4*)(smem + LDS_X);
  f32x4 r4[2], m4[2];
  {
    f32x4 s1[4], s2[4];
    constexpr int LA = ZLA;
    v2u ar[LA + 1][2];
    float pa[4], pb[4];
#define UTILE(u) ((u) < 8 ? (u) >> 2 : (((u) >> 3) + 1) & 3)
#define UKS(u) ((u) < 8 ? (u) & 3 : (u) & 7)
#pragma unroll
    for (int u = 0; u < LA; ++u) { ar[u][0] = AHALF(UTILE(u), UKS(u), 0); ar[u][1] = AHALF(UTILE(u), UKS(u), 1); }
#pragma unroll
    for (int i = 0; i < 6; ++i) cz[i / 3][i % 3] = LOAD_CZ(i / 3, i % 3);
#pragma unroll
    for (int i = 0; i < 4; ++i) gq[i] = __builtin_amdgcn_raw_buffer_load_b32(rws, lane * 8, WS_MODG + (i * 3072 + 5 * 512 + nt * 64) * 8, SC1);
    DMA_W(3, slotB, 0);
    SB();
    sfor<128>([&](auto G) __attribute__((always_inline)) {
      constexpr int m = decltype(G)::value, u = Z2O.u[m], k = Z2O.kind[m], pos = Z2O.pos[m], tu = UTILE(u), i = UKS(u);
      const v4u a = {ar[u % (LA + 1)][0][0], ar[u % (LA + 1)][0][1], ar[u % (LA + 1)][1][0], ar[u % (LA + 1)][1][1]};
      const f32x4 z4 = {0.f, 0.f, 0.f, 0.f};
      if constexpr (k < 3) acc[k][tu] = mfma(a, wf[k][i], (i == 0 && tu >= 2) ? z4 : acc[k][tu]);
      if constexpr (k == 3) s1[tu] = mfma(ones4, a, i == 0 ? z4 : s1[tu]);
      if constexpr (k == 4) s2[tu] = mfma(a, a, i == 0 ? z4 : s2[tu]);
      SB();
      if constexpr (pos < 2 && u + LA < 40) ar[(u + LA) % (LA + 1)][pos] = AHALF(UTILE(u + LA), UKS(u + LA), pos);
      if constexpr (pos < 2 && u >= 36) ah[0][u - 36][pos] = AHALF(u - 36, 0, pos);
      if constexpr (u < 8 && pos == 0 && u != 4) { if constexpr (u > 0) DMA_W(3, slotB, u); }
      if constexpr (u == 4 && pos == 0) DMA_W(3, slotB, 4);
      if constexpr (u >= 12 && u < 15 && pos == 0) st128<SC1>(mkv, rws, z2o[u - 12], 0);
      if constexpr (m == 23 || m == 28 || m == 55 || m == 70) {
        constexpr int x = m == 23 ? 0 : m == 28 ? 1 : m == 55 ? 2 : 3;
        PINA(s1[x]);
        ldsw<float>(dgo[x >> 1] + (x & 1) * 64, s1[x][0]);
        if constexpr (x < 3) ldsw<f32x4>(dgo[2 + (x >> 1)] + (x & 1) * 256, s2[x]);
      }
      if constexpr (m == 71) ldsw<f32x4>(dgo[3] + 256, s2[3]);
      if constexpr (m == 35) __builtin_amdgcn_s_waitcnt(0x0F70);
      if constexpr (m == 46 || m == 51 || m == 56 || m == 61) {
        constexpr int x = (m - 46) / 5 * 2;
        wq[3][x] = READ_W(8192, x); wq[3][x + 1] = READ_W(8192, x + 1);
      }
      if constexpr (m == 50 || m == 60 || m == 65) ldsw<f32x4>(xw + (m == 50 ? 0 : m == 60 ? 1 : 2) * 2048, acc[m == 50 ? 0 : m == 60 ? 1 : 2][2]);
      if constexpr (m == 38 || m == 41 || m == 44 || m == 47) DMA_W(4, slotA, (m - 38) / 3);
      if constexpr (m == 66) DMA_W(4, slotA, 4);
      if constexpr (m == 68 || m == 72 || m == 78) DMA_W(4, slotA, m == 68 ? 5 : m == 72 ? 6 : 7);
      if constexpr (m == 73 || m == 94) bar();
      if constexpr (m >= 74 && m < 77) acc[m - 74][0] = ldsr<f32x4>(xrd + (m - 74) * 2048);
      if constexpr (m == 82 || m == 85 || m == 88) ldsw<f32x4>(xw + (m - 82) / 3 * 2048 + 1024, acc[(m - 82) / 3][3]);
      if constexpr (m >= 95 && m < 98) acc[m - 95][1] = ldsr<f32x4>(xrd + (m - 95) * 2048 + 1024);
      if constexpr (m == 77) {
#pragma unroll
        for (int f = 0; f < 8; ++f) PINA(wq[3][f]);
      }
      if constexpr (m >= 83 && m < 93 && (m - 83) % 3 != 2) {
        constexpr int x = (m - 83) / 3 * 2 + (m - 83) % 3;
        if constexpr (x < 4) pa[x] = ldsr<float>(rdo[0] + x * 256);
        else pb[x - 4] = ldsr<float>(rdo[1] + (x - 4) * 1024);
      }
      if constexpr (m == 93) pb[3] = ldsr<float>(rdo[1] + 3072);
      if constexpr (m == 98) { pa[0] += pa[1]; PIN(pa[0]); }
      if constexpr (m == 99) { pa[2] += pa[3]; PIN(pa[2]); }
      if constexpr (m == 101) { pb[0] += pb[1]; PIN(pb[0]); }
      if constexpr (m == 102) { pb[2] += pb[3]; PIN(pb[2]); }
      if constexpr (m == 104) { pa[0] += pa[2]; PIN(pa[0]); }
      if constexpr (m == 105) { pb[0] += pb[2]; PIN(pb[0]); }
      if constexpr (m == 107) { pa[0] *= (1.0f / D); PIN(pa[0]); }
      if constexpr (m == 108) { pa[1] = pa[0] * pa[0]; PIN(pa[1]); }
      if constexpr (m == 110) { pb[0] = __builtin_fmaf(pb[0], 1.0f / D, -pa[1]); PIN(pb[0]); }
      if constexpr (m == 111) { pb[0] = __builtin_amdgcn_fmed3f(pb[0], 0.f, 3.0e38f); PIN(pb[0]); }
      if constexpr (m == 113) { pb[0] += EPS; PIN(pb[0]); }
      if constexpr (m == 114) { pb[0] = __builtin_amdgcn_rsqf(pb[0]); PIN(pb[0]); }
      if constexpr (m == 116) { pa[0] = lane < 32 ? 1.0f : -pa[0]; PIN(pa[0]); }
      if constexpr (m == 117) { pb[0] *= pa[0]; PIN(pb[0]); }
      if constexpr (m == 119) ldsw<float>(a_fin, pb[0]);
      if constexpr (m == 120 || m == 122 || m == 123 || m == 125) {
        constexpr int x = m == 120 ? 0 : m == 122 ? 1 : m == 123 ? 2 : 3;
        if constexpr (x & 1) r4[x >> 1] = ldsr<f32x4>(a_fr + (x >> 1) * 64);
        else m4[x >> 1] = ldsr<f32x4>(a_fr + 128 + (x >> 1) * 64);
      }
      if constexpr (m >= 100 && m < 124 && (m - 100) % 3 == 0) DMA_W(5, slotB, (m - 100) / 3);
      if constexpr (m == 47) STAMP(1, 46);
      if constexpr (m == 79) STAMP(1, 47);
      SB();
    });
  }
#pragma unroll
  for (int x = 0; x < 3; ++x) {
    const float y = xrow_sum4(__uint_as_float(cz[1][x][0]), __uint_as_float(cz[1][x][1]), __uint_as_float(cz[1][x][0]), __uint_as_float(cz[1][x][1]));
    auto sw = __builtin_amdgcn_permlane16_swap(__float_as_uint(y), __float_as_uint(y), false, false);
    csb[1][x] = f32x2{__uint_as_float(sw[0]), __uint_as_float(sw[1])};
  }
#pragma unroll
  for (int k = 0; k < 6; ++k) PINA(acc[k >> 1][k & 1]);
  STAMP(1, 6);
  asm volatile("s_branch adaln_pad2\n\t.rept 64\n\ts_nop 0\n\t.endr\nadaln_pad2:");
  f32x4 z2v[3][2];
  {
    f32x4 rs[2], rq[2];
#pragma unroll
    for (int t = 0; t < 2; ++t) {
#pragma unroll
      for (int k = 0; k < 3; ++k) {
        const f32x4 a = acc[k][t];
        const f32x2 zl = gelu2(fma2(lo2(a), lo2(r4[t]), fma2(lo2(m4[t]), bc2(csb[1][k][0]), bc2(csb[1][k][1]))));
        const f32x2 zh = gelu2(fma2(hi2(a), hi2(r4[t]), fma2(hi2(m4[t]), bc2(csb[1][k][0]), bc2(csb[1][k][1]))));
        z2v[k][t] = f32x4{zl[0], zl[1], zh[0], zh[1]};
        if (k == 0) { rs[t] = z2v[k][t]; rq[t] = z2v[k][t] * z2v[k][t]; }
        else { rs[t] += z2v[k][t]; rq[t] += z2v[k][t] * z2v[k][t]; }
        if (((t * 3 + k) & 1) == 0) {
#pragma unroll
          for (int f = (t * 3 + k) * 4; f < (t * 3 + k) * 4 + 8; ++f) wg[f % 3][f / 3] = LOAD_WP(0, f % 3, f / 3);
        } else {
          PIN(z2v[k][t]);
          SB();
        }
      }
    }
    STAMP(1, 50);
    float v8[8], v4[4], v2[2];
    const bool b3 = c & 8, b2 = c & 4, b1 = c & 2, b0 = c & 1;
#pragma unroll
    for (int r_ = 0; r_ < 4; ++r_) {
      const float a0 = rs[0][r_] + dpp<0x128>(rs[0][r_]), a1 = rs[1][r_] + dpp<0x128>(rs[1][r_]);
      const float q0 = rq[0][r_] + dpp<0x128>(rq[0][r_]), q1 = rq[1][r_] + dpp<0x128>(rq[1][r_]);
      v8[2 * r_] = b3 ? a1 : a0; v8[2 * r_ + 1] = b3 ? q1 : q0;
    }
#pragma unroll
    for (int j = 0; j < 4; ++j) {
      const float x0 = v8[j] + dpp<0x141>(v8[j]), x1 = v8[4 + j] + dpp<0x141>(v8[4 + j]);
      v4[j] = b2 ? x1 : x0;
    }
#pragma unroll
    for (int j = 0; j < 2; ++j) {
      const float x0 = v4[j] + dpp<0x4E>(v4[j]), x1 = v4[2 + j] + dpp<0x4E>(v4[2 + j]);
      v2[j] = b1 ? x1 : x0;
    }
    const float x0 = v2[0] + dpp<0xB1>(v2[0]), x1 = v2[1] + dpp<0xB1>(v2[1]);
    const float outv = b0 ? x1 : x0;
    __builtin_amdgcn_raw_buffer_store_b32(nomark(__float_as_uint(outv)), rws, lane * 4, WS_ST2 + ((mt * 2 + kh) * 16 + nt * 2 + uh) * 256, SC1);
  }
  STAMP(1, 7);
  SB();
#pragma unroll
  for (int tu = 0; tu < 4; ++tu)
#pragma unroll
    for (int k = 0; k < 3; ++k) acc[k][tu] = f32x4{0.f, 0.f, 0.f, 0.f};
  unsigned k3v[6];
  {
    const unsigned k0 = l16 + w * 6144, w3 = (w + 1) >> 2;
    k3v[0] = k0; k3v[1] = k0 + 2048 - w3 * 1024; k3v[2] = k3v[1] + 1024; k3v[3] = k0 + 5120 - w3 * 2048;
    k3v[4] = k0 + 1024 + (w3 << 30); k3v[5] = k3v[4] + 3072;
  }
  v2u x1h[2][2];
  v4u wo[6][4];
#define LOAD_WO(f) wo[(f) >> 2][(f) & 3] = ld128<SC1>(rws, k3v[(f) >> 2], WS_WOT + (nt * 4 + ((f) & 3)) * 22 * 1024)
  auto dma_sel2 = [&](int i, int x) __attribute__((always_inline)) {
    if (i < 4) dma_sel(IC<6>{}, slotA, (i - 2) * 4 + x); else dma_sel(IC<7>{}, slotB, (i - 4) * 4 + x);
  };
  f32x4 pt[4];
  float* c1s = st + 768 + w * 96;
#define LOAD_PT(i) asf(ld128<SC1>(rws, l16 + (i) * 1024, WS_ST2 + (mt * 2 + kh) * 4096))
  SB();
#pragma unroll
  for (int i = 0; i < 8; ++i) {
#pragma unroll
    for (int j = 0; j < 12; ++j) {
      const int tu = j / 3, k = j % 3;
      const v4u a = {ah[i & 1][tu][0][0], ah[i & 1][tu][0][1], ah[i & 1][tu][1][0], ah[i & 1][tu][1][1]};
      acc[k][tu] = mfma(wg[k][i], a, acc[k][tu]);
      SB();
      if (j < 8 && i < 7) ah[(i + 1) & 1][j >> 1][j & 1] = AHALF(j >> 1, i + 1, j & 1);
      if (j < 4 && i == 7) x1h[j >> 1][j & 1] = trread(smem + x1o + (j >> 1) * 4096 + (j & 1) * 512);
      if (i == 0 && j == 0) asm volatile("s_waitcnt vmcnt(24)" ::: "memory");
      if (i < 2 && j >= 4) wq[4 + i][j - 4] = READ_W(i * 8192, j - 4);
      if (i == 3 && j == 0) {
#pragma unroll
        for (int f = 0; f < 16; ++f) PINA(wq[4 + (f >> 3)][f & 7]);
      }
      if (i == 2 && j == 8) USE(wg[2][7]);
      if (i >= 2 && i < 6 && j == 9) dma_sel2(i, 0);
      if (i >= 3 && i < 7 && (j == 1 || j == 4 || j == 7)) dma_sel2(i - 1, (j + 2) / 3);
      if (j >= 8 && i == 6) pt[j - 8] = LOAD_PT(j - 8);
      if (i == 7 && j >= 8) LOAD_WO(j - 8);
      if (i == 5 && j == 10) {
        asm volatile("s_waitcnt vmcnt(30)" ::: "memory");
        __builtin_amdgcn_s_barrier();
      }
      SB();
    }
  }
  STAMP(1, 8);
  asm volatile("s_branch adaln_pad3\n\t.rept 64\n\ts_nop 0\n\t.endr\nadaln_pad3:");
#pragma unroll
  for (int k = 0; k < 3; ++k)
#pragma unroll
    for (int t = 0; t < 2; ++t) xch[(w * 6 + k * 2 + t) * 64 + lane] = acc[k][2 + t];
  SB();
  {
    unsigned m = 0;
#pragma unroll
    for (int i = 0; i < 4; ++i) m = macc4(m, __builtin_bit_cast(v4u, pt[i]));
    RETRY_BEGIN(m)
#pragma unroll
      for (int i = 0; i < 4; ++i) { pt[i] = LOAD_PT(i); m = macc4(m, __builtin_bit_cast(v4u, pt[i])); }
    RETRY_END(m)
    if (tid < 4) hint_add(p.ws, 64 + (nt * 4 + (mt >> 3)) * 4 + tid);
    const f32x4 sx = (pt[0] + pt[1]) + (pt[2] + pt[3]);
    const float tv = xrow_sum4(sx[0], sx[2], sx[1], sx[3]);
    auto sw = __builtin_amdgcn_permlane32_swap(__float_as_uint(tv), __float_as_uint(tv), false, false);
    const float mm = __uint_as_float(sw[0]) * (1.0f / FH);
    const float r = __builtin_amdgcn_rsqf(fmaxf(__uint_as_float(sw[1]) * (1.0f / FH) - mm * mm, 0.f) + EPS);
    float* f2 = st + 512 + w * 64;
    f2[(q >> 1) * 32 + 2 * c + (q & 1)] = (q >> 1) ? -r * mm : r;
#pragma unroll
    for (int t = 0; t < 2; ++t) { r4[t] = *(const f32x4*)(f2 + q * 8 + t * 4); m4[t] = *(const f32x4*)(f2 + 32 + q * 8 + t * 4); }
#pragma unroll
    for (int k = 0; k < 3; ++k) {
      v4u o;
#pragma unroll
      for (int t = 0; t < 2; ++t) {
        o[2 * t] = nomark(pk2(fma2(lo2(z2v[k][t]), lo2(r4[t]), lo2(m4[t]))));
        o[2 * t + 1] = pk2(fma2(hi2(z2v[k][t]), hi2(r4[t]), hi2(m4[t])));
      }
      st128<SC1>(o, rws, z2o[k], 0);
    }
  }
  STAMP(1, 9);
  c1s[lane] = xrow_sum4(__uint_as_float(cz[0][0][0]), __uint_as_float(cz[0][0][1]), __uint_as_float(cz[0][1][0]), __uint_as_float(cz[0][1][1]));
  c1s[64 + (lane & 31)] = xrow_sum4(__uint_as_float(cz[0][2][0]), __uint_as_float(cz[0][2][1]), __uint_as_float(cz[0][2][0]), __uint_as_float(cz[0][2][1]));
  st[1408 + lane] = rbf((__uint_as_float(gq[0]) + __uint_as_float(gq[1])) + (__uint_as_float(gq[2]) + __uint_as_float(gq[3])));
  SB();
  f32x4 z1f[3][2];
  constexpr int RD = ZRD;
  v4u zf[RD];
#define LOAD_Z(n) ld128<SC1>(rws, zv[((n) >> 1) & 7] + ((n) & 1) * 1024, WS_Z2 + (((n) < 48 ? UT(((n) >> 4) % 3) : UO(((n) >> 4) % 3)) * 64 + w * 16) * 1024 + (((n) >> 4) % 3 == 2 ? oob3 : 0u))
  unsigned zv[8];
#pragma unroll
  for (int i = 0; i < 8; ++i) { zv[i] = l16 + ((CKMAP(i) + kr) & 7) * 2048; PIN(zv[i]); }
  {
    bar();
    STAMP(1, 34);
#pragma unroll
    for (int k = 0; k < 6; ++k) PINA(acc[k >> 1][k & 1]);
    f32x4 oth[3][2], cs1[3], bs1[3];
    float r1[2], m1[2];
#pragma unroll
    for (int t = 0; t < 2; ++t) { r1[t] = fin[t * 16 + c]; m1[t] = fin[32 + t * 16 + c]; }
#pragma unroll
    for (int k = 0; k < 3; ++k) {
      cs1[k] = *(const f32x4*)(c1s + k * 32 + q * 4); bs1[k] = *(const f32x4*)(c1s + k * 32 + 16 + q * 4);
#pragma unroll
      for (int t = 0; t < 2; ++t) oth[k][t] = xch[((w ^ 1) * 6 + k * 2 + t) * 64 + lane];
    }
#pragma unroll
    for (int i = 4; i < WO0; ++i) LOAD_WO(i);
    SB();
    f32x2 z1a[6][2];
#pragma unroll
    for (int i = 0; i < 6; ++i) {
      const int k = i >> 1, t = i & 1;
      const f32x4 a = acc[k][t] + oth[k][t];
      z1a[i][0] = fma2(lo2(a), bc2(r1[t]), fma2(lo2(cs1[k]), bc2(m1[t]), lo2(bs1[k])));
      z1a[i][1] = fma2(hi2(a), bc2(r1[t]), fma2(hi2(cs1[k]), bc2(m1[t]), hi2(bs1[k])));
    }
#pragma unroll
    for (int i = 0; i < GELU_PRE; ++i) {
      const f32x2 lo = gelu2(z1a[i][0]), hi = gelu2(z1a[i][1]);
      z1f[i >> 1][i & 1] = f32x4{lo[0], lo[1], hi[0], hi[1]};
      PIN(z1f[i >> 1][i & 1]);
    }
#pragma unroll
    for (int i = GELU_PRE; i < 6; ++i) { PIN(z1a[i][0]); PIN(z1a[i][1]); }
    SB();
#pragma unroll
    for (int f = 0; f < 16; ++f) wq[6 + (f >> 3)][f & 7] = READ_W((f >> 3) * 8192, f & 7);
    SB();
    STAMP(1, 10);
    hint_wait(rws, 0u, WS_CNT + (64 + (nt * 4 + w) * 4 + (xcd >> 1)) * 128, 8u);
    STAMP(1, 22);
#pragma unroll
    for (int n = 0; n < RD; ++n) zf[n] = LOAD_Z(n);
    SB();
#pragma unroll
    for (int i = GELU_PRE; i < 6; ++i) {
      const f32x2 lo = gelu2(z1a[i][0]), hi = gelu2(z1a[i][1]);
      z1f[i >> 1][i & 1] = f32x4{lo[0], lo[1], hi[0], hi[1]};
      PIN(z1f[i >> 1][i & 1]);
    }
  }
  f32x4* xb = (f32x4*)(smem + LDS_A) + lane;
  f32x4* yb = (f32x4*)(smem + LDS_A + 49152) + lane;
  f32x4 ac2[2][4], ini[4], yo[2];
  const float bs0 = bflo(bspq[0]), bs1_ = bfhi(bspq[0]);
  f32x4 bq0 = {bs0, bs0, bs0, bs0}, bq1 = {bs1_, bs1_, bs1_, bs1_};
  const f32x4 z4 = {0.f, 0.f, 0.f, 0.f};
  PINA(bq0); PINA(bq1);
  unsigned vmk = l16 + WS_V + ((mt * 4 + w) * 22 + pr0) * 1024, o_fl4 = w == 0 ? 0u : OOB;
  gu32* p_fl4 = (gu32*)(p.ws + WS_CNT + (192 + mt * 2 + (tid & 1)) * 128);
  PIN(vmk); PIN(mkv); PIN(o_fl4);
  unsigned voff[2];
  voff[0] = l16 + WS_V + ((mt * 4 + 2 * kh) * 22 + (uo0 >> 1)) * 1024;
  voff[1] = l16 + WS_V + ((mt * 4 + 2 * kh) * 22 + (uo2 >> 1)) * 1024 + (uo2 & 1) * 8 + oob3;
  PIN(voff[0]); PIN(voff[1]);
  float ev[4];
  unsigned eo0, eos, eo1[2];
  float evs[2];
  SB();
  STAMP(1, 11);
  auto v_store = [&](int k, int t, unsigned o0, unsigned o1) __attribute__((always_inline)) {
    st64<SC1>(v2u{o0, o1}, rws, voff[k >> 1] + (k & 1) * 8, t * 22 * 1024);
  };
  sfor<6>([&](auto P) __attribute__((always_inline)) {
    constexpr int ps = decltype(P)::value, cur = ps & 1, prv = cur ^ 1;
    unsigned zm;
    sfor<64>([&](auto G) __attribute__((always_inline)) {
      constexpr int g = decltype(G)::value, ks = g >> 2, tu = g & 3, ck = ks >> 1, k2 = ks & 1, n = ps * 16 + ks;
      const f32x4 cin = ks > 0 ? ac2[cur][tu] : ps >= 3 ? ini[tu] : tu == 0 ? bq0 : tu == 1 ? bq1 : z4;
      ac2[cur][tu] = mfma(zf[n % RD], wq[ck][k2 * 4 + tu], cin);
      SB();
      if constexpr (tu == 1) { zm = ks == 0 ? (zf[n % RD][0] > zf[n % RD][2] ? zf[n % RD][0] : zf[n % RD][2]) : macc(zm, zf[n % RD]); PIN(zm); }
      if constexpr (tu == 3) {
        if constexpr (n + RD < 96) zf[n % RD] = LOAD_Z(n + RD);
        else if constexpr (WO0 + n + RD - 96 < 24) LOAD_WO(WO0 + n + RD - 96);
      }
      if constexpr (ps >= 1 && ps <= 3 && g >= 2 && g < 18 && (g & 3) == 2) xb[((w * 3 + ps - 1) * 4 + (g >> 2)) * 64] = ac2[prv][g >> 2];
      if constexpr (ps >= 4 && (g == 2 || g == 6)) yb[((w * 3 + ps - 4) * 2 + (g >> 2)) * 64] = ac2[prv][2 + (g >> 2)];
      if constexpr (ps >= 1 && g == (ps >= 4 ? 12 : 24)) {
        if constexpr (ps == 4) asm volatile("s_waitcnt vmcnt(20)" ::: "memory");
        bar();
      }
      if constexpr (ps >= 4 && (g == 13 || g == 14)) yo[g - 13] = yb[(((w ^ 1) * 3 + ps - 4) * 2 + g - 13) * 64];
      if constexpr (ps >= 4 && g >= 18 && g < 46 && (g & 1) == 0) {
        constexpr int t = (g - 18) / 14, ph = ((g - 18) % 14) >> 1;
        if constexpr (ph == 0) PINA(ac2[prv][t]);
        if constexpr (ph < 4) { ev[ph] = ac2[prv][t][ph] + yo[t][ph]; PIN(ev[ph]); }
        else if constexpr (ph < 6) { ev[2 * ph - 8] *= z1f[ps - 4][t][2 * ph - 8]; PIN(ev[2 * ph - 8]); ev[2 * ph - 7] *= z1f[ps - 4][t][2 * ph - 7]; PIN(ev[2 * ph - 7]); }
        else { eo0 = nomark(pk2(ev[0], ev[1])); PIN(eo0); }
      }
      if constexpr (ps >= 4 && (g == 29 + 4 || g == 43 + 2)) { eo1[g > 40] = pk2(g > 40 ? ev[2] : evs[0], g > 40 ? ev[3] : evs[1]); PIN(eo1[g > 40]); }
      if constexpr (ps >= 4 && g == 30) { evs[0] = ev[2]; evs[1] = ev[3]; eos = eo0; }
      if constexpr (ps >= 4 && g == 37) v_store(ps - 4, 0, eos, eo1[0]);
      if constexpr (ps >= 4 && g == 46) v_store(ps - 4, 1, eo0, eo1[1]);
      if constexpr (ps >= 2 && ps <= 4 && g >= 48 && g < 56 && (g & 1) == 0) ini[(g - 48) >> 1] = xb[(((w ^ 2) * 3 + ps - 2) * 4 + ((g - 48) >> 1)) * 64];
      if constexpr (ps == 0 && (g == 22 || g == 38 || g == 54)) st128<SC1>(mkv, rws, vmk + (g - 22) / 16 * 1024, g == 54 ? oob3 : 0u);
      if constexpr (ps * 64 + g == HINT4_G) { if (tid < 2) (void)__hip_atomic_fetch_add(p_fl4, 1u, __ATOMIC_RELAXED, __HIP_MEMORY_SCOPE_AGENT); }
      if constexpr (ps == 3 && g == 40) PIN(p_fl4);
      if constexpr (ps == 5 && g == POLL4_G) flagv = __builtin_amdgcn_raw_buffer_load_b32(rws, o_fl4, WS_CNT + (192 + mt * 2 + (xcd & 1)) * 128, SC1);
      if constexpr (g == 0 && ps >= 1 && ps < 5) STAMP(1, 55 + ps);
      SB();
    });
    if (__builtin_expect(any_mark(zm), 0)) {
#pragma unroll
      for (int tu = 0; tu < 4; ++tu) ac2[cur][tu] = ps >= 3 ? xb[(((w ^ 2) * 3 + ps - 3) * 4 + tu) * 64] : tu == 0 ? bq0 : tu == 1 ? bq1 : z4;
      sfor<16>([&](auto K) __attribute__((always_inline)) {
        constexpr int ks = decltype(K)::value, ck = ks >> 1, k2 = ks & 1;
        v4u zz;
        unsigned sp = 0;
        do { MEMFENCE(); zz = LOAD_Z(ps * 16 + ks); } while (any_mark(macc(0u, zz)) & (++sp <= SPIN_CAP));
#pragma unroll
        for (int tu = 0; tu < 4; ++tu) ac2[cur][tu] = mfma(zz, wq[ck][k2 * 4 + tu], ac2[cur][tu]);
      });
    }
  });
  STAMP(1, 12);
  asm volatile("s_branch adaln_pad4\n\t.rept 64\n\ts_nop 0\n\t.endr\nadaln_pad4:");
  {
#pragma unroll
    for (int t = 0; t < 2; ++t) yb[((w * 3 + 2) * 2 + t) * 64] = ac2[1][2 + t];
    PIN(ac2[1][0]); PIN(ac2[1][1]);
    if (w == 0) st[0] = __uint_as_float(flagv < 8u);
    bar();
    late = __float_as_uint(st[0]);
    STAMP(1, 52);
#pragma unroll
    for (int t = 0; t < 2; ++t) yo[t] = yb[(((w ^ 1) * 3 + 2) * 2 + t) * 64];
#pragma unroll
    for (int t = 0; t < 2; ++t) {
      const f32x4 g = (ac2[1][t] + yo[t]) * z1f[2][t];
      v_store(2, t, nomark(pk2(lo2(g))), pk2(hi2(g)));
    }
  }
  STAMP(1, 13);
  SB();
  f32x4 gb4[4];
  f32x2 x1f[2][4];
  STAMP(1, 53);
  if (__builtin_amdgcn_readfirstlane(late)) {
    if (w == 0) hint_wait(rws, 0u, WS_CNT + (192 + mt * 2 + (xcd & 1)) * 128, 8u);
    __builtin_amdgcn_s_barrier();
  }
  MEMFENCE();
  STAMP(1, 23);
  f32x4 a3[4][4];
  {
    unsigned spins = 0;
    for (;;) {
      MEMFENCE();
      v4u vs[6][4];
#define LOAD_V(f) vs[(f) >> 2][(f) & 3] = ld128<SC1>(rws, k3v[(f) >> 2], WS_V + (mt * 4 + (((f) & 3) ^ w)) * 22 * 1024)
#pragma unroll
      for (int f = 0; f < 12; ++f) LOAD_V(f);
      SB();
      unsigned m = 0;
      sfor<96>([&](auto G) __attribute__((always_inline)) {
        constexpr int g = decltype(G)::value, ks = g >> 4, tu = (g >> 2) & 3, e = g & 3;
        a3[e][tu] = mfma(wo[ks][e], vs[ks][tu], ks == 0 ? f32x4{0.f, 0.f, 0.f, 0.f} : a3[e][tu]);
        if constexpr (e == 0 && 12 + (g >> 2) < 24) LOAD_V(12 + (g >> 2));
        if constexpr (e == 1) { m = macc(m, vs[ks][tu]); PIN(m); }
        SB();
      });
      if (!(any_mark(m) & (++spins <= SPIN_CAP))) break;
    }
  }
#pragma unroll
  for (int i = 0; i < 4; ++i) gb4[i] = *(const f32x4*)(st + 1408 + (i >> 1) * 32 + q * 8 + (i & 1) * 4);
#pragma unroll
  for (int i = 0; i < 8; ++i) { x1f[i >> 2][i & 3] = unpk(x1h[i >> 2][(i >> 1) & 1][i & 1]); PIN(x1f[i >> 2][i & 3]); }
  STAMP(1, 14);
  asm volatile("s_branch adaln_pad5\n\t.rept 64\n\ts_nop 0\n\t.endr\nadaln_pad5:");
  {
    f32x4* red = (f32x4*)(smem + LDS_W);
#pragma unroll
    for (int tu = 1; tu < 4; ++tu)
#pragma unroll
      for (int e = 0; e < 4; ++e) red[((w * 3 + tu - 1) * 4 + e) * 64 + lane] = a3[e][tu];
#pragma unroll
    for (int e = 0; e < 4; ++e) PIN(a3[e][0]);
    bar();
    STAMP(1, 54);
    f32x4 o3[4][3];
#pragma unroll
    for (int e = 0; e < 4; ++e)
#pragma unroll
      for (int rel = 1; rel < 4; ++rel) o3[e][rel - 1] = red[(((w ^ rel) * 3 + rel - 1) * 4 + e) * 64 + lane];
    const rsrc_t rout = mkrs(p.out, L * D * 2);
    SB();
#pragma unroll
    for (int pp = 0; pp < 2; ++pp) {
      v4u o;
#pragma unroll
      for (int h = 0; h < 2; ++h) {
        const int e = pp * 2 + h;
        const f32x4 y = (a3[e][0] + o3[e][0]) + (o3[e][1] + o3[e][2]);
        o[2 * h] = pk2(fma2(lo2(y), lo2(gb4[e]), x1f[pp][2 * h]));
        o[2 * h + 1] = pk2(fma2(hi2(y), hi2(gb4[e]), x1f[pp][2 * h + 1]));
      }
      st128<0>(o, rout, perm(w, c) * 1024 + q * 16 + pp * 64, mt * 64 * 1024 + nt * 128);
    }
  }
  if (b == 0 && tid == 0) *(unsigned*)(p.ws + WS_EPOCH) = tag_s;
  STAMP(1, 15);
  STAMP_FLUSH(1);
  asm volatile("adaln_code_end:");
}

extern "C" void kernel_launch(void* const* d_in, const int* in_sizes, int n_in, void* d_out, int out_size, void* d_ws,
                              size_t ws_size, hipStream_t stream) {
  Params p;
  p.u = (const u16*)d_in[0];
  p.lar = (const u16*)d_in[1];
  p.aim = (const u16*)d_in[2];
  p.cre = (const u16*)d_in[3];
  p.cim = (const u16*)d_in[4];
  p.ldt = (const u16*)d_in[5];
  p.dsk = (const u16*)d_in[6];
  p.win = (const u16*)d_in[8];
  p.wsp = (const u16*)d_in[9];
  p.bsp = (const u16*)d_in[10];
  p.wout = (const u16*)d_in[11];
  p.c = (const u16*)d_in[12];
  p.wmod = (const u16*)d_in[13];
  p.out = (u16*)d_out;
  p.ws = (unsigned char*)d_ws;
  (void)hipFuncSetAttribute((const void*)adaln_mega, hipFuncAttributeMaxDynamicSharedMemorySize, MEGA_LDS);
  adaln_mega<<<256, 256, MEGA_LDS, stream>>>(p);
}
```

```cpp
#define LATE_G 40
#include <hip/hip_runtime.h>
#include <hip/hip_bf16.h>
#include <stdint.h>
#include <utility>

#define LAS __attribute__((address_space(3)))
typedef unsigned v4u __attribute__((ext_vector_type(4)));
typedef unsigned v2u __attribute__((ext_vector_type(2)));
typedef float f32x4 __attribute__((ext_vector_type(4)));
typedef float f32x2 __attribute__((ext_vector_type(2)));
typedef short bf16x8 __attribute__((ext_vector_type(8)));
typedef _Float16 f16x8 __attribute__((ext_vector_type(8)));
typedef _Float16 f16x2 __attribute__((ext_vector_type(2)));
typedef __bf16 bf16x2_t __attribute__((ext_vector_type(2)));
typedef unsigned short u16;
typedef __amdgpu_buffer_rsrc_t rsrc_t;

#define STAMP_DECL do { } while (0)
#define STAMP(k, i) do { } while (0)
#define STAMP_FLUSH(k) do { } while (0)

#define OPT_A 1
#define OPT_B 1
#define OPT_C 2
#define OPT_D 1
#define OPT_E 2
#define OPT_F 1
constexpr int L = 2048, D = 512, NM = 128, FH = 688, FF = 1376;
constexpr float EPS = 1e-5f;
constexpr int SC1 = 16;
constexpr unsigned OOB = 0x40000000u;
constexpr unsigned SPIN_CAP = 1u << 14;

constexpr unsigned WS_EPOCH = 0;
constexpr unsigned WS_CNT = 4096;
constexpr unsigned WS_SR = WS_CNT + 512 * 128;
constexpr unsigned WS_SB = WS_SR + 2048 * 8;
constexpr unsigned WS_MODG = WS_SB + 2048 * 8;
constexpr unsigned WS_CSB = WS_MODG + 3072 * 32;
constexpr unsigned WS_ST2 = WS_CSB + 88 * 16 * 32;
constexpr unsigned WS_UG = 1u << 20;
static_assert(WS_ST2 + 32 * 2 * 16 * 32 * 8 <= WS_UG, "small buffers");
constexpr unsigned WS_X1T = WS_UG + 256 * 2048 * 8;
constexpr unsigned WS_WP = WS_X1T + 512 * 2048 * 2;
constexpr unsigned WS_WOT = WS_WP + 88 * 16 * 1024;
constexpr unsigned WS_Z2 = WS_WOT + 32 * 22 * 1024;
constexpr unsigned WS_V = WS_Z2 + 44 * 64 * 1024;
constexpr unsigned WS_END = WS_V + 128 * 22 * 1024;

struct Params {
  const u16 *u, *lar, *aim, *cre, *cim, *ldt, *dsk, *win, *wsp, *bsp, *wout, *c, *wmod;
  u16* out;
  unsigned char* ws;
};

#define DI __device__ __forceinline__
template <int V> struct IC { static constexpr int value = V; };
template <class F, int... I> DI void sfor_(F&& f, std::integer_sequence<int, I...>) { int d[] = {(f(IC<I>{}), 0)...}; (void)d; }
template <int N, class F> DI void sfor(F&& f) { sfor_(f, std::make_integer_sequence<int, N>{}); }
DI float bf2f(unsigned b) { return __builtin_bit_cast(float, b << 16); }
DI float bflo(unsigned w) { return __builtin_bit_cast(float, w << 16); }
DI float bfhi(unsigned w) { return __builtin_bit_cast(float, w & 0xFFFF0000u); }
DI f32x2 unpk(unsigned w) { return f32x2{bflo(w), bfhi(w)}; }
DI unsigned pk2(f32x2 v) { return __builtin_bit_cast(unsigned, __builtin_convertvector(v, bf16x2_t)); }
DI unsigned pk2(float lo, float hi) { return pk2(f32x2{lo, hi}); }
DI unsigned nomark(unsigned x) { return x < 0xFFFFFFFEu ? x : 0xFFFFFFFEu; }
DI float rbf(float x) { return bflo(pk2(x, 0.f)); }
DI f32x2 fma2(f32x2 a, f32x2 b, f32x2 c) { return __builtin_elementwise_fma(a, b, c); }
DI f32x2 lo2(f32x4 v) { return f32x2{v[0], v[1]}; }
DI f32x2 hi2(f32x4 v) { return f32x2{v[2], v[3]}; }
DI f32x2 bc2(float x) { return f32x2{x, x}; }
DI f32x4 mfma(v4u a, v4u b, f32x4 c) {
  return __builtin_amdgcn_mfma_f32_16x16x32_bf16(__builtin_bit_cast(bf16x8, a), __builtin_bit_cast(bf16x8, b), c, 0, 0, 0);
}
DI f32x4 mfma_h(v4u a, v4u b, f32x4 c) {
  return __builtin_amdgcn_mfma_f32_16x16x32_f16(__builtin_bit_cast(f16x8, a), __builtin_bit_cast(f16x8, b), c, 0, 0, 0);
}
DI float dot2(unsigned a, unsigned b, float acc) {
  return __builtin_amdgcn_fdot2_f32_bf16(__builtin_bit_cast(bf16x2_t, a), __builtin_bit_cast(bf16x2_t, b), acc, false);
}
constexpr unsigned ONES = 0x3F803F80u;
DI int perm(int u, int c) { return 32 * (u >> 1) + 8 * (c >> 2) + 4 * (u & 1) + (c & 3); }
template <int CTRL> DI float dpp(float x) {
  return __builtin_bit_cast(float, __builtin_amdgcn_mov_dpp(__builtin_bit_cast(int, x), CTRL, 0xf, 0xf, true));
}
DI float row16_sum(float x) {
  x += dpp<0xB1>(x); x += dpp<0x4E>(x); x += dpp<0x141>(x); x += dpp<0x128>(x);
  return x;
}
DI float row16_max(float x) {
  x = fmaxf(x, dpp<0xB1>(x)); x = fmaxf(x, dpp<0x4E>(x)); x = fmaxf(x, dpp<0x141>(x)); x = fmaxf(x, dpp<0x128>(x));
  return x;
}
DI float xrow_sum(float x) {
  auto s = __builtin_amdgcn_permlane16_swap(__float_as_uint(x), __float_as_uint(x), false, false);
  x = __uint_as_float(s[0]) + __uint_as_float(s[1]);
  auto t = __builtin_amdgcn_permlane32_swap(__float_as_uint(x), __float_as_uint(x), false, false);
  return __uint_as_float(t[0]) + __uint_as_float(t[1]);
}
DI float xrow_sum4(float a, float b, float c, float d) {
  auto s = __builtin_amdgcn_permlane16_swap(__float_as_uint(a), __float_as_uint(b), false, false);
  auto t = __builtin_amdgcn_permlane16_swap(__float_as_uint(c), __float_as_uint(d), false, false);
  const float x = __uint_as_float(s[0]) + __uint_as_float(s[1]), y = __uint_as_float(t[0]) + __uint_as_float(t[1]);
  auto z = __builtin_amdgcn_permlane32_swap(__float_as_uint(x), __float_as_uint(y), false, false);
  return __uint_as_float(z[0]) + __uint_as_float(z[1]);
}
DI float xrow_max(float x) {
  auto s = __builtin_amdgcn_permlane16_swap(__float_as_uint(x), __float_as_uint(x), false, false);
  x = fmaxf(__uint_as_float(s[0]), __uint_as_float(s[1]));
  auto t = __builtin_amdgcn_permlane32_swap(__float_as_uint(x), __float_as_uint(x), false, false);
  return fmaxf(__uint_as_float(t[0]), __uint_as_float(t[1]));
}
DI f32x2 gelu2(f32x2 x) {
  const float k0 = -2.0f * 0.7978845608028654f * 1.4426950408889634f, k1 = k0 * 0.044715f;
  const f32x2 a = x * fma2(x * x, bc2(k1), bc2(k0));
  const f32x2 e = f32x2{__builtin_amdgcn_exp2f(a[0]), __builtin_amdgcn_exp2f(a[1])} + bc2(1.0f);
  return x * f32x2{__builtin_amdgcn_rcpf(e[0]), __builtin_amdgcn_rcpf(e[1])};
}
DI rsrc_t mkrs(const void* ptr, unsigned bytes) { return __builtin_amdgcn_make_buffer_rsrc((void*)ptr, 0, (int)bytes, 0x00020000); }
template <int AUX> DI v4u ld128(rsrc_t rs, unsigned v, unsigned s) { return __builtin_amdgcn_raw_buffer_load_b128(rs, (int)v, (int)s, AUX); }
template <int AUX> DI v2u ld64(rsrc_t rs, unsigned v, unsigned s) { return __builtin_amdgcn_raw_buffer_load_b64(rs, (int)v, (int)s, AUX); }
template <int AUX> DI void st128(v4u x, rsrc_t rs, unsigned v, unsigned s) { __builtin_amdgcn_raw_buffer_store_b128(x, rs, (int)(v + s), 0, AUX); }
template <int AUX> DI void st64(v2u x, rsrc_t rs, unsigned v, unsigned s) { __builtin_amdgcn_raw_buffer_store_b64(x, rs, (int)v, (int)s, AUX); }
DI f32x4 asf(v4u x) { return __builtin_bit_cast(f32x4, x); }
DI float sum4(f32x4 x) { return (x[0] + x[1]) + (x[2] + x[3]); }
DI unsigned umax3(unsigned a, unsigned b, unsigned c) { unsigned m = a > b ? a : b; return m > c ? m : c; }
DI unsigned macc4(unsigned m, v4u x) { return umax3(umax3(m, x[0], x[1]), x[2], x[3]); }
DI unsigned macc(unsigned m, v4u x) { return umax3(m, x[0], x[2]); }
DI bool any_mark(unsigned m) { return __builtin_amdgcn_ballot_w64(m == 0xFFFFFFFFu) != 0ull; }
#define SB() __builtin_amdgcn_sched_barrier(0)
#define PIN(x) asm volatile("" : "+v"(x))
#define PINA(x) asm volatile("" : "+a"(x))
#define USE(x) asm volatile("" ::"v"(x))
#define RETRY_BEGIN(m) if (__builtin_expect(any_mark(m), 0)) { unsigned sp_ = 0; do { MEMFENCE(); m = 0;
#define RETRY_END(m) } while (any_mark(m) & (++sp_ <= SPIN_CAP)); }
#define MEMFENCE() asm volatile("" ::: "memory")
DI void bar() { asm volatile("s_waitcnt lgkmcnt(0)" ::: "memory"); __builtin_amdgcn_s_barrier(); asm volatile("" ::: "memory"); }

DI f32x2 cmul(f32x2 a, f32x2 b) {
  f32x2 t = bc2(a[0]) * b;
  asm("v_pk_fma_f32 %0, %1, %2, %0 op_sel:[1,1,0] op_sel_hi:[1,0,1] neg_lo:[0,1,0]" : "+v"(t) : "v"(a), "v"(b));
  return t;
}
DI f32x2 cexp_p(float are2, float rev_hi, float rev_lo, float p) {
  const float mag = __builtin_amdgcn_exp2f(are2 * p);
  const float hi = rev_hi * p;
  const float fr = (hi - __builtin_rintf(hi)) + rev_lo * p;
  return f32x2{mag * __builtin_amdgcn_cosf(fr), mag * __builtin_amdgcn_sinf(fr)};
}
DI float exp_acc(float x) {
  const float hi = x * 1.4426950408889634f;
  const float lo = __builtin_fmaf(x, 1.4426950408889634f, -hi) + x * 1.925963033500011e-8f;
  const float e = __builtin_amdgcn_exp2f(hi);
  return __builtin_fmaf(e, lo * 0.6931471805599453f, e);
}
DI unsigned pkh(float a, float b) {
  f16x2 v = {(_Float16)a, (_Float16)b};
  return __builtin_bit_cast(unsigned, v);
}

constexpr int CV_RK = 4160;
constexpr int CV_H = 2 * CV_RK;
constexpr int CV_CH = CV_H + 4608;
constexpr int LDS_A = 0;
constexpr int LDS_U = 2 * CV_CH;
constexpr int LDS_GS = LDS_U + 8192;
constexpr int LDS_TAB = 36864;
constexpr int KP_CH = 13840;
constexpr int LDS_KP = LDS_TAB + 2 * 64 * 528 + 16;
constexpr int LDS_LN = 0, LDS_MR = 8448;
constexpr int LDS_WI = 145472, LDS_WO = LDS_WI + 10240;
constexpr int LDS_X = 65536;
constexpr int LDS_S = LDS_X + 24576;
constexpr int LDS_W = LDS_S + 6144;
constexpr int MEGA_LDS = LDS_W + 65536 + 2048;
static_assert(LDS_WO + 4608 <= MEGA_LDS && LDS_KP + 2 * KP_CH <= LDS_WI && LDS_S >= 73728, "lds");
static_assert(LDS_GS + 1280 <= LDS_TAB && LDS_KP + 2 * KP_CH <= MEGA_LDS && MEGA_LDS <= 160 * 1024, "lds");
#define FLAG_AT 48
static_assert(FLAG_AT >= 36 && FLAG_AT <= 56 && FLAG_AT % 4 == 0, "flag position");
#define WF_EARLY 5
#define WO0 12
#define WSP_P0 4
#define HINT4_KS 15
#define ZDEPTH 3
#define GELU_IN 0
#define EPI_W 0
#define MOD_POS 0
#define POLL3 0
#define GELU_PRE 4
#define HINT4_G 314
static_assert(HINT4_G > 4 * 64 + 12, "behind the marker barrier");
#define POLL4_G 48
#define RELABEL 1
#define CKMAP(ck) (RELABEL ? 2 * ((ck) & 3) + ((ck) >> 2) : (ck))
#define POLL_AT 130
#define MOD_MID 1
#define LATE_SLEEP 6
#define SMALL_N 3
#define ZRD 12
#define ZLA 4
#define IN_AUX 0
#define WSP_AUX 0

template <class T> DI T ldsr(unsigned a) { return *(const LAS T*)(size_t)a; }
template <class T> DI void ldsw(unsigned a, T v) { *(LAS T*)(size_t)a = v; }
DI v2u trread(const unsigned char* ptr) {
  typedef short v4i16_t __attribute__((ext_vector_type(4)));
  return __builtin_bit_cast(v2u, __builtin_amdgcn_ds_read_tr16_b64_v4i16((LAS v4i16_t*)ptr));
}
DI v2u trread_a(unsigned a) {
  typedef short v4i16_t __attribute__((ext_vector_type(4)));
  return __builtin_bit_cast(v2u, __builtin_amdgcn_ds_read_tr16_b64_v4i16((LAS v4i16_t*)(size_t)a));
}
struct CvOrder { int qq[36], pp[36]; bool first[36]; };
constexpr CvOrder cv_order() {
  CvOrder o{};
  int n = 0;
  for (int m = 0; m < 8; ++m) {
    for (int j = 0; j < m; ++j) if (m + j < 8) { o.qq[n] = m; o.pp[n] = j; ++n; }
    for (int j = 0; j <= m; ++j) if (m + j < 8) { o.qq[n] = j; o.pp[n] = m; ++n; }
  }
  for (int i = 0; i < 36; ++i) {
    o.first[i] = true;
    for (int j = 0; j < i; ++j) if (o.qq[j] + o.pp[j] == o.qq[i] + o.pp[i]) o.first[i] = false;
  }
  return o;
}
constexpr CvOrder CVO = cv_order();
struct Z2Order { int u[128], kind[128], pos[128]; };
constexpr Z2Order z2_order() {
  Z2Order o{};
  int n = 0;
  for (int u = 0; u < 40; ++u) {
    int pos = 0;
    const bool st = u < 24 && (u < 8 || (u & 7) < 4);
    for (int k = 0; k < 5; ++k)
      if (k < 3 ? u >= 8 : st) { o.u[n] = u; o.kind[n] = k; o.pos[n] = pos++; ++n; }
  }
  return o;
}
constexpr Z2Order Z2O = z2_order();

typedef __attribute__((address_space(1))) unsigned gu32;
DI unsigned tx(unsigned m, unsigned x, unsigned tag) { return __builtin_amdgcn_bitop3_b32(m, x, tag, 0xF6); }
DI unsigned tacc(unsigned m, v4u x, unsigned tag) { return tx(tx(m, x[1], tag), x[3], tag); }
DI bool any_nz(unsigned m) { return __builtin_amdgcn_ballot_w64(m != 0u) != 0ull; }
DI void hint_add(unsigned char* ws, int line) { (void)__hip_atomic_fetch_add((gu32*)(ws + WS_CNT + line * 128), 1u, __ATOMIC_RELAXED, __HIP_MEMORY_SCOPE_AGENT); }
DI void hint_wait(rsrc_t rws, unsigned voff, unsigned soff, unsigned want) {
  unsigned sp = 0;
  for (;;) {
    MEMFENCE();
    const unsigned v = __builtin_amdgcn_raw_buffer_load_b32(rws, (int)voff, (int)soff, SC1);
    if (!((__builtin_amdgcn_ballot_w64(v < want) != 0ull) & (++sp <= SPIN_CAP))) break;
    __builtin_amdgcn_s_sleep(OPT_E);
  }
  MEMFENCE();
}

extern "C" __global__ void __launch_bounds__(256, 1) adaln_mega(Params p) {
  extern __shared__ __attribute__((aligned(16))) unsigned char smem[];
  const int tid = threadIdx.x, lane = tid & 63, w = __builtin_amdgcn_readfirstlane(tid >> 6);
  const int b = blockIdx.x, xcd = b & 7, idx = b >> 3;
  const int pb = xcd * 32 + idx;
  const unsigned l16 = lane * 16;
  STAMP_DECL;
  STAMP(1, 0);
  v2u codepf;
  {
    unsigned long long pc;
    int rel_m, rel_e;
    asm volatile("s_getpc_b64 %0\n\ts_mov_b32 %1, adaln_mega@rel32@lo+4\n\ts_mov_b32 %2, adaln_code_end@rel32@lo+12" : "=s"(pc), "=s"(rel_m), "=s"(rel_e));
    const rsrc_t rcode = mkrs((const void*)(pc + (long long)rel_m), rel_e > rel_m ? (unsigned)(rel_e - rel_m) & ~15u : 0u);
    codepf = ld64<0>(rcode, tid * 8, idx * 2048);
  }
  SB();
  const rsrc_t ru = mkrs(p.u, L * D * 2);
  v4u xr[2];
  xr[0] = ld128<IN_AUX>(ru, l16, (pb * 8 + w * 2) * 1024);
  xr[1] = ld128<IN_AUX>(ru, l16 + 1024, (pb * 8 + w * 2) * 1024);
  const int mkg = b & 3, mcg = b >> 2;
  const unsigned cq = p.c[mkg * 128 + (tid & 3) * 32 + (tid >> 3)];
  const unsigned epoch = *(const unsigned*)(p.ws + WS_EPOCH);
  const rsrc_t rws = mkrs(p.ws, WS_END);
  __builtin_amdgcn_raw_buffer_store_b32(0u, rws, tid < 2 ? tid * 32768u : OOB, WS_CNT + b * 128, SC1);
  SB();
  const int cch = w >> 1, eh = w & 1, ns = tid & 127;
  const unsigned q_cre = p.cre[pb * 256 + tid], q_cim = p.cim[pb * 256 + tid], q_lar = p.lar[pb * 256 + tid], q_aim = p.aim[pb * 256 + tid];
  const unsigned q_ldt = p.ldt[pb * 2 + cch], q_dsk = p.dsk[pb * 2 + cch];
  v4u xm[4];
#define LOAD_XM() { \
  const rsrc_t rmod = mkrs(p.wmod, D * 3072 * 2); \
_Pragma("unroll") \
  for (int i = 0; i < 4; ++i) xm[i] = ld128<IN_AUX>(rmod, (tid >> 3) * 6144 + (tid & 7) * 16, (mkg * 128 + i * 32) * 6144 + mcg * 128 + (b < 192 ? 0u : OOB)); }
  SB();
  LOAD_XM();
  SB();
  STAMP(1, 24);
  const int c = lane & 15, q = lane >> 4;
  const unsigned lds0 = (unsigned)(size_t)(LAS unsigned char*)smem;
  unsigned pos4[9];
  {
    const float f4 = (float)(8188 - tid * 32);
#pragma unroll
    for (int j = 0; j < 10; j += 2) {
      const unsigned pp = pk2(bc2(f4) - f32x2{4.f * j, 4.f * j + 4.f});
      const int p0 = (int)bflo(pp), p1 = (int)bfhi(pp);
      pos4[j] = p0 + ((p0 >> 5) << 2) + lds0 + LDS_KP;
      PIN(pos4[j]);
      if (j + 1 < 9) { pos4[j + 1] = p1 + ((p1 >> 5) << 2) + lds0 + LDS_KP; PIN(pos4[j + 1]); }
    }
  }
  unsigned co[2][4];
  {
    const unsigned b0 = (eh ? 1152 + 576 * q : 144 * (q >> 1) + 4 * (q & 1)) * 4 + 36 * c + lds0 + LDS_KP + cch * KP_CH;
    const unsigned b1 = (q < 2 ? 288 + 144 * q : 568 + 4 * q) * 4 + 36 * c + lds0 + LDS_KP + cch * KP_CH;
    const unsigned s0 = eh ? 576 : 4, s1 = q < 2 ? 8 : 576;
#pragma unroll
    for (int r = 0; r < 4; ++r) { co[0][r] = b0 + r * s0; co[1][r] = b1 + r * s1; PIN(co[0][r]); PIN(co[1][r]); }
  }
  *(unsigned*)(smem + LDS_KP - 16 + cch * KP_CH + (lane & 3) * 4) = 0u;
  unsigned a_pad, a_wi, a_wo, a_tab, gm_off, gc_off;
  a_pad = lds0 + cch * CV_CH + (lane < 32 ? CV_H + l16 : ((lane >> 1) & 1) * CV_RK + 4096 + (lane & 1) * 16);
  a_wi = lds0 + LDS_WI + (w * 32 + 8 * q + ((lane >> 2) & 3)) * 80 + (lane & 3) * 16;
  a_wo = lds0 + LDS_WO + (8 * q + ((lane >> 2) & 3)) * 144 + (w >> 1) * 64 + (lane & 3) * 16 + (w & 1) * 8;
  a_tab = lds0 + LDS_TAB + cch * (64 * 528) + c * 528 + q * 16;
  gm_off = ((4 - cch) * 512 + mkg * 128 + ns) * 8;
  gc_off = (((lane & 7) > 5 ? 0 : ((lane & 7) >> 1)) * 512 + pb * 2 + (lane & 1)) * 8;
  PIN(a_pad); PIN(a_wi); PIN(a_wo); PIN(a_tab); PIN(gm_off); PIN(gc_off);
  unsigned ab[4], bb, dv[4], rdo4[4], xoff;
#pragma unroll
  for (int i = 0; i < 4; ++i) {
    ab[i] = lds0 + cch * CV_CH + (1 - (c & 1)) * CV_RK + 2 * (2030 - 2 * (c >> 1) + 8 * q) - eh * 256 - 192 - 512 - 1024 * i;
    dv[i] = (lane >> 3) * 4096 + (((lane & 7) ^ ((lane >> 4) & 1) ^ (i << 1)) * 16) + i * (32768 - 1024);
    rdo4[i] = lds0 + LDS_W + w * 16384 + ((perm((i & 1) * 2 ^ (2 * (w & 1)), c) * 128 + ((q ^ (c >> 1)) * 16)) ^ ((i >> 1) * 64));
    PIN(ab[i]); PIN(dv[i]); PIN(rdo4[i]);
  }
  bb = lds0 + cch * CV_CH + CV_H + 2 * (256 - 16 + 16 * c + 8 * q) - eh * 256 - 192;
  xoff = WS_X1T + (pb * 2 + cch) * 4096 + eh * 2048 + ((c >> 1) * 32 + ((q & 1) * 4 + 2 * (c & 1) + (q >> 1)) * 4) * 2;
  PIN(bb); PIN(xoff);
  SB();
  STAMP(1, 26);
  {
    *(v4u*)(smem + LDS_LN + (w * 2) * 1040 + l16) = xr[0];
    *(v4u*)(smem + LDS_LN + (w * 2 + 1) * 1040 + l16) = xr[1];
    float sm[2], sq[2];
#pragma unroll
    for (int i = 0; i < 2; ++i) {
      sm[i] = dot2(xr[i][0], ONES, dot2(xr[i][1], ONES, dot2(xr[i][2], ONES, dot2(xr[i][3], ONES, 0.f))));
      sq[i] = dot2(xr[i][0], xr[i][0], dot2(xr[i][1], xr[i][1], dot2(xr[i][2], xr[i][2], dot2(xr[i][3], xr[i][3], 0.f))));
    }
    const float tv = row16_sum(xrow_sum4(sm[0], sm[1], sq[0], sq[1]));
    auto sw = __builtin_amdgcn_permlane32_swap(__float_as_uint(tv), __float_as_uint(tv), false, false);
    const float m = __uint_as_float(sw[0]) * (1.0f / D);
    const float r = __builtin_amdgcn_rsqf(fmaxf(__uint_as_float(sw[1]) * (1.0f / D) - m * m, 0.f) + EPS);
    ((float*)(smem + LDS_GS + 1088))[(q >> 1) * 8 + w * 2 + (q & 1)] = (q >> 1) ? -m * r : r;
  }
  const unsigned tag = epoch * 2654435761u + 0x9E3779B9u;
  STAMP(1, 28);
  asm volatile("s_waitcnt vmcnt(%0)" ::"i"(MOD_POS == 0 ? 8 : 4) : "memory");
  bar();
  STAMP(1, 16);
  {
    unsigned ua[4][2];
#pragma unroll
    for (int i = 0; i < 4; ++i) {
      const unsigned char* src = smem + LDS_LN + (tid & 3) * 2080 + (i * 64 + (tid >> 2)) * 4;
      ua[i][0] = *(const unsigned*)src; ua[i][1] = *(const unsigned*)(src + 1040);
    }
    const unsigned lnv = ((const unsigned*)(smem + LDS_GS + 1088))[c];
    SB();
#pragma unroll
    for (int i = 0; i < 4; ++i) st128<SC1>(v4u{ua[i][0], tag, ua[i][1], tag}, rws, (tid >> 2) * 16384 + (tid & 3) * 16, WS_UG + i * 64 * 16384 + pb * 64);
    st64<SC1>(v2u{lnv, tag}, rws, tid < 16 ? (c >> 3) * 16384 + (c & 7) * 8 : OOB, WS_SR + pb * 64);
  }
  STAMP(1, 17);
  SB();
  const unsigned wp_oob = b < 176 ? 0u : OOB;
  const int wpr = b >> 2, wkg = b & 3;
  v4u xi[2], xo;
  {
    const rsrc_t rwin = mkrs(p.win, D * FF * 2), rwout = mkrs(p.wout, FH * D * 2);
    const int hf = wpr >= 22, pp = wpr - 22 * hf;
    const unsigned v = ((tid >> 2) * (FF * 2) + (tid & 3) * 16) | ((pp == 21 ? tid & 2 : 0) << 29);
#pragma unroll
    for (int i = 0; i < 2; ++i) xi[i] = ld128<IN_AUX>(rwin, v, (wkg * 128 + i * 64) * (FF * 2) + (hf * FH + pp * 32) * 2 + wp_oob);
    xo = ld128<IN_AUX>(rwout, (tid >> 3) * 1024 + (tid & 7) * 16, idx * 32 * 1024 + xcd * 128);
  }
  SB();
  const int d0 = pb * 2, dk = d0 + cch;
  const int mt = (xcd >> 1) * 8 + (idx & 7), nt = (xcd & 1) * 4 + (idx >> 3);
  const int big = (nt + 2) >> 3;
  const int np = 3 - big, pr0 = 3 * nt - big * (nt - 6);
  const int kh = w & 1, uh = w >> 1;
  const int uo0 = pr0 * 2 + uh * (4 - 2 * big), ut0 = pr0 * 2 + (uh ^ 1) * (4 - 2 * big), uo2 = pr0 * 2 + 2 + uh, ut2 = pr0 * 2 + 2 + (uh ^ 1);
#define UO(k) ((k) == 2 ? uo2 : uo0 + (k))
#define UT(k) ((k) == 2 ? ut2 : ut0 + (k))
  const unsigned oob3 = (unsigned)big << 30;
  const int kr = 2 * (idx >> 3) + (xcd & 1) + (OPT_B == 0 ? (idx & 7) : OPT_B == 1 ? 4 * (idx & 1) : OPT_B == 2 ? 2 * (idx & 3) : 0);
  const rsrc_t rwsp = mkrs(p.wsp, L * L * 2);
  const unsigned wp_fr = (wpr * 2 * 16 + wkg * 4 + w) * 1024 + wp_oob;
  v4u wr[1][8];
  const unsigned wsp_v = (lane >> 3) * 4096 + (lane & 7) * 16, wsp_s = mt * 64 * 4096 + w * 1024;
#define LOAD_WSP1(ck, i) wr[ck][i] = ld128<WSP_AUX>(rwsp, wsp_v + (((ck) + kr) & 7) * 128, wsp_s + (i) * 8 * 4096)
#define LOAD_WSPN(n) LOAD_WSP1((n) >> 3, (n) & 7)
  const v4u mk = {~0u, ~0u, ~0u, ~0u};
  unsigned bspq[2];
#pragma unroll
  for (int t = 0; t < 2; ++t) bspq[t] = p.bsp[mt * 64 + perm(2 * kh + t, c)];
  const unsigned tag_s = __builtin_amdgcn_readfirstlane(tag);
  {
    const float dt = rbf(exp_acc(bf2f(q_ldt)));
    const float cre = bf2f(q_cre) * dt, cim = bf2f(q_cim) * dt;
    const float are2 = -rbf(exp_acc(bf2f(q_lar))) * dt * 1.4426950408889634f;
    const float aim = bf2f(q_aim) * dt;
    const float rev_hi = aim * 0.15915494309189535f;
    const float rev_lo = __builtin_fmaf(aim, 0.15915494309189535f, -rev_hi) + aim * 6.4206383e-9f;
    int ex = __builtin_amdgcn_frexp_expf(xrow_max(row16_max(fmaxf(fabsf(cre), fabsf(cim))))) + 1;
    ex = ex < -100 ? -100 : ex;
    const float scale = __builtin_amdgcn_ldexpf(1.f, -ex);
    ((float*)(smem + LDS_GS + 1024))[w] = __builtin_amdgcn_ldexpf(2.f, ex);
    const f32x2 c0 = {cre * scale, cim * scale};
    const f32x2 e1 = cexp_p(are2, rev_hi, rev_lo, 1.f), e8 = cexp_p(are2, rev_hi, rev_lo, 8.f), e64 = cexp_p(are2, rev_hi, rev_lo, 64.f);
    unsigned char* tn = smem + LDS_TAB + cch * (64 * 528) + ns * 4;
#define TROW(r, z) *(unsigned*)(tn + (r) * 528) = pkh((z)[0], -(z)[1])
    SB();
    const f32x2 e128 = cmul(e64, e64), e2 = cmul(e1, e1), e256 = cmul(e128, e128), e4 = cmul(e2, e2), e512 = cmul(e256, e256);
    f32x2 f0 = {1.f, 0.f}, f1 = e64;
    f32x2 ca = cmul(c0, e512), cb = cmul(ca, e512);
#pragma unroll
    for (int j = 0; j < 8; ++j) {
      *(unsigned*)(tn + (48 + j) * 528) = pkh(f0[0], f0[1]);
      *(unsigned*)(tn + (56 + j) * 528) = pkh(f1[0], f1[1]);
      TROW(32 + j, cb);
      f0 = j == 0 ? e8 : cmul(f0, e8); f1 = cmul(f1, e8); cb = cmul(cb, e128);
      if (j < 4) {
        const f32x2 z = cmul(ca, e4);
        TROW(24 + j, ca); TROW(28 + j, z);
        ca = cmul(ca, e128);
      }
      if (j == 3 || j == 7) {
        PIN(f0); PIN(f1); PIN(cb);
        if (WSP_P0 >= 4) { LOAD_WSPN(j >> 2 << 1); LOAD_WSPN((j >> 2 << 1) + 1); }
        SB();
      }
    }
    TROW(40, cb);
    SB();
  {
    const float cv = bf2f(cq);
    const float sl = rbf(cv * __builtin_amdgcn_rcpf(1.0f + __builtin_amdgcn_exp2f(cv * -1.4426950408889634f)));
    const float sc[4] = {dpp<0x00>(sl), dpp<0x55>(sl), dpp<0xAA>(sl), dpp<0xFF>(sl)};
    f32x2 a[4];
#pragma unroll
    for (int i = 0; i < 4; ++i)
#pragma unroll
      for (int j = 0; j < 4; ++j) a[j] = i == 0 ? unpk(xm[i][j]) * bc2(sc[i]) : fma2(unpk(xm[i][j]), bc2(sc[i]), a[j]);
    float* red = (float*)(smem + LDS_MR) + (tid >> 3) * 68 + (tid & 7) * 8;
    *(f32x4*)red = f32x4{a[0][0], a[0][1], a[1][0], a[1][1]};
    *(f32x4*)(red + 4) = f32x4{a[2][0], a[2][1], a[3][0], a[3][1]};
  }
    bar();
    float ms8[8];
#pragma unroll
    for (int i = 0; i < 8; ++i) ms8[i] = ((const float*)(smem + LDS_MR))[(q * 8 + i) * 68 + w * 16 + c];
    SB();
    f32x2 t1[8];
    t1[1] = e1; t1[2] = e2; t1[4] = e4;
    t1[3] = cmul(e2, e1); t1[5] = cmul(e4, e1); t1[6] = cmul(e2, e4); t1[7] = cmul(t1[6], e1);
    const f32x2 c1 = cmul(c0, e128), c2 = cmul(c0, e256), c3 = cmul(c2, e128);
    TROW(0, c0); TROW(8, c1); TROW(16, c2); TROW(20, c3);
    {
    const float s = xrow_sum(((ms8[0] + ms8[1]) + (ms8[2] + ms8[3])) + ((ms8[4] + ms8[5]) + (ms8[6] + ms8[7])));
    st64<SC1>(v2u{__float_as_uint(s), tag}, rws, q == 0 ? c * 8 : OOB, WS_MODG + (mkg * 3072 + mcg * 64 + w * 16) * 8 + (b < 192 ? 0u : OOB));
    }
    SB();
#pragma unroll
    for (int v = 1; v < 8; ++v) {
      const f32x2 z0 = cmul(t1[v], c0), z1 = cmul(t1[v], c1);
      TROW(v, z0); TROW(8 + v, z1);
      if ((v & 1) == 0) {
        const f32x2 z2 = cmul(t1[v], c2), z3 = cmul(t1[v], c3);
        TROW(16 + v / 2, z2); TROW(20 + v / 2, z3);
        if (v < 6 && WSP_P0 >= 8) { LOAD_WSPN(v + 2); LOAD_WSPN(v + 3); }
        SB();
      }
    }
  }
  STAMP(1, 27);
#pragma unroll
  for (int i = 0; i < 2; ++i) *(v4u*)(smem + LDS_WI + (i * 64 + (tid >> 2)) * 80 + (tid & 3) * 16) = xi[i];
  *(v4u*)(smem + LDS_WO + (tid >> 3) * 144 + (tid & 7) * 16) = xo;
  STAMP(1, 29);
  bar();
  STAMP(1, 18);
  SB();
  v4u gr[4], gb_[4];
  v2u gm[4], gc[4];
#define LOAD_GS() _Pragma("unroll") for (int i_ = 0; i_ < 4; ++i_) { gr[i_] = ld128<SC1>(rws, tid * 16, WS_SR + i_ * 4096); gb_[i_] = ld128<SC1>(rws, tid * 16, WS_SB + i_ * 4096); }
#define LOAD_GM() _Pragma("unroll") for (int i_ = 0; i_ < 4; ++i_) { gm[i_] = ld64<SC1>(rws, gm_off, WS_MODG + i_ * 24576); gc[i_] = ld64<SC1>(rws, gc_off, WS_MODG + i_ * 24576); }
  v4u ug[4], wt[2];
#define LOAD_UG() _Pragma("unroll") for (int i_ = 0; i_ < 4; ++i_) ug[i_] = ld128<SC1>(rws, tid * 16, WS_UG + pb * 16384 + i_ * 4096)
  {
    v4u fa[2][8], fb[8];
    const unsigned ta = a_tab + eh * (32 * 528);
#pragma unroll
    for (int ks = 0; ks < 3; ++ks) { fb[ks] = ldsr<v4u>(a_tab + 48 * 528 + ks * 64); fa[0][ks] = ldsr<v4u>(ta + ks * 64); fa[1][ks] = ldsr<v4u>(a_tab + 16 * 528 + ks * 64); }
    SB();
    LOAD_UG();
    SB();
    f32x4 kk[2][2];
    f32x2 us;
    v2u wh_[6];
#pragma unroll
    for (int i = 0; i < 4; ++i) kk[i >> 1][i & 1] = f32x4{0.f, 0.f, 0.f, 0.f};
#pragma unroll
    for (int ks = 0; ks < 8; ++ks) {
      if (ks < 5) { fb[3 + ks] = ldsr<v4u>(a_tab + 48 * 528 + (3 + ks) * 64); fa[0][3 + ks] = ldsr<v4u>(ta + (3 + ks) * 64); fa[1][3 + ks] = ldsr<v4u>(a_tab + 16 * 528 + (3 + ks) * 64); }
      if (ks == 5) { us = *(const f32x2*)(smem + LDS_GS + 1024 + cch * 8); wh_[4] = trread_a(a_wo); wh_[5] = trread_a(a_wo + 576); }
      if (ks == 6) { wh_[0] = trread_a(a_wi); wh_[1] = trread_a(a_wi + 320); wh_[2] = trread_a(a_wi + 8); wh_[3] = trread_a(a_wi + 328); }
      kk[0][ks >> 2] = mfma_h(fa[0][ks], fb[ks], kk[0][ks >> 2]); kk[1][ks >> 2] = mfma_h(fa[1][ks], fb[ks], kk[1][ks >> 2]);
      SB();
    }
    STAMP(1, 37);
    wt[0] = v4u{wh_[0][0], wh_[0][1], wh_[1][0], wh_[1][1]};
    wt[1] = v4u{wh_[2][0], wh_[2][1], wh_[3][0], wh_[3][1]};
    st128<SC1>(v4u{wh_[4][0], wh_[4][1], wh_[5][0], wh_[5][1]}, rws, l16, WS_WOT + ((xcd * 4 + w) * 22 + idx) * 1024 + wp_oob);
    LOAD_GS();
    SB();
#pragma unroll
    for (int i = 0; i < 4; ++i) PINA(kk[i >> 1][i & 1]);
    f32x4 k0 = kk[0][0] * us[0] + kk[0][1] * us[1];
    const f32x4 k1 = kk[1][0] * us[0] + kk[1][1] * us[1];
    k0[0] += ns == 0 ? bf2f(q_dsk) : 0.f;
#pragma unroll
    for (int r = 0; r < 4; ++r) { ldsw<float>(co[0][r], k0[r]); ldsw<float>(co[1][r], k1[r]); }
  }
  STAMP(1, 31);
  bar();
  STAMP(1, 19);
  {
    float v[2][9];
#pragma unroll
    for (int j = 0; j < 9; ++j) v[0][j] = ldsr<float>(pos4[j]);
    LOAD_GM();
#pragma unroll
    for (int j = 0; j < 9; ++j) v[1][j] = ldsr<float>(pos4[j] + KP_CH);
    ldsw<v4u>(a_pad, v4u{0u, 0u, 0u, 0u});
    SB();
    STAMP(1, 36);
#pragma unroll
    for (int ch = 0; ch < 2; ++ch) {
      unsigned char* rk = smem + ch * CV_CH + tid * 16;
#pragma unroll
      for (int sft = 0; sft < 2; ++sft)
        *(v4u*)(rk + sft * CV_RK) = v4u{pk2(v[ch][sft], v[ch][sft + 1]), pk2(v[ch][sft + 2], v[ch][sft + 3]), pk2(v[ch][sft + 4], v[ch][sft + 5]), pk2(v[ch][sft + 6], v[ch][sft + 7])};
    }
#pragma unroll
    for (int i = WSP_P0; i < 8; ++i) LOAD_WSPN(i);
    st128<SC1>(mk, rws, l16, WS_X1T + dk * 4096 + eh * 2048);
    st128<SC1>(mk, rws, l16 + 1024, WS_X1T + dk * 4096 + eh * 2048);
    __builtin_amdgcn_raw_buffer_store_b32(~0u, rws, lane * 4, WS_ST2 + ((mt * 2 + kh) * 16 + nt * 2 + uh) * 256, SC1);
    SB();
    STAMP(1, 32);
    unsigned m = 0;
#pragma unroll
    for (int i = 0; i < 4; ++i) { m = tacc(m, ug[i], tag); m = tacc(m, gr[i], tag); m = tacc(m, gb_[i], tag); }
    if (__builtin_expect(any_nz(m), 0)) {
      unsigned sp = 0;
      do {
        MEMFENCE(); LOAD_UG(); LOAD_GS(); m = 0;
#pragma unroll
        for (int i = 0; i < 4; ++i) { m = tacc(m, ug[i], tag); m = tacc(m, gr[i], tag); m = tacc(m, gb_[i], tag); }
      } while (any_nz(m) & (++sp <= SPIN_CAP));
    }
    f32x2 nn[4][2];
#pragma unroll
    for (int i = 0; i < 4; ++i) {
      nn[i][0] = fma2(unpk(ug[i][0]), bc2(__uint_as_float(gr[i][0])), bc2(__uint_as_float(gb_[i][0])));
      nn[i][1] = fma2(unpk(ug[i][2]), bc2(__uint_as_float(gr[i][2])), bc2(__uint_as_float(gb_[i][2])));
      PIN(nn[i][0]); PIN(nn[i][1]);
      *(unsigned*)(smem + LDS_U + i * 1024 + tid * 4) = __builtin_amdgcn_perm(ug[i][2], ug[i][0], 0x05040100u);
      *(unsigned*)(smem + LDS_U + 4096 + i * 1024 + tid * 4) = __builtin_amdgcn_perm(ug[i][2], ug[i][0], 0x07060302u);
    }
    SB();
    STAMP(1, 33);
    m = 0;
#pragma unroll
    for (int i = 0; i < 4; ++i) m = tx(tx(m, gm[i][1], tag), gc[i][1], tag);
    if (__builtin_expect(any_nz(m), 0)) {
      unsigned sp = 0;
      do {
        __builtin_amdgcn_s_sleep(3);
        MEMFENCE(); LOAD_GM(); m = 0;
#pragma unroll
        for (int i = 0; i < 4; ++i) m = tx(tx(m, gm[i][1], tag), gc[i][1], tag);
      } while (any_nz(m) & (++sp <= SPIN_CAP));
    }
    const float mv = rbf((__uint_as_float(gm[0][0]) + __uint_as_float(gm[1][0])) + (__uint_as_float(gm[2][0]) + __uint_as_float(gm[3][0])));
    *(u16*)(smem + LDS_GS + tid * 2) = (u16)pk2(mv + (cch ? 0.f : 1.0f), 0.f);
    const float cv = rbf((__uint_as_float(gc[0][0]) + __uint_as_float(gc[1][0])) + (__uint_as_float(gc[2][0]) + __uint_as_float(gc[3][0])));
    const float gav = dpp<0x104>(cv), gav2 = dpp<0x102>(cv);
    const float hbv = cv * gav, hsv = rbf(1.0f + cv) * gav2;
    const f32x2 hb = {__builtin_bit_cast(float, __builtin_amdgcn_readlane(__builtin_bit_cast(int, hbv), 0)), __builtin_bit_cast(float, __builtin_amdgcn_readlane(__builtin_bit_cast(int, hbv), 1))};
    const f32x2 hs = {__builtin_bit_cast(float, __builtin_amdgcn_readlane(__builtin_bit_cast(int, hsv), 2)), __builtin_bit_cast(float, __builtin_amdgcn_readlane(__builtin_bit_cast(int, hsv), 3))};
    SB();
    bspq[0] |= bspq[1] << 16;
    PIN(bspq[0]);
    STAMP(1, 38);
#pragma unroll
    for (int i = 0; i < 4; ++i) {
      const f32x2 h0 = fma2(nn[i][0], hs, hb), h1 = fma2(nn[i][1], hs, hb);
      *(unsigned*)(smem + CV_H + 512 + i * 1024 + tid * 4) = pk2(h0[0], h1[0]);
      *(unsigned*)(smem + CV_CH + CV_H + 512 + i * 1024 + tid * 4) = pk2(h0[1], h1[1]);
    }
  }
  asm volatile("" ::"v"(codepf));
  STAMP(1, 39);
  bar();
  STAMP(1, 1);
#define CV_A(ee, k) ({ const unsigned a_ = ab[(k) >> 1] + 192 - 64 * (ee) + 512 * (1 - ((k) & 1)); v4u{ldsr<unsigned>(a_), ldsr<unsigned>(a_ + 4), ldsr<unsigned>(a_ + 8), ldsr<unsigned>(a_ + 12)}; })
#define CV_B(ee, k) ldsr<v4u>(bb + 192 - 64 * (ee) + 512 * (k))
  const unsigned slotA = LDS_W + w * 16384, slotB = slotA + 8192;
  v4u wq[8][8];
#define READ_W(dl, f) ldsr<v4u>(rdo4[(((f) >> 1) & 1) + ((f) >> 2) * 2] + (dl) + ((f) & 1) * 512)
  unsigned dso;
#define DMA_W(ck, slot, i) do { \
    if (((i) & 3) == 0) asm volatile("s_add_u32 m0, %1, %2\n\ts_add_u32 %0, %3, %4\n\tbuffer_load_dwordx4 %5, %6, %0 offen lds" \
                 : "=&s"(dso) : "s"(lds0 + (slot)), "i"((i) * 1024), "s"(wsp_s + ((CKMAP(ck) + kr) & 7) * 128), "i"((i) * 32768), "v"(dv[0]), "s"(rwsp) : "scc", "memory"); \
    else asm volatile("buffer_load_dwordx4 %0, %1, %2 offen offset:%3 lds" :: "v"(dv[(i) & 3]), "s"(rwsp), "s"(dso), "i"(((i) & 3) * 1024) : "memory"); } while (0)
  auto dma_sel = [&](auto CK, unsigned slot, int i) __attribute__((always_inline)) {
    constexpr int ck = decltype(CK)::value;
    switch (i) {
      case 0: DMA_W(ck, slot, 0); break; case 1: DMA_W(ck, slot, 1); break; case 2: DMA_W(ck, slot, 2); break; case 3: DMA_W(ck, slot, 3); break;
      case 4: DMA_W(ck, slot, 4); break; case 5: DMA_W(ck, slot, 5); break; case 6: DMA_W(ck, slot, 6); break; default: DMA_W(ck, slot, 7); break;
    }
  };
  v4u cA[2][8], cB[2][8];
  v4u ones4 = {ONES, ONES, ONES, ONES};
  PIN(ones4);
  f32x4 cb4[4];
  {
    const v4u gbf = *(const v4u*)(smem + LDS_GS + w * 64 + q * 16), sbf = *(const v4u*)(smem + LDS_GS + 256 + w * 64 + q * 16);
#pragma unroll
    for (int k = 0; k < 4; ++k) { cA[0][k] = CV_A(0, k); cB[0][k] = CV_B(0, k); }
    SB();
    const f32x4 z4 = {0.f, 0.f, 0.f, 0.f};
#pragma unroll
    for (int e = 0; e < 2; ++e) {
      v4u o;
#pragma unroll
      for (int jj = 0; jj < 4; ++jj) o[jj] = pk2(unpk(wt[e][jj]) * unpk(gbf[jj]));
      st128<SC1>(o, rws, l16 + e * 16384, WS_WP + wp_fr);
      cb4[2 * e + 1] = mfma(sbf, wt[e], z4);
      cb4[2 * e] = mfma(ones4, o, z4);
    }
  }
  SB();
  f32x4 cacc[8];
  v2u ue[4];
  float uf[16];
  unsigned flagv;
  unsigned wro[4], a_cp, a_ue, o_csb, o_flag;
  gu32* p_flag = (gu32*)(p.ws + WS_CNT + (256 + (tid & 7) * 16 + (idx & 15)) * 128);
  {
    f32x2 cp[4];
    float s0, s1, s2, s3;
    sfor<144>([&](auto G) __attribute__((always_inline)) {
      constexpr int g = decltype(G)::value, ee = g / 36, j = g % 36, t = CVO.pp[j] + CVO.qq[j];
      constexpr bool first = ee == 0 && CVO.first[j];
      cacc[t] = mfma(cA[ee & 1][CVO.qq[j]], cB[ee & 1][CVO.pp[j]], first ? f32x4{0.f, 0.f, 0.f, 0.f} : cacc[t]);
      SB();
      if constexpr (g < 8) {
        if constexpr (g & 1) cB[0][4 + (g >> 1)] = CV_B(0, 4 + (g >> 1));
        else cA[0][4 + (g >> 1)] = CV_A(0, 4 + (g >> 1));
      }
      constexpr int jn = ee == 0 ? (j >= 12 && (j - 12) % 3 != 2 ? (j - 12) / 3 * 2 + (j - 12) % 3 : -1) : (j & 1) ? j >> 1 : -1;
      if constexpr (ee < 3 && jn >= 0 && jn < 16) {
        if constexpr (jn & 1) cB[(ee + 1) & 1][jn >> 1] = CV_B(ee + 1, jn >> 1);
        else cA[(ee + 1) & 1][jn >> 1] = CV_A(ee + 1, jn >> 1);
      }
      if constexpr (g == 8) { a_cp = lds0 + LDS_S + 4096 + c * 16; PIN(a_cp); }
      if constexpr (g == 9) { cb4[0][1] = cb4[1][0]; cb4[0][2] = cb4[2][0]; PINA(cb4[0]); }
      if constexpr (g == 10) { cb4[0][3] = cb4[3][0]; PINA(cb4[0]); }
      if constexpr (g == 11) ldsw<f32x4>(a_cp + w * 256, cb4[0]);
      if constexpr (g == 17) { a_cp += (q & 1) * 8; PIN(a_cp); }
      if constexpr (g == 20) { o_csb = q * 512 + c * 8; PIN(o_csb); }
      if constexpr (g == 23) { o_csb = tid < 32 ? o_csb : OOB; PIN(o_csb); }
      if constexpr (g == 26) { PIN(p_flag); }
      if constexpr (g == FLAG_AT) {
        STAMP(1, 42);
        asm volatile("s_waitcnt vmcnt(%0) lgkmcnt(8)" ::"i"(FLAG_AT > 38 ? 4 + (FLAG_AT - 38 + 3) / 4 : 4) : "memory");
        __builtin_amdgcn_s_barrier();
        if (tid < 8) (void)__hip_atomic_fetch_add(p_flag, 1u, __ATOMIC_RELAXED, __HIP_MEMORY_SCOPE_AGENT);
        STAMP(1, 43);
      }
      if constexpr (g == FLAG_AT + 4 || g == FLAG_AT + 8) {
        cp[(g - FLAG_AT - 4) / 2] = ldsr<f32x2>(a_cp + (g - FLAG_AT - 4) * 128);
        cp[(g - FLAG_AT - 4) / 2 + 1] = ldsr<f32x2>(a_cp + (g - FLAG_AT - 4) * 128 + 256);
      }
      if constexpr (g == FLAG_AT + 16) { s0 = cp[0][0] + cp[1][0]; PIN(s0); s2 = cp[0][1] + cp[1][1]; PIN(s2); }
      if constexpr (g == FLAG_AT + 20) { s1 = cp[2][0] + cp[3][0]; PIN(s1); s3 = cp[2][1] + cp[3][1]; PIN(s3); }
      if constexpr (g == FLAG_AT + 24) { s0 += s1; PIN(s0); s2 += s3; PIN(s2); }
      if constexpr (g == FLAG_AT + 28) st64<SC1>(v2u{__float_as_uint(s0), __float_as_uint(s2)}, rws, o_csb, WS_CSB + wpr * 1024 + wkg * 128 + wp_oob);
      if constexpr (g == 36) STAMP(1, 40);
      if constexpr (g == 72) STAMP(1, 41);
      if constexpr (g == 14 || g == 29 || g == 32 || g == 35) DMA_W(1, slotA, g == 14 ? 0 : (g - 26) / 3);
      if constexpr (g >= 38 && g < 86 && (g & 3) == 2) DMA_W(1 + (g + 10) / 64, g < 54 ? slotA : slotB, (4 + (g - 38) / 4) & 7);
      if constexpr (g == 88) asm volatile("s_waitcnt vmcnt(8)" ::: "memory");
      if constexpr (g >= 90 && g < 106 && (g & 1) == 0) wq[1][(g - 90) >> 1] = READ_W(0, (g - 90) >> 1);
      if constexpr (g >= 68 && g < 72) { wro[g - 68] = (lds0 + slotA + (lane >> 3) * 128 + (((lane & 7) ^ ((lane >> 4) & 1)) * 16)) ^ ((g - 68) * 32); PIN(wro[g - 68]); }
      if constexpr (g == 105) { a_ue = lds0 + LDS_U + cch * 4096 + eh * 2048 + c * 32 + q * 8; PIN(a_ue); }
      if constexpr (g == 106 || g == 107) { ue[(g - 106) * 2] = ldsr<v2u>(a_ue + (g - 106) * 1024); ue[(g - 106) * 2 + 1] = ldsr<v2u>(a_ue + (g - 106) * 1024 + 512); }
      if constexpr (g >= 108 && g < 140 && (g & 3) == 0) ldsw<v4u>(wro[((g - 108) >> 2) & 3] + ((g - 108) >> 2) * 1024, wr[0][(g - 108) >> 2]);
      if constexpr (g >= 109 && g < 125 && (g & 1) == 1) {
        constexpr int x = (g - 109) >> 1;
        if constexpr (x >= 2) { uf[2 * (x - 2)] = bflo(ue[(x - 2) >> 1][x & 1]); PIN(uf[2 * (x - 2)]); uf[2 * (x - 2) + 1] = bfhi(ue[(x - 2) >> 1][x & 1]); PIN(uf[2 * (x - 2) + 1]); }
      }
      if constexpr (g == 118 || g == 122) { constexpr int x = 6 + (g == 122); uf[2 * x] = bflo(ue[3][x & 1]); PIN(uf[2 * x]); uf[2 * x + 1] = bfhi(ue[3][x & 1]); PIN(uf[2 * x + 1]); }
      if constexpr (g == 110) { o_flag = w == 0 ? c * 128 : OOB; PIN(o_flag); }
      if constexpr (g == 126) asm volatile("s_waitcnt vmcnt(0)" ::: "memory");
      if constexpr (g >= 127 && g < 143 && (g & 1) == 1) wq[2][(g - 127) >> 1] = READ_W(8192, (g - 127) >> 1);
      if constexpr (g == POLL_AT) flagv = __builtin_amdgcn_raw_buffer_load_b32(rws, o_flag, WS_CNT + (256 + xcd * 16) * 128, SC1);
      SB();
    });
  }
  STAMP(1, 2);
  unsigned late;
  {
    f32x4* red = (f32x4*)(smem + LDS_X) + w * 256 + lane;
    f32x4 keep[4];
    if (eh) {
#pragma unroll
      for (int t = 0; t < 4; ++t) red[t * 64] = cacc[t];
      SB();
#pragma unroll
      for (int t = 0; t < 4; ++t) { PINA(cacc[4 + t]); keep[t] = cacc[4 + t]; PIN(keep[t]); }
    } else {
#pragma unroll
      for (int t = 0; t < 4; ++t) red[t * 64] = cacc[4 + t];
      SB();
#pragma unroll
      for (int t = 0; t < 4; ++t) { PINA(cacc[t]); keep[t] = cacc[t]; PIN(keep[t]); }
    }
    f32x2 pre[4][2];
#pragma unroll
    for (int t = 0; t < 4; ++t) {
      pre[t][0] = f32x2{uf[4 * t], uf[4 * t + 1]} + lo2(keep[t]);
      pre[t][1] = f32x2{uf[4 * t + 2], uf[4 * t + 3]} + hi2(keep[t]);
      PIN(pre[t][0]); PIN(pre[t][1]);
    }
    if (w == 0) *(unsigned*)(smem + LDS_S) = __builtin_amdgcn_ballot_w64(flagv < 16u) != 0ull;
    bar();
    late = *(const unsigned*)(smem + LDS_S);
    const f32x4* rd = (const f32x4*)(smem + LDS_X) + (w ^ 1) * 256 + lane;
    f32x4 oth[4];
#pragma unroll
    for (int t = 0; t < 4; ++t) oth[t] = rd[t * 64];
#pragma unroll
    for (int t = 0; t < 4; ++t) {
      v2u o;
      o[0] = nomark(pk2(pre[t][0] + lo2(oth[t])));
      o[1] = pk2(pre[t][1] + hi2(oth[t]));
      st64<SC1>(o, rws, xoff + t * 512, 0);
    }
  }
  STAMP(1, 3);
  SB();
  v4u wf[3][8];
  const unsigned rot = uh * 4;
#define LOAD_WP(half, k, i) ld128<SC1>(rws, l16 + ((i) & 3) * 1024, WS_WP + (((half) * 44 + UO(k)) * 16 + kh * 8 + (((i) & 4) ^ rot)) * 1024 + ((k) == 2 ? oob3 : 0u))
#define LOAD_XS(i) ld128<SC1>(rws, wsp_v, WS_X1T + ((i) * 32 + w * 8) * 4096 + mt * 128)
  v4u xs[16];
  unsigned tb[2][4], xdst;
  SB();
  int x1o;
  unsigned z2o[3], dgo[4], rdo[2], xw, xrd, a_fin, a_fr;
  v4u mkv = mk;
  v2u cz[2][3];
  f32x2 csb[2][3];
  unsigned gq[4];
#define LOAD_CZ(hf, kc) ld64<SC1>(rws, lane * 8, WS_CSB + ((hf) * 44 + UO(kc)) * 512 + ((kc) == 2 ? oob3 : 0u))
  {
    STAMP(1, 20);
    if (__builtin_amdgcn_readfirstlane(late)) {
      if (w == 0) { hint_wait(rws, c * 128, WS_CNT + (256 + xcd * 16) * 128, 16u); __builtin_amdgcn_s_sleep(LATE_SLEEP); }
      __builtin_amdgcn_s_barrier();
    }
    MEMFENCE();
    STAMP(1, 21);
#pragma unroll
    for (int i = 0; i < 3; ++i)
#pragma unroll
      for (int k = 0; k < 3; ++k) wf[k][i] = LOAD_WP(1, k, i);
    SB();
#pragma unroll
  for (int f = 0; f < 8; ++f) wq[0][f] = READ_W(0, f);
#pragma unroll
  for (int f = 0; f < 24; ++f) PINA(wq[f >> 3][f & 7]);
    int lz = lane;
    PIN(lz);
  {
    const int row = 8 * (lz >> 4) + ((lz >> 2) & 3);
    const int sg = ((row >> 1) & 1) | (((row >> 3) & 1) << 1);
#pragma unroll
    for (int tu = 0; tu < 4; ++tu) {
      const unsigned t0 = lds0 + LDS_A + kh * 32768 + row * 128 + ((tu ^ (2 * kh) ^ sg) * 32) + (lz & 3) * 8;
      tb[0][tu] = t0 + uh * 16384;
      tb[1][tu] = t0 + (uh ^ 1) * 16384;
    }
    const int rl = w * 8 + (lz >> 3), chn = lz & 7;
    const int sg2 = ((rl >> 1) & 1) | (((rl >> 3) & 1) << 1);
    xdst = lds0 + LDS_A + rl * 128 + (((chn >> 1) ^ sg2) * 32) + (chn & 1) * 16;
  }
  {
    const int row = 8 * (lz >> 4) + ((lz >> 2) & 3);
    const int sg = ((row >> 1) & 1) | (((row >> 3) & 1) << 1);
    x1o = LDS_A + nt * 8192 + row * 128 + ((w ^ sg) * 32) + (lz & 3) * 8;
  }
#pragma unroll
  for (int k = 0; k < 3; ++k) { z2o[k] = lz * 16 + WS_Z2 + (UO(k) * 64 + mt * 2 + kh) * 1024 + (k == 2 ? oob3 : 0u); }
#pragma unroll
    for (int tu = 0; tu < 8; ++tu) PIN(tb[tu >> 2][tu & 3]);
    PIN(xdst); PIN(x1o);
    {
      const int c2 = lz & 15;
      const bool dg = (c2 >> 2) == (lz >> 4);
      const unsigned dump = lds0 + LDS_W + 65536 + lz * 16;
      dgo[0] = dg ? lds0 + LDS_S + (w * 64 + kh * 32 + c2) * 4 : dump; dgo[1] = dg ? lds0 + LDS_S + (w * 64 + (kh ^ 1) * 32 + c2) * 4 : dump;
      dgo[2] = dg ? lds0 + LDS_S + 2048 + (w * 64 + kh * 32 + c2) * 16 : dump; dgo[3] = dg ? lds0 + LDS_S + 2048 + (w * 64 + (kh ^ 1) * 32 + c2) * 16 : dump;
      rdo[0] = lds0 + LDS_S + (kh * 32 + (lz & 31)) * 4; rdo[1] = lds0 + LDS_S + 2048 + (kh * 32 + (lz & 31)) * 16 + (lz & 3) * 4;
      xw = lds0 + LDS_X + w * 6144 + lz * 16; xrd = lds0 + LDS_X + (w ^ 1) * 6144 + lz * 16;
      a_fin = lds0 + LDS_S + 1024 + w * 256 + lz * 4; a_fr = lds0 + LDS_S + 1024 + w * 256 + (lz >> 4) * 16;
      PIN(dgo[0]); PIN(dgo[1]); PIN(dgo[2]); PIN(dgo[3]); PIN(rdo[0]); PIN(rdo[1]); PIN(xw); PIN(xrd); PIN(a_fin); PIN(a_fr); PIN(mkv);
    }
#pragma unroll
    for (int k = 0; k < 3; ++k) PIN(z2o[k]);
    SB();
#pragma unroll
    for (int i = 3; i < WF_EARLY; ++i)
#pragma unroll
      for (int k = 0; k < 3; ++k) wf[k][i] = LOAD_WP(1, k, i);
    SB();
#pragma unroll
    for (int i = 0; i < 16; ++i) xs[i] = LOAD_XS(i);
    SB();
    STAMP(1, 44);
    unsigned m = 0;
#pragma unroll
    for (int i = 0; i < 16; ++i) {
      m = macc(m, xs[i]); ldsw<v4u>(xdst + i * 4096, xs[i]);
      if (WF_EARLY * 3 + i < 24) wf[(WF_EARLY * 3 + i) % 3][(WF_EARLY * 3 + i) / 3] = LOAD_WP(1, (WF_EARLY * 3 + i) % 3, (WF_EARLY * 3 + i) / 3);
      SB();
    }
    PIN(m);
    STAMP(1, 45);
    RETRY_BEGIN(m)
#pragma unroll
      for (int i = 0; i < 16; ++i) xs[i] = LOAD_XS(i);
      SB();
#pragma unroll
      for (int i = 0; i < 16; ++i) { m = macc(m, xs[i]); ldsw<v4u>(xdst + i * 4096, xs[i]); }
    RETRY_END(m)
  }
  STAMP(1, 4);
  bar();
  STAMP(1, 5);
#define AHALF(tu, i, h) trread_a(tb[(i) >> 2][tu] + ((i) & 3) * 4096 + (h) * 512)
  f32x4 acc[3][4];
  v4u wg[3][8];
  v2u ah[2][4][2];
  float* st = (float*)(smem + LDS_S);
  float* fin = st + 256 + w * 64;
  f32x4* xch = (f32x4*)(smem + LDS_X);
  f32x4 r4[2], m4[2];
  {
    f32x4 s1[4], s2[4];
    constexpr int LA = ZLA;
    v2u ar[LA + 1][2];
    float pa[4], pb[4];
#define UTILE(u) ((u) < 8 ? (u) >> 2 : (((u) >> 3) + 1) & 3)
#define UKS(u) ((u) < 8 ? (u) & 3 : (u) & 7)
#pragma unroll
    for (int u = 0; u < LA; ++u) { ar[u][0] = AHALF(UTILE(u), UKS(u), 0); ar[u][1] = AHALF(UTILE(u), UKS(u), 1); }
#pragma unroll
    for (int i = 0; i < 6; ++i) cz[i / 3][i % 3] = LOAD_CZ(i / 3, i % 3);
#pragma unroll
    for (int i = 0; i < 4; ++i) gq[i] = __builtin_amdgcn_raw_buffer_load_b32(rws, lane * 8, WS_MODG + (i * 3072 + 5 * 512 + nt * 64) * 8, SC1);
    DMA_W(3, slotB, 0);
    SB();
    sfor<128>([&](auto G) __attribute__((always_inline)) {
      constexpr int m = decltype(G)::value, u = Z2O.u[m], k = Z2O.kind[m], pos = Z2O.pos[m], tu = UTILE(u), i = UKS(u);
      const v4u a = {ar[u % (LA + 1)][0][0], ar[u % (LA + 1)][0][1], ar[u % (LA + 1)][1][0], ar[u % (LA + 1)][1][1]};
      const f32x4 z4 = {0.f, 0.f, 0.f, 0.f};
      if constexpr (k < 3) acc[k][tu] = mfma(a, wf[k][i], (i == 0 && tu >= 2) ? z4 : acc[k][tu]);
      if constexpr (k == 3) s1[tu] = mfma(ones4, a, i == 0 ? z4 : s1[tu]);
      if constexpr (k == 4) s2[tu] = mfma(a, a, i == 0 ? z4 : s2[tu]);
      SB();
      if constexpr (pos < 2 && u + LA < 40) ar[(u + LA) % (LA + 1)][pos] = AHALF(UTILE(u + LA), UKS(u + LA), pos);
      if constexpr (pos < 2 && u >= 36) ah[0][u - 36][pos] = AHALF(u - 36, 0, pos);
      if constexpr (u < 8 && pos == 0 && u != 4) { if constexpr (u > 0) DMA_W(3, slotB, u); }
      if constexpr (u == 4 && pos == 0) DMA_W(3, slotB, 4);
      if constexpr (u >= 12 && u < 15 && pos == 0) st128<SC1>(mkv, rws, z2o[u - 12], 0);
      if constexpr (m == 23 || m == 28 || m == 55 || m == 70) {
        constexpr int x = m == 23 ? 0 : m == 28 ? 1 : m == 55 ? 2 : 3;
        PINA(s1[x]);
        ldsw<float>(dgo[x >> 1] + (x & 1) * 64, s1[x][0]);
        if constexpr (x < 3) ldsw<f32x4>(dgo[2 + (x >> 1)] + (x & 1) * 256, s2[x]);
      }
      if constexpr (m == 71) ldsw<f32x4>(dgo[3] + 256, s2[3]);
      if constexpr (m == 35) __builtin_amdgcn_s_waitcnt(0x0F70);
      if constexpr (m == 46 || m == 51 || m == 56 || m == 61) {
        constexpr int x = (m - 46) / 5 * 2;
        wq[3][x] = READ_W(8192, x); wq[3][x + 1] = READ_W(8192, x + 1);
      }
      if constexpr (m == 50 || m == 60 || m == 65) ldsw<f32x4>(xw + (m == 50 ? 0 : m == 60 ? 1 : 2) * 2048, acc[m == 50 ? 0 : m == 60 ? 1 : 2][2]);
      if constexpr (m == 38 || m == 41 || m == 44 || m == 47) DMA_W(4, slotA, (m - 38) / 3);
      if constexpr (m == 66) DMA_W(4, slotA, 4);
      if constexpr (m == 68 || m == 72 || m == 78) DMA_W(4, slotA, m == 68 ? 5 : m == 72 ? 6 : 7);
      if constexpr (m == 73 || m == 94) bar();
      if constexpr (m >= 74 && m < 77) acc[m - 74][0] = ldsr<f32x4>(xrd + (m - 74) * 2048);
      if constexpr (m == 82 || m == 85 || m == 88) ldsw<f32x4>(xw + (m - 82) / 3 * 2048 + 1024, acc[(m - 82) / 3][3]);
      if constexpr (m >= 95 && m < 98) acc[m - 95][1] = ldsr<f32x4>(xrd + (m - 95) * 2048 + 1024);
      if constexpr (m == 77) {
#pragma unroll
        for (int f = 0; f < 8; ++f) PINA(wq[3][f]);
      }
      if constexpr (m >= 83 && m < 93 && (m - 83) % 3 != 2) {
        constexpr int x = (m - 83) / 3 * 2 + (m - 83) % 3;
        if constexpr (x < 4) pa[x] = ldsr<float>(rdo[0] + x * 256);
        else pb[x - 4] = ldsr<float>(rdo[1] + (x - 4) * 1024);
      }
      if constexpr (m == 93) pb[3] = ldsr<float>(rdo[1] + 3072);
      if constexpr (m == 98) { pa[0] += pa[1]; PIN(pa[0]); }
      if constexpr (m == 99) { pa[2] += pa[3]; PIN(pa[2]); }
      if constexpr (m == 101) { pb[0] += pb[1]; PIN(pb[0]); }
      if constexpr (m == 102) { pb[2] += pb[3]; PIN(pb[2]); }
      if constexpr (m == 104) { pa[0] += pa[2]; PIN(pa[0]); }
      if constexpr (m == 105) { pb[0] += pb[2]; PIN(pb[0]); }
      if constexpr (m == 107) { pa[0] *= (1.0f / D); PIN(pa[0]); }
      if constexpr (m == 108) { pa[1] = pa[0] * pa[0]; PIN(pa[1]); }
      if constexpr (m == 110) { pb[0] = __builtin_fmaf(pb[0], 1.0f / D, -pa[1]); PIN(pb[0]); }
      if constexpr (m == 111) { pb[0] = __builtin_amdgcn_fmed3f(pb[0], 0.f, 3.0e38f); PIN(pb[0]); }
      if constexpr (m == 113) { pb[0] += EPS; PIN(pb[0]); }
      if constexpr (m == 114) { pb[0] = __builtin_amdgcn_rsqf(pb[0]); PIN(pb[0]); }
      if constexpr (m == 116) { pa[0] = lane < 32 ? 1.0f : -pa[0]; PIN(pa[0]); }
      if constexpr (m == 117) { pb[0] *= pa[0]; PIN(pb[0]); }
      if constexpr (m == 119) ldsw<float>(a_fin, pb[0]);
      if constexpr (m == 120 || m == 122 || m == 123 || m == 125) {
        constexpr int x = m == 120 ? 0 : m == 122 ? 1 : m == 123 ? 2 : 3;
        if constexpr (x & 1) r4[x >> 1] = ldsr<f32x4>(a_fr + (x >> 1) * 64);
        else m4[x >> 1] = ldsr<f32x4>(a_fr + 128 + (x >> 1) * 64);
      }
      if constexpr (m >= 100 && m < 124 && (m - 100) % 3 == 0) DMA_W(5, slotB, (m - 100) / 3);
      if constexpr (m == 47) STAMP(1, 46);
      if constexpr (m == 79) STAMP(1, 47);
      SB();
    });
  }
#pragma unroll
  for (int x = 0; x < 3; ++x) {
    const float y = xrow_sum4(__uint_as_float(cz[1][x][0]), __uint_as_float(cz[1][x][1]), __uint_as_float(cz[1][x][0]), __uint_as_float(cz[1][x][1]));
    auto sw = __builtin_amdgcn_permlane16_swap(__float_as_uint(y), __float_as_uint(y), false, false);
    csb[1][x] = f32x2{__uint_as_float(sw[0]), __uint_as_float(sw[1])};
  }
#pragma unroll
  for (int k = 0; k < 6; ++k) PINA(acc[k >> 1][k & 1]);
  STAMP(1, 6);
  f32x4 z2v[3][2];
  {
    f32x4 rs[2], rq[2];
#pragma unroll
    for (int t = 0; t < 2; ++t) {
#pragma unroll
      for (int k = 0; k < 3; ++k) {
        const f32x4 a = acc[k][t];
        const f32x2 zl = gelu2(fma2(lo2(a), lo2(r4[t]), fma2(lo2(m4[t]), bc2(csb[1][k][0]), bc2(csb[1][k][1]))));
        const f32x2 zh = gelu2(fma2(hi2(a), hi2(r4[t]), fma2(hi2(m4[t]), bc2(csb[1][k][0]), bc2(csb[1][k][1]))));
        z2v[k][t] = f32x4{zl[0], zl[1], zh[0], zh[1]};
        if (k == 0) { rs[t] = z2v[k][t]; rq[t] = z2v[k][t] * z2v[k][t]; }
        else { rs[t] += z2v[k][t]; rq[t] += z2v[k][t] * z2v[k][t]; }
        if (((t * 3 + k) & 1) == 0) {
#pragma unroll
          for (int f = (t * 3 + k) * 4; f < (t * 3 + k) * 4 + 8; ++f) wg[f % 3][f / 3] = LOAD_WP(0, f % 3, f / 3);
        } else {
          PIN(z2v[k][t]);
          SB();
        }
      }
    }
    STAMP(1, 50);
    float v8[8], v4[4], v2[2];
    const bool b3 = c & 8, b2 = c & 4, b1 = c & 2, b0 = c & 1;
#pragma unroll
    for (int r_ = 0; r_ < 4; ++r_) {
      const float a0 = rs[0][r_] + dpp<0x128>(rs[0][r_]), a1 = rs[1][r_] + dpp<0x128>(rs[1][r_]);
      const float q0 = rq[0][r_] + dpp<0x128>(rq[0][r_]), q1 = rq[1][r_] + dpp<0x128>(rq[1][r_]);
      v8[2 * r_] = b3 ? a1 : a0; v8[2 * r_ + 1] = b3 ? q1 : q0;
    }
#pragma unroll
    for (int j = 0; j < 4; ++j) {
      const float x0 = v8[j] + dpp<0x141>(v8[j]), x1 = v8[4 + j] + dpp<0x141>(v8[4 + j]);
      v4[j] = b2 ? x1 : x0;
    }
#pragma unroll
    for (int j = 0; j < 2; ++j) {
      const float x0 = v4[j] + dpp<0x4E>(v4[j]), x1 = v4[2 + j] + dpp<0x4E>(v4[2 + j]);
      v2[j] = b1 ? x1 : x0;
    }
    const float x0 = v2[0] + dpp<0xB1>(v2[0]), x1 = v2[1] + dpp<0xB1>(v2[1]);
    const float outv = b0 ? x1 : x0;
    __builtin_amdgcn_raw_buffer_store_b32(nomark(__float_as_uint(outv)), rws, lane * 4, WS_ST2 + ((mt * 2 + kh) * 16 + nt * 2 + uh) * 256, SC1);
  }
  STAMP(1, 7);
  SB();
#pragma unroll
  for (int tu = 0; tu < 4; ++tu)
#pragma unroll
    for (int k = 0; k < 3; ++k) acc[k][tu] = f32x4{0.f, 0.f, 0.f, 0.f};
  unsigned k3v[6];
  {
    const unsigned k0 = l16 + w * 6144, w3 = (w + 1) >> 2;
    k3v[0] = k0; k3v[1] = k0 + 2048 - w3 * 1024; k3v[2] = k3v[1] + 1024; k3v[3] = k0 + 5120 - w3 * 2048;
    k3v[4] = k0 + 1024 + (w3 << 30); k3v[5] = k3v[4] + 3072;
  }
  v2u x1h[2][2];
  v4u wo[6][4];
#define LOAD_WO(f) wo[(f) >> 2][(f) & 3] = ld128<SC1>(rws, k3v[(f) >> 2], WS_WOT + (nt * 4 + ((f) & 3)) * 22 * 1024)
  auto dma_sel2 = [&](int i, int x) __attribute__((always_inline)) {
    if (i < 4) dma_sel(IC<6>{}, slotA, (i - 2) * 4 + x); else dma_sel(IC<7>{}, slotB, (i - 4) * 4 + x);
  };
  f32x4 pt[4];
  float* c1s = st + 768 + w * 96;
#define LOAD_PT(i) asf(ld128<SC1>(rws, l16 + (i) * 1024, WS_ST2 + (mt * 2 + kh) * 4096))
  SB();
#pragma unroll
  for (int i = 0; i < 8; ++i) {
#pragma unroll
    for (int j = 0; j < 12; ++j) {
      const int tu = j / 3, k = j % 3;
      const v4u a = {ah[i & 1][tu][0][0], ah[i & 1][tu][0][1], ah[i & 1][tu][1][0], ah[i & 1][tu][1][1]};
      acc[k][tu] = mfma(wg[k][i], a, acc[k][tu]);
      SB();
      if (j < 8 && i < 7) ah[(i + 1) & 1][j >> 1][j & 1] = AHALF(j >> 1, i + 1, j & 1);
      if (j < 4 && i == 7) x1h[j >> 1][j & 1] = trread(smem + x1o + (j >> 1) * 4096 + (j & 1) * 512);
      if (i == 0 && j == 0) asm volatile("s_waitcnt vmcnt(24)" ::: "memory");
      if (i < 2 && j >= 4) wq[4 + i][j - 4] = READ_W(i * 8192, j - 4);
      if (i == 3 && j == 0) {
#pragma unroll
        for (int f = 0; f < 16; ++f) PINA(wq[4 + (f >> 3)][f & 7]);
      }
      if (i == 2 && j == 8) USE(wg[2][7]);
      if (i >= 2 && i < 6 && j == 9) dma_sel2(i, 0);
      if (i >= 3 && i < 7 && (j == 1 || j == 4 || j == 7)) dma_sel2(i - 1, (j + 2) / 3);
      if (j >= 8 && i == 6) pt[j - 8] = LOAD_PT(j - 8);
      if (i == 7 && j >= 8) LOAD_WO(j - 8);
      if (i == 5 && j == 10) {
        asm volatile("s_waitcnt vmcnt(30)" ::: "memory");
        __builtin_amdgcn_s_barrier();
      }
      SB();
    }
  }
  STAMP(1, 8);
#pragma unroll
  for (int k = 0; k < 3; ++k)
#pragma unroll
    for (int t = 0; t < 2; ++t) xch[(w * 6 + k * 2 + t) * 64 + lane] = acc[k][2 + t];
  SB();
  {
    unsigned m = 0;
#pragma unroll
    for (int i = 0; i < 4; ++i) m = macc4(m, __builtin_bit_cast(v4u, pt[i]));
    RETRY_BEGIN(m)
#pragma unroll
      for (int i = 0; i < 4; ++i) { pt[i] = LOAD_PT(i); m = macc4(m, __builtin_bit_cast(v4u, pt[i])); }
    RETRY_END(m)
    if (tid < 4) hint_add(p.ws, 64 + (nt * 4 + (mt >> 3)) * 4 + tid);
    const f32x4 sx = (pt[0] + pt[1]) + (pt[2] + pt[3]);
    const float tv = xrow_sum4(sx[0], sx[2], sx[1], sx[3]);
    auto sw = __builtin_amdgcn_permlane32_swap(__float_as_uint(tv), __float_as_uint(tv), false, false);
    const float mm = __uint_as_float(sw[0]) * (1.0f / FH);
    const float r = __builtin_amdgcn_rsqf(fmaxf(__uint_as_float(sw[1]) * (1.0f / FH) - mm * mm, 0.f) + EPS);
    float* f2 = st + 512 + w * 64;
    f2[(q >> 1) * 32 + 2 * c + (q & 1)] = (q >> 1) ? -r * mm : r;
#pragma unroll
    for (int t = 0; t < 2; ++t) { r4[t] = *(const f32x4*)(f2 + q * 8 + t * 4); m4[t] = *(const f32x4*)(f2 + 32 + q * 8 + t * 4); }
#pragma unroll
    for (int k = 0; k < 3; ++k) {
      v4u o;
#pragma unroll
      for (int t = 0; t < 2; ++t) {
        o[2 * t] = nomark(pk2(fma2(lo2(z2v[k][t]), lo2(r4[t]), lo2(m4[t]))));
        o[2 * t + 1] = pk2(fma2(hi2(z2v[k][t]), hi2(r4[t]), hi2(m4[t])));
      }
      st128<SC1>(o, rws, z2o[k], 0);
    }
  }
  STAMP(1, 9);
  c1s[lane] = xrow_sum4(__uint_as_float(cz[0][0][0]), __uint_as_float(cz[0][0][1]), __uint_as_float(cz[0][1][0]), __uint_as_float(cz[0][1][1]));
  c1s[64 + (lane & 31)] = xrow_sum4(__uint_as_float(cz[0][2][0]), __uint_as_float(cz[0][2][1]), __uint_as_float(cz[0][2][0]), __uint_as_float(cz[0][2][1]));
  st[1408 + lane] = rbf((__uint_as_float(gq[0]) + __uint_as_float(gq[1])) + (__uint_as_float(gq[2]) + __uint_as_float(gq[3])));
  SB();
  f32x4 z1f[3][2];
  constexpr int RD = ZRD;
  v4u zf[RD];
#define LOAD_Z(n) ld128<SC1>(rws, zv[((n) >> 1) & 7] + ((n) & 1) * 1024, WS_Z2 + (((n) < 48 ? UT(((n) >> 4) % 3) : UO(((n) >> 4) % 3)) * 64 + w * 16) * 1024 + (((n) >> 4) % 3 == 2 ? oob3 : 0u))
  unsigned zv[8];
#pragma unroll
  for (int i = 0; i < 8; ++i) { zv[i] = l16 + ((CKMAP(i) + kr) & 7) * 2048; PIN(zv[i]); }
  {
    bar();
    STAMP(1, 34);
#pragma unroll
    for (int k = 0; k < 6; ++k) PINA(acc[k >> 1][k & 1]);
    f32x4 oth[3][2], cs1[3], bs1[3];
    float r1[2], m1[2];
#pragma unroll
    for (int t = 0; t < 2; ++t) { r1[t] = fin[t * 16 + c]; m1[t] = fin[32 + t * 16 + c]; }
#pragma unroll
    for (int k = 0; k < 3; ++k) {
      cs1[k] = *(const f32x4*)(c1s + k * 32 + q * 4); bs1[k] = *(const f32x4*)(c1s + k * 32 + 16 + q * 4);
#pragma unroll
      for (int t = 0; t < 2; ++t) oth[k][t] = xch[((w ^ 1) * 6 + k * 2 + t) * 64 + lane];
    }
#pragma unroll
    for (int i = 4; i < WO0; ++i) LOAD_WO(i);
    SB();
    f32x2 z1a[6][2];
#pragma unroll
    for (int i = 0; i < 6; ++i) {
      const int k = i >> 1, t = i & 1;
      const f32x4 a = acc[k][t] + oth[k][t];
      z1a[i][0] = fma2(lo2(a), bc2(r1[t]), fma2(lo2(cs1[k]), bc2(m1[t]), lo2(bs1[k])));
      z1a[i][1] = fma2(hi2(a), bc2(r1[t]), fma2(hi2(cs1[k]), bc2(m1[t]), hi2(bs1[k])));
    }
#pragma unroll
    for (int i = 0; i < GELU_PRE; ++i) {
      const f32x2 lo = gelu2(z1a[i][0]), hi = gelu2(z1a[i][1]);
      z1f[i >> 1][i & 1] = f32x4{lo[0], lo[1], hi[0], hi[1]};
      PIN(z1f[i >> 1][i & 1]);
    }
#pragma unroll
    for (int i = GELU_PRE; i < 6; ++i) { PIN(z1a[i][0]); PIN(z1a[i][1]); }
    SB();
#pragma unroll
    for (int f = 0; f < 16; ++f) wq[6 + (f >> 3)][f & 7] = READ_W((f >> 3) * 8192, f & 7);
    SB();
    STAMP(1, 10);
    hint_wait(rws, 0u, WS_CNT + (64 + (nt * 4 + w) * 4 + (xcd >> 1)) * 128, 8u);
    STAMP(1, 22);
#pragma unroll
    for (int n = 0; n < RD; ++n) zf[n] = LOAD_Z(n);
    SB();
#pragma unroll
    for (int i = GELU_PRE; i < 6; ++i) {
      const f32x2 lo = gelu2(z1a[i][0]), hi = gelu2(z1a[i][1]);
      z1f[i >> 1][i & 1] = f32x4{lo[0], lo[1], hi[0], hi[1]};
      PIN(z1f[i >> 1][i & 1]);
    }
  }
  f32x4* xb = (f32x4*)(smem + LDS_A) + lane;
  f32x4* yb = (f32x4*)(smem + LDS_A + 49152) + lane;
  f32x4 ac2[2][4], ini[4], yo[2];
  const float bs0 = bflo(bspq[0]), bs1_ = bfhi(bspq[0]);
  f32x4 bq0 = {bs0, bs0, bs0, bs0}, bq1 = {bs1_, bs1_, bs1_, bs1_};
  const f32x4 z4 = {0.f, 0.f, 0.f, 0.f};
  PINA(bq0); PINA(bq1);
  unsigned vmk = l16 + WS_V + ((mt * 4 + w) * 22 + pr0) * 1024;
  gu32* p_fl4 = (gu32*)(p.ws + WS_CNT + (192 + mt * 2 + (tid & 1)) * 128);
  PIN(vmk); PIN(mkv);
  unsigned voff[2];
  voff[0] = l16 + WS_V + ((mt * 4 + 2 * kh) * 22 + (uo0 >> 1)) * 1024;
  voff[1] = l16 + WS_V + ((mt * 4 + 2 * kh) * 22 + (uo2 >> 1)) * 1024 + (uo2 & 1) * 8 + oob3;
  PIN(voff[0]); PIN(voff[1]);
  float ev[4];
  unsigned eo0, eos, eo1[2];
  float evs[2];
  SB();
  STAMP(1, 11);
  auto v_store = [&](int k, int t, unsigned o0, unsigned o1) __attribute__((always_inline)) {
    st64<SC1>(v2u{o0, o1}, rws, voff[k >> 1] + (k & 1) * 8, t * 22 * 1024);
  };
  sfor<6>([&](auto P) __attribute__((always_inline)) {
    constexpr int ps = decltype(P)::value, cur = ps & 1, prv = cur ^ 1;
    unsigned zm;
    sfor<64>([&](auto G) __attribute__((always_inline)) {
      constexpr int g = decltype(G)::value, ks = g >> 2, tu = g & 3, ck = ks >> 1, k2 = ks & 1, n = ps * 16 + ks;
      const f32x4 cin = ks > 0 ? ac2[cur][tu] : ps >= 3 ? ini[tu] : tu == 0 ? bq0 : tu == 1 ? bq1 : z4;
      ac2[cur][tu] = mfma(zf[n % RD], wq[ck][k2 * 4 + tu], cin);
      SB();
      if constexpr (tu == 1) { zm = ks == 0 ? (zf[n % RD][0] > zf[n % RD][2] ? zf[n % RD][0] : zf[n % RD][2]) : macc(zm, zf[n % RD]); PIN(zm); }
      if constexpr (tu == 3) {
        if constexpr (n + RD < 96) zf[n % RD] = LOAD_Z(n + RD);
        else if constexpr (WO0 + n + RD - 96 < 24) LOAD_WO(WO0 + n + RD - 96);
      }
      if constexpr (ps >= 1 && ps <= 3 && g >= 2 && g < 18 && (g & 3) == 2) xb[((w * 3 + ps - 1) * 4 + (g >> 2)) * 64] = ac2[prv][g >> 2];
      if constexpr (ps >= 4 && (g == 2 || g == 6)) yb[((w * 3 + ps - 4) * 2 + (g >> 2)) * 64] = ac2[prv][2 + (g >> 2)];
      if constexpr (ps >= 1 && g == (ps >= 4 ? 12 : 24)) {
        if constexpr (ps == 4) asm volatile("s_waitcnt vmcnt(20)" ::: "memory");
        bar();
      }
      if constexpr (ps >= 4 && (g == 13 || g == 14)) yo[g - 13] = yb[(((w ^ 1) * 3 + ps - 4) * 2 + g - 13) * 64];
      if constexpr (ps >= 4 && g >= 18 && g < 46 && (g & 1) == 0) {
        constexpr int t = (g - 18) / 14, ph = ((g - 18) % 14) >> 1;
        if constexpr (ph == 0) PINA(ac2[prv][t]);
        if constexpr (ph < 4) { ev[ph] = ac2[prv][t][ph] + yo[t][ph]; PIN(ev[ph]); }
        else if constexpr (ph < 6) { ev[2 * ph - 8] *= z1f[ps - 4][t][2 * ph - 8]; PIN(ev[2 * ph - 8]); ev[2 * ph - 7] *= z1f[ps - 4][t][2 * ph - 7]; PIN(ev[2 * ph - 7]); }
        else { eo0 = nomark(pk2(ev[0], ev[1])); PIN(eo0); }
      }
      if constexpr (ps >= 4 && (g == 29 + 4 || g == 43 + 2)) { eo1[g > 40] = pk2(g > 40 ? ev[2] : evs[0], g > 40 ? ev[3] : evs[1]); PIN(eo1[g > 40]); }
      if constexpr (ps >= 4 && g == 30) { evs[0] = ev[2]; evs[1] = ev[3]; eos = eo0; }
      if constexpr (ps >= 4 && g == 37) v_store(ps - 4, 0, eos, eo1[0]);
      if constexpr (ps >= 4 && g == 46) v_store(ps - 4, 1, eo0, eo1[1]);
      if constexpr (ps >= 2 && ps <= 4 && g >= 48 && g < 56 && (g & 1) == 0) ini[(g - 48) >> 1] = xb[(((w ^ 2) * 3 + ps - 2) * 4 + ((g - 48) >> 1)) * 64];
      if constexpr (ps == 0 && (g == 22 || g == 38 || g == 54)) st128<SC1>(mkv, rws, vmk + (g - 22) / 16 * 1024, g == 54 ? oob3 : 0u);
      if constexpr (ps * 64 + g == HINT4_G) { if (tid < 2) (void)__hip_atomic_fetch_add(p_fl4, 1u, __ATOMIC_RELAXED, __HIP_MEMORY_SCOPE_AGENT); }
      if constexpr (ps == 3 && g == 40) PIN(p_fl4);
      if constexpr (ps == 5 && g == POLL4_G) flagv = __builtin_amdgcn_raw_buffer_load_b32(rws, 0, WS_CNT + (192 + mt * 2 + (xcd & 1)) * 128, SC1);
      if constexpr (g == 0 && ps >= 1 && ps < 5) STAMP(1, 55 + ps);
      SB();
    });
    if (__builtin_expect(any_mark(zm), 0)) {
#pragma unroll
      for (int tu = 0; tu < 4; ++tu) ac2[cur][tu] = ps >= 3 ? xb[(((w ^ 2) * 3 + ps - 3) * 4 + tu) * 64] : tu == 0 ? bq0 : tu == 1 ? bq1 : z4;
      sfor<16>([&](auto K) __attribute__((always_inline)) {
        constexpr int ks = decltype(K)::value, ck = ks >> 1, k2 = ks & 1;
        v4u zz;
        unsigned sp = 0;
        do { MEMFENCE(); zz = LOAD_Z(ps * 16 + ks); } while (any_mark(macc(0u, zz)) & (++sp <= SPIN_CAP));
#pragma unroll
        for (int tu = 0; tu < 4; ++tu) ac2[cur][tu] = mfma(zz, wq[ck][k2 * 4 + tu], ac2[cur][tu]);
      });
    }
  });
  STAMP(1, 12);
  {
#pragma unroll
    for (int t = 0; t < 2; ++t) yb[((w * 3 + 2) * 2 + t) * 64] = ac2[1][2 + t];
    PIN(ac2[1][0]); PIN(ac2[1][1]);
    bar();
    STAMP(1, 52);
#pragma unroll
    for (int t = 0; t < 2; ++t) yo[t] = yb[(((w ^ 1) * 3 + 2) * 2 + t) * 64];
#pragma unroll
    for (int t = 0; t < 2; ++t) {
      const f32x4 g = (ac2[1][t] + yo[t]) * z1f[2][t];
      v_store(2, t, nomark(pk2(lo2(g))), pk2(hi2(g)));
    }
  }
  STAMP(1, 13);
  SB();
  f32x4 gb4[4];
  f32x2 x1f[2][4];
  STAMP(1, 53);
  if (__builtin_amdgcn_readfirstlane(flagv) < 8u) hint_wait(rws, 0u, WS_CNT + (192 + mt * 2 + (xcd & 1)) * 128, 8u);
  MEMFENCE();
  STAMP(1, 23);
  f32x4 a3[4][4];
  v4u vs[6][4];
  unsigned a_rw, a_rr[3], o_out;
#define LOAD_V(f) vs[(f) >> 2][(f) & 3] = ld128<SC1>(rws, k3v[(f) >> 2], WS_V + (mt * 4 + (((f) & 3) ^ w)) * 22 * 1024)
  {
    unsigned spins = 0;
    for (;;) {
      MEMFENCE();
#pragma unroll
      for (int f = 0; f < 12; ++f) LOAD_V(f);
      SB();
#pragma unroll
      for (int i = 0; i < 4; ++i) gb4[i] = *(const f32x4*)(st + 1408 + (i >> 1) * 32 + q * 8 + (i & 1) * 4);
#pragma unroll
      for (int i = 0; i < 8; ++i) { x1f[i >> 2][i & 3] = unpk(x1h[i >> 2][(i >> 1) & 1][i & 1]); PIN(x1f[i >> 2][i & 3]); }
      int lz = lane;
      PIN(lz);
      a_rw = lds0 + LDS_W + w * 12288 + lz * 16;
#pragma unroll
      for (int rel = 1; rel < 4; ++rel) { a_rr[rel - 1] = lds0 + LDS_W + ((w ^ rel) * 3 + rel - 1) * 4096 + lz * 16; PIN(a_rr[rel - 1]); }
      o_out = (mt * 64 + perm(w, lz & 15)) * 1024 + (lz >> 4) * 16 + nt * 128;
      PIN(a_rw); PIN(o_out);
      SB();
      unsigned m = 0;
      sfor<64>([&](auto G) __attribute__((always_inline)) {
        constexpr int g = decltype(G)::value, ks = g >> 4, tu = (g >> 2) & 3, e = g & 3;
        a3[e][tu] = mfma(wo[ks][e], vs[ks][tu], ks == 0 ? f32x4{0.f, 0.f, 0.f, 0.f} : a3[e][tu]);
        SB();
        if constexpr (e == 0 && g < 16) LOAD_V(12 + (g >> 2));
        if constexpr (g >= LATE_G && g < LATE_G + 16 && (g & 1) == 0) LOAD_V(16 + (((g - LATE_G) >> 1) & 1) * 4 + ((((g - LATE_G) >> 2) + 1) & 3));
        if constexpr (e == 1) { m = macc(m, vs[ks][tu]); PIN(m); }
        SB();
      });
      if (!(any_mark(m) & (++spins <= SPIN_CAP))) break;
    }
    unsigned m = 0;
#pragma unroll
    for (int f = 16; f < 24; ++f) m = macc(m, vs[f >> 2][f & 3]);
    RETRY_BEGIN(m)
#pragma unroll
      for (int f = 16; f < 24; ++f) { LOAD_V(f); m = macc(m, vs[f >> 2][f & 3]); }
    RETRY_END(m)
    sfor<32>([&](auto G) __attribute__((always_inline)) {
      constexpr int g = decltype(G)::value, tu = ((g >> 3) + 1) & 3, ks = 4 + ((g >> 2) & 1), e = g & 3;
      a3[e][tu] = mfma(wo[ks][e], vs[ks][tu], a3[e][tu]);
      SB();
      if constexpr (g >= 8 && (g & 1) == 0) ldsw<f32x4>(a_rw + (((g >> 3) - 1) * 4 + ((g >> 1) & 3)) * 1024, a3[(g >> 1) & 3][g >> 3]);
      SB();
    });
  }
  STAMP(1, 14);
  {
#pragma unroll
    for (int e = 0; e < 4; ++e) PIN(a3[e][0]);
    bar();
    STAMP(1, 54);
    f32x4 o3[4][3];
#pragma unroll
    for (int e = 0; e < 4; ++e)
#pragma unroll
      for (int rel = 1; rel < 4; ++rel) o3[e][rel - 1] = ldsr<f32x4>(a_rr[rel - 1] + e * 1024);
    const rsrc_t rout = mkrs(p.out, L * D * 2);
    SB();
#pragma unroll
    for (int pp = 0; pp < 2; ++pp) {
      v4u o;
#pragma unroll
      for (int h = 0; h < 2; ++h) {
        const int e = pp * 2 + h;
        const f32x4 y = (a3[e][0] + o3[e][0]) + (o3[e][1] + o3[e][2]);
        o[2 * h] = pk2(fma2(lo2(y), lo2(gb4[e]), x1f[pp][2 * h]));
        o[2 * h + 1] = pk2(fma2(hi2(y), hi2(gb4[e]), x1f[pp][2 * h + 1]));
      }
      st128<0>(o, rout, o_out + pp * 64, 0);
    }
  }
  if (b == 0 && tid == 0) *(unsigned*)(p.ws + WS_EPOCH) = tag_s;
  STAMP(1, 15);
  STAMP_FLUSH(1);
  asm volatile("adaln_code_end:");
}

extern "C" void kernel_launch(void* const* d_in, const int* in_sizes, int n_in, void* d_out, int out_size, void* d_ws,
                              size_t ws_size, hipStream_t stream) {
  Params p;
  p.u = (const u16*)d_in[0];
  p.lar = (const u16*)d_in[1];
  p.aim = (const u16*)d_in[2];
  p.cre = (const u16*)d_in[3];
  p.cim = (const u16*)d_in[4];
  p.ldt = (const u16*)d_in[5];
  p.dsk = (const u16*)d_in[6];
  p.win = (const u16*)d_in[8];
  p.wsp = (const u16*)d_in[9];
  p.bsp = (const u16*)d_in[10];
  p.wout = (const u16*)d_in[11];
  p.c = (const u16*)d_in[12];
  p.wmod = (const u16*)d_in[13];
  p.out = (u16*)d_out;
  p.ws = (unsigned char*)d_ws;
  (void)hipFuncSetAttribute((const void*)adaln_mega, hipFuncAttributeMaxDynamicSharedMemorySize, MEGA_LDS);
  adaln_mega<<<256, 256, MEGA_LDS, stream>>>(p);
}
```

```cpp
#define HINT4_G 260
#include <hip/hip_runtime.h>
#include <hip/hip_bf16.h>
#include <stdint.h>
#include <utility>

#define LAS __attribute__((address_space(3)))
typedef unsigned v4u __attribute__((ext_vector_type(4)));
typedef unsigned v2u __attribute__((ext_vector_type(2)));
typedef float f32x4 __attribute__((ext_vector_type(4)));
typedef float f32x2 __attribute__((ext_vector_type(2)));
typedef short bf16x8 __attribute__((ext_vector_type(8)));
typedef _Float16 f16x8 __attribute__((ext_vector_type(8)));
typedef _Float16 f16x2 __attribute__((ext_vector_type(2)));
typedef __bf16 bf16x2_t __attribute__((ext_vector_type(2)));
typedef unsigned short u16;
typedef __amdgpu_buffer_rsrc_t rsrc_t;

#define STAMP_DECL do { } while (0)
#define STAMP(k, i) do { } while (0)
#define STAMP_FLUSH(k) do { } while (0)

#define OPT_A 1
#define OPT_B 1
#define OPT_C 2
#define OPT_D 1
#define OPT_E 2
#define OPT_F 1
constexpr int L = 2048, D = 512, NM = 128, FH = 688, FF = 1376;
constexpr float EPS = 1e-5f;
constexpr int SC1 = 16;
constexpr unsigned OOB = 0x40000000u;
constexpr unsigned SPIN_CAP = 1u << 14;

constexpr unsigned WS_EPOCH = 0;
constexpr unsigned WS_CNT = 4096;
constexpr unsigned WS_SR = WS_CNT + 512 * 128;
constexpr unsigned WS_SB = WS_SR + 2048 * 8;
constexpr unsigned WS_MODG = WS_SB + 2048 * 8;
constexpr unsigned WS_CSB = WS_MODG + 3072 * 32;
constexpr unsigned WS_ST2 = WS_CSB + 88 * 16 * 32;
constexpr unsigned WS_UG = 1u << 20;
static_assert(WS_ST2 + 32 * 2 * 16 * 32 * 8 <= WS_UG, "small buffers");
constexpr unsigned WS_X1T = WS_UG + 256 * 2048 * 8;
constexpr unsigned WS_WP = WS_X1T + 512 * 2048 * 2;
constexpr unsigned WS_WOT = WS_WP + 88 * 16 * 1024;
constexpr unsigned WS_Z2 = WS_WOT + 32 * 22 * 1024;
constexpr unsigned WS_V = WS_Z2 + 44 * 64 * 1024;
constexpr unsigned WS_END = WS_V + 128 * 22 * 1024;

struct Params {
  const u16 *u, *lar, *aim, *cre, *cim, *ldt, *dsk, *win, *wsp, *bsp, *wout, *c, *wmod;
  u16* out;
  unsigned char* ws;
};

#define DI __device__ __forceinline__
template <int V> struct IC { static constexpr int value = V; };
template <class F, int... I> DI void sfor_(F&& f, std::integer_sequence<int, I...>) { int d[] = {(f(IC<I>{}), 0)...}; (void)d; }
template <int N, class F> DI void sfor(F&& f) { sfor_(f, std::make_integer_sequence<int, N>{}); }
DI float bf2f(unsigned b) { return __builtin_bit_cast(float, b << 16); }
DI float bflo(unsigned w) { return __builtin_bit_cast(float, w << 16); }
DI float bfhi(unsigned w) { return __builtin_bit_cast(float, w & 0xFFFF0000u); }
DI f32x2 unpk(unsigned w) { return f32x2{bflo(w), bfhi(w)}; }
DI unsigned pk2(f32x2 v) { return __builtin_bit_cast(unsigned, __builtin_convertvector(v, bf16x2_t)); }
DI unsigned pk2(float lo, float hi) { return pk2(f32x2{lo, hi}); }
DI unsigned nomark(unsigned x) { return x < 0xFFFFFFFEu ? x : 0xFFFFFFFEu; }
DI float rbf(float x) { return bflo(pk2(x, 0.f)); }
DI f32x2 fma2(f32x2 a, f32x2 b, f32x2 c) { return __builtin_elementwise_fma(a, b, c); }
DI f32x2 lo2(f32x4 v) { return f32x2{v[0], v[1]}; }
DI f32x2 hi2(f32x4 v) { return f32x2{v[2], v[3]}; }
DI f32x2 bc2(float x) { return f32x2{x, x}; }
DI f32x4 mfma(v4u a, v4u b, f32x4 c) {
  return __builtin_amdgcn_mfma_f32_16x16x32_bf16(__builtin_bit_cast(bf16x8, a), __builtin_bit_cast(bf16x8, b), c, 0, 0, 0);
}
DI f32x4 mfma_h(v4u a, v4u b, f32x4 c) {
  return __builtin_amdgcn_mfma_f32_16x16x32_f16(__builtin_bit_cast(f16x8, a), __builtin_bit_cast(f16x8, b), c, 0, 0, 0);
}
DI float dot2(unsigned a, unsigned b, float acc) {
  return __builtin_amdgcn_fdot2_f32_bf16(__builtin_bit_cast(bf16x2_t, a), __builtin_bit_cast(bf16x2_t, b), acc, false);
}
constexpr unsigned ONES = 0x3F803F80u;
DI int perm(int u, int c) { return 32 * (u >> 1) + 8 * (c >> 2) + 4 * (u & 1) + (c & 3); }
template <int CTRL> DI float dpp(float x) {
  return __builtin_bit_cast(float, __builtin_amdgcn_mov_dpp(__builtin_bit_cast(int, x), CTRL, 0xf, 0xf, true));
}
DI float row16_sum(float x) {
  x += dpp<0xB1>(x); x += dpp<0x4E>(x); x += dpp<0x141>(x); x += dpp<0x128>(x);
  return x;
}
DI float row16_max(float x) {
  x = fmaxf(x, dpp<0xB1>(x)); x = fmaxf(x, dpp<0x4E>(x)); x = fmaxf(x, dpp<0x141>(x)); x = fmaxf(x, dpp<0x128>(x));
  return x;
}
DI float xrow_sum(float x) {
  auto s = __builtin_amdgcn_permlane16_swap(__float_as_uint(x), __float_as_uint(x), false, false);
  x = __uint_as_float(s[0]) + __uint_as_float(s[1]);
  auto t = __builtin_amdgcn_permlane32_swap(__float_as_uint(x), __float_as_uint(x), false, false);
  return __uint_as_float(t[0]) + __uint_as_float(t[1]);
}
DI float xrow_sum4(float a, float b, float c, float d) {
  auto s = __builtin_amdgcn_permlane16_swap(__float_as_uint(a), __float_as_uint(b), false, false);
  auto t = __builtin_amdgcn_permlane16_swap(__float_as_uint(c), __float_as_uint(d), false, false);
  const float x = __uint_as_float(s[0]) + __uint_as_float(s[1]), y = __uint_as_float(t[0]) + __uint_as_float(t[1]);
  auto z = __builtin_amdgcn_permlane32_swap(__float_as_uint(x), __float_as_uint(y), false, false);
  return __uint_as_float(z[0]) + __uint_as_float(z[1]);
}
DI float xrow_max(float x) {
  auto s = __builtin_amdgcn_permlane16_swap(__float_as_uint(x), __float_as_uint(x), false, false);
  x = fmaxf(__uint_as_float(s[0]), __uint_as_float(s[1]));
  auto t = __builtin_amdgcn_permlane32_swap(__float_as_uint(x), __float_as_uint(x), false, false);
  return fmaxf(__uint_as_float(t[0]), __uint_as_float(t[1]));
}
DI f32x2 gelu2(f32x2 x) {
  const float k0 = -2.0f * 0.7978845608028654f * 1.4426950408889634f, k1 = k0 * 0.044715f;
  const f32x2 a = x * fma2(x * x, bc2(k1), bc2(k0));
  const f32x2 e = f32x2{__builtin_amdgcn_exp2f(a[0]), __builtin_amdgcn_exp2f(a[1])} + bc2(1.0f);
  return x * f32x2{__builtin_amdgcn_rcpf(e[0]), __builtin_amdgcn_rcpf(e[1])};
}
DI rsrc_t mkrs(const void* ptr, unsigned bytes) { return __builtin_amdgcn_make_buffer_rsrc((void*)ptr, 0, (int)bytes, 0x00020000); }
template <int AUX> DI v4u ld128(rsrc_t rs, unsigned v, unsigned s) { return __builtin_amdgcn_raw_buffer_load_b128(rs, (int)v, (int)s, AUX); }
template <int AUX> DI v2u ld64(rsrc_t rs, unsigned v, unsigned s) { return __builtin_amdgcn_raw_buffer_load_b64(rs, (int)v, (int)s, AUX); }
template <int AUX> DI void st128(v4u x, rsrc_t rs, unsigned v, unsigned s) { __builtin_amdgcn_raw_buffer_store_b128(x, rs, (int)(v + s), 0, AUX); }
template <int AUX> DI void st64(v2u x, rsrc_t rs, unsigned v, unsigned s) { __builtin_amdgcn_raw_buffer_store_b64(x, rs, (int)v, (int)s, AUX); }
DI f32x4 asf(v4u x) { return __builtin_bit_cast(f32x4, x); }
DI float sum4(f32x4 x) { return (x[0] + x[1]) + (x[2] + x[3]); }
DI unsigned umax3(unsigned a, unsigned b, unsigned c) { unsigned m = a > b ? a : b; return m > c ? m : c; }
DI unsigned macc4(unsigned m, v4u x) { return umax3(umax3(m, x[0], x[1]), x[2], x[3]); }
DI unsigned macc(unsigned m, v4u x) { return umax3(m, x[0], x[2]); }
DI bool any_mark(unsigned m) { return __builtin_amdgcn_ballot_w64(m == 0xFFFFFFFFu) != 0ull; }
#define SB() __builtin_amdgcn_sched_barrier(0)
#define PIN(x) asm volatile("" : "+v"(x))
#define PINA(x) asm volatile("" : "+a"(x))
#define USE(x) asm volatile("" ::"v"(x))
#define RETRY_BEGIN(m) if (__builtin_expect(any_mark(m), 0)) { unsigned sp_ = 0; do { MEMFENCE(); m = 0;
#define RETRY_END(m) } while (any_mark(m) & (++sp_ <= SPIN_CAP)); }
#define MEMFENCE() asm volatile("" ::: "memory")
DI void bar() { asm volatile("s_waitcnt lgkmcnt(0)" ::: "memory"); __builtin_amdgcn_s_barrier(); asm volatile("" ::: "memory"); }

DI f32x2 cmul(f32x2 a, f32x2 b) {
  f32x2 t = bc2(a[0]) * b;
  asm("v_pk_fma_f32 %0, %1, %2, %0 op_sel:[1,1,0] op_sel_hi:[1,0,1] neg_lo:[0,1,0]" : "+v"(t) : "v"(a), "v"(b));
  return t;
}
DI f32x2 cexp_p(float are2, float rev_hi, float rev_lo, float p) {
  const float mag = __builtin_amdgcn_exp2f(are2 * p);
  const float hi = rev_hi * p;
  const float fr = (hi - __builtin_rintf(hi)) + rev_lo * p;
  return f32x2{mag * __builtin_amdgcn_cosf(fr), mag * __builtin_amdgcn_sinf(fr)};
}
DI float exp_acc(float x) {
  const float hi = x * 1.4426950408889634f;
  const float lo = __builtin_fmaf(x, 1.4426950408889634f, -hi) + x * 1.925963033500011e-8f;
  const float e = __builtin_amdgcn_exp2f(hi);
  return __builtin_fmaf(e, lo * 0.6931471805599453f, e);
}
DI unsigned pkh(float a, float b) {
  f16x2 v = {(_Float16)a, (_Float16)b};
  return __builtin_bit_cast(unsigned, v);
}

constexpr int CV_RK = 4160;
constexpr int CV_H = 2 * CV_RK;
constexpr int CV_CH = CV_H + 4608;
constexpr int LDS_A = 0;
constexpr int LDS_U = 2 * CV_CH;
constexpr int LDS_GS = LDS_U + 8192;
constexpr int LDS_TAB = 36864;
constexpr int KP_CH = 13840;
constexpr int LDS_KP = LDS_TAB + 2 * 64 * 528 + 16;
constexpr int LDS_LN = 0, LDS_MR = 8448;
constexpr int LDS_WI = 145472, LDS_WO = LDS_WI + 10240;
constexpr int LDS_X = 65536;
constexpr int LDS_S = LDS_X + 24576;
constexpr int LDS_W = LDS_S + 6144;
constexpr int MEGA_LDS = LDS_W + 65536 + 2048;
static_assert(LDS_WO + 4608 <= MEGA_LDS && LDS_KP + 2 * KP_CH <= LDS_WI && LDS_S >= 73728, "lds");
static_assert(LDS_GS + 1280 <= LDS_TAB && LDS_KP + 2 * KP_CH <= MEGA_LDS && MEGA_LDS <= 160 * 1024, "lds");
#define FLAG_AT 48
static_assert(FLAG_AT >= 36 && FLAG_AT <= 56 && FLAG_AT % 4 == 0, "flag position");
#define WF_EARLY 5
#define WO0 12
#define WSP_P0 4
#define HINT4_KS 15
#define ZDEPTH 3
#define GELU_IN 0
#define EPI_W 0
#define MOD_POS 0
#define POLL3 0
#define GELU_PRE 4
static_assert(HINT4_G > 3 * 64 + 12, "behind the marker barrier");
#define POLL4_G 16
#define RELABEL 1
#define CKMAP(ck) (RELABEL ? 2 * ((ck) & 3) + ((ck) >> 2) : (ck))
#define POLL_AT 130
#define MOD_MID 1
#define LATE_SLEEP 6
#define SMALL_N 3
#define ZRD 12
#define ZLA 4
#define LATE_G 40
#define VPRE_G 37
#define IN_AUX 0
#define WSP_AUX 0

template <class T> DI T ldsr(unsigned a) { return *(const LAS T*)(size_t)a; }
template <class T> DI void ldsw(unsigned a, T v) { *(LAS T*)(size_t)a = v; }
DI v2u trread(const unsigned char* ptr) {
  typedef short v4i16_t __attribute__((ext_vector_type(4)));
  return __builtin_bit_cast(v2u, __builtin_amdgcn_ds_read_tr16_b64_v4i16((LAS v4i16_t*)ptr));
}
DI v2u trread_a(unsigned a) {
  typedef short v4i16_t __attribute__((ext_vector_type(4)));
  return __builtin_bit_cast(v2u, __builtin_amdgcn_ds_read_tr16_b64_v4i16((LAS v4i16_t*)(size_t)a));
}
struct CvOrder { int qq[36], pp[36]; bool first[36]; };
constexpr CvOrder cv_order() {
  CvOrder o{};
  int n = 0;
  for (int m = 0; m < 8; ++m) {
    for (int j = 0; j < m; ++j) if (m + j < 8) { o.qq[n] = m; o.pp[n] = j; ++n; }
    for (int j = 0; j <= m; ++j) if (m + j < 8) { o.qq[n] = j; o.pp[n] = m; ++n; }
  }
  for (int i = 0; i < 36; ++i) {
    o.first[i] = true;
    for (int j = 0; j < i; ++j) if (o.qq[j] + o.pp[j] == o.qq[i] + o.pp[i]) o.first[i] = false;
  }
  return o;
}
constexpr CvOrder CVO = cv_order();
struct Z2Order { int u[128], kind[128], pos[128]; };
constexpr Z2Order z2_order() {
  Z2Order o{};
  int n = 0;
  for (int u = 0; u < 40; ++u) {
    int pos = 0;
    const bool st = u < 24 && (u < 8 || (u & 7) < 4);
    for (int k = 0; k < 5; ++k)
      if (k < 3 ? u >= 8 : st) { o.u[n] = u; o.kind[n] = k; o.pos[n] = pos++; ++n; }
  }
  return o;
}
constexpr Z2Order Z2O = z2_order();

typedef __attribute__((address_space(1))) unsigned gu32;
DI unsigned tx(unsigned m, unsigned x, unsigned tag) { return __builtin_amdgcn_bitop3_b32(m, x, tag, 0xF6); }
DI unsigned tacc(unsigned m, v4u x, unsigned tag) { return tx(tx(m, x[1], tag), x[3], tag); }
DI bool any_nz(unsigned m) { return __builtin_amdgcn_ballot_w64(m != 0u) != 0ull; }
DI void hint_add(unsigned char* ws, int line) { (void)__hip_atomic_fetch_add((gu32*)(ws + WS_CNT + line * 128), 1u, __ATOMIC_RELAXED, __HIP_MEMORY_SCOPE_AGENT); }
DI void hint_wait(rsrc_t rws, unsigned voff, unsigned soff, unsigned want) {
  unsigned sp = 0;
  for (;;) {
    MEMFENCE();
    const unsigned v = __builtin_amdgcn_raw_buffer_load_b32(rws, (int)voff, (int)soff, SC1);
    if (!((__builtin_amdgcn_ballot_w64(v < want) != 0ull) & (++sp <= SPIN_CAP))) break;
    __builtin_amdgcn_s_sleep(OPT_E);
  }
  MEMFENCE();
}

extern "C" __global__ void __launch_bounds__(256, 1) adaln_mega(Params p) {
  extern __shared__ __attribute__((aligned(16))) unsigned char smem[];
  const int tid = threadIdx.x, lane = tid & 63, w = __builtin_amdgcn_readfirstlane(tid >> 6);
  const int b = blockIdx.x, xcd = b & 7, idx = b >> 3;
  const int pb = xcd * 32 + idx;
  const unsigned l16 = lane * 16;
  STAMP_DECL;
  STAMP(1, 0);
  v2u codepf;
  {
    unsigned long long pc;
    int rel_m, rel_e;
    asm volatile("s_getpc_b64 %0\n\ts_mov_b32 %1, adaln_mega@rel32@lo+4\n\ts_mov_b32 %2, adaln_code_end@rel32@lo+12" : "=s"(pc), "=s"(rel_m), "=s"(rel_e));
    const rsrc_t rcode = mkrs((const void*)(pc + (long long)rel_m), rel_e > rel_m ? (unsigned)(rel_e - rel_m) & ~15u : 0u);
    codepf = ld64<0>(rcode, tid * 8, idx * 2048);
  }
  SB();
  const rsrc_t ru = mkrs(p.u, L * D * 2);
  v4u xr[2];
  xr[0] = ld128<IN_AUX>(ru, l16, (pb * 8 + w * 2) * 1024);
  xr[1] = ld128<IN_AUX>(ru, l16 + 1024, (pb * 8 + w * 2) * 1024);
  const int mkg = b & 3, mcg = b >> 2;
  const unsigned cq = p.c[mkg * 128 + (tid & 3) * 32 + (tid >> 3)];
  const unsigned epoch = *(const unsigned*)(p.ws + WS_EPOCH);
  const rsrc_t rws = mkrs(p.ws, WS_END);
  __builtin_amdgcn_raw_buffer_store_b32(0u, rws, tid < 2 ? tid * 32768u : OOB, WS_CNT + b * 128, SC1);
  SB();
  const int cch = w >> 1, eh = w & 1, ns = tid & 127;
  const unsigned q_cre = p.cre[pb * 256 + tid], q_cim = p.cim[pb * 256 + tid], q_lar = p.lar[pb * 256 + tid], q_aim = p.aim[pb * 256 + tid];
  const unsigned q_ldt = p.ldt[pb * 2 + cch], q_dsk = p.dsk[pb * 2 + cch];
  v4u xm[4];
#define LOAD_XM() { \
  const rsrc_t rmod = mkrs(p.wmod, D * 3072 * 2); \
_Pragma("unroll") \
  for (int i = 0; i < 4; ++i) xm[i] = ld128<IN_AUX>(rmod, (tid >> 3) * 6144 + (tid & 7) * 16, (mkg * 128 + i * 32) * 6144 + mcg * 128 + (b < 192 ? 0u : OOB)); }
  SB();
  LOAD_XM();
  SB();
  STAMP(1, 24);
  const int c = lane & 15, q = lane >> 4;
  const unsigned lds0 = (unsigned)(size_t)(LAS unsigned char*)smem;
  unsigned pos4[9];
  {
    const float f4 = (float)(8188 - tid * 32);
#pragma unroll
    for (int j = 0; j < 10; j += 2) {
      const unsigned pp = pk2(bc2(f4) - f32x2{4.f * j, 4.f * j + 4.f});
      const int p0 = (int)bflo(pp), p1 = (int)bfhi(pp);
      pos4[j] = p0 + ((p0 >> 5) << 2) + lds0 + LDS_KP;
      PIN(pos4[j]);
      if (j + 1 < 9) { pos4[j + 1] = p1 + ((p1 >> 5) << 2) + lds0 + LDS_KP; PIN(pos4[j + 1]); }
    }
  }
  unsigned co[2][4];
  {
    const unsigned b0 = (eh ? 1152 + 576 * q : 144 * (q >> 1) + 4 * (q & 1)) * 4 + 36 * c + lds0 + LDS_KP + cch * KP_CH;
    const unsigned b1 = (q < 2 ? 288 + 144 * q : 568 + 4 * q) * 4 + 36 * c + lds0 + LDS_KP + cch * KP_CH;
    const unsigned s0 = eh ? 576 : 4, s1 = q < 2 ? 8 : 576;
#pragma unroll
    for (int r = 0; r < 4; ++r) { co[0][r] = b0 + r * s0; co[1][r] = b1 + r * s1; PIN(co[0][r]); PIN(co[1][r]); }
  }
  *(unsigned*)(smem + LDS_KP - 16 + cch * KP_CH + (lane & 3) * 4) = 0u;
  unsigned a_pad, a_wi, a_wo, a_tab, gm_off, gc_off;
  a_pad = lds0 + cch * CV_CH + (lane < 32 ? CV_H + l16 : ((lane >> 1) & 1) * CV_RK + 4096 + (lane & 1) * 16);
  a_wi = lds0 + LDS_WI + (w * 32 + 8 * q + ((lane >> 2) & 3)) * 80 + (lane & 3) * 16;
  a_wo = lds0 + LDS_WO + (8 * q + ((lane >> 2) & 3)) * 144 + (w >> 1) * 64 + (lane & 3) * 16 + (w & 1) * 8;
  a_tab = lds0 + LDS_TAB + cch * (64 * 528) + c * 528 + q * 16;
  gm_off = ((4 - cch) * 512 + mkg * 128 + ns) * 8;
  gc_off = (((lane & 7) > 5 ? 0 : ((lane & 7) >> 1)) * 512 + pb * 2 + (lane & 1)) * 8;
  PIN(a_pad); PIN(a_wi); PIN(a_wo); PIN(a_tab); PIN(gm_off); PIN(gc_off);
  unsigned ab[4], bb, dv[4], rdo4[4], xoff;
#pragma unroll
  for (int i = 0; i < 4; ++i) {
    ab[i] = lds0 + cch * CV_CH + (1 - (c & 1)) * CV_RK + 2 * (2030 - 2 * (c >> 1) + 8 * q) - eh * 256 - 192 - 512 - 1024 * i;
    dv[i] = (lane >> 3) * 4096 + (((lane & 7) ^ ((lane >> 4) & 1) ^ (i << 1)) * 16) + i * (32768 - 1024);
    rdo4[i] = lds0 + LDS_W + w * 16384 + ((perm((i & 1) * 2 ^ (2 * (w & 1)), c) * 128 + ((q ^ (c >> 1)) * 16)) ^ ((i >> 1) * 64));
    PIN(ab[i]); PIN(dv[i]); PIN(rdo4[i]);
  }
  bb = lds0 + cch * CV_CH + CV_H + 2 * (256 - 16 + 16 * c + 8 * q) - eh * 256 - 192;
  xoff = WS_X1T + (pb * 2 + cch) * 4096 + eh * 2048 + ((c >> 1) * 32 + ((q & 1) * 4 + 2 * (c & 1) + (q >> 1)) * 4) * 2;
  PIN(bb); PIN(xoff);
  SB();
  STAMP(1, 26);
  {
    *(v4u*)(smem + LDS_LN + (w * 2) * 1040 + l16) = xr[0];
    *(v4u*)(smem + LDS_LN + (w * 2 + 1) * 1040 + l16) = xr[1];
    float sm[2], sq[2];
#pragma unroll
    for (int i = 0; i < 2; ++i) {
      sm[i] = dot2(xr[i][0], ONES, dot2(xr[i][1], ONES, dot2(xr[i][2], ONES, dot2(xr[i][3], ONES, 0.f))));
      sq[i] = dot2(xr[i][0], xr[i][0], dot2(xr[i][1], xr[i][1], dot2(xr[i][2], xr[i][2], dot2(xr[i][3], xr[i][3], 0.f))));
    }
    const float tv = row16_sum(xrow_sum4(sm[0], sm[1], sq[0], sq[1]));
    auto sw = __builtin_amdgcn_permlane32_swap(__float_as_uint(tv), __float_as_uint(tv), false, false);
    const float m = __uint_as_float(sw[0]) * (1.0f / D);
    const float r = __builtin_amdgcn_rsqf(fmaxf(__uint_as_float(sw[1]) * (1.0f / D) - m * m, 0.f) + EPS);
    ((float*)(smem + LDS_GS + 1088))[(q >> 1) * 8 + w * 2 + (q & 1)] = (q >> 1) ? -m * r : r;
  }
  const unsigned tag = epoch * 2654435761u + 0x9E3779B9u;
  STAMP(1, 28);
  asm volatile("s_waitcnt vmcnt(%0)" ::"i"(MOD_POS == 0 ? 8 : 4) : "memory");
  bar();
  STAMP(1, 16);
  {
    unsigned ua[4][2];
#pragma unroll
    for (int i = 0; i < 4; ++i) {
      const unsigned char* src = smem + LDS_LN + (tid & 3) * 2080 + (i * 64 + (tid >> 2)) * 4;
      ua[i][0] = *(const unsigned*)src; ua[i][1] = *(const unsigned*)(src + 1040);
    }
    const unsigned lnv = ((const unsigned*)(smem + LDS_GS + 1088))[c];
    SB();
#pragma unroll
    for (int i = 0; i < 4; ++i) st128<SC1>(v4u{ua[i][0], tag, ua[i][1], tag}, rws, (tid >> 2) * 16384 + (tid & 3) * 16, WS_UG + i * 64 * 16384 + pb * 64);
    st64<SC1>(v2u{lnv, tag}, rws, tid < 16 ? (c >> 3) * 16384 + (c & 7) * 8 : OOB, WS_SR + pb * 64);
  }
  STAMP(1, 17);
  SB();
  const unsigned wp_oob = b < 176 ? 0u : OOB;
  const int wpr = b >> 2, wkg = b & 3;
  v4u xi[2], xo;
  {
    const rsrc_t rwin = mkrs(p.win, D * FF * 2), rwout = mkrs(p.wout, FH * D * 2);
    const int hf = wpr >= 22, pp = wpr - 22 * hf;
    const unsigned v = ((tid >> 2) * (FF * 2) + (tid & 3) * 16) | ((pp == 21 ? tid & 2 : 0) << 29);
#pragma unroll
    for (int i = 0; i < 2; ++i) xi[i] = ld128<IN_AUX>(rwin, v, (wkg * 128 + i * 64) * (FF * 2) + (hf * FH + pp * 32) * 2 + wp_oob);
    xo = ld128<IN_AUX>(rwout, (tid >> 3) * 1024 + (tid & 7) * 16, idx * 32 * 1024 + xcd * 128);
  }
  SB();
  const int d0 = pb * 2, dk = d0 + cch;
  const int mt = (xcd >> 1) * 8 + (idx & 7), nt = (xcd & 1) * 4 + (idx >> 3);
  const int big = (nt + 2) >> 3;
  const int np = 3 - big, pr0 = 3 * nt - big * (nt - 6);
  const int kh = w & 1, uh = w >> 1;
  const int uo0 = pr0 * 2 + uh * (4 - 2 * big), ut0 = pr0 * 2 + (uh ^ 1) * (4 - 2 * big), uo2 = pr0 * 2 + 2 + uh, ut2 = pr0 * 2 + 2 + (uh ^ 1);
#define UO(k) ((k) == 2 ? uo2 : uo0 + (k))
#define UT(k) ((k) == 2 ? ut2 : ut0 + (k))
  const unsigned oob3 = (unsigned)big << 30;
  const int kr = 2 * (idx >> 3) + (xcd & 1) + (OPT_B == 0 ? (idx & 7) : OPT_B == 1 ? 4 * (idx & 1) : OPT_B == 2 ? 2 * (idx & 3) : 0);
  const rsrc_t rwsp = mkrs(p.wsp, L * L * 2);
  const unsigned wp_fr = (wpr * 2 * 16 + wkg * 4 + w) * 1024 + wp_oob;
  v4u wr[1][8];
  const unsigned wsp_v = (lane >> 3) * 4096 + (lane & 7) * 16, wsp_s = mt * 64 * 4096 + w * 1024;
#define LOAD_WSP1(ck, i) wr[ck][i] = ld128<WSP_AUX>(rwsp, wsp_v + (((ck) + kr) & 7) * 128, wsp_s + (i) * 8 * 4096)
#define LOAD_WSPN(n) LOAD_WSP1((n) >> 3, (n) & 7)
  const v4u mk = {~0u, ~0u, ~0u, ~0u};
  unsigned bspq[2];
#pragma unroll
  for (int t = 0; t < 2; ++t) bspq[t] = p.bsp[mt * 64 + perm(2 * kh + t, c)];
  const unsigned tag_s = __builtin_amdgcn_readfirstlane(tag);
  {
    const float dt = rbf(exp_acc(bf2f(q_ldt)));
    const float cre = bf2f(q_cre) * dt, cim = bf2f(q_cim) * dt;
    const float are2 = -rbf(exp_acc(bf2f(q_lar))) * dt * 1.4426950408889634f;
    const float aim = bf2f(q_aim) * dt;
    const float rev_hi = aim * 0.15915494309189535f;
    const float rev_lo = __builtin_fmaf(aim, 0.15915494309189535f, -rev_hi) + aim * 6.4206383e-9f;
    int ex = __builtin_amdgcn_frexp_expf(xrow_max(row16_max(fmaxf(fabsf(cre), fabsf(cim))))) + 1;
    ex = ex < -100 ? -100 : ex;
    const float scale = __builtin_amdgcn_ldexpf(1.f, -ex);
    ((float*)(smem + LDS_GS + 1024))[w] = __builtin_amdgcn_ldexpf(2.f, ex);
    const f32x2 c0 = {cre * scale, cim * scale};
    const f32x2 e1 = cexp_p(are2, rev_hi, rev_lo, 1.f), e8 = cexp_p(are2, rev_hi, rev_lo, 8.f), e64 = cexp_p(are2, rev_hi, rev_lo, 64.f);
    unsigned char* tn = smem + LDS_TAB + cch * (64 * 528) + ns * 4;
#define TROW(r, z) *(unsigned*)(tn + (r) * 528) = pkh((z)[0], -(z)[1])
    SB();
    const f32x2 e128 = cmul(e64, e64), e2 = cmul(e1, e1), e256 = cmul(e128, e128), e4 = cmul(e2, e2), e512 = cmul(e256, e256);
    f32x2 f0 = {1.f, 0.f}, f1 = e64;
    f32x2 ca = cmul(c0, e512), cb = cmul(ca, e512);
#pragma unroll
    for (int j = 0; j < 8; ++j) {
      *(unsigned*)(tn + (48 + j) * 528) = pkh(f0[0], f0[1]);
      *(unsigned*)(tn + (56 + j) * 528) = pkh(f1[0], f1[1]);
      TROW(32 + j, cb);
      f0 = j == 0 ? e8 : cmul(f0, e8); f1 = cmul(f1, e8); cb = cmul(cb, e128);
      if (j < 4) {
        const f32x2 z = cmul(ca, e4);
        TROW(24 + j, ca); TROW(28 + j, z);
        ca = cmul(ca, e128);
      }
      if (j == 3 || j == 7) {
        PIN(f0); PIN(f1); PIN(cb);
        if (WSP_P0 >= 4) { LOAD_WSPN(j >> 2 << 1); LOAD_WSPN((j >> 2 << 1) + 1); }
        SB();
      }
    }
    TROW(40, cb);
    SB();
  {
    const float cv = bf2f(cq);
    const float sl = rbf(cv * __builtin_amdgcn_rcpf(1.0f + __builtin_amdgcn_exp2f(cv * -1.4426950408889634f)));
    const float sc[4] = {dpp<0x00>(sl), dpp<0x55>(sl), dpp<0xAA>(sl), dpp<0xFF>(sl)};
    f32x2 a[4];
#pragma unroll
    for (int i = 0; i < 4; ++i)
#pragma unroll
      for (int j = 0; j < 4; ++j) a[j] = i == 0 ? unpk(xm[i][j]) * bc2(sc[i]) : fma2(unpk(xm[i][j]), bc2(sc[i]), a[j]);
    float* red = (float*)(smem + LDS_MR) + (tid >> 3) * 68 + (tid & 7) * 8;
    *(f32x4*)red = f32x4{a[0][0], a[0][1], a[1][0], a[1][1]};
    *(f32x4*)(red + 4) = f32x4{a[2][0], a[2][1], a[3][0], a[3][1]};
  }
    bar();
    float ms8[8];
#pragma unroll
    for (int i = 0; i < 8; ++i) ms8[i] = ((const float*)(smem + LDS_MR))[(q * 8 + i) * 68 + w * 16 + c];
    SB();
    f32x2 t1[8];
    t1[1] = e1; t1[2] = e2; t1[4] = e4;
    t1[3] = cmul(e2, e1); t1[5] = cmul(e4, e1); t1[6] = cmul(e2, e4); t1[7] = cmul(t1[6], e1);
    const f32x2 c1 = cmul(c0, e128), c2 = cmul(c0, e256), c3 = cmul(c2, e128);
    TROW(0, c0); TROW(8, c1); TROW(16, c2); TROW(20, c3);
    {
    const float s = xrow_sum(((ms8[0] + ms8[1]) + (ms8[2] + ms8[3])) + ((ms8[4] + ms8[5]) + (ms8[6] + ms8[7])));
    st64<SC1>(v2u{__float_as_uint(s), tag}, rws, q == 0 ? c * 8 : OOB, WS_MODG + (mkg * 3072 + mcg * 64 + w * 16) * 8 + (b < 192 ? 0u : OOB));
    }
    SB();
#pragma unroll
    for (int v = 1; v < 8; ++v) {
      const f32x2 z0 = cmul(t1[v], c0), z1 = cmul(t1[v], c1);
      TROW(v, z0); TROW(8 + v, z1);
      if ((v & 1) == 0) {
        const f32x2 z2 = cmul(t1[v], c2), z3 = cmul(t1[v], c3);
        TROW(16 + v / 2, z2); TROW(20 + v / 2, z3);
        if (v < 6 && WSP_P0 >= 8) { LOAD_WSPN(v + 2); LOAD_WSPN(v + 3); }
        SB();
      }
    }
  }
  STAMP(1, 27);
#pragma unroll
  for (int i = 0; i < 2; ++i) *(v4u*)(smem + LDS_WI + (i * 64 + (tid >> 2)) * 80 + (tid & 3) * 16) = xi[i];
  *(v4u*)(smem + LDS_WO + (tid >> 3) * 144 + (tid & 7) * 16) = xo;
  STAMP(1, 29);
  bar();
  STAMP(1, 18);
  SB();
  v4u gr[4], gb_[4];
  v2u gm[4], gc[4];
#define LOAD_GS() _Pragma("unroll") for (int i_ = 0; i_ < 4; ++i_) { gr[i_] = ld128<SC1>(rws, tid * 16, WS_SR + i_ * 4096); gb_[i_] = ld128<SC1>(rws, tid * 16, WS_SB + i_ * 4096); }
#define LOAD_GM() _Pragma("unroll") for (int i_ = 0; i_ < 4; ++i_) { gm[i_] = ld64<SC1>(rws, gm_off, WS_MODG + i_ * 24576); gc[i_] = ld64<SC1>(rws, gc_off, WS_MODG + i_ * 24576); }
  v4u ug[4], wt[2];
#define LOAD_UG() _Pragma("unroll") for (int i_ = 0; i_ < 4; ++i_) ug[i_] = ld128<SC1>(rws, tid * 16, WS_UG + pb * 16384 + i_ * 4096)
  {
    v4u fa[2][8], fb[8];
    const unsigned ta = a_tab + eh * (32 * 528);
#pragma unroll
    for (int ks = 0; ks < 3; ++ks) { fb[ks] = ldsr<v4u>(a_tab + 48 * 528 + ks * 64); fa[0][ks] = ldsr<v4u>(ta + ks * 64); fa[1][ks] = ldsr<v4u>(a_tab + 16 * 528 + ks * 64); }
    SB();
    LOAD_UG();
    SB();
    f32x4 kk[2][2];
    f32x2 us;
    v2u wh_[6];
#pragma unroll
    for (int i = 0; i < 4; ++i) kk[i >> 1][i & 1] = f32x4{0.f, 0.f, 0.f, 0.f};
#pragma unroll
    for (int ks = 0; ks < 8; ++ks) {
      if (ks < 5) { fb[3 + ks] = ldsr<v4u>(a_tab + 48 * 528 + (3 + ks) * 64); fa[0][3 + ks] = ldsr<v4u>(ta + (3 + ks) * 64); fa[1][3 + ks] = ldsr<v4u>(a_tab + 16 * 528 + (3 + ks) * 64); }
      if (ks == 5) { us = *(const f32x2*)(smem + LDS_GS + 1024 + cch * 8); wh_[4] = trread_a(a_wo); wh_[5] = trread_a(a_wo + 576); }
      if (ks == 6) { wh_[0] = trread_a(a_wi); wh_[1] = trread_a(a_wi + 320); wh_[2] = trread_a(a_wi + 8); wh_[3] = trread_a(a_wi + 328); }
      kk[0][ks >> 2] = mfma_h(fa[0][ks], fb[ks], kk[0][ks >> 2]); kk[1][ks >> 2] = mfma_h(fa[1][ks], fb[ks], kk[1][ks >> 2]);
      SB();
    }
    STAMP(1, 37);
    wt[0] = v4u{wh_[0][0], wh_[0][1], wh_[1][0], wh_[1][1]};
    wt[1] = v4u{wh_[2][0], wh_[2][1], wh_[3][0], wh_[3][1]};
    st128<SC1>(v4u{wh_[4][0], wh_[4][1], wh_[5][0], wh_[5][1]}, rws, l16, WS_WOT + ((xcd * 4 + w) * 22 + idx) * 1024 + wp_oob);
    LOAD_GS();
    SB();
#pragma unroll
    for (int i = 0; i < 4; ++i) PINA(kk[i >> 1][i & 1]);
    f32x4 k0 = kk[0][0] * us[0] + kk[0][1] * us[1];
    const f32x4 k1 = kk[1][0] * us[0] + kk[1][1] * us[1];
    k0[0] += ns == 0 ? bf2f(q_dsk) : 0.f;
#pragma unroll
    for (int r = 0; r < 4; ++r) { ldsw<float>(co[0][r], k0[r]); ldsw<float>(co[1][r], k1[r]); }
  }
  STAMP(1, 31);
  bar();
  STAMP(1, 19);
  {
    float v[2][9];
#pragma unroll
    for (int j = 0; j < 9; ++j) v[0][j] = ldsr<float>(pos4[j]);
    LOAD_GM();
#pragma unroll
    for (int j = 0; j < 9; ++j) v[1][j] = ldsr<float>(pos4[j] + KP_CH);
    ldsw<v4u>(a_pad, v4u{0u, 0u, 0u, 0u});
    SB();
    STAMP(1, 36);
#pragma unroll
    for (int ch = 0; ch < 2; ++ch) {
      unsigned char* rk = smem + ch * CV_CH + tid * 16;
#pragma unroll
      for (int sft = 0; sft < 2; ++sft)
        *(v4u*)(rk + sft * CV_RK) = v4u{pk2(v[ch][sft], v[ch][sft + 1]), pk2(v[ch][sft + 2], v[ch][sft + 3]), pk2(v[ch][sft + 4], v[ch][sft + 5]), pk2(v[ch][sft + 6], v[ch][sft + 7])};
    }
#pragma unroll
    for (int i = WSP_P0; i < 8; ++i) LOAD_WSPN(i);
    st128<SC1>(mk, rws, l16, WS_X1T + dk * 4096 + eh * 2048);
    st128<SC1>(mk, rws, l16 + 1024, WS_X1T + dk * 4096 + eh * 2048);
    __builtin_amdgcn_raw_buffer_store_b32(~0u, rws, lane * 4, WS_ST2 + ((mt * 2 + kh) * 16 + nt * 2 + uh) * 256, SC1);
    SB();
    STAMP(1, 32);
    unsigned m = 0;
#pragma unroll
    for (int i = 0; i < 4; ++i) { m = tacc(m, ug[i], tag); m = tacc(m, gr[i], tag); m = tacc(m, gb_[i], tag); }
    if (__builtin_expect(any_nz(m), 0)) {
      unsigned sp = 0;
      do {
        MEMFENCE(); LOAD_UG(); LOAD_GS(); m = 0;
#pragma unroll
        for (int i = 0; i < 4; ++i) { m = tacc(m, ug[i], tag); m = tacc(m, gr[i], tag); m = tacc(m, gb_[i], tag); }
      } while (any_nz(m) & (++sp <= SPIN_CAP));
    }
    f32x2 nn[4][2];
#pragma unroll
    for (int i = 0; i < 4; ++i) {
      nn[i][0] = fma2(unpk(ug[i][0]), bc2(__uint_as_float(gr[i][0])), bc2(__uint_as_float(gb_[i][0])));
      nn[i][1] = fma2(unpk(ug[i][2]), bc2(__uint_as_float(gr[i][2])), bc2(__uint_as_float(gb_[i][2])));
      PIN(nn[i][0]); PIN(nn[i][1]);
      *(unsigned*)(smem + LDS_U + i * 1024 + tid * 4) = __builtin_amdgcn_perm(ug[i][2], ug[i][0], 0x05040100u);
      *(unsigned*)(smem + LDS_U + 4096 + i * 1024 + tid * 4) = __builtin_amdgcn_perm(ug[i][2], ug[i][0], 0x07060302u);
    }
    SB();
    STAMP(1, 33);
    m = 0;
#pragma unroll
    for (int i = 0; i < 4; ++i) m = tx(tx(m, gm[i][1], tag), gc[i][1], tag);
    if (__builtin_expect(any_nz(m), 0)) {
      unsigned sp = 0;
      do {
        __builtin_amdgcn_s_sleep(3);
        MEMFENCE(); LOAD_GM(); m = 0;
#pragma unroll
        for (int i = 0; i < 4; ++i) m = tx(tx(m, gm[i][1], tag), gc[i][1], tag);
      } while (any_nz(m) & (++sp <= SPIN_CAP));
    }
    const float mv = rbf((__uint_as_float(gm[0][0]) + __uint_as_float(gm[1][0])) + (__uint_as_float(gm[2][0]) + __uint_as_float(gm[3][0])));
    *(u16*)(smem + LDS_GS + tid * 2) = (u16)pk2(mv + (cch ? 0.f : 1.0f), 0.f);
    const float cv = rbf((__uint_as_float(gc[0][0]) + __uint_as_float(gc[1][0])) + (__uint_as_float(gc[2][0]) + __uint_as_float(gc[3][0])));
    const float gav = dpp<0x104>(cv), gav2 = dpp<0x102>(cv);
    const float hbv = cv * gav, hsv = rbf(1.0f + cv) * gav2;
    const f32x2 hb = {__builtin_bit_cast(float, __builtin_amdgcn_readlane(__builtin_bit_cast(int, hbv), 0)), __builtin_bit_cast(float, __builtin_amdgcn_readlane(__builtin_bit_cast(int, hbv), 1))};
    const f32x2 hs = {__builtin_bit_cast(float, __builtin_amdgcn_readlane(__builtin_bit_cast(int, hsv), 2)), __builtin_bit_cast(float, __builtin_amdgcn_readlane(__builtin_bit_cast(int, hsv), 3))};
    SB();
    bspq[0] |= bspq[1] << 16;
    PIN(bspq[0]);
    STAMP(1, 38);
#pragma unroll
    for (int i = 0; i < 4; ++i) {
      const f32x2 h0 = fma2(nn[i][0], hs, hb), h1 = fma2(nn[i][1], hs, hb);
      *(unsigned*)(smem + CV_H + 512 + i * 1024 + tid * 4) = pk2(h0[0], h1[0]);
      *(unsigned*)(smem + CV_CH + CV_H + 512 + i * 1024 + tid * 4) = pk2(h0[1], h1[1]);
    }
  }
  asm volatile("" ::"v"(codepf));
  STAMP(1, 39);
  bar();
  STAMP(1, 1);
#define CV_A(ee, k) ({ const unsigned a_ = ab[(k) >> 1] + 192 - 64 * (ee) + 512 * (1 - ((k) & 1)); v4u{ldsr<unsigned>(a_), ldsr<unsigned>(a_ + 4), ldsr<unsigned>(a_ + 8), ldsr<unsigned>(a_ + 12)}; })
#define CV_B(ee, k) ldsr<v4u>(bb + 192 - 64 * (ee) + 512 * (k))
  const unsigned slotA = LDS_W + w * 16384, slotB = slotA + 8192;
  v4u wq[8][8];
#define READ_W(dl, f) ldsr<v4u>(rdo4[(((f) >> 1) & 1) + ((f) >> 2) * 2] + (dl) + ((f) & 1) * 512)
  unsigned dso;
#define DMA_W(ck, slot, i) do { \
    if (((i) & 3) == 0) asm volatile("s_add_u32 m0, %1, %2\n\ts_add_u32 %0, %3, %4\n\tbuffer_load_dwordx4 %5, %6, %0 offen lds" \
                 : "=&s"(dso) : "s"(lds0 + (slot)), "i"((i) * 1024), "s"(wsp_s + ((CKMAP(ck) + kr) & 7) * 128), "i"((i) * 32768), "v"(dv[0]), "s"(rwsp) : "scc", "memory"); \
    else asm volatile("buffer_load_dwordx4 %0, %1, %2 offen offset:%3 lds" :: "v"(dv[(i) & 3]), "s"(rwsp), "s"(dso), "i"(((i) & 3) * 1024) : "memory"); } while (0)
  auto dma_sel = [&](auto CK, unsigned slot, int i) __attribute__((always_inline)) {
    constexpr int ck = decltype(CK)::value;
    switch (i) {
      case 0: DMA_W(ck, slot, 0); break; case 1: DMA_W(ck, slot, 1); break; case 2: DMA_W(ck, slot, 2); break; case 3: DMA_W(ck, slot, 3); break;
      case 4: DMA_W(ck, slot, 4); break; case 5: DMA_W(ck, slot, 5); break; case 6: DMA_W(ck, slot, 6); break; default: DMA_W(ck, slot, 7); break;
    }
  };
  v4u cA[2][8], cB[2][8];
  v4u ones4 = {ONES, ONES, ONES, ONES};
  PIN(ones4);
  f32x4 cb4[4];
  {
    const v4u gbf = *(const v4u*)(smem + LDS_GS + w * 64 + q * 16), sbf = *(const v4u*)(smem + LDS_GS + 256 + w * 64 + q * 16);
#pragma unroll
    for (int k = 0; k < 4; ++k) { cA[0][k] = CV_A(0, k); cB[0][k] = CV_B(0, k); }
    SB();
    const f32x4 z4 = {0.f, 0.f, 0.f, 0.f};
#pragma unroll
    for (int e = 0; e < 2; ++e) {
      v4u o;
#pragma unroll
      for (int jj = 0; jj < 4; ++jj) o[jj] = pk2(unpk(wt[e][jj]) * unpk(gbf[jj]));
      st128<SC1>(o, rws, l16 + e * 16384, WS_WP + wp_fr);
      cb4[2 * e + 1] = mfma(sbf, wt[e], z4);
      cb4[2 * e] = mfma(ones4, o, z4);
    }
  }
  SB();
  f32x4 cacc[8];
  v2u ue[4];
  float uf[16];
  unsigned flagv;
  unsigned wro[4], a_cp, a_ue, o_csb, o_flag;
  gu32* p_flag = (gu32*)(p.ws + WS_CNT + (256 + (tid & 7) * 16 + (idx & 15)) * 128);
  {
    f32x2 cp[4];
    float s0, s1, s2, s3;
    sfor<144>([&](auto G) __attribute__((always_inline)) {
      constexpr int g = decltype(G)::value, ee = g / 36, j = g % 36, t = CVO.pp[j] + CVO.qq[j];
      constexpr bool first = ee == 0 && CVO.first[j];
      cacc[t] = mfma(cA[ee & 1][CVO.qq[j]], cB[ee & 1][CVO.pp[j]], first ? f32x4{0.f, 0.f, 0.f, 0.f} : cacc[t]);
      SB();
      if constexpr (g < 8) {
        if constexpr (g & 1) cB[0][4 + (g >> 1)] = CV_B(0, 4 + (g >> 1));
        else cA[0][4 + (g >> 1)] = CV_A(0, 4 + (g >> 1));
      }
      constexpr int jn = ee == 0 ? (j >= 12 && (j - 12) % 3 != 2 ? (j - 12) / 3 * 2 + (j - 12) % 3 : -1) : (j & 1) ? j >> 1 : -1;
      if constexpr (ee < 3 && jn >= 0 && jn < 16) {
        if constexpr (jn & 1) cB[(ee + 1) & 1][jn >> 1] = CV_B(ee + 1, jn >> 1);
        else cA[(ee + 1) & 1][jn >> 1] = CV_A(ee + 1, jn >> 1);
      }
      if constexpr (g == 8) { a_cp = lds0 + LDS_S + 4096 + c * 16; PIN(a_cp); }
      if constexpr (g == 9) { cb4[0][1] = cb4[1][0]; cb4[0][2] = cb4[2][0]; PINA(cb4[0]); }
      if constexpr (g == 10) { cb4[0][3] = cb4[3][0]; PINA(cb4[0]); }
      if constexpr (g == 11) ldsw<f32x4>(a_cp + w * 256, cb4[0]);
      if constexpr (g == 17) { a_cp += (q & 1) * 8; PIN(a_cp); }
      if constexpr (g == 20) { o_csb = q * 512 + c * 8; PIN(o_csb); }
      if constexpr (g == 23) { o_csb = tid < 32 ? o_csb : OOB; PIN(o_csb); }
      if constexpr (g == 26) { PIN(p_flag); }
      if constexpr (g == FLAG_AT) {
        STAMP(1, 42);
        asm volatile("s_waitcnt vmcnt(%0) lgkmcnt(8)" ::"i"(FLAG_AT > 38 ? 4 + (FLAG_AT - 38 + 3) / 4 : 4) : "memory");
        __builtin_amdgcn_s_barrier();
        if (tid < 8) (void)__hip_atomic_fetch_add(p_flag, 1u, __ATOMIC_RELAXED, __HIP_MEMORY_SCOPE_AGENT);
        STAMP(1, 43);
      }
      if constexpr (g == FLAG_AT + 4 || g == FLAG_AT + 8) {
        cp[(g - FLAG_AT - 4) / 2] = ldsr<f32x2>(a_cp + (g - FLAG_AT - 4) * 128);
        cp[(g - FLAG_AT - 4) / 2 + 1] = ldsr<f32x2>(a_cp + (g - FLAG_AT - 4) * 128 + 256);
      }
      if constexpr (g == FLAG_AT + 16) { s0 = cp[0][0] + cp[1][0]; PIN(s0); s2 = cp[0][1] + cp[1][1]; PIN(s2); }
      if constexpr (g == FLAG_AT + 20) { s1 = cp[2][0] + cp[3][0]; PIN(s1); s3 = cp[2][1] + cp[3][1]; PIN(s3); }
      if constexpr (g == FLAG_AT + 24) { s0 += s1; PIN(s0); s2 += s3; PIN(s2); }
      if constexpr (g == FLAG_AT + 28) st64<SC1>(v2u{__float_as_uint(s0), __float_as_uint(s2)}, rws, o_csb, WS_CSB + wpr * 1024 + wkg * 128 + wp_oob);
      if constexpr (g == 36) STAMP(1, 40);
      if constexpr (g == 72) STAMP(1, 41);
      if constexpr (g == 14 || g == 29 || g == 32 || g == 35) DMA_W(1, slotA, g == 14 ? 0 : (g - 26) / 3);
      if constexpr (g >= 38 && g < 86 && (g & 3) == 2) DMA_W(1 + (g + 10) / 64, g < 54 ? slotA : slotB, (4 + (g - 38) / 4) & 7);
      if constexpr (g == 88) asm volatile("s_waitcnt vmcnt(8)" ::: "memory");
      if constexpr (g >= 90 && g < 106 && (g & 1) == 0) wq[1][(g - 90) >> 1] = READ_W(0, (g - 90) >> 1);
      if constexpr (g >= 68 && g < 72) { wro[g - 68] = (lds0 + slotA + (lane >> 3) * 128 + (((lane & 7) ^ ((lane >> 4) & 1)) * 16)) ^ ((g - 68) * 32); PIN(wro[g - 68]); }
      if constexpr (g == 105) { a_ue = lds0 + LDS_U + cch * 4096 + eh * 2048 + c * 32 + q * 8; PIN(a_ue); }
      if constexpr (g == 106 || g == 107) { ue[(g - 106) * 2] = ldsr<v2u>(a_ue + (g - 106) * 1024); ue[(g - 106) * 2 + 1] = ldsr<v2u>(a_ue + (g - 106) * 1024 + 512); }
      if constexpr (g >= 108 && g < 140 && (g & 3) == 0) ldsw<v4u>(wro[((g - 108) >> 2) & 3] + ((g - 108) >> 2) * 1024, wr[0][(g - 108) >> 2]);
      if constexpr (g >= 109 && g < 125 && (g & 1) == 1) {
        constexpr int x = (g - 109) >> 1;
        if constexpr (x >= 2) { uf[2 * (x - 2)] = bflo(ue[(x - 2) >> 1][x & 1]); PIN(uf[2 * (x - 2)]); uf[2 * (x - 2) + 1] = bfhi(ue[(x - 2) >> 1][x & 1]); PIN(uf[2 * (x - 2) + 1]); }
      }
      if constexpr (g == 118 || g == 122) { constexpr int x = 6 + (g == 122); uf[2 * x] = bflo(ue[3][x & 1]); PIN(uf[2 * x]); uf[2 * x + 1] = bfhi(ue[3][x & 1]); PIN(uf[2 * x + 1]); }
      if constexpr (g == 110) { o_flag = w == 0 ? c * 128 : OOB; PIN(o_flag); }
      if constexpr (g == 126) asm volatile("s_waitcnt vmcnt(0)" ::: "memory");
      if constexpr (g >= 127 && g < 143 && (g & 1) == 1) wq[2][(g - 127) >> 1] = READ_W(8192, (g - 127) >> 1);
      if constexpr (g == POLL_AT) flagv = __builtin_amdgcn_raw_buffer_load_b32(rws, o_flag, WS_CNT + (256 + xcd * 16) * 128, SC1);
      SB();
    });
  }
  STAMP(1, 2);
  unsigned late;
  {
    f32x4* red = (f32x4*)(smem + LDS_X) + w * 256 + lane;
    f32x4 keep[4];
    if (eh) {
#pragma unroll
      for (int t = 0; t < 4; ++t) red[t * 64] = cacc[t];
      SB();
#pragma unroll
      for (int t = 0; t < 4; ++t) { PINA(cacc[4 + t]); keep[t] = cacc[4 + t]; PIN(keep[t]); }
    } else {
#pragma unroll
      for (int t = 0; t < 4; ++t) red[t * 64] = cacc[4 + t];
      SB();
#pragma unroll
      for (int t = 0; t < 4; ++t) { PINA(cacc[t]); keep[t] = cacc[t]; PIN(keep[t]); }
    }
    f32x2 pre[4][2];
#pragma unroll
    for (int t = 0; t < 4; ++t) {
      pre[t][0] = f32x2{uf[4 * t], uf[4 * t + 1]} + lo2(keep[t]);
      pre[t][1] = f32x2{uf[4 * t + 2], uf[4 * t + 3]} + hi2(keep[t]);
      PIN(pre[t][0]); PIN(pre[t][1]);
    }
    if (w == 0) *(unsigned*)(smem + LDS_S) = __builtin_amdgcn_ballot_w64(flagv < 16u) != 0ull;
    bar();
    late = *(const unsigned*)(smem + LDS_S);
    const f32x4* rd = (const f32x4*)(smem + LDS_X) + (w ^ 1) * 256 + lane;
    f32x4 oth[4];
#pragma unroll
    for (int t = 0; t < 4; ++t) oth[t] = rd[t * 64];
#pragma unroll
    for (int t = 0; t < 4; ++t) {
      v2u o;
      o[0] = nomark(pk2(pre[t][0] + lo2(oth[t])));
      o[1] = pk2(pre[t][1] + hi2(oth[t]));
      st64<SC1>(o, rws, xoff + t * 512, 0);
    }
  }
  STAMP(1, 3);
  SB();
  v4u wf[3][8];
  const unsigned rot = uh * 4;
#define LOAD_WP(half, k, i) ld128<SC1>(rws, l16 + ((i) & 3) * 1024, WS_WP + (((half) * 44 + UO(k)) * 16 + kh * 8 + (((i) & 4) ^ rot)) * 1024 + ((k) == 2 ? oob3 : 0u))
#define LOAD_XS(i) ld128<SC1>(rws, wsp_v, WS_X1T + ((i) * 32 + w * 8) * 4096 + mt * 128)
  v4u xs[16];
  unsigned tb[2][4], xdst;
  SB();
  int x1o;
  unsigned z2o[3], dgo[4], rdo[2], xw, xrd, a_fin, a_fr;
  v4u mkv = mk;
  v2u cz[2][3];
  f32x2 csb[2][3];
  unsigned gq[4];
#define LOAD_CZ(hf, kc) ld64<SC1>(rws, lane * 8, WS_CSB + ((hf) * 44 + UO(kc)) * 512 + ((kc) == 2 ? oob3 : 0u))
  {
    STAMP(1, 20);
    if (__builtin_amdgcn_readfirstlane(late)) {
      if (w == 0) { hint_wait(rws, c * 128, WS_CNT + (256 + xcd * 16) * 128, 16u); __builtin_amdgcn_s_sleep(LATE_SLEEP); }
      __builtin_amdgcn_s_barrier();
    }
    MEMFENCE();
    STAMP(1, 21);
#pragma unroll
    for (int i = 0; i < 3; ++i)
#pragma unroll
      for (int k = 0; k < 3; ++k) wf[k][i] = LOAD_WP(1, k, i);
    SB();
#pragma unroll
  for (int f = 0; f < 8; ++f) wq[0][f] = READ_W(0, f);
#pragma unroll
  for (int f = 0; f < 24; ++f) PINA(wq[f >> 3][f & 7]);
    int lz = lane;
    PIN(lz);
  {
    const int row = 8 * (lz >> 4) + ((lz >> 2) & 3);
    const int sg = ((row >> 1) & 1) | (((row >> 3) & 1) << 1);
#pragma unroll
    for (int tu = 0; tu < 4; ++tu) {
      const unsigned t0 = lds0 + LDS_A + kh * 32768 + row * 128 + ((tu ^ (2 * kh) ^ sg) * 32) + (lz & 3) * 8;
      tb[0][tu] = t0 + uh * 16384;
      tb[1][tu] = t0 + (uh ^ 1) * 16384;
    }
    const int rl = w * 8 + (lz >> 3), chn = lz & 7;
    const int sg2 = ((rl >> 1) & 1) | (((rl >> 3) & 1) << 1);
    xdst = lds0 + LDS_A + rl * 128 + (((chn >> 1) ^ sg2) * 32) + (chn & 1) * 16;
  }
  {
    const int row = 8 * (lz >> 4) + ((lz >> 2) & 3);
    const int sg = ((row >> 1) & 1) | (((row >> 3) & 1) << 1);
    x1o = LDS_A + nt * 8192 + row * 128 + ((w ^ sg) * 32) + (lz & 3) * 8;
  }
#pragma unroll
  for (int k = 0; k < 3; ++k) { z2o[k] = lz * 16 + WS_Z2 + (UO(k) * 64 + mt * 2 + kh) * 1024 + (k == 2 ? oob3 : 0u); }
#pragma unroll
    for (int tu = 0; tu < 8; ++tu) PIN(tb[tu >> 2][tu & 3]);
    PIN(xdst); PIN(x1o);
    {
      const int c2 = lz & 15;
      const bool dg = (c2 >> 2) == (lz >> 4);
      const unsigned dump = lds0 + LDS_W + 65536 + lz * 16;
      dgo[0] = dg ? lds0 + LDS_S + (w * 64 + kh * 32 + c2) * 4 : dump; dgo[1] = dg ? lds0 + LDS_S + (w * 64 + (kh ^ 1) * 32 + c2) * 4 : dump;
      dgo[2] = dg ? lds0 + LDS_S + 2048 + (w * 64 + kh * 32 + c2) * 16 : dump; dgo[3] = dg ? lds0 + LDS_S + 2048 + (w * 64 + (kh ^ 1) * 32 + c2) * 16 : dump;
      rdo[0] = lds0 + LDS_S + (kh * 32 + (lz & 31)) * 4; rdo[1] = lds0 + LDS_S + 2048 + (kh * 32 + (lz & 31)) * 16 + (lz & 3) * 4;
      xw = lds0 + LDS_X + w * 6144 + lz * 16; xrd = lds0 + LDS_X + (w ^ 1) * 6144 + lz * 16;
      a_fin = lds0 + LDS_S + 1024 + w * 256 + lz * 4; a_fr = lds0 + LDS_S + 1024 + w * 256 + (lz >> 4) * 16;
      PIN(dgo[0]); PIN(dgo[1]); PIN(dgo[2]); PIN(dgo[3]); PIN(rdo[0]); PIN(rdo[1]); PIN(xw); PIN(xrd); PIN(a_fin); PIN(a_fr); PIN(mkv);
    }
#pragma unroll
    for (int k = 0; k < 3; ++k) PIN(z2o[k]);
    SB();
#pragma unroll
    for (int i = 3; i < WF_EARLY; ++i)
#pragma unroll
      for (int k = 0; k < 3; ++k) wf[k][i] = LOAD_WP(1, k, i);
    SB();
#pragma unroll
    for (int i = 0; i < 16; ++i) xs[i] = LOAD_XS(i);
    SB();
    STAMP(1, 44);
    unsigned m = 0;
#pragma unroll
    for (int i = 0; i < 16; ++i) {
      m = macc(m, xs[i]); ldsw<v4u>(xdst + i * 4096, xs[i]);
      if (WF_EARLY * 3 + i < 24) wf[(WF_EARLY * 3 + i) % 3][(WF_EARLY * 3 + i) / 3] = LOAD_WP(1, (WF_EARLY * 3 + i) % 3, (WF_EARLY * 3 + i) / 3);
      SB();
    }
    PIN(m);
    STAMP(1, 45);
    RETRY_BEGIN(m)
#pragma unroll
      for (int i = 0; i < 16; ++i) xs[i] = LOAD_XS(i);
      SB();
#pragma unroll
      for (int i = 0; i < 16; ++i) { m = macc(m, xs[i]); ldsw<v4u>(xdst + i * 4096, xs[i]); }
    RETRY_END(m)
  }
  STAMP(1, 4);
  bar();
  STAMP(1, 5);
#define AHALF(tu, i, h) trread_a(tb[(i) >> 2][tu] + ((i) & 3) * 4096 + (h) * 512)
  f32x4 acc[3][4];
  v4u wg[3][8];
  v2u ah[2][4][2];
  float* st = (float*)(smem + LDS_S);
  float* fin = st + 256 + w * 64;
  f32x4* xch = (f32x4*)(smem + LDS_X);
  f32x4 r4[2], m4[2];
  {
    f32x4 s1[4], s2[4];
    constexpr int LA = ZLA;
    v2u ar[LA + 1][2];
    float pa[4], pb[4];
#define UTILE(u) ((u) < 8 ? (u) >> 2 : (((u) >> 3) + 1) & 3)
#define UKS(u) ((u) < 8 ? (u) & 3 : (u) & 7)
#pragma unroll
    for (int u = 0; u < LA; ++u) { ar[u][0] = AHALF(UTILE(u), UKS(u), 0); ar[u][1] = AHALF(UTILE(u), UKS(u), 1); }
#pragma unroll
    for (int i = 0; i < 6; ++i) cz[i / 3][i % 3] = LOAD_CZ(i / 3, i % 3);
#pragma unroll
    for (int i = 0; i < 4; ++i) gq[i] = __builtin_amdgcn_raw_buffer_load_b32(rws, lane * 8, WS_MODG + (i * 3072 + 5 * 512 + nt * 64) * 8, SC1);
    DMA_W(3, slotB, 0);
    SB();
    sfor<128>([&](auto G) __attribute__((always_inline)) {
      constexpr int m = decltype(G)::value, u = Z2O.u[m], k = Z2O.kind[m], pos = Z2O.pos[m], tu = UTILE(u), i = UKS(u);
      const v4u a = {ar[u % (LA + 1)][0][0], ar[u % (LA + 1)][0][1], ar[u % (LA + 1)][1][0], ar[u % (LA + 1)][1][1]};
      const f32x4 z4 = {0.f, 0.f, 0.f, 0.f};
      if constexpr (k < 3) acc[k][tu] = mfma(a, wf[k][i], (i == 0 && tu >= 2) ? z4 : acc[k][tu]);
      if constexpr (k == 3) s1[tu] = mfma(ones4, a, i == 0 ? z4 : s1[tu]);
      if constexpr (k == 4) s2[tu] = mfma(a, a, i == 0 ? z4 : s2[tu]);
      SB();
      if constexpr (pos < 2 && u + LA < 40) ar[(u + LA) % (LA + 1)][pos] = AHALF(UTILE(u + LA), UKS(u + LA), pos);
      if constexpr (pos < 2 && u >= 36) ah[0][u - 36][pos] = AHALF(u - 36, 0, pos);
      if constexpr (u < 8 && pos == 0 && u != 4) { if constexpr (u > 0) DMA_W(3, slotB, u); }
      if constexpr (u == 4 && pos == 0) DMA_W(3, slotB, 4);
      if constexpr (u >= 12 && u < 15 && pos == 0) st128<SC1>(mkv, rws, z2o[u - 12], 0);
      if constexpr (m == 23 || m == 28 || m == 55 || m == 70) {
        constexpr int x = m == 23 ? 0 : m == 28 ? 1 : m == 55 ? 2 : 3;
        PINA(s1[x]);
        ldsw<float>(dgo[x >> 1] + (x & 1) * 64, s1[x][0]);
        if constexpr (x < 3) ldsw<f32x4>(dgo[2 + (x >> 1)] + (x & 1) * 256, s2[x]);
      }
      if constexpr (m == 71) ldsw<f32x4>(dgo[3] + 256, s2[3]);
      if constexpr (m == 35) __builtin_amdgcn_s_waitcnt(0x0F70);
      if constexpr (m == 46 || m == 51 || m == 56 || m == 61) {
        constexpr int x = (m - 46) / 5 * 2;
        wq[3][x] = READ_W(8192, x); wq[3][x + 1] = READ_W(8192, x + 1);
      }
      if constexpr (m == 50 || m == 60 || m == 65) ldsw<f32x4>(xw + (m == 50 ? 0 : m == 60 ? 1 : 2) * 2048, acc[m == 50 ? 0 : m == 60 ? 1 : 2][2]);
      if constexpr (m == 38 || m == 41 || m == 44 || m == 47) DMA_W(4, slotA, (m - 38) / 3);
      if constexpr (m == 66) DMA_W(4, slotA, 4);
      if constexpr (m == 68 || m == 72 || m == 78) DMA_W(4, slotA, m == 68 ? 5 : m == 72 ? 6 : 7);
      if constexpr (m == 73 || m == 94) bar();
      if constexpr (m >= 74 && m < 77) acc[m - 74][0] = ldsr<f32x4>(xrd + (m - 74) * 2048);
      if constexpr (m == 82 || m == 85 || m == 88) ldsw<f32x4>(xw + (m - 82) / 3 * 2048 + 1024, acc[(m - 82) / 3][3]);
      if constexpr (m >= 95 && m < 98) acc[m - 95][1] = ldsr<f32x4>(xrd + (m - 95) * 2048 + 1024);
      if constexpr (m == 77) {
#pragma unroll
        for (int f = 0; f < 8; ++f) PINA(wq[3][f]);
      }
      if constexpr (m >= 83 && m < 93 && (m - 83) % 3 != 2) {
        constexpr int x = (m - 83) / 3 * 2 + (m - 83) % 3;
        if constexpr (x < 4) pa[x] = ldsr<float>(rdo[0] + x * 256);
        else pb[x - 4] = ldsr<float>(rdo[1] + (x - 4) * 1024);
      }
      if constexpr (m == 93) pb[3] = ldsr<float>(rdo[1] + 3072);
      if constexpr (m == 98) { pa[0] += pa[1]; PIN(pa[0]); }
      if constexpr (m == 99) { pa[2] += pa[3]; PIN(pa[2]); }
      if constexpr (m == 101) { pb[0] += pb[1]; PIN(pb[0]); }
      if constexpr (m == 102) { pb[2] += pb[3]; PIN(pb[2]); }
      if constexpr (m == 104) { pa[0] += pa[2]; PIN(pa[0]); }
      if constexpr (m == 105) { pb[0] += pb[2]; PIN(pb[0]); }
      if constexpr (m == 107) { pa[0] *= (1.0f / D); PIN(pa[0]); }
      if constexpr (m == 108) { pa[1] = pa[0] * pa[0]; PIN(pa[1]); }
      if constexpr (m == 110) { pb[0] = __builtin_fmaf(pb[0], 1.0f / D, -pa[1]); PIN(pb[0]); }
      if constexpr (m == 111) { pb[0] = __builtin_amdgcn_fmed3f(pb[0], 0.f, 3.0e38f); PIN(pb[0]); }
      if constexpr (m == 113) { pb[0] += EPS; PIN(pb[0]); }
      if constexpr (m == 114) { pb[0] = __builtin_amdgcn_rsqf(pb[0]); PIN(pb[0]); }
      if constexpr (m == 116) { pa[0] = lane < 32 ? 1.0f : -pa[0]; PIN(pa[0]); }
      if constexpr (m == 117) { pb[0] *= pa[0]; PIN(pb[0]); }
      if constexpr (m == 119) ldsw<float>(a_fin, pb[0]);
      if constexpr (m == 120 || m == 122 || m == 123 || m == 125) {
        constexpr int x = m == 120 ? 0 : m == 122 ? 1 : m == 123 ? 2 : 3;
        if constexpr (x & 1) r4[x >> 1] = ldsr<f32x4>(a_fr + (x >> 1) * 64);
        else m4[x >> 1] = ldsr<f32x4>(a_fr + 128 + (x >> 1) * 64);
      }
      if constexpr (m >= 100 && m < 124 && (m - 100) % 3 == 0) DMA_W(5, slotB, (m - 100) / 3);
      if constexpr (m == 47) STAMP(1, 46);
      if constexpr (m == 79) STAMP(1, 47);
      SB();
    });
  }
#pragma unroll
  for (int x = 0; x < 3; ++x) {
    const float y = xrow_sum4(__uint_as_float(cz[1][x][0]), __uint_as_float(cz[1][x][1]), __uint_as_float(cz[1][x][0]), __uint_as_float(cz[1][x][1]));
    auto sw = __builtin_amdgcn_permlane16_swap(__float_as_uint(y), __float_as_uint(y), false, false);
    csb[1][x] = f32x2{__uint_as_float(sw[0]), __uint_as_float(sw[1])};
  }
#pragma unroll
  for (int k = 0; k < 6; ++k) PINA(acc[k >> 1][k & 1]);
  STAMP(1, 6);
  f32x4 z2v[3][2];
  {
    f32x4 rs[2], rq[2];
#pragma unroll
    for (int t = 0; t < 2; ++t) {
#pragma unroll
      for (int k = 0; k < 3; ++k) {
        const f32x4 a = acc[k][t];
        const f32x2 zl = gelu2(fma2(lo2(a), lo2(r4[t]), fma2(lo2(m4[t]), bc2(csb[1][k][0]), bc2(csb[1][k][1]))));
        const f32x2 zh = gelu2(fma2(hi2(a), hi2(r4[t]), fma2(hi2(m4[t]), bc2(csb[1][k][0]), bc2(csb[1][k][1]))));
        z2v[k][t] = f32x4{zl[0], zl[1], zh[0], zh[1]};
        if (k == 0) { rs[t] = z2v[k][t]; rq[t] = z2v[k][t] * z2v[k][t]; }
        else { rs[t] += z2v[k][t]; rq[t] += z2v[k][t] * z2v[k][t]; }
        if (((t * 3 + k) & 1) == 0) {
#pragma unroll
          for (int f = (t * 3 + k) * 4; f < (t * 3 + k) * 4 + 8; ++f) wg[f % 3][f / 3] = LOAD_WP(0, f % 3, f / 3);
        } else {
          PIN(z2v[k][t]);
          SB();
        }
      }
    }
    STAMP(1, 50);
    float v8[8], v4[4], v2[2];
    const bool b3 = c & 8, b2 = c & 4, b1 = c & 2, b0 = c & 1;
#pragma unroll
    for (int r_ = 0; r_ < 4; ++r_) {
      const float a0 = rs[0][r_] + dpp<0x128>(rs[0][r_]), a1 = rs[1][r_] + dpp<0x128>(rs[1][r_]);
      const float q0 = rq[0][r_] + dpp<0x128>(rq[0][r_]), q1 = rq[1][r_] + dpp<0x128>(rq[1][r_]);
      v8[2 * r_] = b3 ? a1 : a0; v8[2 * r_ + 1] = b3 ? q1 : q0;
    }
#pragma unroll
    for (int j = 0; j < 4; ++j) {
      const float x0 = v8[j] + dpp<0x141>(v8[j]), x1 = v8[4 + j] + dpp<0x141>(v8[4 + j]);
      v4[j] = b2 ? x1 : x0;
    }
#pragma unroll
    for (int j = 0; j < 2; ++j) {
      const float x0 = v4[j] + dpp<0x4E>(v4[j]), x1 = v4[2 + j] + dpp<0x4E>(v4[2 + j]);
      v2[j] = b1 ? x1 : x0;
    }
    const float x0 = v2[0] + dpp<0xB1>(v2[0]), x1 = v2[1] + dpp<0xB1>(v2[1]);
    const float outv = b0 ? x1 : x0;
    __builtin_amdgcn_raw_buffer_store_b32(nomark(__float_as_uint(outv)), rws, lane * 4, WS_ST2 + ((mt * 2 + kh) * 16 + nt * 2 + uh) * 256, SC1);
  }
  STAMP(1, 7);
  SB();
#pragma unroll
  for (int tu = 0; tu < 4; ++tu)
#pragma unroll
    for (int k = 0; k < 3; ++k) acc[k][tu] = f32x4{0.f, 0.f, 0.f, 0.f};
  unsigned k3v[6];
  {
    const unsigned k0 = l16 + w * 6144, w3 = (w + 1) >> 2;
    k3v[0] = k0; k3v[1] = k0 + 2048 - w3 * 1024; k3v[2] = k3v[1] + 1024; k3v[3] = k0 + 5120 - w3 * 2048;
    k3v[4] = k0 + 1024 + (w3 << 30); k3v[5] = k3v[4] + 3072;
  }
  v2u x1h[2][2];
  v4u wo[6][4];
#define LOAD_WO(f) wo[(f) >> 2][(f) & 3] = ld128<SC1>(rws, k3v[(f) >> 2], WS_WOT + (nt * 4 + ((f) & 3)) * 22 * 1024)
  auto dma_sel2 = [&](int i, int x) __attribute__((always_inline)) {
    if (i < 4) dma_sel(IC<6>{}, slotA, (i - 2) * 4 + x); else dma_sel(IC<7>{}, slotB, (i - 4) * 4 + x);
  };
  f32x4 pt[4];
  float* c1s = st + 768 + w * 96;
#define LOAD_PT(i) asf(ld128<SC1>(rws, l16 + (i) * 1024, WS_ST2 + (mt * 2 + kh) * 4096))
  SB();
#pragma unroll
  for (int i = 0; i < 8; ++i) {
#pragma unroll
    for (int j = 0; j < 12; ++j) {
      const int tu = j / 3, k = j % 3;
      const v4u a = {ah[i & 1][tu][0][0], ah[i & 1][tu][0][1], ah[i & 1][tu][1][0], ah[i & 1][tu][1][1]};
      acc[k][tu] = mfma(wg[k][i], a, acc[k][tu]);
      SB();
      if (j < 8 && i < 7) ah[(i + 1) & 1][j >> 1][j & 1] = AHALF(j >> 1, i + 1, j & 1);
      if (j < 4 && i == 7) x1h[j >> 1][j & 1] = trread(smem + x1o + (j >> 1) * 4096 + (j & 1) * 512);
      if (i == 0 && j == 0) asm volatile("s_waitcnt vmcnt(24)" ::: "memory");
      if (i < 2 && j >= 4) wq[4 + i][j - 4] = READ_W(i * 8192, j - 4);
      if (i == 3 && j == 0) {
#pragma unroll
        for (int f = 0; f < 16; ++f) PINA(wq[4 + (f >> 3)][f & 7]);
      }
      if (i == 2 && j == 8) USE(wg[2][7]);
      if (i >= 2 && i < 6 && j == 9) dma_sel2(i, 0);
      if (i >= 3 && i < 7 && (j == 1 || j == 4 || j == 7)) dma_sel2(i - 1, (j + 2) / 3);
      if (j >= 8 && i == 6) pt[j - 8] = LOAD_PT(j - 8);
      if (i == 7 && j >= 8) LOAD_WO(j - 8);
      if (i == 5 && j == 10) {
        asm volatile("s_waitcnt vmcnt(30)" ::: "memory");
        __builtin_amdgcn_s_barrier();
      }
      SB();
    }
  }
  STAMP(1, 8);
#pragma unroll
  for (int k = 0; k < 3; ++k)
#pragma unroll
    for (int t = 0; t < 2; ++t) xch[(w * 6 + k * 2 + t) * 64 + lane] = acc[k][2 + t];
  SB();
  {
    unsigned m = 0;
#pragma unroll
    for (int i = 0; i < 4; ++i) m = macc4(m, __builtin_bit_cast(v4u, pt[i]));
    RETRY_BEGIN(m)
#pragma unroll
      for (int i = 0; i < 4; ++i) { pt[i] = LOAD_PT(i); m = macc4(m, __builtin_bit_cast(v4u, pt[i])); }
    RETRY_END(m)
    if (tid < 4) hint_add(p.ws, 64 + (nt * 4 + (mt >> 3)) * 4 + tid);
    const f32x4 sx = (pt[0] + pt[1]) + (pt[2] + pt[3]);
    const float tv = xrow_sum4(sx[0], sx[2], sx[1], sx[3]);
    auto sw = __builtin_amdgcn_permlane32_swap(__float_as_uint(tv), __float_as_uint(tv), false, false);
    const float mm = __uint_as_float(sw[0]) * (1.0f / FH);
    const float r = __builtin_amdgcn_rsqf(fmaxf(__uint_as_float(sw[1]) * (1.0f / FH) - mm * mm, 0.f) + EPS);
    float* f2 = st + 512 + w * 64;
    f2[(q >> 1) * 32 + 2 * c + (q & 1)] = (q >> 1) ? -r * mm : r;
#pragma unroll
    for (int t = 0; t < 2; ++t) { r4[t] = *(const f32x4*)(f2 + q * 8 + t * 4); m4[t] = *(const f32x4*)(f2 + 32 + q * 8 + t * 4); }
#pragma unroll
    for (int k = 0; k < 3; ++k) {
      v4u o;
#pragma unroll
      for (int t = 0; t < 2; ++t) {
        o[2 * t] = nomark(pk2(fma2(lo2(z2v[k][t]), lo2(r4[t]), lo2(m4[t]))));
        o[2 * t + 1] = pk2(fma2(hi2(z2v[k][t]), hi2(r4[t]), hi2(m4[t])));
      }
      st128<SC1>(o, rws, z2o[k], 0);
    }
  }
  STAMP(1, 9);
  c1s[lane] = xrow_sum4(__uint_as_float(cz[0][0][0]), __uint_as_float(cz[0][0][1]), __uint_as_float(cz[0][1][0]), __uint_as_float(cz[0][1][1]));
  c1s[64 + (lane & 31)] = xrow_sum4(__uint_as_float(cz[0][2][0]), __uint_as_float(cz[0][2][1]), __uint_as_float(cz[0][2][0]), __uint_as_float(cz[0][2][1]));
  st[1408 + lane] = rbf((__uint_as_float(gq[0]) + __uint_as_float(gq[1])) + (__uint_as_float(gq[2]) + __uint_as_float(gq[3])));
  SB();
  f32x4 z1f[3][2];
  constexpr int RD = ZRD;
  v4u zf[RD];
#define ZUNIT(ps) ((ps) < 2 ? ut0 + (ps) : (ps) < 4 ? uo0 + (ps) - 2 : (ps) == 4 ? ut2 : uo2)
#define LOAD_Z(n) ld128<SC1>(rws, zv[((n) >> 1) & 7] + ((n) & 1) * 1024, WS_Z2 + (ZUNIT((n) >> 4) * 64 + w * 16) * 1024 + ((n) >= 64 ? oob3 : 0u))
  unsigned zv[8];
#pragma unroll
  for (int i = 0; i < 8; ++i) { zv[i] = l16 + ((CKMAP(i) + kr) & 7) * 2048; PIN(zv[i]); }
  {
    bar();
    STAMP(1, 34);
#pragma unroll
    for (int k = 0; k < 6; ++k) PINA(acc[k >> 1][k & 1]);
    f32x4 oth[3][2], cs1[3], bs1[3];
    float r1[2], m1[2];
#pragma unroll
    for (int t = 0; t < 2; ++t) { r1[t] = fin[t * 16 + c]; m1[t] = fin[32 + t * 16 + c]; }
#pragma unroll
    for (int k = 0; k < 3; ++k) {
      cs1[k] = *(const f32x4*)(c1s + k * 32 + q * 4); bs1[k] = *(const f32x4*)(c1s + k * 32 + 16 + q * 4);
#pragma unroll
      for (int t = 0; t < 2; ++t) oth[k][t] = xch[((w ^ 1) * 6 + k * 2 + t) * 64 + lane];
    }
#pragma unroll
    for (int i = 4; i < WO0; ++i) LOAD_WO(i);
    SB();
    f32x2 z1a[6][2];
#pragma unroll
    for (int i = 0; i < 6; ++i) {
      const int k = i >> 1, t = i & 1;
      const f32x4 a = acc[k][t] + oth[k][t];
      z1a[i][0] = fma2(lo2(a), bc2(r1[t]), fma2(lo2(cs1[k]), bc2(m1[t]), lo2(bs1[k])));
      z1a[i][1] = fma2(hi2(a), bc2(r1[t]), fma2(hi2(cs1[k]), bc2(m1[t]), hi2(bs1[k])));
    }
#pragma unroll
    for (int i = 0; i < GELU_PRE; ++i) {
      const f32x2 lo = gelu2(z1a[i][0]), hi = gelu2(z1a[i][1]);
      z1f[i >> 1][i & 1] = f32x4{lo[0], lo[1], hi[0], hi[1]};
      PIN(z1f[i >> 1][i & 1]);
    }
#pragma unroll
    for (int i = GELU_PRE; i < 6; ++i) { PIN(z1a[i][0]); PIN(z1a[i][1]); }
    SB();
#pragma unroll
    for (int f = 0; f < 16; ++f) wq[6 + (f >> 3)][f & 7] = READ_W((f >> 3) * 8192, f & 7);
    SB();
    STAMP(1, 10);
    hint_wait(rws, 0u, WS_CNT + (64 + (nt * 4 + w) * 4 + (xcd >> 1)) * 128, 8u);
    STAMP(1, 22);
#pragma unroll
    for (int n = 0; n < RD; ++n) zf[n] = LOAD_Z(n);
    SB();
#pragma unroll
    for (int i = GELU_PRE; i < 6; ++i) {
      const f32x2 lo = gelu2(z1a[i][0]), hi = gelu2(z1a[i][1]);
      z1f[i >> 1][i & 1] = f32x4{lo[0], lo[1], hi[0], hi[1]};
      PIN(z1f[i >> 1][i & 1]);
    }
  }
  f32x4* xb = (f32x4*)(smem + LDS_A) + lane;
  f32x4* yb = (f32x4*)(smem + LDS_A + 49152) + lane;
  f32x4 ac2[2][4], ini[4], yo[2];
  const float bs0 = bflo(bspq[0]), bs1_ = bfhi(bspq[0]);
  f32x4 bq0 = {bs0, bs0, bs0, bs0}, bq1 = {bs1_, bs1_, bs1_, bs1_};
  const f32x4 z4 = {0.f, 0.f, 0.f, 0.f};
  PINA(bq0); PINA(bq1);
  unsigned vmk = l16 + WS_V + ((mt * 4 + w) * 22 + pr0) * 1024;
  gu32* p_fl4 = (gu32*)(p.ws + WS_CNT + (192 + mt * 2 + (tid & 1)) * 128);
  PIN(vmk); PIN(mkv);
  unsigned voff[3];
  voff[0] = l16 + WS_V + ((mt * 4 + 2 * kh) * 22 + (uo0 >> 1)) * 1024;
  voff[1] = l16 + WS_V + ((mt * 4 + 2 * kh) * 22 + (uo2 >> 1)) * 1024 + (uo2 & 1) * 8 + oob3;
  voff[2] = voff[0] + 22 * 1024;
  PIN(voff[0]); PIN(voff[1]); PIN(voff[2]);
  float ev[4];
  v4u eo[2];
  f32x4 yx[2][2];
  v4u vs[6][4];
#define LOAD_V(f) vs[(f) >> 2][(f) & 3] = ld128<SC1>(rws, k3v[(f) >> 2], WS_V + (mt * 4 + (((f) & 3) ^ w)) * 22 * 1024)
  SB();
  STAMP(1, 11);
  sfor<6>([&](auto P) __attribute__((always_inline)) {
    constexpr int ps = decltype(P)::value, cur = ps & 1, prv = cur ^ 1;
    constexpr bool prevT = ps == 1 || ps == 2 || ps == 5, epi = ps == 3 || ps == 4;
    unsigned zm;
    sfor<64>([&](auto G) __attribute__((always_inline)) {
      constexpr int g = decltype(G)::value, ks = g >> 2, tu = g & 3, ck = ks >> 1, k2 = ks & 1, n = ps * 16 + ks;
      const f32x4 cin = ks > 0 ? ac2[cur][tu] : (ps == 2 || ps == 3) ? ini[tu] : ps == 5 ? z4 : tu == 0 ? bq0 : tu == 1 ? bq1 : z4;
      ac2[cur][tu] = mfma(zf[n % RD], wq[ck][k2 * 4 + tu], cin);
      SB();
      if constexpr (tu == 1) { zm = ks == 0 ? (zf[n % RD][0] > zf[n % RD][2] ? zf[n % RD][0] : zf[n % RD][2]) : macc(zm, zf[n % RD]); PIN(zm); }
      if constexpr (tu == 3) {
        if constexpr (n + RD < 96) zf[n % RD] = LOAD_Z(n + RD);
        else if constexpr (WO0 + n + RD - 96 < 16) LOAD_WO(WO0 + n + RD - 96);
      }
      if constexpr (prevT && g >= 2 && g < 18 && (g & 3) == 2) xb[((w * 3 + (ps == 5 ? 2 : ps - 1)) * 4 + (g >> 2)) * 64] = ac2[prv][g >> 2];
      if constexpr (epi && (g == 2 || g == 6)) yb[((w * 3 + ps - 3) * 2 + (g >> 2)) * 64] = ac2[prv][2 + (g >> 2)];
      if constexpr (ps >= 1 && g == (epi ? 12 : 24)) {
        if constexpr (ps == 3) asm volatile("s_waitcnt vmcnt(20)" ::: "memory");
        bar();
      }
      if constexpr (epi && (g == 13 || g == 14)) yo[g - 13] = yb[(((w ^ 1) * 3 + ps - 3) * 2 + g - 13) * 64];
      if constexpr (epi && g >= 18 && g < 50 && (g & 1) == 0) {
        constexpr int t = (g - 18) / 16, ph = ((g - 18) % 16) >> 1, h = (ps - 3) * 2;
        if constexpr (ph == 0) PINA(ac2[prv][t]);
        if constexpr (ph < 4) { ev[ph] = ac2[prv][t][ph] + yo[t][ph]; PIN(ev[ph]); }
        else if constexpr (ph < 6) { ev[2 * ph - 8] *= z1f[ps - 3][t][2 * ph - 8]; PIN(ev[2 * ph - 8]); ev[2 * ph - 7] *= z1f[ps - 3][t][2 * ph - 7]; PIN(ev[2 * ph - 7]); }
        else if constexpr (ph == 6) { eo[t][h] = nomark(pk2(ev[0], ev[1])); PIN(eo[t]); }
        else {
          eo[t][h + 1] = pk2(ev[2], ev[3]);
          if constexpr (ps == 4) st128<SC1>(eo[t], rws, voff[0 + 2 * t], 0);
          else PIN(eo[t]);
        }
      }
      if constexpr ((ps == 1 || ps == 2) && g >= 48 && g < 56 && (g & 1) == 0) ini[(g - 48) >> 1] = xb[(((w ^ 2) * 3 + ps - 1) * 4 + ((g - 48) >> 1)) * 64];
      if constexpr (ps == 5 && (g == 25 || g == 26)) { yx[0][g - 25] = xb[(((w ^ 2) * 3 + 2) * 4 + g - 25) * 64]; yx[1][g - 25] = xb[(((w ^ 3) * 3 + 2) * 4 + 2 + g - 25) * 64]; }
      if constexpr (ps == 0 && (g == 22 || g == 38 || g == 54)) st128<SC1>(mkv, rws, vmk + (g - 22) / 16 * 1024, g == 54 ? oob3 : 0u);
      if constexpr (ps * 64 + g == HINT4_G) { if (tid < 2) (void)__hip_atomic_fetch_add(p_fl4, 1u, __ATOMIC_RELAXED, __HIP_MEMORY_SCOPE_AGENT); }
      if constexpr (ps == 2 && g == 40) PIN(p_fl4);
      if constexpr (ps == 5 && g == POLL4_G) flagv = __builtin_amdgcn_raw_buffer_load_b32(rws, 0, WS_CNT + (192 + mt * 2 + (xcd & 1)) * 128, SC1);
      if constexpr (ps == 5 && g == VPRE_G) {
        if (__builtin_expect(__builtin_amdgcn_readfirstlane(flagv) < 8u, 0)) hint_wait(rws, 0u, WS_CNT + (192 + mt * 2 + (xcd & 1)) * 128, 8u);
        MEMFENCE();
      }
      if constexpr (ps == 5 && g > VPRE_G && (g & 1) == 0 && (g - VPRE_G - 1) / 2 < 12) LOAD_V((g - VPRE_G - 1) / 2);
      if constexpr (g == 0 && ps >= 1 && ps < 5) STAMP(1, 55 + ps);
      SB();
    });
    if (__builtin_expect(any_mark(zm), 0)) {
      int l2 = lane;
      PIN(l2);
#pragma unroll
      for (int tu = 0; tu < 4; ++tu) ac2[cur][tu] = (ps == 2 || ps == 3) ? xb[(((w ^ 2) * 3 + ps - 2) * 4 + tu) * 64] : ps == 5 ? z4 : tu == 0 ? bq0 : tu == 1 ? bq1 : z4;
#pragma unroll 1
      for (int ks = 0; ks < 16; ++ks) {
        const int kk = ((CKMAP(ks >> 1) + kr) & 7) * 2 + (ks & 1);
        v4u zz, ww[4];
        unsigned sp = 0;
        do { MEMFENCE(); zz = ld128<SC1>(rws, l2 * 16 + kk * 1024, WS_Z2 + (ZUNIT(ps) * 64 + w * 16) * 1024 + (ps >= 4 ? oob3 : 0u)); } while (any_mark(macc(0u, zz)) & (++sp <= SPIN_CAP));
#pragma unroll
        for (int tu = 0; tu < 4; ++tu) ww[tu] = ld128<0>(rwsp, perm(tu ^ (2 * kh), l2 & 15) * 4096 + (l2 >> 4) * 16 + kk * 64, wsp_s);
#pragma unroll
        for (int tu = 0; tu < 4; ++tu) ac2[cur][tu] = mfma(zz, ww[tu], ac2[cur][tu]);
      }
    }
  });
  STAMP(1, 12);
  {
#pragma unroll
    for (int t = 0; t < 2; ++t) yb[((w * 3 + 2) * 2 + t) * 64] = ac2[1][2 + t];
#pragma unroll
    for (int t = 0; t < 2; ++t) { yx[0][t] = (yx[0][t] + yx[1][t]) + ac2[1][t]; PIN(yx[0][t]); }
    bar();
    STAMP(1, 52);
#pragma unroll
    for (int t = 0; t < 2; ++t) yo[t] = yb[(((w ^ 1) * 3 + 2) * 2 + t) * 64];
#pragma unroll
    for (int t = 0; t < 2; ++t) {
      const f32x4 g = (yx[0][t] + yo[t]) * z1f[2][t];
      st64<SC1>(v2u{nomark(pk2(lo2(g))), pk2(hi2(g))}, rws, voff[1], t * 22 * 1024);
    }
  }
  STAMP(1, 13);
  SB();
  f32x4 gb4[4];
  f32x2 x1f[2][4];
  STAMP(1, 53);
  STAMP(1, 23);
  f32x4 a3[4][4];
  unsigned a_rw, a_rr[3], o_out;

  {
    {
      unsigned m = 0;
      sfor<64>([&](auto G) __attribute__((always_inline)) {
        constexpr int g = decltype(G)::value, ks = g >> 4, tu = (g >> 2) & 3, e = g & 3;
        a3[e][tu] = mfma(wo[ks][e], vs[ks][tu], ks == 0 ? f32x4{0.f, 0.f, 0.f, 0.f} : a3[e][tu]);
        SB();
        if constexpr (e == 0 && g < 16) LOAD_V(12 + (g >> 2));
        if constexpr (e == 2 && g < 32) LOAD_WO(16 + (g >> 2));
        if constexpr (g >= LATE_G && g < LATE_G + 16 && (g & 1) == 0) LOAD_V(16 + (((g - LATE_G) >> 1) & 1) * 4 + ((((g - LATE_G) >> 2) + 1) & 3));
        if constexpr (e == 1) { m = macc(m, vs[ks][tu]); PIN(m); }
        SB();
      });
      if (__builtin_expect(any_mark(m), 0)) {
        sfor<4>([&](auto K) __attribute__((always_inline)) {
          constexpr int ks = decltype(K)::value;
          unsigned sp = 0;
          do {
            MEMFENCE();
            m = 0;
#pragma unroll
            for (int tu = 0; tu < 4; ++tu) { LOAD_V(ks * 4 + tu); m = macc(m, vs[ks][tu]); }
          } while (any_mark(m) & (++sp <= SPIN_CAP));
#pragma unroll
          for (int tu = 0; tu < 4; ++tu)
#pragma unroll
            for (int e = 0; e < 4; ++e) a3[e][tu] = mfma(wo[ks][e], vs[ks][tu], ks == 0 ? f32x4{0.f, 0.f, 0.f, 0.f} : a3[e][tu]);
        });
      }
    }
#pragma unroll
    for (int i = 0; i < 4; ++i) gb4[i] = *(const f32x4*)(st + 1408 + (i >> 1) * 32 + q * 8 + (i & 1) * 4);
#pragma unroll
    for (int i = 0; i < 8; ++i) { x1f[i >> 2][i & 3] = unpk(x1h[i >> 2][(i >> 1) & 1][i & 1]); PIN(x1f[i >> 2][i & 3]); }
    int lz = lane;
    PIN(lz);
    a_rw = lds0 + LDS_W + w * 12288 + lz * 16;
#pragma unroll
    for (int rel = 1; rel < 4; ++rel) { a_rr[rel - 1] = lds0 + LDS_W + ((w ^ rel) * 3 + rel - 1) * 4096 + lz * 16; PIN(a_rr[rel - 1]); }
    o_out = (mt * 64 + perm(w, lz & 15)) * 1024 + (lz >> 4) * 16 + nt * 128;
    PIN(a_rw); PIN(o_out);
    SB();
    unsigned m = 0;
#pragma unroll
    for (int f = 16; f < 24; ++f) m = macc(m, vs[f >> 2][f & 3]);
    RETRY_BEGIN(m)
#pragma unroll
      for (int f = 16; f < 24; ++f) { LOAD_V(f); m = macc(m, vs[f >> 2][f & 3]); }
    RETRY_END(m)
    sfor<32>([&](auto G) __attribute__((always_inline)) {
      constexpr int g = decltype(G)::value, tu = ((g >> 3) + 1) & 3, ks = 4 + ((g >> 2) & 1), e = g & 3;
      a3[e][tu] = mfma(wo[ks][e], vs[ks][tu], a3[e][tu]);
      SB();
      if constexpr (g >= 8 && (g & 1) == 0) ldsw<f32x4>(a_rw + (((g >> 3) - 1) * 4 + ((g >> 1) & 3)) * 1024, a3[(g >> 1) & 3][g >> 3]);
      SB();
    });
  }
  STAMP(1, 14);
  {
#pragma unroll
    for (int e = 0; e < 4; ++e) PIN(a3[e][0]);
    bar();
    STAMP(1, 54);
    f32x4 o3[4][3];
#pragma unroll
    for (int e = 0; e < 4; ++e)
#pragma unroll
      for (int rel = 1; rel < 4; ++rel) o3[e][rel - 1] = ldsr<f32x4>(a_rr[rel - 1] + e * 1024);
    const rsrc_t rout = mkrs(p.out, L * D * 2);
    SB();
#pragma unroll
    for (int pp = 0; pp < 2; ++pp) {
      v4u o;
#pragma unroll
      for (int h = 0; h < 2; ++h) {
        const int e = pp * 2 + h;
        const f32x4 y = (a3[e][0] + o3[e][0]) + (o3[e][1] + o3[e][2]);
        o[2 * h] = pk2(fma2(lo2(y), lo2(gb4[e]), x1f[pp][2 * h]));
        o[2 * h + 1] = pk2(fma2(hi2(y), hi2(gb4[e]), x1f[pp][2 * h + 1]));
      }
      st128<0>(o, rout, o_out + pp * 64, 0);
    }
  }
  if (b == 0 && tid == 0) *(unsigned*)(p.ws + WS_EPOCH) = tag_s;
  STAMP(1, 15);
  STAMP_FLUSH(1);
  asm volatile("adaln_code_end:");
}

extern "C" void kernel_launch(void* const* d_in, const int* in_sizes, int n_in, void* d_out, int out_size, void* d_ws,
                              size_t ws_size, hipStream_t stream) {
  Params p;
  p.u = (const u16*)d_in[0];
  p.lar = (const u16*)d_in[1];
  p.aim = (const u16*)d_in[2];
  p.cre = (const u16*)d_in[3];
  p.cim = (const u16*)d_in[4];
  p.ldt = (const u16*)d_in[5];
  p.dsk = (const u16*)d_in[6];
  p.win = (const u16*)d_in[8];
  p.wsp = (const u16*)d_in[9];
  p.bsp = (const u16*)d_in[10];
  p.wout = (const u16*)d_in[11];
  p.c = (const u16*)d_in[12];
  p.wmod = (const u16*)d_in[13];
  p.out = (u16*)d_out;
  p.ws = (unsigned char*)d_ws;
  (void)hipFuncSetAttribute((const void*)adaln_mega, hipFuncAttributeMaxDynamicSharedMemorySize, MEGA_LDS);
  adaln_mega<<<256, 256, MEGA_LDS, stream>>>(p);
}
```

```cpp
#define INL2 0
#include <hip/hip_runtime.h>
#include <hip/hip_bf16.h>
#include <stdint.h>
#include <utility>

#define LAS __attribute__((address_space(3)))
typedef unsigned v4u __attribute__((ext_vector_type(4)));
typedef unsigned v2u __attribute__((ext_vector_type(2)));
typedef float f32x4 __attribute__((ext_vector_type(4)));
typedef float f32x2 __attribute__((ext_vector_type(2)));
typedef short bf16x8 __attribute__((ext_vector_type(8)));
typedef _Float16 f16x8 __attribute__((ext_vector_type(8)));
typedef _Float16 f16x2 __attribute__((ext_vector_type(2)));
typedef __bf16 bf16x2_t __attribute__((ext_vector_type(2)));
typedef unsigned short u16;
typedef __amdgpu_buffer_rsrc_t rsrc_t;

#define STAMP_DECL do { } while (0)
#define STAMP(k, i) do { } while (0)
#define STAMP_FLUSH(k) do { } while (0)

#define OPT_A 1
#define OPT_B 1
#define OPT_C 2
#define OPT_D 1
#define OPT_E 2
#define OPT_F 1
#define INL1 1
#define GM_SLEEP 0
constexpr int L = 2048, D = 512, NM = 128, FH = 688, FF = 1376;
constexpr float EPS = 1e-5f;
constexpr int SC1 = 16;
constexpr unsigned OOB = 0x40000000u;
constexpr unsigned SPIN_CAP = 1u << 14;

constexpr unsigned WS_EPOCH = 0;
constexpr unsigned WS_CNT = 4096;
constexpr unsigned WS_SR = WS_CNT + 512 * 128;
constexpr unsigned WS_SB = WS_SR + 2048 * 8;
constexpr unsigned WS_MODG = WS_SB + 2048 * 8;
constexpr unsigned WS_CSB = WS_MODG + 3072 * 32;
constexpr unsigned WS_ST2 = WS_CSB + 88 * 16 * 32;
constexpr unsigned WS_UG = 1u << 20;
static_assert(WS_ST2 + 32 * 2 * 16 * 32 * 8 <= WS_UG, "small buffers");
constexpr unsigned WS_X1T = WS_UG + 256 * 2048 * 8;
constexpr unsigned WS_WP = WS_X1T + 512 * 2048 * 2;
constexpr unsigned WS_WOT = WS_WP + 88 * 16 * 1024;
constexpr unsigned WS_Z2 = WS_WOT + 32 * 22 * 1024;
constexpr unsigned WS_V = WS_Z2 + 44 * 64 * 1024;
constexpr unsigned WS_END = WS_V + 128 * 22 * 1024;

struct Params {
  const u16 *u, *lar, *aim, *cre, *cim, *ldt, *dsk, *win, *wsp, *bsp, *wout, *c, *wmod;
  u16* out;
  unsigned char* ws;
};

#define DI __device__ __forceinline__
template <int V> struct IC { static constexpr int value = V; };
template <class F, int... I> DI void sfor_(F&& f, std::integer_sequence<int, I...>) { int d[] = {(f(IC<I>{}), 0)...}; (void)d; }
template <int N, class F> DI void sfor(F&& f) { sfor_(f, std::make_integer_sequence<int, N>{}); }
DI float bf2f(unsigned b) { return __builtin_bit_cast(float, b << 16); }
DI float bflo(unsigned w) { return __builtin_bit_cast(float, w << 16); }
DI float bfhi(unsigned w) { return __builtin_bit_cast(float, w & 0xFFFF0000u); }
DI f32x2 unpk(unsigned w) { return f32x2{bflo(w), bfhi(w)}; }
DI unsigned pk2(f32x2 v) { return __builtin_bit_cast(unsigned, __builtin_convertvector(v, bf16x2_t)); }
DI unsigned pk2(float lo, float hi) { return pk2(f32x2{lo, hi}); }
DI unsigned nomark(unsigned x) { return x < 0xFFFFFFFEu ? x : 0xFFFFFFFEu; }
DI float rbf(float x) { return bflo(pk2(x, 0.f)); }
DI f32x2 fma2(f32x2 a, f32x2 b, f32x2 c) { return __builtin_elementwise_fma(a, b, c); }
DI f32x2 lo2(f32x4 v) { return f32x2{v[0], v[1]}; }
DI f32x2 hi2(f32x4 v) { return f32x2{v[2], v[3]}; }
DI f32x2 bc2(float x) { return f32x2{x, x}; }
DI f32x4 mfma(v4u a, v4u b, f32x4 c) {
  return __builtin_amdgcn_mfma_f32_16x16x32_bf16(__builtin_bit_cast(bf16x8, a), __builtin_bit_cast(bf16x8, b), c, 0, 0, 0);
}
DI f32x4 mfma_h(v4u a, v4u b, f32x4 c) {
  return __builtin_amdgcn_mfma_f32_16x16x32_f16(__builtin_bit_cast(f16x8, a), __builtin_bit_cast(f16x8, b), c, 0, 0, 0);
}
DI float dot2(unsigned a, unsigned b, float acc) {
  return __builtin_amdgcn_fdot2_f32_bf16(__builtin_bit_cast(bf16x2_t, a), __builtin_bit_cast(bf16x2_t, b), acc, false);
}
constexpr unsigned ONES = 0x3F803F80u;
DI int perm(int u, int c) { return 32 * (u >> 1) + 8 * (c >> 2) + 4 * (u & 1) + (c & 3); }
template <int CTRL> DI float dpp(float x) {
  return __builtin_bit_cast(float, __builtin_amdgcn_mov_dpp(__builtin_bit_cast(int, x), CTRL, 0xf, 0xf, true));
}
DI float row16_sum(float x) {
  x += dpp<0xB1>(x); x += dpp<0x4E>(x); x += dpp<0x141>(x); x += dpp<0x128>(x);
  return x;
}
DI float row16_max(float x) {
  x = fmaxf(x, dpp<0xB1>(x)); x = fmaxf(x, dpp<0x4E>(x)); x = fmaxf(x, dpp<0x141>(x)); x = fmaxf(x, dpp<0x128>(x));
  return x;
}
DI float xrow_sum(float x) {
  auto s = __builtin_amdgcn_permlane16_swap(__float_as_uint(x), __float_as_uint(x), false, false);
  x = __uint_as_float(s[0]) + __uint_as_float(s[1]);
  auto t = __builtin_amdgcn_permlane32_swap(__float_as_uint(x), __float_as_uint(x), false, false);
  return __uint_as_float(t[0]) + __uint_as_float(t[1]);
}
DI float xrow_sum4(float a, float b, float c, float d) {
  auto s = __builtin_amdgcn_permlane16_swap(__float_as_uint(a), __float_as_uint(b), false, false);
  auto t = __builtin_amdgcn_permlane16_swap(__float_as_uint(c), __float_as_uint(d), false, false);
  const float x = __uint_as_float(s[0]) + __uint_as_float(s[1]), y = __uint_as_float(t[0]) + __uint_as_float(t[1]);
  auto z = __builtin_amdgcn_permlane32_swap(__float_as_uint(x), __float_as_uint(y), false, false);
  return __uint_as_float(z[0]) + __uint_as_float(z[1]);
}
DI float xrow_max(float x) {
  auto s = __builtin_amdgcn_permlane16_swap(__float_as_uint(x), __float_as_uint(x), false, false);
  x = fmaxf(__uint_as_float(s[0]), __uint_as_float(s[1]));
  auto t = __builtin_amdgcn_permlane32_swap(__float_as_uint(x), __float_as_uint(x), false, false);
  return fmaxf(__uint_as_float(t[0]), __uint_as_float(t[1]));
}
DI f32x2 gelu2(f32x2 x) {
  const float k0 = -2.0f * 0.7978845608028654f * 1.4426950408889634f, k1 = k0 * 0.044715f;
  const f32x2 a = x * fma2(x * x, bc2(k1), bc2(k0));
  const f32x2 e = f32x2{__builtin_amdgcn_exp2f(a[0]), __builtin_amdgcn_exp2f(a[1])} + bc2(1.0f);
  return x * f32x2{__builtin_amdgcn_rcpf(e[0]), __builtin_amdgcn_rcpf(e[1])};
}
DI rsrc_t mkrs(const void* ptr, unsigned bytes) { return __builtin_amdgcn_make_buffer_rsrc((void*)ptr, 0, (int)bytes, 0x00020000); }
template <int AUX> DI v4u ld128(rsrc_t rs, unsigned v, unsigned s) { return __builtin_amdgcn_raw_buffer_load_b128(rs, (int)v, (int)s, AUX); }
template <int AUX> DI v2u ld64(rsrc_t rs, unsigned v, unsigned s) { return __builtin_amdgcn_raw_buffer_load_b64(rs, (int)v, (int)s, AUX); }
template <int AUX> DI void st128(v4u x, rsrc_t rs, unsigned v, unsigned s) { __builtin_amdgcn_raw_buffer_store_b128(x, rs, (int)(v + s), 0, AUX); }
template <int AUX> DI void st64(v2u x, rsrc_t rs, unsigned v, unsigned s) { __builtin_amdgcn_raw_buffer_store_b64(x, rs, (int)v, (int)s, AUX); }
DI f32x4 asf(v4u x) { return __builtin_bit_cast(f32x4, x); }
DI float sum4(f32x4 x) { return (x[0] + x[1]) + (x[2] + x[3]); }
DI unsigned umax3(unsigned a, unsigned b, unsigned c) { unsigned m = a > b ? a : b; return m > c ? m : c; }
DI unsigned macc4(unsigned m, v4u x) { return umax3(umax3(m, x[0], x[1]), x[2], x[3]); }
DI unsigned macc(unsigned m, v4u x) { return umax3(m, x[0], x[2]); }
DI bool any_mark(unsigned m) { return __builtin_amdgcn_ballot_w64(m == 0xFFFFFFFFu) != 0ull; }
#define SB() __builtin_amdgcn_sched_barrier(0)
#define PIN(x) asm volatile("" : "+v"(x))
#define PINA(x) asm volatile("" : "+a"(x))
#define USE(x) asm volatile("" ::"v"(x))
#define RETRY_BEGIN(m) if (__builtin_expect(any_mark(m), INL2)) { unsigned sp_ = 0; do { MEMFENCE(); m = 0;
#define RETRY_END(m) } while (any_mark(m) & (++sp_ <= SPIN_CAP)); }
#define MEMFENCE() asm volatile("" ::: "memory")
DI void bar() { asm volatile("s_waitcnt lgkmcnt(0)" ::: "memory"); __builtin_amdgcn_s_barrier(); asm volatile("" ::: "memory"); }

DI f32x2 cmul(f32x2 a, f32x2 b) {
  f32x2 t = bc2(a[0]) * b;
  asm("v_pk_fma_f32 %0, %1, %2, %0 op_sel:[1,1,0] op_sel_hi:[1,0,1] neg_lo:[0,1,0]" : "+v"(t) : "v"(a), "v"(b));
  return t;
}
DI f32x2 cexp_p(float are2, float rev_hi, float rev_lo, float p) {
  const float mag = __builtin_amdgcn_exp2f(are2 * p);
  const float hi = rev_hi * p;
  const float fr = (hi - __builtin_rintf(hi)) + rev_lo * p;
  return f32x2{mag * __builtin_amdgcn_cosf(fr), mag * __builtin_amdgcn_sinf(fr)};
}
DI float exp_acc(float x) {
  const float hi = x * 1.4426950408889634f;
  const float lo = __builtin_fmaf(x, 1.4426950408889634f, -hi) + x * 1.925963033500011e-8f;
  const float e = __builtin_amdgcn_exp2f(hi);
  return __builtin_fmaf(e, lo * 0.6931471805599453f, e);
}
DI unsigned pkh(float a, float b) {
  f16x2 v = {(_Float16)a, (_Float16)b};
  return __builtin_bit_cast(unsigned, v);
}

constexpr int CV_RK = 4160;
constexpr int CV_H = 2 * CV_RK;
constexpr int CV_CH = CV_H + 4608;
constexpr int LDS_A = 0;
constexpr int LDS_U = 2 * CV_CH;
constexpr int LDS_GS = LDS_U + 8192;
constexpr int LDS_TAB = 36864;
constexpr int KP_CH = 13840;
constexpr int LDS_KP = LDS_TAB + 2 * 64 * 528 + 16;
constexpr int LDS_LN = 0, LDS_MR = 8448;
constexpr int LDS_WI = 145472, LDS_WO = LDS_WI + 10240;
constexpr int LDS_X = 65536;
constexpr int LDS_S = LDS_X + 24576;
constexpr int LDS_W = LDS_S + 6144;
constexpr int MEGA_LDS = LDS_W + 65536 + 2048;
static_assert(LDS_WO + 4608 <= MEGA_LDS && LDS_KP + 2 * KP_CH <= LDS_WI && LDS_S >= 73728, "lds");
static_assert(LDS_GS + 1280 <= LDS_TAB && LDS_KP + 2 * KP_CH <= MEGA_LDS && MEGA_LDS <= 160 * 1024, "lds");
#define FLAG_AT 48
static_assert(FLAG_AT >= 36 && FLAG_AT <= 56 && FLAG_AT % 4 == 0, "flag position");
#define WF_EARLY 5
#define WO0 12
#define WSP_P0 4
#define HINT4_KS 15
#define ZDEPTH 3
#define GELU_IN 0
#define EPI_W 0
#define MOD_POS 0
#define POLL3 0
#define GELU_PRE 4
#define HINT4_G 280
static_assert(HINT4_G > 3 * 64 + 12, "behind the marker barrier");
#define POLL4_G 16
#define RELABEL 1
#define CKMAP(ck) (RELABEL ? 2 * ((ck) & 3) + ((ck) >> 2) : (ck))
#define POLL_AT 130
#define MOD_MID 1
#define LATE_SLEEP 6
#define SMALL_N 3
#define ZRD 12
#define ZLA 4
#define LATE_G 44
#define VPRE_G 37
#define IN_AUX 0
#define WSP_AUX 0

template <class T> DI T ldsr(unsigned a) { return *(const LAS T*)(size_t)a; }
template <class T> DI void ldsw(unsigned a, T v) { *(LAS T*)(size_t)a = v; }
DI v2u trread(const unsigned char* ptr) {
  typedef short v4i16_t __attribute__((ext_vector_type(4)));
  return __builtin_bit_cast(v2u, __builtin_amdgcn_ds_read_tr16_b64_v4i16((LAS v4i16_t*)ptr));
}
DI v2u trread_a(unsigned a) {
  typedef short v4i16_t __attribute__((ext_vector_type(4)));
  return __builtin_bit_cast(v2u, __builtin_amdgcn_ds_read_tr16_b64_v4i16((LAS v4i16_t*)(size_t)a));
}
struct CvOrder { int qq[36], pp[36]; bool first[36]; };
constexpr CvOrder cv_order() {
  CvOrder o{};
  int n = 0;
  for (int m = 0; m < 8; ++m) {
    for (int j = 0; j < m; ++j) if (m + j < 8) { o.qq[n] = m; o.pp[n] = j; ++n; }
    for (int j = 0; j <= m; ++j) if (m + j < 8) { o.qq[n] = j; o.pp[n] = m; ++n; }
  }
  for (int i = 0; i < 36; ++i) {
    o.first[i] = true;
    for (int j = 0; j < i; ++j) if (o.qq[j] + o.pp[j] == o.qq[i] + o.pp[i]) o.first[i] = false;
  }
  return o;
}
constexpr CvOrder CVO = cv_order();
struct Z2Order { int u[128], kind[128], pos[128]; };
constexpr Z2Order z2_order() {
  Z2Order o{};
  int n = 0;
  for (int u = 0; u < 40; ++u) {
    int pos = 0;
    const bool st = u < 24 && (u < 8 || (u & 7) < 4);
    for (int k = 0; k < 5; ++k)
      if (k < 3 ? u >= 8 : st) { o.u[n] = u; o.kind[n] = k; o.pos[n] = pos++; ++n; }
  }
  return o;
}
constexpr Z2Order Z2O = z2_order();

typedef __attribute__((address_space(1))) unsigned gu32;
DI unsigned tx(unsigned m, unsigned x, unsigned tag) { return __builtin_amdgcn_bitop3_b32(m, x, tag, 0xF6); }
DI unsigned tacc(unsigned m, v4u x, unsigned tag) { return tx(tx(m, x[1], tag), x[3], tag); }
DI bool any_nz(unsigned m) { return __builtin_amdgcn_ballot_w64(m != 0u) != 0ull; }
DI void hint_add(unsigned char* ws, int line) { (void)__hip_atomic_fetch_add((gu32*)(ws + WS_CNT + line * 128), 1u, __ATOMIC_RELAXED, __HIP_MEMORY_SCOPE_AGENT); }
DI void hint_wait(rsrc_t rws, unsigned voff, unsigned soff, unsigned want) {
  unsigned sp = 0;
  for (;;) {
    MEMFENCE();
    const unsigned v = __builtin_amdgcn_raw_buffer_load_b32(rws, (int)voff, (int)soff, SC1);
    if (!((__builtin_amdgcn_ballot_w64(v < want) != 0ull) & (++sp <= SPIN_CAP))) break;
    __builtin_amdgcn_s_sleep(OPT_E);
  }
  MEMFENCE();
}

extern "C" __global__ void __launch_bounds__(256, 1) adaln_mega(Params p) {
  extern __shared__ __attribute__((aligned(16))) unsigned char smem[];
  const int tid = threadIdx.x, lane = tid & 63, w = __builtin_amdgcn_readfirstlane(tid >> 6);
  const int b = blockIdx.x, xcd = b & 7, idx = b >> 3;
  const int pb = xcd * 32 + idx;
  const unsigned l16 = lane * 16;
  STAMP_DECL;
  STAMP(1, 0);
  v2u codepf;
  {
    unsigned long long pc;
    int rel_m, rel_e;
    asm volatile("s_getpc_b64 %0\n\ts_mov_b32 %1, adaln_mega@rel32@lo+4\n\ts_mov_b32 %2, adaln_code_end@rel32@lo+12" : "=s"(pc), "=s"(rel_m), "=s"(rel_e));
    const rsrc_t rcode = mkrs((const void*)(pc + (long long)rel_m), rel_e > rel_m ? (unsigned)(rel_e - rel_m) & ~15u : 0u);
    codepf = ld64<0>(rcode, tid * 8, idx * 2048);
  }
  SB();
  const rsrc_t ru = mkrs(p.u, L * D * 2);
  v4u xr[2];
  xr[0] = ld128<IN_AUX>(ru, l16, (pb * 8 + w * 2) * 1024);
  xr[1] = ld128<IN_AUX>(ru, l16 + 1024, (pb * 8 + w * 2) * 1024);
  const int mkg = b & 3, mcg = b >> 2;
  const unsigned cq = p.c[mkg * 128 + (tid & 3) * 32 + (tid >> 3)];
  const unsigned epoch = *(const unsigned*)(p.ws + WS_EPOCH);
  const rsrc_t rws = mkrs(p.ws, WS_END);
  __builtin_amdgcn_raw_buffer_store_b32(0u, rws, tid < 2 ? tid * 32768u : OOB, WS_CNT + b * 128, SC1);
  SB();
  const int cch = w >> 1, eh = w & 1, ns = tid & 127;
  const unsigned q_cre = p.cre[pb * 256 + tid], q_cim = p.cim[pb * 256 + tid], q_lar = p.lar[pb * 256 + tid], q_aim = p.aim[pb * 256 + tid];
  const unsigned q_ldt = p.ldt[pb * 2 + cch], q_dsk = p.dsk[pb * 2 + cch];
  v4u xm[4];
#define LOAD_XM() { \
  const rsrc_t rmod = mkrs(p.wmod, D * 3072 * 2); \
_Pragma("unroll") \
  for (int i = 0; i < 4; ++i) xm[i] = ld128<IN_AUX>(rmod, (tid >> 3) * 6144 + (tid & 7) * 16, (mkg * 128 + i * 32) * 6144 + mcg * 128 + (b < 192 ? 0u : OOB)); }
  SB();
  LOAD_XM();
  SB();
  STAMP(1, 24);
  const int c = lane & 15, q = lane >> 4;
  const unsigned lds0 = (unsigned)(size_t)(LAS unsigned char*)smem;
  unsigned pos4[9];
  {
    const float f4 = (float)(8188 - tid * 32);
#pragma unroll
    for (int j = 0; j < 10; j += 2) {
      const unsigned pp = pk2(bc2(f4) - f32x2{4.f * j, 4.f * j + 4.f});
      const int p0 = (int)bflo(pp), p1 = (int)bfhi(pp);
      pos4[j] = p0 + ((p0 >> 5) << 2) + lds0 + LDS_KP;
      PIN(pos4[j]);
      if (j + 1 < 9) { pos4[j + 1] = p1 + ((p1 >> 5) << 2) + lds0 + LDS_KP; PIN(pos4[j + 1]); }
    }
  }
  unsigned co[2][4];
  {
    const unsigned b0 = (eh ? 1152 + 576 * q : 144 * (q >> 1) + 4 * (q & 1)) * 4 + 36 * c + lds0 + LDS_KP + cch * KP_CH;
    const unsigned b1 = (q < 2 ? 288 + 144 * q : 568 + 4 * q) * 4 + 36 * c + lds0 + LDS_KP + cch * KP_CH;
    const unsigned s0 = eh ? 576 : 4, s1 = q < 2 ? 8 : 576;
#pragma unroll
    for (int r = 0; r < 4; ++r) { co[0][r] = b0 + r * s0; co[1][r] = b1 + r * s1; PIN(co[0][r]); PIN(co[1][r]); }
  }
  *(unsigned*)(smem + LDS_KP - 16 + cch * KP_CH + (lane & 3) * 4) = 0u;
  unsigned a_pad, a_wi, a_wo, a_tab, gm_off, gc_off;
  a_pad = lds0 + cch * CV_CH + (lane < 32 ? CV_H + l16 : ((lane >> 1) & 1) * CV_RK + 4096 + (lane & 1) * 16);
  a_wi = lds0 + LDS_WI + (w * 32 + 8 * q + ((lane >> 2) & 3)) * 80 + (lane & 3) * 16;
  a_wo = lds0 + LDS_WO + (8 * q + ((lane >> 2) & 3)) * 144 + (w >> 1) * 64 + (lane & 3) * 16 + (w & 1) * 8;
  a_tab = lds0 + LDS_TAB + cch * (64 * 528) + c * 528 + q * 16;
  gm_off = ((4 - cch) * 512 + mkg * 128 + ns) * 8;
  gc_off = (((lane & 7) > 5 ? 0 : ((lane & 7) >> 1)) * 512 + pb * 2 + (lane & 1)) * 8;
  PIN(a_pad); PIN(a_wi); PIN(a_wo); PIN(a_tab); PIN(gm_off); PIN(gc_off);
  unsigned ab[4], bb, dv[4], rdo4[4], xoff;
#pragma unroll
  for (int i = 0; i < 4; ++i) {
    ab[i] = lds0 + cch * CV_CH + (1 - (c & 1)) * CV_RK + 2 * (2030 - 2 * (c >> 1) + 8 * q) - eh * 256 - 192 - 512 - 1024 * i;
    dv[i] = (lane >> 3) * 4096 + (((lane & 7) ^ ((lane >> 4) & 1) ^ (i << 1)) * 16) + i * (32768 - 1024);
    rdo4[i] = lds0 + LDS_W + w * 16384 + ((perm((i & 1) * 2 ^ (2 * (w & 1)), c) * 128 + ((q ^ (c >> 1)) * 16)) ^ ((i >> 1) * 64));
    PIN(ab[i]); PIN(dv[i]); PIN(rdo4[i]);
  }
  bb = lds0 + cch * CV_CH + CV_H + 2 * (256 - 16 + 16 * c + 8 * q) - eh * 256 - 192;
  xoff = WS_X1T + (pb * 2 + cch) * 4096 + eh * 2048 + ((c >> 1) * 32 + ((q & 1) * 4 + 2 * (c & 1) + (q >> 1)) * 4) * 2;
  PIN(bb); PIN(xoff);
  SB();
  STAMP(1, 26);
  {
    *(v4u*)(smem + LDS_LN + (w * 2) * 1040 + l16) = xr[0];
    *(v4u*)(smem + LDS_LN + (w * 2 + 1) * 1040 + l16) = xr[1];
    float sm[2], sq[2];
#pragma unroll
    for (int i = 0; i < 2; ++i) {
      sm[i] = dot2(xr[i][0], ONES, dot2(xr[i][1], ONES, dot2(xr[i][2], ONES, dot2(xr[i][3], ONES, 0.f))));
      sq[i] = dot2(xr[i][0], xr[i][0], dot2(xr[i][1], xr[i][1], dot2(xr[i][2], xr[i][2], dot2(xr[i][3], xr[i][3], 0.f))));
    }
    const float tv = row16_sum(xrow_sum4(sm[0], sm[1], sq[0], sq[1]));
    auto sw = __builtin_amdgcn_permlane32_swap(__float_as_uint(tv), __float_as_uint(tv), false, false);
    const float m = __uint_as_float(sw[0]) * (1.0f / D);
    const float r = __builtin_amdgcn_rsqf(fmaxf(__uint_as_float(sw[1]) * (1.0f / D) - m * m, 0.f) + EPS);
    ((float*)(smem + LDS_GS + 1088))[(q >> 1) * 8 + w * 2 + (q & 1)] = (q >> 1) ? -m * r : r;
  }
  const unsigned tag = epoch * 2654435761u + 0x9E3779B9u;
  STAMP(1, 28);
  asm volatile("s_waitcnt vmcnt(%0)" ::"i"(MOD_POS == 0 ? 8 : 4) : "memory");
  bar();
  STAMP(1, 16);
  {
    unsigned ua[4][2];
#pragma unroll
    for (int i = 0; i < 4; ++i) {
      const unsigned char* src = smem + LDS_LN + (tid & 3) * 2080 + (i * 64 + (tid >> 2)) * 4;
      ua[i][0] = *(const unsigned*)src; ua[i][1] = *(const unsigned*)(src + 1040);
    }
    const unsigned lnv = ((const unsigned*)(smem + LDS_GS + 1088))[c];
    SB();
#pragma unroll
    for (int i = 0; i < 4; ++i) st128<SC1>(v4u{ua[i][0], tag, ua[i][1], tag}, rws, (tid >> 2) * 16384 + (tid & 3) * 16, WS_UG + i * 64 * 16384 + pb * 64);
    st64<SC1>(v2u{lnv, tag}, rws, tid < 16 ? (c >> 3) * 16384 + (c & 7) * 8 : OOB, WS_SR + pb * 64);
  }
  STAMP(1, 17);
  SB();
  const unsigned wp_oob = b < 176 ? 0u : OOB;
  const int wpr = b >> 2, wkg = b & 3;
  v4u xi[2], xo;
  {
    const rsrc_t rwin = mkrs(p.win, D * FF * 2), rwout = mkrs(p.wout, FH * D * 2);
    const int hf = wpr >= 22, pp = wpr - 22 * hf;
    const unsigned v = ((tid >> 2) * (FF * 2) + (tid & 3) * 16) | ((pp == 21 ? tid & 2 : 0) << 29);
#pragma unroll
    for (int i = 0; i < 2; ++i) xi[i] = ld128<IN_AUX>(rwin, v, (wkg * 128 + i * 64) * (FF * 2) + (hf * FH + pp * 32) * 2 + wp_oob);
    xo = ld128<IN_AUX>(rwout, (tid >> 3) * 1024 + (tid & 7) * 16, idx * 32 * 1024 + xcd * 128);
  }
  SB();
  const int d0 = pb * 2, dk = d0 + cch;
  const int mt = (xcd >> 1) * 8 + (idx & 7), nt = (xcd & 1) * 4 + (idx >> 3);
  const int big = (nt + 2) >> 3;
  const int np = 3 - big, pr0 = 3 * nt - big * (nt - 6);
  const int kh = w & 1, uh = w >> 1;
  const int uo0 = pr0 * 2 + uh * (4 - 2 * big), ut0 = pr0 * 2 + (uh ^ 1) * (4 - 2 * big), uo2 = pr0 * 2 + 2 + uh, ut2 = pr0 * 2 + 2 + (uh ^ 1);
#define UO(k) ((k) == 2 ? uo2 : uo0 + (k))
#define UT(k) ((k) == 2 ? ut2 : ut0 + (k))
  const unsigned oob3 = (unsigned)big << 30;
  const int kr = 2 * (idx >> 3) + (xcd & 1) + (OPT_B == 0 ? (idx & 7) : OPT_B == 1 ? 4 * (idx & 1) : OPT_B == 2 ? 2 * (idx & 3) : 0);
  const rsrc_t rwsp = mkrs(p.wsp, L * L * 2);
  const unsigned wp_fr = (wpr * 2 * 16 + wkg * 4 + w) * 1024 + wp_oob;
  v4u wr[1][8];
  const unsigned wsp_v = (lane >> 3) * 4096 + (lane & 7) * 16, wsp_s = mt * 64 * 4096 + w * 1024;
#define LOAD_WSP1(ck, i) wr[ck][i] = ld128<WSP_AUX>(rwsp, wsp_v + (((ck) + kr) & 7) * 128, wsp_s + (i) * 8 * 4096)
#define LOAD_WSPN(n) LOAD_WSP1((n) >> 3, (n) & 7)
  const v4u mk = {~0u, ~0u, ~0u, ~0u};
  unsigned bspq[2];
#pragma unroll
  for (int t = 0; t < 2; ++t) bspq[t] = p.bsp[mt * 64 + perm(2 * kh + t, c)];
  const unsigned tag_s = __builtin_amdgcn_readfirstlane(tag);
  {
    const float dt = rbf(exp_acc(bf2f(q_ldt)));
    const float cre = bf2f(q_cre) * dt, cim = bf2f(q_cim) * dt;
    const float are2 = -rbf(exp_acc(bf2f(q_lar))) * dt * 1.4426950408889634f;
    const float aim = bf2f(q_aim) * dt;
    const float rev_hi = aim * 0.15915494309189535f;
    const float rev_lo = __builtin_fmaf(aim, 0.15915494309189535f, -rev_hi) + aim * 6.4206383e-9f;
    int ex = __builtin_amdgcn_frexp_expf(xrow_max(row16_max(fmaxf(fabsf(cre), fabsf(cim))))) + 1;
    ex = ex < -100 ? -100 : ex;
    const float scale = __builtin_amdgcn_ldexpf(1.f, -ex);
    ((float*)(smem + LDS_GS + 1024))[w] = __builtin_amdgcn_ldexpf(2.f, ex);
    const f32x2 c0 = {cre * scale, cim * scale};
    const f32x2 e1 = cexp_p(are2, rev_hi, rev_lo, 1.f), e8 = cexp_p(are2, rev_hi, rev_lo, 8.f), e64 = cexp_p(are2, rev_hi, rev_lo, 64.f);
    unsigned char* tn = smem + LDS_TAB + cch * (64 * 528) + ns * 4;
#define TROW(r, z) *(unsigned*)(tn + (r) * 528) = pkh((z)[0], -(z)[1])
    SB();
    const f32x2 e128 = cmul(e64, e64), e2 = cmul(e1, e1), e256 = cmul(e128, e128), e4 = cmul(e2, e2), e512 = cmul(e256, e256);
    f32x2 f0 = {1.f, 0.f}, f1 = e64;
    f32x2 ca = cmul(c0, e512), cb = cmul(ca, e512);
#pragma unroll
    for (int j = 0; j < 8; ++j) {
      *(unsigned*)(tn + (48 + j) * 528) = pkh(f0[0], f0[1]);
      *(unsigned*)(tn + (56 + j) * 528) = pkh(f1[0], f1[1]);
      TROW(32 + j, cb);
      f0 = j == 0 ? e8 : cmul(f0, e8); f1 = cmul(f1, e8); cb = cmul(cb, e128);
      if (j < 4) {
        const f32x2 z = cmul(ca, e4);
        TROW(24 + j, ca); TROW(28 + j, z);
        ca = cmul(ca, e128);
      }
      if (j == 3 || j == 7) {
        PIN(f0); PIN(f1); PIN(cb);
        if (WSP_P0 >= 4) { LOAD_WSPN(j >> 2 << 1); LOAD_WSPN((j >> 2 << 1) + 1); }
        SB();
      }
    }
    TROW(40, cb);
    SB();
  {
    const float cv = bf2f(cq);
    const float sl = rbf(cv * __builtin_amdgcn_rcpf(1.0f + __builtin_amdgcn_exp2f(cv * -1.4426950408889634f)));
    const float sc[4] = {dpp<0x00>(sl), dpp<0x55>(sl), dpp<0xAA>(sl), dpp<0xFF>(sl)};
    f32x2 a[4];
#pragma unroll
    for (int i = 0; i < 4; ++i)
#pragma unroll
      for (int j = 0; j < 4; ++j) a[j] = i == 0 ? unpk(xm[i][j]) * bc2(sc[i]) : fma2(unpk(xm[i][j]), bc2(sc[i]), a[j]);
    float* red = (float*)(smem + LDS_MR) + (tid >> 3) * 68 + (tid & 7) * 8;
    *(f32x4*)red = f32x4{a[0][0], a[0][1], a[1][0], a[1][1]};
    *(f32x4*)(red + 4) = f32x4{a[2][0], a[2][1], a[3][0], a[3][1]};
  }
    bar();
    float ms8[8];
#pragma unroll
    for (int i = 0; i < 8; ++i) ms8[i] = ((const float*)(smem + LDS_MR))[(q * 8 + i) * 68 + w * 16 + c];
    SB();
    f32x2 t1[8];
    t1[1] = e1; t1[2] = e2; t1[4] = e4;
    t1[3] = cmul(e2, e1); t1[5] = cmul(e4, e1); t1[6] = cmul(e2, e4); t1[7] = cmul(t1[6], e1);
    const f32x2 c1 = cmul(c0, e128), c2 = cmul(c0, e256), c3 = cmul(c2, e128);
    TROW(0, c0); TROW(8, c1); TROW(16, c2); TROW(20, c3);
    {
    const float s = xrow_sum(((ms8[0] + ms8[1]) + (ms8[2] + ms8[3])) + ((ms8[4] + ms8[5]) + (ms8[6] + ms8[7])));
    st64<SC1>(v2u{__float_as_uint(s), tag}, rws, q == 0 ? c * 8 : OOB, WS_MODG + (mkg * 3072 + mcg * 64 + w * 16) * 8 + (b < 192 ? 0u : OOB));
    }
    SB();
#pragma unroll
    for (int v = 1; v < 8; ++v) {
      const f32x2 z0 = cmul(t1[v], c0), z1 = cmul(t1[v], c1);
      TROW(v, z0); TROW(8 + v, z1);
      if ((v & 1) == 0) {
        const f32x2 z2 = cmul(t1[v], c2), z3 = cmul(t1[v], c3);
        TROW(16 + v / 2, z2); TROW(20 + v / 2, z3);
        if (v < 6 && WSP_P0 >= 8) { LOAD_WSPN(v + 2); LOAD_WSPN(v + 3); }
        SB();
      }
    }
  }
  STAMP(1, 27);
#pragma unroll
  for (int i = 0; i < 2; ++i) *(v4u*)(smem + LDS_WI + (i * 64 + (tid >> 2)) * 80 + (tid & 3) * 16) = xi[i];
  *(v4u*)(smem + LDS_WO + (tid >> 3) * 144 + (tid & 7) * 16) = xo;
  STAMP(1, 29);
  bar();
  STAMP(1, 18);
  SB();
  v4u gr[4], gb_[4];
  v2u gm[4], gc[4];
#define LOAD_GS() _Pragma("unroll") for (int i_ = 0; i_ < 4; ++i_) { gr[i_] = ld128<SC1>(rws, tid * 16, WS_SR + i_ * 4096); gb_[i_] = ld128<SC1>(rws, tid * 16, WS_SB + i_ * 4096); }
#define LOAD_GM() _Pragma("unroll") for (int i_ = 0; i_ < 4; ++i_) { gm[i_] = ld64<SC1>(rws, gm_off, WS_MODG + i_ * 24576); gc[i_] = ld64<SC1>(rws, gc_off, WS_MODG + i_ * 24576); }
  v4u ug[4], wt[2];
#define LOAD_UG() _Pragma("unroll") for (int i_ = 0; i_ < 4; ++i_) ug[i_] = ld128<SC1>(rws, tid * 16, WS_UG + pb * 16384 + i_ * 4096)
  {
    v4u fa[2][8], fb[8];
    const unsigned ta = a_tab + eh * (32 * 528);
#pragma unroll
    for (int ks = 0; ks < 3; ++ks) { fb[ks] = ldsr<v4u>(a_tab + 48 * 528 + ks * 64); fa[0][ks] = ldsr<v4u>(ta + ks * 64); fa[1][ks] = ldsr<v4u>(a_tab + 16 * 528 + ks * 64); }
    SB();
    LOAD_UG();
    SB();
    f32x4 kk[2][2];
    f32x2 us;
    v2u wh_[6];
#pragma unroll
    for (int i = 0; i < 4; ++i) kk[i >> 1][i & 1] = f32x4{0.f, 0.f, 0.f, 0.f};
#pragma unroll
    for (int ks = 0; ks < 8; ++ks) {
      if (ks < 5) { fb[3 + ks] = ldsr<v4u>(a_tab + 48 * 528 + (3 + ks) * 64); fa[0][3 + ks] = ldsr<v4u>(ta + (3 + ks) * 64); fa[1][3 + ks] = ldsr<v4u>(a_tab + 16 * 528 + (3 + ks) * 64); }
      if (ks == 5) { us = *(const f32x2*)(smem + LDS_GS + 1024 + cch * 8); wh_[4] = trread_a(a_wo); wh_[5] = trread_a(a_wo + 576); }
      if (ks == 6) { wh_[0] = trread_a(a_wi); wh_[1] = trread_a(a_wi + 320); wh_[2] = trread_a(a_wi + 8); wh_[3] = trread_a(a_wi + 328); }
      kk[0][ks >> 2] = mfma_h(fa[0][ks], fb[ks], kk[0][ks >> 2]); kk[1][ks >> 2] = mfma_h(fa[1][ks], fb[ks], kk[1][ks >> 2]);
      SB();
    }
    STAMP(1, 37);
    wt[0] = v4u{wh_[0][0], wh_[0][1], wh_[1][0], wh_[1][1]};
    wt[1] = v4u{wh_[2][0], wh_[2][1], wh_[3][0], wh_[3][1]};
    st128<SC1>(v4u{wh_[4][0], wh_[4][1], wh_[5][0], wh_[5][1]}, rws, l16, WS_WOT + ((xcd * 4 + w) * 22 + idx) * 1024 + wp_oob);
    LOAD_GS();
    SB();
#pragma unroll
    for (int i = 0; i < 4; ++i) PINA(kk[i >> 1][i & 1]);
    f32x4 k0 = kk[0][0] * us[0] + kk[0][1] * us[1];
    const f32x4 k1 = kk[1][0] * us[0] + kk[1][1] * us[1];
    k0[0] += ns == 0 ? bf2f(q_dsk) : 0.f;
#pragma unroll
    for (int r = 0; r < 4; ++r) { ldsw<float>(co[0][r], k0[r]); ldsw<float>(co[1][r], k1[r]); }
  }
  STAMP(1, 31);
  bar();
  STAMP(1, 19);
  {
    float v[2][9];
#pragma unroll
    for (int j = 0; j < 9; ++j) v[0][j] = ldsr<float>(pos4[j]);
    LOAD_GM();
#pragma unroll
    for (int j = 0; j < 9; ++j) v[1][j] = ldsr<float>(pos4[j] + KP_CH);
    ldsw<v4u>(a_pad, v4u{0u, 0u, 0u, 0u});
    SB();
    STAMP(1, 36);
#pragma unroll
    for (int ch = 0; ch < 2; ++ch) {
      unsigned char* rk = smem + ch * CV_CH + tid * 16;
#pragma unroll
      for (int sft = 0; sft < 2; ++sft)
        *(v4u*)(rk + sft * CV_RK) = v4u{pk2(v[ch][sft], v[ch][sft + 1]), pk2(v[ch][sft + 2], v[ch][sft + 3]), pk2(v[ch][sft + 4], v[ch][sft + 5]), pk2(v[ch][sft + 6], v[ch][sft + 7])};
    }
#pragma unroll
    for (int i = WSP_P0; i < 8; ++i) LOAD_WSPN(i);
    st128<SC1>(mk, rws, l16, WS_X1T + dk * 4096 + eh * 2048);
    st128<SC1>(mk, rws, l16 + 1024, WS_X1T + dk * 4096 + eh * 2048);
    __builtin_amdgcn_raw_buffer_store_b32(~0u, rws, lane * 4, WS_ST2 + ((mt * 2 + kh) * 16 + nt * 2 + uh) * 256, SC1);
    SB();
    STAMP(1, 32);
    unsigned m = 0;
#pragma unroll
    for (int i = 0; i < 4; ++i) { m = tacc(m, ug[i], tag); m = tacc(m, gr[i], tag); m = tacc(m, gb_[i], tag); }
    if (__builtin_expect(any_nz(m), INL1)) {
      unsigned sp = 0;
      do {
        MEMFENCE(); LOAD_UG(); LOAD_GS(); m = 0;
#pragma unroll
        for (int i = 0; i < 4; ++i) { m = tacc(m, ug[i], tag); m = tacc(m, gr[i], tag); m = tacc(m, gb_[i], tag); }
      } while (any_nz(m) & (++sp <= SPIN_CAP));
    }
    f32x2 nn[4][2];
#pragma unroll
    for (int i = 0; i < 4; ++i) {
      nn[i][0] = fma2(unpk(ug[i][0]), bc2(__uint_as_float(gr[i][0])), bc2(__uint_as_float(gb_[i][0])));
      nn[i][1] = fma2(unpk(ug[i][2]), bc2(__uint_as_float(gr[i][2])), bc2(__uint_as_float(gb_[i][2])));
      PIN(nn[i][0]); PIN(nn[i][1]);
      *(unsigned*)(smem + LDS_U + i * 1024 + tid * 4) = __builtin_amdgcn_perm(ug[i][2], ug[i][0], 0x05040100u);
      *(unsigned*)(smem + LDS_U + 4096 + i * 1024 + tid * 4) = __builtin_amdgcn_perm(ug[i][2], ug[i][0], 0x07060302u);
    }
    SB();
    STAMP(1, 33);
    m = 0;
#pragma unroll
    for (int i = 0; i < 4; ++i) m = tx(tx(m, gm[i][1], tag), gc[i][1], tag);
    if (__builtin_expect(any_nz(m), INL1)) {
      unsigned sp = 0;
      do {
        __builtin_amdgcn_s_sleep(GM_SLEEP);
        MEMFENCE(); LOAD_GM(); m = 0;
#pragma unroll
        for (int i = 0; i < 4; ++i) m = tx(tx(m, gm[i][1], tag), gc[i][1], tag);
      } while (any_nz(m) & (++sp <= SPIN_CAP));
    }
    const float mv = rbf((__uint_as_float(gm[0][0]) + __uint_as_float(gm[1][0])) + (__uint_as_float(gm[2][0]) + __uint_as_float(gm[3][0])));
    *(u16*)(smem + LDS_GS + tid * 2) = (u16)pk2(mv + (cch ? 0.f : 1.0f), 0.f);
    const float cv = rbf((__uint_as_float(gc[0][0]) + __uint_as_float(gc[1][0])) + (__uint_as_float(gc[2][0]) + __uint_as_float(gc[3][0])));
    const float gav = dpp<0x104>(cv), gav2 = dpp<0x102>(cv);
    const float hbv = cv * gav, hsv = rbf(1.0f + cv) * gav2;
    const f32x2 hb = {__builtin_bit_cast(float, __builtin_amdgcn_readlane(__builtin_bit_cast(int, hbv), 0)), __builtin_bit_cast(float, __builtin_amdgcn_readlane(__builtin_bit_cast(int, hbv), 1))};
    const f32x2 hs = {__builtin_bit_cast(float, __builtin_amdgcn_readlane(__builtin_bit_cast(int, hsv), 2)), __builtin_bit_cast(float, __builtin_amdgcn_readlane(__builtin_bit_cast(int, hsv), 3))};
    SB();
    bspq[0] |= bspq[1] << 16;
    PIN(bspq[0]);
    STAMP(1, 38);
#pragma unroll
    for (int i = 0; i < 4; ++i) {
      const f32x2 h0 = fma2(nn[i][0], hs, hb), h1 = fma2(nn[i][1], hs, hb);
      *(unsigned*)(smem + CV_H + 512 + i * 1024 + tid * 4) = pk2(h0[0], h1[0]);
      *(unsigned*)(smem + CV_CH + CV_H + 512 + i * 1024 + tid * 4) = pk2(h0[1], h1[1]);
    }
  }
  asm volatile("" ::"v"(codepf));
  STAMP(1, 39);
  bar();
  STAMP(1, 1);
#define CV_A(ee, k) ({ const unsigned a_ = ab[(k) >> 1] + 192 - 64 * (ee) + 512 * (1 - ((k) & 1)); v4u{ldsr<unsigned>(a_), ldsr<unsigned>(a_ + 4), ldsr<unsigned>(a_ + 8), ldsr<unsigned>(a_ + 12)}; })
#define CV_B(ee, k) ldsr<v4u>(bb + 192 - 64 * (ee) + 512 * (k))
  const unsigned slotA = LDS_W + w * 16384, slotB = slotA + 8192;
  v4u wq[8][8];
#define READ_W(dl, f) ldsr<v4u>(rdo4[(((f) >> 1) & 1) + ((f) >> 2) * 2] + (dl) + ((f) & 1) * 512)
  unsigned dso;
#define DMA_W(ck, slot, i) do { \
    if (((i) & 3) == 0) asm volatile("s_add_u32 m0, %1, %2\n\ts_add_u32 %0, %3, %4\n\tbuffer_load_dwordx4 %5, %6, %0 offen lds" \
                 : "=&s"(dso) : "s"(lds0 + (slot)), "i"((i) * 1024), "s"(wsp_s + ((CKMAP(ck) + kr) & 7) * 128), "i"((i) * 32768), "v"(dv[0]), "s"(rwsp) : "scc", "memory"); \
    else asm volatile("buffer_load_dwordx4 %0, %1, %2 offen offset:%3 lds" :: "v"(dv[(i) & 3]), "s"(rwsp), "s"(dso), "i"(((i) & 3) * 1024) : "memory"); } while (0)
  auto dma_sel = [&](auto CK, unsigned slot, int i) __attribute__((always_inline)) {
    constexpr int ck = decltype(CK)::value;
    switch (i) {
      case 0: DMA_W(ck, slot, 0); break; case 1: DMA_W(ck, slot, 1); break; case 2: DMA_W(ck, slot, 2); break; case 3: DMA_W(ck, slot, 3); break;
      case 4: DMA_W(ck, slot, 4); break; case 5: DMA_W(ck, slot, 5); break; case 6: DMA_W(ck, slot, 6); break; default: DMA_W(ck, slot, 7); break;
    }
  };
  v4u cA[2][8], cB[2][8];
  v4u ones4 = {ONES, ONES, ONES, ONES};
  PIN(ones4);
  f32x4 cb4[4];
  {
    const v4u gbf = *(const v4u*)(smem + LDS_GS + w * 64 + q * 16), sbf = *(const v4u*)(smem + LDS_GS + 256 + w * 64 + q * 16);
#pragma unroll
    for (int k = 0; k < 4; ++k) { cA[0][k] = CV_A(0, k); cB[0][k] = CV_B(0, k); }
    SB();
    const f32x4 z4 = {0.f, 0.f, 0.f, 0.f};
#pragma unroll
    for (int e = 0; e < 2; ++e) {
      v4u o;
#pragma unroll
      for (int jj = 0; jj < 4; ++jj) o[jj] = pk2(unpk(wt[e][jj]) * unpk(gbf[jj]));
      st128<SC1>(o, rws, l16 + e * 16384, WS_WP + wp_fr);
      cb4[2 * e + 1] = mfma(sbf, wt[e], z4);
      cb4[2 * e] = mfma(ones4, o, z4);
    }
  }
  SB();
  f32x4 cacc[8];
  v2u ue[4];
  float uf[16];
  unsigned flagv;
  unsigned wro[4], a_cp, a_ue, o_csb, o_flag;
  gu32* p_flag = (gu32*)(p.ws + WS_CNT + (256 + (tid & 7) * 16 + (idx & 15)) * 128);
  {
    f32x2 cp[4];
    float s0, s1, s2, s3;
    sfor<144>([&](auto G) __attribute__((always_inline)) {
      constexpr int g = decltype(G)::value, ee = g / 36, j = g % 36, t = CVO.pp[j] + CVO.qq[j];
      constexpr bool first = ee == 0 && CVO.first[j];
      cacc[t] = mfma(cA[ee & 1][CVO.qq[j]], cB[ee & 1][CVO.pp[j]], first ? f32x4{0.f, 0.f, 0.f, 0.f} : cacc[t]);
      SB();
      if constexpr (g < 8) {
        if constexpr (g & 1) cB[0][4 + (g >> 1)] = CV_B(0, 4 + (g >> 1));
        else cA[0][4 + (g >> 1)] = CV_A(0, 4 + (g >> 1));
      }
      constexpr int jn = ee == 0 ? (j >= 12 && (j - 12) % 3 != 2 ? (j - 12) / 3 * 2 + (j - 12) % 3 : -1) : (j & 1) ? j >> 1 : -1;
      if constexpr (ee < 3 && jn >= 0 && jn < 16) {
        if constexpr (jn & 1) cB[(ee + 1) & 1][jn >> 1] = CV_B(ee + 1, jn >> 1);
        else cA[(ee + 1) & 1][jn >> 1] = CV_A(ee + 1, jn >> 1);
      }
      if constexpr (g == 8) { a_cp = lds0 + LDS_S + 4096 + c * 16; PIN(a_cp); }
      if constexpr (g == 9) { cb4[0][1] = cb4[1][0]; cb4[0][2] = cb4[2][0]; PINA(cb4[0]); }
      if constexpr (g == 10) { cb4[0][3] = cb4[3][0]; PINA(cb4[0]); }
      if constexpr (g == 11) ldsw<f32x4>(a_cp + w * 256, cb4[0]);
      if constexpr (g == 17) { a_cp += (q & 1) * 8; PIN(a_cp); }
      if constexpr (g == 20) { o_csb = q * 512 + c * 8; PIN(o_csb); }
      if constexpr (g == 23) { o_csb = tid < 32 ? o_csb : OOB; PIN(o_csb); }
      if constexpr (g == 26) { PIN(p_flag); }
      if constexpr (g == FLAG_AT) {
        STAMP(1, 42);
        asm volatile("s_waitcnt vmcnt(%0) lgkmcnt(8)" ::"i"(FLAG_AT > 38 ? 4 + (FLAG_AT - 38 + 3) / 4 : 4) : "memory");
        __builtin_amdgcn_s_barrier();
        if (tid < 8) (void)__hip_atomic_fetch_add(p_flag, 1u, __ATOMIC_RELAXED, __HIP_MEMORY_SCOPE_AGENT);
        STAMP(1, 43);
      }
      if constexpr (g == FLAG_AT + 4 || g == FLAG_AT + 8) {
        cp[(g - FLAG_AT - 4) / 2] = ldsr<f32x2>(a_cp + (g - FLAG_AT - 4) * 128);
        cp[(g - FLAG_AT - 4) / 2 + 1] = ldsr<f32x2>(a_cp + (g - FLAG_AT - 4) * 128 + 256);
      }
      if constexpr (g == FLAG_AT + 16) { s0 = cp[0][0] + cp[1][0]; PIN(s0); s2 = cp[0][1] + cp[1][1]; PIN(s2); }
      if constexpr (g == FLAG_AT + 20) { s1 = cp[2][0] + cp[3][0]; PIN(s1); s3 = cp[2][1] + cp[3][1]; PIN(s3); }
      if constexpr (g == FLAG_AT + 24) { s0 += s1; PIN(s0); s2 += s3; PIN(s2); }
      if constexpr (g == FLAG_AT + 28) st64<SC1>(v2u{__float_as_uint(s0), __float_as_uint(s2)}, rws, o_csb, WS_CSB + wpr * 1024 + wkg * 128 + wp_oob);
      if constexpr (g == 36) STAMP(1, 40);
      if constexpr (g == 72) STAMP(1, 41);
      if constexpr (g == 14 || g == 29 || g == 32 || g == 35) DMA_W(1, slotA, g == 14 ? 0 : (g - 26) / 3);
      if constexpr (g >= 38 && g < 86 && (g & 3) == 2) DMA_W(1 + (g + 10) / 64, g < 54 ? slotA : slotB, (4 + (g - 38) / 4) & 7);
      if constexpr (g == 88) asm volatile("s_waitcnt vmcnt(8)" ::: "memory");
      if constexpr (g >= 90 && g < 106 && (g & 1) == 0) wq[1][(g - 90) >> 1] = READ_W(0, (g - 90) >> 1);
      if constexpr (g >= 68 && g < 72) { wro[g - 68] = (lds0 + slotA + (lane >> 3) * 128 + (((lane & 7) ^ ((lane >> 4) & 1)) * 16)) ^ ((g - 68) * 32); PIN(wro[g - 68]); }
      if constexpr (g == 105) { a_ue = lds0 + LDS_U + cch * 4096 + eh * 2048 + c * 32 + q * 8; PIN(a_ue); }
      if constexpr (g == 106 || g == 107) { ue[(g - 106) * 2] = ldsr<v2u>(a_ue + (g - 106) * 1024); ue[(g - 106) * 2 + 1] = ldsr<v2u>(a_ue + (g - 106) * 1024 + 512); }
      if constexpr (g >= 108 && g < 140 && (g & 3) == 0) ldsw<v4u>(wro[((g - 108) >> 2) & 3] + ((g - 108) >> 2) * 1024, wr[0][(g - 108) >> 2]);
      if constexpr (g >= 109 && g < 125 && (g & 1) == 1) {
        constexpr int x = (g - 109) >> 1;
        if constexpr (x >= 2) { uf[2 * (x - 2)] = bflo(ue[(x - 2) >> 1][x & 1]); PIN(uf[2 * (x - 2)]); uf[2 * (x - 2) + 1] = bfhi(ue[(x - 2) >> 1][x & 1]); PIN(uf[2 * (x - 2) + 1]); }
      }
      if constexpr (g == 118 || g == 122) { constexpr int x = 6 + (g == 122); uf[2 * x] = bflo(ue[3][x & 1]); PIN(uf[2 * x]); uf[2 * x + 1] = bfhi(ue[3][x & 1]); PIN(uf[2 * x + 1]); }
      if constexpr (g == 110) { o_flag = w == 0 ? c * 128 : OOB; PIN(o_flag); }
      if constexpr (g == 126) asm volatile("s_waitcnt vmcnt(0)" ::: "memory");
      if constexpr (g >= 127 && g < 143 && (g & 1) == 1) wq[2][(g - 127) >> 1] = READ_W(8192, (g - 127) >> 1);
      if constexpr (g == POLL_AT) flagv = __builtin_amdgcn_raw_buffer_load_b32(rws, o_flag, WS_CNT + (256 + xcd * 16) * 128, SC1);
      SB();
    });
  }
  STAMP(1, 2);
  unsigned late;
  {
    f32x4* red = (f32x4*)(smem + LDS_X) + w * 256 + lane;
    f32x4 keep[4];
    if (eh) {
#pragma unroll
      for (int t = 0; t < 4; ++t) red[t * 64] = cacc[t];
      SB();
#pragma unroll
      for (int t = 0; t < 4; ++t) { PINA(cacc[4 + t]); keep[t] = cacc[4 + t]; PIN(keep[t]); }
    } else {
#pragma unroll
      for (int t = 0; t < 4; ++t) red[t * 64] = cacc[4 + t];
      SB();
#pragma unroll
      for (int t = 0; t < 4; ++t) { PINA(cacc[t]); keep[t] = cacc[t]; PIN(keep[t]); }
    }
    f32x2 pre[4][2];
#pragma unroll
    for (int t = 0; t < 4; ++t) {
      pre[t][0] = f32x2{uf[4 * t], uf[4 * t + 1]} + lo2(keep[t]);
      pre[t][1] = f32x2{uf[4 * t + 2], uf[4 * t + 3]} + hi2(keep[t]);
      PIN(pre[t][0]); PIN(pre[t][1]);
    }
    if (w == 0) *(unsigned*)(smem + LDS_S) = __builtin_amdgcn_ballot_w64(flagv < 16u) != 0ull;
    bar();
    late = *(const unsigned*)(smem + LDS_S);
    const f32x4* rd = (const f32x4*)(smem + LDS_X) + (w ^ 1) * 256 + lane;
    f32x4 oth[4];
#pragma unroll
    for (int t = 0; t < 4; ++t) oth[t] = rd[t * 64];
#pragma unroll
    for (int t = 0; t < 4; ++t) {
      v2u o;
      o[0] = nomark(pk2(pre[t][0] + lo2(oth[t])));
      o[1] = pk2(pre[t][1] + hi2(oth[t]));
      st64<SC1>(o, rws, xoff + t * 512, 0);
    }
  }
  STAMP(1, 3);
  SB();
  v4u wf[3][8];
  const unsigned rot = uh * 4;
#define LOAD_WP(half, k, i) ld128<SC1>(rws, l16 + ((i) & 3) * 1024, WS_WP + (((half) * 44 + UO(k)) * 16 + kh * 8 + (((i) & 4) ^ rot)) * 1024 + ((k) == 2 ? oob3 : 0u))
#define LOAD_XS(i) ld128<SC1>(rws, wsp_v, WS_X1T + ((i) * 32 + w * 8) * 4096 + mt * 128)
  v4u xs[16];
  unsigned tb[2][4], xdst;
  SB();
  int x1o;
  unsigned z2o[3], dgo[4], rdo[2], xw, xrd, a_fin, a_fr;
  v4u mkv = mk;
  v2u cz[2][3];
  f32x2 csb[2][3];
  unsigned gq[4];
#define LOAD_CZ(hf, kc) ld64<SC1>(rws, lane * 8, WS_CSB + ((hf) * 44 + UO(kc)) * 512 + ((kc) == 2 ? oob3 : 0u))
  {
    STAMP(1, 20);
    if (__builtin_amdgcn_readfirstlane(late)) {
      if (w == 0) { hint_wait(rws, c * 128, WS_CNT + (256 + xcd * 16) * 128, 16u); __builtin_amdgcn_s_sleep(LATE_SLEEP); }
      __builtin_amdgcn_s_barrier();
    }
    MEMFENCE();
    STAMP(1, 21);
#pragma unroll
    for (int i = 0; i < 3; ++i)
#pragma unroll
      for (int k = 0; k < 3; ++k) wf[k][i] = LOAD_WP(1, k, i);
    SB();
#pragma unroll
  for (int f = 0; f < 8; ++f) wq[0][f] = READ_W(0, f);
#pragma unroll
  for (int f = 0; f < 24; ++f) PINA(wq[f >> 3][f & 7]);
    int lz = lane;
    PIN(lz);
  {
    const int row = 8 * (lz >> 4) + ((lz >> 2) & 3);
    const int sg = ((row >> 1) & 1) | (((row >> 3) & 1) << 1);
#pragma unroll
    for (int tu = 0; tu < 4; ++tu) {
      const unsigned t0 = lds0 + LDS_A + kh * 32768 + row * 128 + ((tu ^ (2 * kh) ^ sg) * 32) + (lz & 3) * 8;
      tb[0][tu] = t0 + uh * 16384;
      tb[1][tu] = t0 + (uh ^ 1) * 16384;
    }
    const int rl = w * 8 + (lz >> 3), chn = lz & 7;
    const int sg2 = ((rl >> 1) & 1) | (((rl >> 3) & 1) << 1);
    xdst = lds0 + LDS_A + rl * 128 + (((chn >> 1) ^ sg2) * 32) + (chn & 1) * 16;
  }
  {
    const int row = 8 * (lz >> 4) + ((lz >> 2) & 3);
    const int sg = ((row >> 1) & 1) | (((row >> 3) & 1) << 1);
    x1o = LDS_A + nt * 8192 + row * 128 + ((w ^ sg) * 32) + (lz & 3) * 8;
  }
#pragma unroll
  for (int k = 0; k < 3; ++k) { z2o[k] = lz * 16 + WS_Z2 + (UO(k) * 64 + mt * 2 + kh) * 1024 + (k == 2 ? oob3 : 0u); }
#pragma unroll
    for (int tu = 0; tu < 8; ++tu) PIN(tb[tu >> 2][tu & 3]);
    PIN(xdst); PIN(x1o);
    {
      const int c2 = lz & 15;
      const bool dg = (c2 >> 2) == (lz >> 4);
      const unsigned dump = lds0 + LDS_W + 65536 + lz * 16;
      dgo[0] = dg ? lds0 + LDS_S + (w * 64 + kh * 32 + c2) * 4 : dump; dgo[1] = dg ? lds0 + LDS_S + (w * 64 + (kh ^ 1) * 32 + c2) * 4 : dump;
      dgo[2] = dg ? lds0 + LDS_S + 2048 + (w * 64 + kh * 32 + c2) * 16 : dump; dgo[3] = dg ? lds0 + LDS_S + 2048 + (w * 64 + (kh ^ 1) * 32 + c2) * 16 : dump;
      rdo[0] = lds0 + LDS_S + (kh * 32 + (lz & 31)) * 4; rdo[1] = lds0 + LDS_S + 2048 + (kh * 32 + (lz & 31)) * 16 + (lz & 3) * 4;
      xw = lds0 + LDS_X + w * 6144 + lz * 16; xrd = lds0 + LDS_X + (w ^ 1) * 6144 + lz * 16;
      a_fin = lds0 + LDS_S + 1024 + w * 256 + lz * 4; a_fr = lds0 + LDS_S + 1024 + w * 256 + (lz >> 4) * 16;
      PIN(dgo[0]); PIN(dgo[1]); PIN(dgo[2]); PIN(dgo[3]); PIN(rdo[0]); PIN(rdo[1]); PIN(xw); PIN(xrd); PIN(a_fin); PIN(a_fr); PIN(mkv);
    }
#pragma unroll
    for (int k = 0; k < 3; ++k) PIN(z2o[k]);
    SB();
#pragma unroll
    for (int i = 3; i < WF_EARLY; ++i)
#pragma unroll
      for (int k = 0; k < 3; ++k) wf[k][i] = LOAD_WP(1, k, i);
    SB();
#pragma unroll
    for (int i = 0; i < 16; ++i) xs[i] = LOAD_XS(i);
    SB();
    STAMP(1, 44);
    unsigned m = 0;
#pragma unroll
    for (int i = 0; i < 16; ++i) {
      m = macc(m, xs[i]); ldsw<v4u>(xdst + i * 4096, xs[i]);
      if (WF_EARLY * 3 + i < 24) wf[(WF_EARLY * 3 + i) % 3][(WF_EARLY * 3 + i) / 3] = LOAD_WP(1, (WF_EARLY * 3 + i) % 3, (WF_EARLY * 3 + i) / 3);
      SB();
    }
    PIN(m);
    STAMP(1, 45);
    RETRY_BEGIN(m)
#pragma unroll
      for (int i = 0; i < 16; ++i) xs[i] = LOAD_XS(i);
      SB();
#pragma unroll
      for (int i = 0; i < 16; ++i) { m = macc(m, xs[i]); ldsw<v4u>(xdst + i * 4096, xs[i]); }
    RETRY_END(m)
  }
  STAMP(1, 4);
  bar();
  STAMP(1, 5);
#define AHALF(tu, i, h) trread_a(tb[(i) >> 2][tu] + ((i) & 3) * 4096 + (h) * 512)
  f32x4 acc[3][4];
  v4u wg[3][8];
  v2u ah[2][4][2];
  float* st = (float*)(smem + LDS_S);
  float* fin = st + 256 + w * 64;
  f32x4* xch = (f32x4*)(smem + LDS_X);
  f32x4 r4[2], m4[2];
  {
    f32x4 s1[4], s2[4];
    constexpr int LA = ZLA;
    v2u ar[LA + 1][2];
    float pa[4], pb[4];
#define UTILE(u) ((u) < 8 ? (u) >> 2 : (((u) >> 3) + 1) & 3)
#define UKS(u) ((u) < 8 ? (u) & 3 : (u) & 7)
#pragma unroll
    for (int u = 0; u < LA; ++u) { ar[u][0] = AHALF(UTILE(u), UKS(u), 0); ar[u][1] = AHALF(UTILE(u), UKS(u), 1); }
#pragma unroll
    for (int i = 0; i < 6; ++i) cz[i / 3][i % 3] = LOAD_CZ(i / 3, i % 3);
#pragma unroll
    for (int i = 0; i < 4; ++i) gq[i] = __builtin_amdgcn_raw_buffer_load_b32(rws, lane * 8, WS_MODG + (i * 3072 + 5 * 512 + nt * 64) * 8, SC1);
    DMA_W(3, slotB, 0);
    SB();
    sfor<128>([&](auto G) __attribute__((always_inline)) {
      constexpr int m = decltype(G)::value, u = Z2O.u[m], k = Z2O.kind[m], pos = Z2O.pos[m], tu = UTILE(u), i = UKS(u);
      const v4u a = {ar[u % (LA + 1)][0][0], ar[u % (LA + 1)][0][1], ar[u % (LA + 1)][1][0], ar[u % (LA + 1)][1][1]};
      const f32x4 z4 = {0.f, 0.f, 0.f, 0.f};
      if constexpr (k < 3) acc[k][tu] = mfma(a, wf[k][i], (i == 0 && tu >= 2) ? z4 : acc[k][tu]);
      if constexpr (k == 3) s1[tu] = mfma(ones4, a, i == 0 ? z4 : s1[tu]);
      if constexpr (k == 4) s2[tu] = mfma(a, a, i == 0 ? z4 : s2[tu]);
      SB();
      if constexpr (pos < 2 && u + LA < 40) ar[(u + LA) % (LA + 1)][pos] = AHALF(UTILE(u + LA), UKS(u + LA), pos);
      if constexpr (pos < 2 && u >= 36) ah[0][u - 36][pos] = AHALF(u - 36, 0, pos);
      if constexpr (u < 8 && pos == 0 && u != 4) { if constexpr (u > 0) DMA_W(3, slotB, u); }
      if constexpr (u == 4 && pos == 0) DMA_W(3, slotB, 4);
      if constexpr (u >= 12 && u < 15 && pos == 0) st128<SC1>(mkv, rws, z2o[u - 12], 0);
      if constexpr (m == 23 || m == 28 || m == 55 || m == 70) {
        constexpr int x = m == 23 ? 0 : m == 28 ? 1 : m == 55 ? 2 : 3;
        PINA(s1[x]);
        ldsw<float>(dgo[x >> 1] + (x & 1) * 64, s1[x][0]);
        if constexpr (x < 3) ldsw<f32x4>(dgo[2 + (x >> 1)] + (x & 1) * 256, s2[x]);
      }
      if constexpr (m == 71) ldsw<f32x4>(dgo[3] + 256, s2[3]);
      if constexpr (m == 35) __builtin_amdgcn_s_waitcnt(0x0F70);
      if constexpr (m == 46 || m == 51 || m == 56 || m == 61) {
        constexpr int x = (m - 46) / 5 * 2;
        wq[3][x] = READ_W(8192, x); wq[3][x + 1] = READ_W(8192, x + 1);
      }
      if constexpr (m == 50 || m == 60 || m == 65) ldsw<f32x4>(xw + (m == 50 ? 0 : m == 60 ? 1 : 2) * 2048, acc[m == 50 ? 0 : m == 60 ? 1 : 2][2]);
      if constexpr (m == 38 || m == 41 || m == 44 || m == 47) DMA_W(4, slotA, (m - 38) / 3);
      if constexpr (m == 66) DMA_W(4, slotA, 4);
      if constexpr (m == 68 || m == 72 || m == 78) DMA_W(4, slotA, m == 68 ? 5 : m == 72 ? 6 : 7);
      if constexpr (m == 73 || m == 94) bar();
      if constexpr (m >= 74 && m < 77) acc[m - 74][0] = ldsr<f32x4>(xrd + (m - 74) * 2048);
      if constexpr (m == 82 || m == 85 || m == 88) ldsw<f32x4>(xw + (m - 82) / 3 * 2048 + 1024, acc[(m - 82) / 3][3]);
      if constexpr (m >= 95 && m < 98) acc[m - 95][1] = ldsr<f32x4>(xrd + (m - 95) * 2048 + 1024);
      if constexpr (m == 77) {
#pragma unroll
        for (int f = 0; f < 8; ++f) PINA(wq[3][f]);
      }
      if constexpr (m >= 83 && m < 93 && (m - 83) % 3 != 2) {
        constexpr int x = (m - 83) / 3 * 2 + (m - 83) % 3;
        if constexpr (x < 4) pa[x] = ldsr<float>(rdo[0] + x * 256);
        else pb[x - 4] = ldsr<float>(rdo[1] + (x - 4) * 1024);
      }
      if constexpr (m == 93) pb[3] = ldsr<float>(rdo[1] + 3072);
      if constexpr (m == 98) { pa[0] += pa[1]; PIN(pa[0]); }
      if constexpr (m == 99) { pa[2] += pa[3]; PIN(pa[2]); }
      if constexpr (m == 101) { pb[0] += pb[1]; PIN(pb[0]); }
      if constexpr (m == 102) { pb[2] += pb[3]; PIN(pb[2]); }
      if constexpr (m == 104) { pa[0] += pa[2]; PIN(pa[0]); }
      if constexpr (m == 105) { pb[0] += pb[2]; PIN(pb[0]); }
      if constexpr (m == 107) { pa[0] *= (1.0f / D); PIN(pa[0]); }
      if constexpr (m == 108) { pa[1] = pa[0] * pa[0]; PIN(pa[1]); }
      if constexpr (m == 110) { pb[0] = __builtin_fmaf(pb[0], 1.0f / D, -pa[1]); PIN(pb[0]); }
      if constexpr (m == 111) { pb[0] = __builtin_amdgcn_fmed3f(pb[0], 0.f, 3.0e38f); PIN(pb[0]); }
      if constexpr (m == 113) { pb[0] += EPS; PIN(pb[0]); }
      if constexpr (m == 114) { pb[0] = __builtin_amdgcn_rsqf(pb[0]); PIN(pb[0]); }
      if constexpr (m == 116) { pa[0] = lane < 32 ? 1.0f : -pa[0]; PIN(pa[0]); }
      if constexpr (m == 117) { pb[0] *= pa[0]; PIN(pb[0]); }
      if constexpr (m == 119) ldsw<float>(a_fin, pb[0]);
      if constexpr (m == 120 || m == 122 || m == 123 || m == 125) {
        constexpr int x = m == 120 ? 0 : m == 122 ? 1 : m == 123 ? 2 : 3;
        if constexpr (x & 1) r4[x >> 1] = ldsr<f32x4>(a_fr + (x >> 1) * 64);
        else m4[x >> 1] = ldsr<f32x4>(a_fr + 128 + (x >> 1) * 64);
      }
      if constexpr (m >= 100 && m < 124 && (m - 100) % 3 == 0) DMA_W(5, slotB, (m - 100) / 3);
      if constexpr (m == 47) STAMP(1, 46);
      if constexpr (m == 79) STAMP(1, 47);
      SB();
    });
  }
#pragma unroll
  for (int x = 0; x < 3; ++x) {
    const float y = xrow_sum4(__uint_as_float(cz[1][x][0]), __uint_as_float(cz[1][x][1]), __uint_as_float(cz[1][x][0]), __uint_as_float(cz[1][x][1]));
    auto sw = __builtin_amdgcn_permlane16_swap(__float_as_uint(y), __float_as_uint(y), false, false);
    csb[1][x] = f32x2{__uint_as_float(sw[0]), __uint_as_float(sw[1])};
  }
#pragma unroll
  for (int k = 0; k < 6; ++k) PINA(acc[k >> 1][k & 1]);
  STAMP(1, 6);
  f32x4 z2v[3][2];
  {
    f32x4 rs[2], rq[2];
#pragma unroll
    for (int t = 0; t < 2; ++t) {
#pragma unroll
      for (int k = 0; k < 3; ++k) {
        const f32x4 a = acc[k][t];
        const f32x2 zl = gelu2(fma2(lo2(a), lo2(r4[t]), fma2(lo2(m4[t]), bc2(csb[1][k][0]), bc2(csb[1][k][1]))));
        const f32x2 zh = gelu2(fma2(hi2(a), hi2(r4[t]), fma2(hi2(m4[t]), bc2(csb[1][k][0]), bc2(csb[1][k][1]))));
        z2v[k][t] = f32x4{zl[0], zl[1], zh[0], zh[1]};
        if (k == 0) { rs[t] = z2v[k][t]; rq[t] = z2v[k][t] * z2v[k][t]; }
        else { rs[t] += z2v[k][t]; rq[t] += z2v[k][t] * z2v[k][t]; }
        if (((t * 3 + k) & 1) == 0) {
#pragma unroll
          for (int f = (t * 3 + k) * 4; f < (t * 3 + k) * 4 + 8; ++f) wg[f % 3][f / 3] = LOAD_WP(0, f % 3, f / 3);
        } else {
          PIN(z2v[k][t]);
          SB();
        }
      }
    }
    STAMP(1, 50);
    float v8[8], v4[4], v2[2];
    const bool b3 = c & 8, b2 = c & 4, b1 = c & 2, b0 = c & 1;
#pragma unroll
    for (int r_ = 0; r_ < 4; ++r_) {
      const float a0 = rs[0][r_] + dpp<0x128>(rs[0][r_]), a1 = rs[1][r_] + dpp<0x128>(rs[1][r_]);
      const float q0 = rq[0][r_] + dpp<0x128>(rq[0][r_]), q1 = rq[1][r_] + dpp<0x128>(rq[1][r_]);
      v8[2 * r_] = b3 ? a1 : a0; v8[2 * r_ + 1] = b3 ? q1 : q0;
    }
#pragma unroll
    for (int j = 0; j < 4; ++j) {
      const float x0 = v8[j] + dpp<0x141>(v8[j]), x1 = v8[4 + j] + dpp<0x141>(v8[4 + j]);
      v4[j] = b2 ? x1 : x0;
    }
#pragma unroll
    for (int j = 0; j < 2; ++j) {
      const float x0 = v4[j] + dpp<0x4E>(v4[j]), x1 = v4[2 + j] + dpp<0x4E>(v4[2 + j]);
      v2[j] = b1 ? x1 : x0;
    }
    const float x0 = v2[0] + dpp<0xB1>(v2[0]), x1 = v2[1] + dpp<0xB1>(v2[1]);
    const float outv = b0 ? x1 : x0;
    __builtin_amdgcn_raw_buffer_store_b32(nomark(__float_as_uint(outv)), rws, lane * 4, WS_ST2 + ((mt * 2 + kh) * 16 + nt * 2 + uh) * 256, SC1);
  }
  STAMP(1, 7);
  SB();
#pragma unroll
  for (int tu = 0; tu < 4; ++tu)
#pragma unroll
    for (int k = 0; k < 3; ++k) acc[k][tu] = f32x4{0.f, 0.f, 0.f, 0.f};
  unsigned k3v[6];
  {
    const unsigned k0 = l16 + w * 6144, w3 = (w + 1) >> 2;
    k3v[0] = k0; k3v[1] = k0 + 2048 - w3 * 1024; k3v[2] = k3v[1] + 1024; k3v[3] = k0 + 5120 - w3 * 2048;
    k3v[4] = k0 + 1024 + (w3 << 30); k3v[5] = k3v[4] + 3072;
  }
  v2u x1h[2][2];
  v4u wo[6][4];
#define LOAD_WO(f) wo[(f) >> 2][(f) & 3] = ld128<SC1>(rws, k3v[(f) >> 2], WS_WOT + (nt * 4 + ((f) & 3)) * 22 * 1024)
  auto dma_sel2 = [&](int i, int x) __attribute__((always_inline)) {
    if (i < 4) dma_sel(IC<6>{}, slotA, (i - 2) * 4 + x); else dma_sel(IC<7>{}, slotB, (i - 4) * 4 + x);
  };
  f32x4 pt[4];
  float* c1s = st + 768 + w * 96;
#define LOAD_PT(i) asf(ld128<SC1>(rws, l16 + (i) * 1024, WS_ST2 + (mt * 2 + kh) * 4096))
  SB();
#pragma unroll
  for (int i = 0; i < 8; ++i) {
#pragma unroll
    for (int j = 0; j < 12; ++j) {
      const int tu = j / 3, k = j % 3;
      const v4u a = {ah[i & 1][tu][0][0], ah[i & 1][tu][0][1], ah[i & 1][tu][1][0], ah[i & 1][tu][1][1]};
      acc[k][tu] = mfma(wg[k][i], a, acc[k][tu]);
      SB();
      if (j < 8 && i < 7) ah[(i + 1) & 1][j >> 1][j & 1] = AHALF(j >> 1, i + 1, j & 1);
      if (j < 4 && i == 7) x1h[j >> 1][j & 1] = trread(smem + x1o + (j >> 1) * 4096 + (j & 1) * 512);
      if (i == 0 && j == 0) asm volatile("s_waitcnt vmcnt(24)" ::: "memory");
      if (i < 2 && j >= 4) wq[4 + i][j - 4] = READ_W(i * 8192, j - 4);
      if (i == 3 && j == 0) {
#pragma unroll
        for (int f = 0; f < 16; ++f) PINA(wq[4 + (f >> 3)][f & 7]);
      }
      if (i == 2 && j == 8) USE(wg[2][7]);
      if (i >= 2 && i < 6 && j == 9) dma_sel2(i, 0);
      if (i >= 3 && i < 7 && (j == 1 || j == 4 || j == 7)) dma_sel2(i - 1, (j + 2) / 3);
      if (j >= 8 && i == 6) pt[j - 8] = LOAD_PT(j - 8);
      if (i == 7 && j >= 8) LOAD_WO(j - 8);
      if (i == 5 && j == 10) {
        asm volatile("s_waitcnt vmcnt(30)" ::: "memory");
        __builtin_amdgcn_s_barrier();
      }
      SB();
    }
  }
  STAMP(1, 8);
#pragma unroll
  for (int k = 0; k < 3; ++k)
#pragma unroll
    for (int t = 0; t < 2; ++t) xch[(w * 6 + k * 2 + t) * 64 + lane] = acc[k][2 + t];
  SB();
  {
    unsigned m = 0;
#pragma unroll
    for (int i = 0; i < 4; ++i) m = macc4(m, __builtin_bit_cast(v4u, pt[i]));
    RETRY_BEGIN(m)
#pragma unroll
      for (int i = 0; i < 4; ++i) { pt[i] = LOAD_PT(i); m = macc4(m, __builtin_bit_cast(v4u, pt[i])); }
    RETRY_END(m)
    if (tid < 4) hint_add(p.ws, 64 + (nt * 4 + (mt >> 3)) * 4 + tid);
    const f32x4 sx = (pt[0] + pt[1]) + (pt[2] + pt[3]);
    const float tv = xrow_sum4(sx[0], sx[2], sx[1], sx[3]);
    auto sw = __builtin_amdgcn_permlane32_swap(__float_as_uint(tv), __float_as_uint(tv), false, false);
    const float mm = __uint_as_float(sw[0]) * (1.0f / FH);
    const float r = __builtin_amdgcn_rsqf(fmaxf(__uint_as_float(sw[1]) * (1.0f / FH) - mm * mm, 0.f) + EPS);
    float* f2 = st + 512 + w * 64;
    f2[(q >> 1) * 32 + 2 * c + (q & 1)] = (q >> 1) ? -r * mm : r;
#pragma unroll
    for (int t = 0; t < 2; ++t) { r4[t] = *(const f32x4*)(f2 + q * 8 + t * 4); m4[t] = *(const f32x4*)(f2 + 32 + q * 8 + t * 4); }
#pragma unroll
    for (int k = 0; k < 3; ++k) {
      v4u o;
#pragma unroll
      for (int t = 0; t < 2; ++t) {
        o[2 * t] = nomark(pk2(fma2(lo2(z2v[k][t]), lo2(r4[t]), lo2(m4[t]))));
        o[2 * t + 1] = pk2(fma2(hi2(z2v[k][t]), hi2(r4[t]), hi2(m4[t])));
      }
      st128<SC1>(o, rws, z2o[k], 0);
    }
  }
  STAMP(1, 9);
  c1s[lane] = xrow_sum4(__uint_as_float(cz[0][0][0]), __uint_as_float(cz[0][0][1]), __uint_as_float(cz[0][1][0]), __uint_as_float(cz[0][1][1]));
  c1s[64 + (lane & 31)] = xrow_sum4(__uint_as_float(cz[0][2][0]), __uint_as_float(cz[0][2][1]), __uint_as_float(cz[0][2][0]), __uint_as_float(cz[0][2][1]));
  st[1408 + lane] = rbf((__uint_as_float(gq[0]) + __uint_as_float(gq[1])) + (__uint_as_float(gq[2]) + __uint_as_float(gq[3])));
  SB();
  f32x4 z1f[3][2];
  constexpr int RD = ZRD;
  v4u zf[RD];
#define ZUNIT(ps) ((ps) < 2 ? ut0 + (ps) : (ps) < 4 ? uo0 + (ps) - 2 : (ps) == 4 ? ut2 : uo2)
#define LOAD_Z(n) ld128<SC1>(rws, zv[((n) >> 1) & 7] + ((n) & 1) * 1024, WS_Z2 + (ZUNIT((n) >> 4) * 64 + w * 16) * 1024 + ((n) >= 64 ? oob3 : 0u))
  unsigned zv[8];
#pragma unroll
  for (int i = 0; i < 8; ++i) { zv[i] = l16 + ((CKMAP(i) + kr) & 7) * 2048; PIN(zv[i]); }
  {
    bar();
    STAMP(1, 34);
#pragma unroll
    for (int k = 0; k < 6; ++k) PINA(acc[k >> 1][k & 1]);
    f32x4 oth[3][2], cs1[3], bs1[3];
    float r1[2], m1[2];
#pragma unroll
    for (int t = 0; t < 2; ++t) { r1[t] = fin[t * 16 + c]; m1[t] = fin[32 + t * 16 + c]; }
#pragma unroll
    for (int k = 0; k < 3; ++k) {
      cs1[k] = *(const f32x4*)(c1s + k * 32 + q * 4); bs1[k] = *(const f32x4*)(c1s + k * 32 + 16 + q * 4);
#pragma unroll
      for (int t = 0; t < 2; ++t) oth[k][t] = xch[((w ^ 1) * 6 + k * 2 + t) * 64 + lane];
    }
#pragma unroll
    for (int i = 4; i < WO0; ++i) LOAD_WO(i);
    SB();
    f32x2 z1a[6][2];
#pragma unroll
    for (int i = 0; i < 6; ++i) {
      const int k = i >> 1, t = i & 1;
      const f32x4 a = acc[k][t] + oth[k][t];
      z1a[i][0] = fma2(lo2(a), bc2(r1[t]), fma2(lo2(cs1[k]), bc2(m1[t]), lo2(bs1[k])));
      z1a[i][1] = fma2(hi2(a), bc2(r1[t]), fma2(hi2(cs1[k]), bc2(m1[t]), hi2(bs1[k])));
    }
#pragma unroll
    for (int i = 0; i < GELU_PRE; ++i) {
      const f32x2 lo = gelu2(z1a[i][0]), hi = gelu2(z1a[i][1]);
      z1f[i >> 1][i & 1] = f32x4{lo[0], lo[1], hi[0], hi[1]};
      PIN(z1f[i >> 1][i & 1]);
    }
#pragma unroll
    for (int i = GELU_PRE; i < 6; ++i) { PIN(z1a[i][0]); PIN(z1a[i][1]); }
    SB();
#pragma unroll
    for (int f = 0; f < 16; ++f) wq[6 + (f >> 3)][f & 7] = READ_W((f >> 3) * 8192, f & 7);
    SB();
    STAMP(1, 10);
    hint_wait(rws, 0u, WS_CNT + (64 + (nt * 4 + w) * 4 + (xcd >> 1)) * 128, 8u);
    STAMP(1, 22);
#pragma unroll
    for (int n = 0; n < RD; ++n) zf[n] = LOAD_Z(n);
    SB();
#pragma unroll
    for (int i = GELU_PRE; i < 6; ++i) {
      const f32x2 lo = gelu2(z1a[i][0]), hi = gelu2(z1a[i][1]);
      z1f[i >> 1][i & 1] = f32x4{lo[0], lo[1], hi[0], hi[1]};
      PIN(z1f[i >> 1][i & 1]);
    }
  }
  f32x4* xb = (f32x4*)(smem + LDS_A) + lane;
  f32x4* yb = (f32x4*)(smem + LDS_A + 49152) + lane;
  f32x4 ac2[2][4], ini[4], yo[2];
  const float bs0 = bflo(bspq[0]), bs1_ = bfhi(bspq[0]);
  f32x4 bq0 = {bs0, bs0, bs0, bs0}, bq1 = {bs1_, bs1_, bs1_, bs1_};
  const f32x4 z4 = {0.f, 0.f, 0.f, 0.f};
  PINA(bq0); PINA(bq1);
  unsigned vmk = l16 + WS_V + ((mt * 4 + w) * 22 + pr0) * 1024;
  gu32* p_fl4 = (gu32*)(p.ws + WS_CNT + (192 + mt * 2 + (tid & 1)) * 128);
  PIN(vmk); PIN(mkv);
  unsigned voff[3];
  voff[0] = l16 + WS_V + ((mt * 4 + 2 * kh) * 22 + (uo0 >> 1)) * 1024;
  voff[1] = l16 + WS_V + ((mt * 4 + 2 * kh) * 22 + (uo2 >> 1)) * 1024 + (uo2 & 1) * 8 + oob3;
  voff[2] = voff[0] + 22 * 1024;
  PIN(voff[0]); PIN(voff[1]); PIN(voff[2]);
  float ev[4];
  v4u eo[2];
  f32x4 yx[2][2];
  v4u vs[6][4];
#define LOAD_V(f) vs[(f) >> 2][(f) & 3] = ld128<SC1>(rws, k3v[(f) >> 2], WS_V + (mt * 4 + (((f) & 3) ^ w)) * 22 * 1024)
  SB();
  STAMP(1, 11);
  sfor<6>([&](auto P) __attribute__((always_inline)) {
    constexpr int ps = decltype(P)::value, cur = ps & 1, prv = cur ^ 1;
    constexpr bool prevT = ps == 1 || ps == 2 || ps == 5, epi = ps == 3 || ps == 4;
    unsigned zm;
    sfor<64>([&](auto G) __attribute__((always_inline)) {
      constexpr int g = decltype(G)::value, ks = g >> 2, tu = g & 3, ck = ks >> 1, k2 = ks & 1, n = ps * 16 + ks;
      const f32x4 cin = ks > 0 ? ac2[cur][tu] : (ps == 2 || ps == 3) ? ini[tu] : ps == 5 ? z4 : tu == 0 ? bq0 : tu == 1 ? bq1 : z4;
      ac2[cur][tu] = mfma(zf[n % RD], wq[ck][k2 * 4 + tu], cin);
      SB();
      if constexpr (tu == 1) { zm = ks == 0 ? (zf[n % RD][0] > zf[n % RD][2] ? zf[n % RD][0] : zf[n % RD][2]) : macc(zm, zf[n % RD]); PIN(zm); }
      if constexpr (tu == 3) {
        if constexpr (n + RD < 96) zf[n % RD] = LOAD_Z(n + RD);
        else if constexpr (WO0 + n + RD - 96 < 16) LOAD_WO(WO0 + n + RD - 96);
      }
      if constexpr (prevT && g >= 2 && g < 18 && (g & 3) == 2) xb[((w * 3 + (ps == 5 ? 2 : ps - 1)) * 4 + (g >> 2)) * 64] = ac2[prv][g >> 2];
      if constexpr (epi && (g == 2 || g == 6)) yb[((w * 3 + ps - 3) * 2 + (g >> 2)) * 64] = ac2[prv][2 + (g >> 2)];
      if constexpr (ps >= 1 && g == (epi ? 12 : 24)) {
        if constexpr (ps == 3) asm volatile("s_waitcnt vmcnt(20)" ::: "memory");
        bar();
      }
      if constexpr (epi && (g == 13 || g == 14)) yo[g - 13] = yb[(((w ^ 1) * 3 + ps - 3) * 2 + g - 13) * 64];
      if constexpr (epi && g >= 18 && g < 50 && (g & 1) == 0) {
        constexpr int t = (g - 18) / 16, ph = ((g - 18) % 16) >> 1, h = (ps - 3) * 2;
        if constexpr (ph == 0) PINA(ac2[prv][t]);
        if constexpr (ph < 4) { ev[ph] = ac2[prv][t][ph] + yo[t][ph]; PIN(ev[ph]); }
        else if constexpr (ph < 6) { ev[2 * ph - 8] *= z1f[ps - 3][t][2 * ph - 8]; PIN(ev[2 * ph - 8]); ev[2 * ph - 7] *= z1f[ps - 3][t][2 * ph - 7]; PIN(ev[2 * ph - 7]); }
        else if constexpr (ph == 6) { eo[t][h] = nomark(pk2(ev[0], ev[1])); PIN(eo[t]); }
        else {
          eo[t][h + 1] = pk2(ev[2], ev[3]);
          if constexpr (ps == 4) st128<SC1>(eo[t], rws, voff[0 + 2 * t], 0);
          else PIN(eo[t]);
        }
      }
      if constexpr ((ps == 1 || ps == 2) && g >= 48 && g < 56 && (g & 1) == 0) ini[(g - 48) >> 1] = xb[(((w ^ 2) * 3 + ps - 1) * 4 + ((g - 48) >> 1)) * 64];
      if constexpr (ps == 5 && (g == 25 || g == 26)) { yx[0][g - 25] = xb[(((w ^ 2) * 3 + 2) * 4 + g - 25) * 64]; yx[1][g - 25] = xb[(((w ^ 3) * 3 + 2) * 4 + 2 + g - 25) * 64]; }
      if constexpr (ps == 0 && (g == 22 || g == 38 || g == 54)) st128<SC1>(mkv, rws, vmk + (g - 22) / 16 * 1024, g == 54 ? oob3 : 0u);
      if constexpr (ps * 64 + g == HINT4_G) { if (tid < 2) (void)__hip_atomic_fetch_add(p_fl4, 1u, __ATOMIC_RELAXED, __HIP_MEMORY_SCOPE_AGENT); }
      if constexpr (ps == 2 && g == 40) PIN(p_fl4);
      if constexpr (ps == 5 && g == POLL4_G) flagv = __builtin_amdgcn_raw_buffer_load_b32(rws, 0, WS_CNT + (192 + mt * 2 + (xcd & 1)) * 128, SC1);
      if constexpr (ps == 5 && g == VPRE_G) {
        if (__builtin_expect(__builtin_amdgcn_readfirstlane(flagv) < 8u, 0)) hint_wait(rws, 0u, WS_CNT + (192 + mt * 2 + (xcd & 1)) * 128, 8u);
        MEMFENCE();
      }
      if constexpr (ps == 5 && g > VPRE_G && (g & 1) == 0 && (g - VPRE_G - 1) / 2 < 12) LOAD_V((g - VPRE_G - 1) / 2);
      if constexpr (g == 0 && ps >= 1 && ps < 5) STAMP(1, 55 + ps);
      SB();
    });
    if (__builtin_expect(any_mark(zm), 0)) {
      int l2 = lane;
      PIN(l2);
#pragma unroll
      for (int tu = 0; tu < 4; ++tu) ac2[cur][tu] = (ps == 2 || ps == 3) ? xb[(((w ^ 2) * 3 + ps - 2) * 4 + tu) * 64] : ps == 5 ? z4 : tu == 0 ? bq0 : tu == 1 ? bq1 : z4;
      if constexpr (ps < 5) {
        sfor<4>([&](auto K) __attribute__((always_inline)) {
          constexpr int k4 = decltype(K)::value * 4;
          v4u zz[4];
          unsigned sp = 0;
          do {
            MEMFENCE();
            zm = 0;
#pragma unroll
            for (int i = 0; i < 4; ++i) { zz[i] = LOAD_Z(ps * 16 + k4 + i); zm = macc(zm, zz[i]); }
          } while (any_mark(zm) & (++sp <= SPIN_CAP));
#pragma unroll
          for (int i = 0; i < 4; ++i)
#pragma unroll
            for (int tu = 0; tu < 4; ++tu) ac2[cur][tu] = mfma(zz[i], wq[(k4 + i) >> 1][(i & 1) * 4 + tu], ac2[cur][tu]);
        });
      } else
#pragma unroll 1
      for (int ks = 0; ks < 16; ++ks) {
        const int kk = ((CKMAP(ks >> 1) + kr) & 7) * 2 + (ks & 1);
        v4u zz, ww[4];
        unsigned sp = 0;
        do { MEMFENCE(); zz = ld128<SC1>(rws, l2 * 16 + kk * 1024, WS_Z2 + (ZUNIT(ps) * 64 + w * 16) * 1024 + (ps >= 4 ? oob3 : 0u)); } while (any_mark(macc(0u, zz)) & (++sp <= SPIN_CAP));
#pragma unroll
        for (int tu = 0; tu < 4; ++tu) ww[tu] = ld128<0>(rwsp, perm(tu ^ (2 * kh), l2 & 15) * 4096 + (l2 >> 4) * 16 + kk * 64, wsp_s);
#pragma unroll
        for (int tu = 0; tu < 4; ++tu) ac2[cur][tu] = mfma(zz, ww[tu], ac2[cur][tu]);
      }
    }
  });
  STAMP(1, 12);
  {
#pragma unroll
    for (int t = 0; t < 2; ++t) yb[((w * 3 + 2) * 2 + t) * 64] = ac2[1][2 + t];
#pragma unroll
    for (int t = 0; t < 2; ++t) { yx[0][t] = (yx[0][t] + yx[1][t]) + ac2[1][t]; PIN(yx[0][t]); }
    bar();
    STAMP(1, 52);
#pragma unroll
    for (int t = 0; t < 2; ++t) yo[t] = yb[(((w ^ 1) * 3 + 2) * 2 + t) * 64];
#pragma unroll
    for (int t = 0; t < 2; ++t) {
      const f32x4 g = (yx[0][t] + yo[t]) * z1f[2][t];
      st64<SC1>(v2u{nomark(pk2(lo2(g))), pk2(hi2(g))}, rws, voff[1], t * 22 * 1024);
    }
  }
  STAMP(1, 13);
  SB();
  f32x4 gb4[4];
  f32x2 x1f[2][4];
  STAMP(1, 53);
  STAMP(1, 23);
  f32x4 a3[4][4];
  unsigned a_rw, a_rr[3], o_out;

  {
    {
      unsigned m = 0;
      sfor<64>([&](auto G) __attribute__((always_inline)) {
        constexpr int g = decltype(G)::value, ks = g >> 4, tu = (g >> 2) & 3, e = g & 3;
        a3[e][tu] = mfma(wo[ks][e], vs[ks][tu], ks == 0 ? f32x4{0.f, 0.f, 0.f, 0.f} : a3[e][tu]);
        SB();
        if constexpr (e == 0 && g < 16) LOAD_V(12 + (g >> 2));
        if constexpr (e == 2 && g < 32) LOAD_WO(16 + (g >> 2));
        if constexpr (g >= LATE_G && g < LATE_G + 16 && (g & 1) == 0) LOAD_V(16 + (((g - LATE_G) >> 1) & 1) * 4 + ((((g - LATE_G) >> 2) + 1) & 3));
        if constexpr (e == 1) { m = macc(m, vs[ks][tu]); PIN(m); }
        SB();
      });
      if (__builtin_expect(any_mark(m), 0)) {
        sfor<4>([&](auto K) __attribute__((always_inline)) {
          constexpr int ks = decltype(K)::value;
          unsigned sp = 0;
          do {
            MEMFENCE();
            m = 0;
#pragma unroll
            for (int tu = 0; tu < 4; ++tu) { LOAD_V(ks * 4 + tu); m = macc(m, vs[ks][tu]); }
          } while (any_mark(m) & (++sp <= SPIN_CAP));
#pragma unroll
          for (int tu = 0; tu < 4; ++tu)
#pragma unroll
            for (int e = 0; e < 4; ++e) a3[e][tu] = mfma(wo[ks][e], vs[ks][tu], ks == 0 ? f32x4{0.f, 0.f, 0.f, 0.f} : a3[e][tu]);
        });
      }
    }
#pragma unroll
    for (int i = 0; i < 4; ++i) gb4[i] = *(const f32x4*)(st + 1408 + (i >> 1) * 32 + q * 8 + (i & 1) * 4);
#pragma unroll
    for (int i = 0; i < 8; ++i) { x1f[i >> 2][i & 3] = unpk(x1h[i >> 2][(i >> 1) & 1][i & 1]); PIN(x1f[i >> 2][i & 3]); }
    int lz = lane;
    PIN(lz);
    a_rw = lds0 + LDS_W + w * 12288 + lz * 16;
#pragma unroll
    for (int rel = 1; rel < 4; ++rel) { a_rr[rel - 1] = lds0 + LDS_W + ((w ^ rel) * 3 + rel - 1) * 4096 + lz * 16; PIN(a_rr[rel - 1]); }
    o_out = (mt * 64 + perm(w, lz & 15)) * 1024 + (lz >> 4) * 16 + nt * 128;
    PIN(a_rw); PIN(o_out);
    SB();
    unsigned m = 0;
#pragma unroll
    for (int f = 16; f < 24; ++f) m = macc(m, vs[f >> 2][f & 3]);
    RETRY_BEGIN(m)
#pragma unroll
      for (int f = 16; f < 24; ++f) { LOAD_V(f); m = macc(m, vs[f >> 2][f & 3]); }
    RETRY_END(m)
    sfor<32>([&](auto G) __attribute__((always_inline)) {
      constexpr int g = decltype(G)::value, tu = ((g >> 3) + 1) & 3, ks = 4 + ((g >> 2) & 1), e = g & 3;
      a3[e][tu] = mfma(wo[ks][e], vs[ks][tu], a3[e][tu]);
      SB();
      if constexpr (g >= 8 && (g & 1) == 0) ldsw<f32x4>(a_rw + (((g >> 3) - 1) * 4 + ((g >> 1) & 3)) * 1024, a3[(g >> 1) & 3][g >> 3]);
      SB();
    });
  }
  STAMP(1, 14);
  {
#pragma unroll
    for (int e = 0; e < 4; ++e) PIN(a3[e][0]);
    bar();
    STAMP(1, 54);
    f32x4 o3[4][3];
#pragma unroll
    for (int e = 0; e < 4; ++e)
#pragma unroll
      for (int rel = 1; rel < 4; ++rel) o3[e][rel - 1] = ldsr<f32x4>(a_rr[rel - 1] + e * 1024);
    const rsrc_t rout = mkrs(p.out, L * D * 2);
    SB();
#pragma unroll
    for (int pp = 0; pp < 2; ++pp) {
      v4u o;
#pragma unroll
      for (int h = 0; h < 2; ++h) {
        const int e = pp * 2 + h;
        const f32x4 y = (a3[e][0] + o3[e][0]) + (o3[e][1] + o3[e][2]);
        o[2 * h] = pk2(fma2(lo2(y), lo2(gb4[e]), x1f[pp][2 * h]));
        o[2 * h + 1] = pk2(fma2(hi2(y), hi2(gb4[e]), x1f[pp][2 * h + 1]));
      }
      st128<0>(o, rout, o_out + pp * 64, 0);
    }
  }
  if (b == 0 && tid == 0) *(unsigned*)(p.ws + WS_EPOCH) = tag_s;
  STAMP(1, 15);
  STAMP_FLUSH(1);
  asm volatile("adaln_code_end:");
}

extern "C" void kernel_launch(void* const* d_in, const int* in_sizes, int n_in, void* d_out, int out_size, void* d_ws,
                              size_t ws_size, hipStream_t stream) {
  Params p;
  p.u = (const u16*)d_in[0];
  p.lar = (const u16*)d_in[1];
  p.aim = (const u16*)d_in[2];
  p.cre = (const u16*)d_in[3];
  p.cim = (const u16*)d_in[4];
  p.ldt = (const u16*)d_in[5];
  p.dsk = (const u16*)d_in[6];
  p.win = (const u16*)d_in[8];
  p.wsp = (const u16*)d_in[9];
  p.bsp = (const u16*)d_in[10];
  p.wout = (const u16*)d_in[11];
  p.c = (const u16*)d_in[12];
  p.wmod = (const u16*)d_in[13];
  p.out = (u16*)d_out;
  p.ws = (unsigned char*)d_ws;
  (void)hipFuncSetAttribute((const void*)adaln_mega, hipFuncAttributeMaxDynamicSharedMemorySize, MEGA_LDS);
  adaln_mega<<<256, 256, MEGA_LDS, stream>>>(p);
}
```

```cpp
#include <hip/hip_runtime.h>
#include <hip/hip_bf16.h>
#include <stdint.h>
#include <utility>

#define LAS __attribute__((address_space(3)))
typedef unsigned v4u __attribute__((ext_vector_type(4)));
typedef unsigned v2u __attribute__((ext_vector_type(2)));
typedef float f32x4 __attribute__((ext_vector_type(4)));
typedef float f32x2 __attribute__((ext_vector_type(2)));
typedef short bf16x8 __attribute__((ext_vector_type(8)));
typedef _Float16 f16x8 __attribute__((ext_vector_type(8)));
typedef _Float16 f16x2 __attribute__((ext_vector_type(2)));
typedef __bf16 bf16x2_t __attribute__((ext_vector_type(2)));
typedef unsigned short u16;
typedef __amdgpu_buffer_rsrc_t rsrc_t;

constexpr int L = 2048, D = 512, NM = 128, FH = 688, FF = 1376;
constexpr float EPS = 1e-5f;
constexpr int SC1 = 16;
constexpr unsigned OOB = 0x40000000u;
constexpr unsigned SPIN_CAP = 1u << 16;

constexpr unsigned WS_EPOCH = 0;
constexpr unsigned WS_CNT = 4096;
constexpr unsigned WS_SR = WS_CNT + 512 * 128;
constexpr unsigned WS_SB = WS_SR + 2048 * 8;
constexpr unsigned WS_MODG = WS_SB + 2048 * 8;
constexpr unsigned WS_CSB = WS_MODG + 3072 * 32;
constexpr unsigned WS_ST2 = WS_CSB + 88 * 16 * 32;
constexpr unsigned WS_UG = 1u << 20;
static_assert(WS_ST2 + 32 * 2 * 16 * 32 * 8 <= WS_UG, "small buffers");
constexpr unsigned WS_X1T = WS_UG + 256 * 2048 * 8;
constexpr unsigned WS_WP = WS_X1T + 512 * 2048 * 2;
constexpr unsigned WS_WOT = WS_WP + 88 * 16 * 1024;
constexpr unsigned WS_Z2 = WS_WOT + 32 * 22 * 1024;
constexpr unsigned WS_V = WS_Z2 + 44 * 64 * 1024;
constexpr unsigned WS_END = WS_V + 128 * 22 * 1024;

struct Params {
  const u16 *u, *lar, *aim, *cre, *cim, *ldt, *dsk, *win, *wsp, *bsp, *wout, *c, *wmod;
  u16* out;
  unsigned char* ws;
};

#define DI __device__ __forceinline__
template <int V> struct IC { static constexpr int value = V; };
template <class F, int... I> DI void sfor_(F&& f, std::integer_sequence<int, I...>) { int d[] = {(f(IC<I>{}), 0)...}; (void)d; }
template <int N, class F> DI void sfor(F&& f) { sfor_(f, std::make_integer_sequence<int, N>{}); }
DI float bf2f(unsigned b) { return __builtin_bit_cast(float, b << 16); }
DI float bflo(unsigned w) { return __builtin_bit_cast(float, w << 16); }
DI float bfhi(unsigned w) { return __builtin_bit_cast(float, w & 0xFFFF0000u); }
DI f32x2 unpk(unsigned w) { return f32x2{bflo(w), bfhi(w)}; }
DI unsigned pk2(f32x2 v) { return __builtin_bit_cast(unsigned, __builtin_convertvector(v, bf16x2_t)); }
DI unsigned pk2(float lo, float hi) { return pk2(f32x2{lo, hi}); }
DI unsigned nomark(unsigned x) { return x < 0xFFFFFFFEu ? x : 0xFFFFFFFEu; }
DI float rbf(float x) { return bflo(pk2(x, 0.f)); }
DI f32x2 fma2(f32x2 a, f32x2 b, f32x2 c) { return __builtin_elementwise_fma(a, b, c); }
DI f32x2 lo2(f32x4 v) { return f32x2{v[0], v[1]}; }
DI f32x2 hi2(f32x4 v) { return f32x2{v[2], v[3]}; }
DI f32x2 bc2(float x) { return f32x2{x, x}; }
DI f32x4 mfma(v4u a, v4u b, f32x4 c) {
  return __builtin_amdgcn_mfma_f32_16x16x32_bf16(__builtin_bit_cast(bf16x8, a), __builtin_bit_cast(bf16x8, b), c, 0, 0, 0);
}
DI f32x4 mfma_h(v4u a, v4u b, f32x4 c) {
  return __builtin_amdgcn_mfma_f32_16x16x32_f16(__builtin_bit_cast(f16x8, a), __builtin_bit_cast(f16x8, b), c, 0, 0, 0);
}
DI float dot2(unsigned a, unsigned b, float acc) {
  return __builtin_amdgcn_fdot2_f32_bf16(__builtin_bit_cast(bf16x2_t, a), __builtin_bit_cast(bf16x2_t, b), acc, false);
}
constexpr unsigned ONES = 0x3F803F80u;
DI int perm(int u, int c) { return 32 * (u >> 1) + 8 * (c >> 2) + 4 * (u & 1) + (c & 3); }
template <int CTRL> DI float dpp(float x) {
  return __builtin_bit_cast(float, __builtin_amdgcn_mov_dpp(__builtin_bit_cast(int, x), CTRL, 0xf, 0xf, true));
}
DI float row16_sum(float x) {
  x += dpp<0xB1>(x); x += dpp<0x4E>(x); x += dpp<0x141>(x); x += dpp<0x128>(x);
  return x;
}
DI float row16_max(float x) {
  x = fmaxf(x, dpp<0xB1>(x)); x = fmaxf(x, dpp<0x4E>(x)); x = fmaxf(x, dpp<0x141>(x)); x = fmaxf(x, dpp<0x128>(x));
  return x;
}
DI float xrow_sum(float x) {
  auto s = __builtin_amdgcn_permlane16_swap(__float_as_uint(x), __float_as_uint(x), false, false);
  x = __uint_as_float(s[0]) + __uint_as_float(s[1]);
  auto t = __builtin_amdgcn_permlane32_swap(__float_as_uint(x), __float_as_uint(x), false, false);
  return __uint_as_float(t[0]) + __uint_as_float(t[1]);
}
DI float xrow_sum4(float a, float b, float c, float d) {
  auto s = __builtin_amdgcn_permlane16_swap(__float_as_uint(a), __float_as_uint(b), false, false);
  auto t = __builtin_amdgcn_permlane16_swap(__float_as_uint(c), __float_as_uint(d), false, false);
  const float x = __uint_as_float(s[0]) + __uint_as_float(s[1]), y = __uint_as_float(t[0]) + __uint_as_float(t[1]);
  auto z = __builtin_amdgcn_permlane32_swap(__float_as_uint(x), __float_as_uint(y), false, false);
  return __uint_as_float(z[0]) + __uint_as_float(z[1]);
}
DI float xrow_max(float x) {
  auto s = __builtin_amdgcn_permlane16_swap(__float_as_uint(x), __float_as_uint(x), false, false);
  x = fmaxf(__uint_as_float(s[0]), __uint_as_float(s[1]));
  auto t = __builtin_amdgcn_permlane32_swap(__float_as_uint(x), __float_as_uint(x), false, false);
  return fmaxf(__uint_as_float(t[0]), __uint_as_float(t[1]));
}
DI f32x2 gelu2(f32x2 x) {
  const float k0 = -2.0f * 0.7978845608028654f * 1.4426950408889634f, k1 = k0 * 0.044715f;
  const f32x2 a = x * fma2(x * x, bc2(k1), bc2(k0));
  const f32x2 e = f32x2{__builtin_amdgcn_exp2f(a[0]), __builtin_amdgcn_exp2f(a[1])} + bc2(1.0f);
  return x * f32x2{__builtin_amdgcn_rcpf(e[0]), __builtin_amdgcn_rcpf(e[1])};
}
DI rsrc_t mkrs(const void* ptr, unsigned bytes) { return __builtin_amdgcn_make_buffer_rsrc((void*)ptr, 0, (int)bytes, 0x00020000); }
template <int AUX> DI v4u ld128(rsrc_t rs, unsigned v, unsigned s) { return __builtin_amdgcn_raw_buffer_load_b128(rs, (int)v, (int)s, AUX); }
template <int AUX> DI v2u ld64(rsrc_t rs, unsigned v, unsigned s) { return __builtin_amdgcn_raw_buffer_load_b64(rs, (int)v, (int)s, AUX); }
template <int AUX> DI void st128(v4u x, rsrc_t rs, unsigned v, unsigned s) { __builtin_amdgcn_raw_buffer_store_b128(x, rs, (int)(v + s), 0, AUX); }
template <int AUX> DI void st64(v2u x, rsrc_t rs, unsigned v, unsigned s) { __builtin_amdgcn_raw_buffer_store_b64(x, rs, (int)v, (int)s, AUX); }
DI f32x4 asf(v4u x) { return __builtin_bit_cast(f32x4, x); }
DI float sum4(f32x4 x) { return (x[0] + x[1]) + (x[2] + x[3]); }
DI unsigned umax3(unsigned a, unsigned b, unsigned c) { unsigned m = a > b ? a : b; return m > c ? m : c; }
DI unsigned macc4(unsigned m, v4u x) { return umax3(umax3(m, x[0], x[1]), x[2], x[3]); }
DI unsigned macc(unsigned m, v4u x) { return umax3(m, x[0], x[2]); }
DI bool any_mark(unsigned m) { return __builtin_amdgcn_ballot_w64(m == 0xFFFFFFFFu) != 0ull; }
#define SB() __builtin_amdgcn_sched_barrier(0)
#define PIN(x) asm volatile("" : "+v"(x))
#define PINA(x) asm volatile("" : "+a"(x))
#define USE(x) asm volatile("" ::"v"(x))
#define RETRY_BEGIN(m) if (__builtin_expect(any_mark(m), 0)) { unsigned sp_ = 0; do { MEMFENCE(); m = 0;
#define RETRY_END(m) } while (any_mark(m) & (++sp_ <= SPIN_CAP)); }
#define MEMFENCE() asm volatile("" ::: "memory")
DI void bar() { asm volatile("s_waitcnt lgkmcnt(0)" ::: "memory"); __builtin_amdgcn_s_barrier(); asm volatile("" ::: "memory"); }

DI f32x2 cmul(f32x2 a, f32x2 b) {
  f32x2 t = bc2(a[0]) * b;
  asm("v_pk_fma_f32 %0, %1, %2, %0 op_sel:[1,1,0] op_sel_hi:[1,0,1] neg_lo:[0,1,0]" : "+v"(t) : "v"(a), "v"(b));
  return t;
}
DI f32x2 cexp_p(float are2, float rev_hi, float rev_lo, float p) {
  const float mag = __builtin_amdgcn_exp2f(are2 * p);
  const float hi = rev_hi * p;
  const float fr = (hi - __builtin_rintf(hi)) + rev_lo * p;
  return f32x2{mag * __builtin_amdgcn_cosf(fr), mag * __builtin_amdgcn_sinf(fr)};
}
DI float exp_acc(float x) {
  const float hi = x * 1.4426950408889634f;
  const float lo = __builtin_fmaf(x, 1.4426950408889634f, -hi) + x * 1.925963033500011e-8f;
  const float e = __builtin_amdgcn_exp2f(hi);
  return __builtin_fmaf(e, lo * 0.6931471805599453f, e);
}
DI unsigned pkh(float a, float b) {
  f16x2 v = {(_Float16)a, (_Float16)b};
  return __builtin_bit_cast(unsigned, v);
}

constexpr int CV_RK = 4160;
constexpr int CV_H = 2 * CV_RK;
constexpr int CV_CH = CV_H + 4608;
constexpr int LDS_A = 0;
constexpr int LDS_U = 2 * CV_CH;
constexpr int LDS_GS = LDS_U + 8192;
constexpr int LDS_TAB = 36864;
constexpr int KP_CH = 13840;
constexpr int LDS_KP = LDS_TAB + 2 * 64 * 528 + 16;
constexpr int LDS_LN = 0, LDS_MR = 8448;
constexpr int LDS_WI = 145472, LDS_WO = LDS_WI + 10240;
constexpr int LDS_X = 65536;
constexpr int LDS_S = LDS_X + 24576;
constexpr int LDS_W = LDS_S + 6144;
constexpr int MEGA_LDS = LDS_W + 65536 + 2048;
static_assert(LDS_WO + 4608 <= MEGA_LDS && LDS_KP + 2 * KP_CH <= LDS_WI && LDS_S >= 73728, "lds");
static_assert(LDS_GS + 1280 <= LDS_TAB && LDS_KP + 2 * KP_CH <= MEGA_LDS && MEGA_LDS <= 160 * 1024, "lds");
constexpr int FLAG_AT = 48, POLL_AT = 130, WF_EARLY = 5, LATE_SLEEP = 6, GELU_PRE = 4, WO0 = 12, HINT4_G = 280, POLL4_G = 16, VPRE_G = 37, LATE_G = 44;
static_assert(FLAG_AT >= 36 && FLAG_AT <= 56 && FLAG_AT % 4 == 0, "flag position");
static_assert(HINT4_G > 3 * 64 + 12, "behind the marker barrier");
#define CKMAP(ck) ((int)((0x75316420u >> (4 * (ck))) & 7u))

template <class T> DI T ldsr(unsigned a) { return *(const LAS T*)(size_t)a; }
template <class T> DI void ldsw(unsigned a, T v) { *(LAS T*)(size_t)a = v; }
DI v2u trread(const unsigned char* ptr) {
  typedef short v4i16_t __attribute__((ext_vector_type(4)));
  return __builtin_bit_cast(v2u, __builtin_amdgcn_ds_read_tr16_b64_v4i16((LAS v4i16_t*)ptr));
}
DI v2u trread_a(unsigned a) {
  typedef short v4i16_t __attribute__((ext_vector_type(4)));
  return __builtin_bit_cast(v2u, __builtin_amdgcn_ds_read_tr16_b64_v4i16((LAS v4i16_t*)(size_t)a));
}
struct CvOrder { int qq[36], pp[36]; bool first[36]; };
constexpr CvOrder cv_order() {
  CvOrder o{};
  int n = 0;
  for (int m = 0; m < 8; ++m) {
    for (int j = 0; j < m; ++j) if (m + j < 8) { o.qq[n] = m; o.pp[n] = j; ++n; }
    for (int j = 0; j <= m; ++j) if (m + j < 8) { o.qq[n] = j; o.pp[n] = m; ++n; }
  }
  for (int i = 0; i < 36; ++i) {
    o.first[i] = true;
    for (int j = 0; j < i; ++j) if (o.qq[j] + o.pp[j] == o.qq[i] + o.pp[i]) o.first[i] = false;
  }
  return o;
}
constexpr CvOrder CVO = cv_order();
struct Z2Order { int u[128], kind[128], pos[128]; };
constexpr Z2Order z2_order() {
  Z2Order o{};
  int n = 0;
  for (int u = 0; u < 40; ++u) {
    int pos = 0;
    const bool st = u < 24 && (u < 8 || (u & 7) < 4);
    for (int k = 0; k < 5; ++k)
      if (k < 3 ? u >= 8 : st) { o.u[n] = u; o.kind[n] = k; o.pos[n] = pos++; ++n; }
  }
  return o;
}
constexpr Z2Order Z2O = z2_order();

typedef __attribute__((address_space(1))) unsigned gu32;
DI unsigned tx(unsigned m, unsigned x, unsigned tag) { return __builtin_amdgcn_bitop3_b32(m, x, tag, 0xF6); }
DI unsigned tacc(unsigned m, v4u x, unsigned tag) { return tx(tx(m, x[1], tag), x[3], tag); }
DI bool any_nz(unsigned m) { return __builtin_amdgcn_ballot_w64(m != 0u) != 0ull; }
DI void hint_add(unsigned char* ws, int line) { (void)__hip_atomic_fetch_add((gu32*)(ws + WS_CNT + line * 128), 1u, __ATOMIC_RELAXED, __HIP_MEMORY_SCOPE_AGENT); }
DI void hint_wait(rsrc_t rws, unsigned voff, unsigned soff, unsigned want) {
  unsigned sp = 0;
  for (;;) {
    MEMFENCE();
    const unsigned v = __builtin_amdgcn_raw_buffer_load_b32(rws, (int)voff, (int)soff, SC1);
    if (!((__builtin_amdgcn_ballot_w64(v < want) != 0ull) & (++sp <= SPIN_CAP))) break;
    __builtin_amdgcn_s_sleep(2);
  }
  MEMFENCE();
}

extern "C" __global__ void __launch_bounds__(256, 1) adaln_mega(Params p) {
  extern __shared__ __attribute__((aligned(16))) unsigned char smem[];
  const int tid = threadIdx.x, lane = tid & 63, w = __builtin_amdgcn_readfirstlane(tid >> 6);
  const int b = blockIdx.x, xcd = b & 7, idx = b >> 3;
  const int pb = xcd * 32 + idx;
  const unsigned l16 = lane * 16;
  v2u codepf;
  {
    unsigned long long pc;
    int rel_m, rel_e;
    asm volatile("s_getpc_b64 %0\n\ts_mov_b32 %1, adaln_mega@rel32@lo+4\n\ts_mov_b32 %2, adaln_code_end@rel32@lo+12" : "=s"(pc), "=s"(rel_m), "=s"(rel_e));
    const rsrc_t rcode = mkrs((const void*)(pc + (long long)rel_m), rel_e > rel_m ? (unsigned)(rel_e - rel_m) & ~15u : 0u);
    codepf = ld64<0>(rcode, tid * 8, idx * 2048);
  }
  SB();
  const rsrc_t ru = mkrs(p.u, L * D * 2);
  v4u xr[2];
  xr[0] = ld128<0>(ru, l16, (pb * 8 + w * 2) * 1024);
  xr[1] = ld128<0>(ru, l16 + 1024, (pb * 8 + w * 2) * 1024);
  const int mkg = b & 3, mcg = b >> 2;
  const unsigned cq = p.c[mkg * 128 + (tid & 3) * 32 + (tid >> 3)];
  const unsigned epoch = *(const unsigned*)(p.ws + WS_EPOCH);
  const rsrc_t rws = mkrs(p.ws, WS_END);
  __builtin_amdgcn_raw_buffer_store_b32(0u, rws, tid < 2 ? tid * 32768u : OOB, WS_CNT + b * 128, SC1);
  SB();
  const int cch = w >> 1, eh = w & 1, ns = tid & 127;
  const unsigned q_cre = p.cre[pb * 256 + tid], q_cim = p.cim[pb * 256 + tid], q_lar = p.lar[pb * 256 + tid], q_aim = p.aim[pb * 256 + tid];
  const unsigned q_ldt = p.ldt[pb * 2 + cch], q_dsk = p.dsk[pb * 2 + cch];
  v4u xm[4];
#define LOAD_XM() { \
  const rsrc_t rmod = mkrs(p.wmod, D * 3072 * 2); \
_Pragma("unroll") \
  for (int i = 0; i < 4; ++i) xm[i] = ld128<0>(rmod, (tid >> 3) * 6144 + (tid & 7) * 16, (mkg * 128 + i * 32) * 6144 + mcg * 128 + (b < 192 ? 0u : OOB)); }
  SB();
  LOAD_XM();
  SB();
  const int c = lane & 15, q = lane >> 4;
  const unsigned lds0 = (unsigned)(size_t)(LAS unsigned char*)smem;
  unsigned pos4[9];
  {
    const float f4 = (float)(8188 - tid * 32);
#pragma unroll
    for (int j = 0; j < 10; j += 2) {
      const unsigned pp = pk2(bc2(f4) - f32x2{4.f * j, 4.f * j + 4.f});
      const int p0 = (int)bflo(pp), p1 = (int)bfhi(pp);
      pos4[j] = p0 + ((p0 >> 5) << 2) + lds0 + LDS_KP;
      PIN(pos4[j]);
      if (j + 1 < 9) { pos4[j + 1] = p1 + ((p1 >> 5) << 2) + lds0 + LDS_KP; PIN(pos4[j + 1]); }
    }
  }
  unsigned co[2][4];
  {
    const unsigned b0 = (eh ? 1152 + 576 * q : 144 * (q >> 1) + 4 * (q & 1)) * 4 + 36 * c + lds0 + LDS_KP + cch * KP_CH;
    const unsigned b1 = (q < 2 ? 288 + 144 * q : 568 + 4 * q) * 4 + 36 * c + lds0 + LDS_KP + cch * KP_CH;
    const unsigned s0 = eh ? 576 : 4, s1 = q < 2 ? 8 : 576;
#pragma unroll
    for (int r = 0; r < 4; ++r) { co[0][r] = b0 + r * s0; co[1][r] = b1 + r * s1; PIN(co[0][r]); PIN(co[1][r]); }
  }
  *(unsigned*)(smem + LDS_KP - 16 + cch * KP_CH + (lane & 3) * 4) = 0u;
  unsigned a_pad, a_wi, a_wo, a_tab, gm_off, gc_off;
  a_pad = lds0 + cch * CV_CH + (lane < 32 ? CV_H + l16 : ((lane >> 1) & 1) * CV_RK + 4096 + (lane & 1) * 16);
  a_wi = lds0 + LDS_WI + (w * 32 + 8 * q + ((lane >> 2) & 3)) * 80 + (lane & 3) * 16;
  a_wo = lds0 + LDS_WO + (8 * q + ((lane >> 2) & 3)) * 144 + (w >> 1) * 64 + (lane & 3) * 16 + (w & 1) * 8;
  a_tab = lds0 + LDS_TAB + cch * (64 * 528) + c * 528 + q * 16;
  gm_off = ((4 - cch) * 512 + mkg * 128 + ns) * 8;
  gc_off = (((lane & 7) > 5 ? 0 : ((lane & 7) >> 1)) * 512 + pb * 2 + (lane & 1)) * 8;
  PIN(a_pad); PIN(a_wi); PIN(a_wo); PIN(a_tab); PIN(gm_off); PIN(gc_off);
  unsigned ab[4], bb, dv[4], rdo4[4], xoff;
#pragma unroll
  for (int i = 0; i < 4; ++i) {
    ab[i] = lds0 + cch * CV_CH + (1 - (c & 1)) * CV_RK + 2 * (2030 - 2 * (c >> 1) + 8 * q) - eh * 256 - 192 - 512 - 1024 * i;
    dv[i] = (lane >> 3) * 4096 + (((lane & 7) ^ ((lane >> 4) & 1) ^ (i << 1)) * 16) + i * (32768 - 1024);
    rdo4[i] = lds0 + LDS_W + w * 16384 + ((perm((i & 1) * 2 ^ (2 * (w & 1)), c) * 128 + ((q ^ (c >> 1)) * 16)) ^ ((i >> 1) * 64));
    PIN(ab[i]); PIN(dv[i]); PIN(rdo4[i]);
  }
  bb = lds0 + cch * CV_CH + CV_H + 2 * (256 - 16 + 16 * c + 8 * q) - eh * 256 - 192;
  xoff = WS_X1T + (pb * 2 + cch) * 4096 + eh * 2048 + ((c >> 1) * 32 + ((q & 1) * 4 + 2 * (c & 1) + (q >> 1)) * 4) * 2;
  PIN(bb); PIN(xoff);
  SB();
  {
    *(v4u*)(smem + LDS_LN + (w * 2) * 1040 + l16) = xr[0];
    *(v4u*)(smem + LDS_LN + (w * 2 + 1) * 1040 + l16) = xr[1];
    float sm[2], sq[2];
#pragma unroll
    for (int i = 0; i < 2; ++i) {
      sm[i] = dot2(xr[i][0], ONES, dot2(xr[i][1], ONES, dot2(xr[i][2], ONES, dot2(xr[i][3], ONES, 0.f))));
      sq[i] = dot2(xr[i][0], xr[i][0], dot2(xr[i][1], xr[i][1], dot2(xr[i][2], xr[i][2], dot2(xr[i][3], xr[i][3], 0.f))));
    }
    const float tv = row16_sum(xrow_sum4(sm[0], sm[1], sq[0], sq[1]));
    auto sw = __builtin_amdgcn_permlane32_swap(__float_as_uint(tv), __float_as_uint(tv), false, false);
    const float m = __uint_as_float(sw[0]) * (1.0f / D);
    const float r = __builtin_amdgcn_rsqf(fmaxf(__uint_as_float(sw[1]) * (1.0f / D) - m * m, 0.f) + EPS);
    ((float*)(smem + LDS_GS + 1088))[(q >> 1) * 8 + w * 2 + (q & 1)] = (q >> 1) ? -m * r : r;
  }
  const unsigned tag = epoch * 2654435761u + 0x9E3779B9u;
  asm volatile("s_waitcnt vmcnt(8)" ::: "memory");
  bar();
  {
    unsigned ua[4][2];
#pragma unroll
    for (int i = 0; i < 4; ++i) {
      const unsigned char* src = smem + LDS_LN + (tid & 3) * 2080 + (i * 64 + (tid >> 2)) * 4;
      ua[i][0] = *(const unsigned*)src; ua[i][1] = *(const unsigned*)(src + 1040);
    }
    const unsigned lnv = ((const unsigned*)(smem + LDS_GS + 1088))[c];
    SB();
#pragma unroll
    for (int i = 0; i < 4; ++i) st128<SC1>(v4u{ua[i][0], tag, ua[i][1], tag}, rws, (tid >> 2) * 16384 + (tid & 3) * 16, WS_UG + i * 64 * 16384 + pb * 64);
    st64<SC1>(v2u{lnv, tag}, rws, tid < 16 ? (c >> 3) * 16384 + (c & 7) * 8 : OOB, WS_SR + pb * 64);
  }
  SB();
  const unsigned wp_oob = b < 176 ? 0u : OOB;
  const int wpr = b >> 2, wkg = b & 3;
  v4u xi[2], xo;
  {
    const rsrc_t rwin = mkrs(p.win, D * FF * 2), rwout = mkrs(p.wout, FH * D * 2);
    const int hf = wpr >= 22, pp = wpr - 22 * hf;
    const unsigned v = ((tid >> 2) * (FF * 2) + (tid & 3) * 16) | ((pp == 21 ? tid & 2 : 0) << 29);
#pragma unroll
    for (int i = 0; i < 2; ++i) xi[i] = ld128<0>(rwin, v, (wkg * 128 + i * 64) * (FF * 2) + (hf * FH + pp * 32) * 2 + wp_oob);
    xo = ld128<0>(rwout, (tid >> 3) * 1024 + (tid & 7) * 16, idx * 32 * 1024 + xcd * 128);
  }
  SB();
  const int d0 = pb * 2, dk = d0 + cch;
  const int mt = (xcd >> 1) * 8 + (idx & 7), nt = (xcd & 1) * 4 + (idx >> 3);
  const int big = (nt + 2) >> 3;
  const int np = 3 - big, pr0 = 3 * nt - big * (nt - 6);
  const int kh = w & 1, uh = w >> 1;
  const int uo0 = pr0 * 2 + uh * (4 - 2 * big), ut0 = pr0 * 2 + (uh ^ 1) * (4 - 2 * big), uo2 = pr0 * 2 + 2 + uh, ut2 = pr0 * 2 + 2 + (uh ^ 1);
#define UO(k) ((k) == 2 ? uo2 : uo0 + (k))
#define UT(k) ((k) == 2 ? ut2 : ut0 + (k))
  const unsigned oob3 = (unsigned)big << 30;
  const int kr = 2 * (idx >> 3) + (xcd & 1) + 4 * (idx & 1);
  const rsrc_t rwsp = mkrs(p.wsp, L * L * 2);
  const unsigned wp_fr = (wpr * 2 * 16 + wkg * 4 + w) * 1024 + wp_oob;
  v4u wr[1][8];
  const unsigned wsp_v = (lane >> 3) * 4096 + (lane & 7) * 16, wsp_s = mt * 64 * 4096 + w * 1024;
#define LOAD_WSP1(ck, i) wr[ck][i] = ld128<0>(rwsp, wsp_v + (((ck) + kr) & 7) * 128, wsp_s + (i) * 8 * 4096)
#define LOAD_WSPN(n) LOAD_WSP1((n) >> 3, (n) & 7)
  const v4u mk = {~0u, ~0u, ~0u, ~0u};
  unsigned bspq[2];
#pragma unroll
  for (int t = 0; t < 2; ++t) bspq[t] = p.bsp[mt * 64 + perm(2 * kh + t, c)];
  const unsigned tag_s = __builtin_amdgcn_readfirstlane(tag);
  {
    const float dt = rbf(exp_acc(bf2f(q_ldt)));
    const float cre = bf2f(q_cre) * dt, cim = bf2f(q_cim) * dt;
    const float are2 = -rbf(exp_acc(bf2f(q_lar))) * dt * 1.4426950408889634f;
    const float aim = bf2f(q_aim) * dt;
    const float rev_hi = aim * 0.15915494309189535f;
    const float rev_lo = __builtin_fmaf(aim, 0.15915494309189535f, -rev_hi) + aim * 6.4206383e-9f;
    int ex = __builtin_amdgcn_frexp_expf(xrow_max(row16_max(fmaxf(fabsf(cre), fabsf(cim))))) + 1;
    ex = ex < -100 ? -100 : ex;
    const float scale = __builtin_amdgcn_ldexpf(1.f, -ex);
    ((float*)(smem + LDS_GS + 1024))[w] = __builtin_amdgcn_ldexpf(2.f, ex);
    const f32x2 c0 = {cre * scale, cim * scale};
    const f32x2 e1 = cexp_p(are2, rev_hi, rev_lo, 1.f), e8 = cexp_p(are2, rev_hi, rev_lo, 8.f), e64 = cexp_p(are2, rev_hi, rev_lo, 64.f);
    unsigned char* tn = smem + LDS_TAB + cch * (64 * 528) + ns * 4;
#define TROW(r, z) *(unsigned*)(tn + (r) * 528) = pkh((z)[0], -(z)[1])
    SB();
    const f32x2 e128 = cmul(e64, e64), e2 = cmul(e1, e1), e256 = cmul(e128, e128), e4 = cmul(e2, e2), e512 = cmul(e256, e256);
    f32x2 f0 = {1.f, 0.f}, f1 = e64;
    f32x2 ca = cmul(c0, e512), cb = cmul(ca, e512);
#pragma unroll
    for (int j = 0; j < 8; ++j) {
      *(unsigned*)(tn + (48 + j) * 528) = pkh(f0[0], f0[1]);
      *(unsigned*)(tn + (56 + j) * 528) = pkh(f1[0], f1[1]);
      TROW(32 + j, cb);
      f0 = j == 0 ? e8 : cmul(f0, e8); f1 = cmul(f1, e8); cb = cmul(cb, e128);
      if (j < 4) {
        const f32x2 z = cmul(ca, e4);
        TROW(24 + j, ca); TROW(28 + j, z);
        ca = cmul(ca, e128);
      }
      if (j == 3 || j == 7) {
        PIN(f0); PIN(f1); PIN(cb);
        LOAD_WSPN(j >> 2 << 1); LOAD_WSPN((j >> 2 << 1) + 1);
        SB();
      }
    }
    TROW(40, cb);
    SB();
  {
    const float cv = bf2f(cq);
    const float sl = rbf(cv * __builtin_amdgcn_rcpf(1.0f + __builtin_amdgcn_exp2f(cv * -1.4426950408889634f)));
    const float sc[4] = {dpp<0x00>(sl), dpp<0x55>(sl), dpp<0xAA>(sl), dpp<0xFF>(sl)};
    f32x2 a[4];
#pragma unroll
    for (int i = 0; i < 4; ++i)
#pragma unroll
      for (int j = 0; j < 4; ++j) a[j] = i == 0 ? unpk(xm[i][j]) * bc2(sc[i]) : fma2(unpk(xm[i][j]), bc2(sc[i]), a[j]);
    float* red = (float*)(smem + LDS_MR) + (tid >> 3) * 68 + (tid & 7) * 8;
    *(f32x4*)red = f32x4{a[0][0], a[0][1], a[1][0], a[1][1]};
    *(f32x4*)(red + 4) = f32x4{a[2][0], a[2][1], a[3][0], a[3][1]};
  }
    bar();
    float ms8[8];
#pragma unroll
    for (int i = 0; i < 8; ++i) ms8[i] = ((const float*)(smem + LDS_MR))[(q * 8 + i) * 68 + w * 16 + c];
    SB();
    f32x2 t1[8];
    t1[1] = e1; t1[2] = e2; t1[4] = e4;
    t1[3] = cmul(e2, e1); t1[5] = cmul(e4, e1); t1[6] = cmul(e2, e4); t1[7] = cmul(t1[6], e1);
    const f32x2 c1 = cmul(c0, e128), c2 = cmul(c0, e256), c3 = cmul(c2, e128);
    TROW(0, c0); TROW(8, c1); TROW(16, c2); TROW(20, c3);
    {
    const float s = xrow_sum(((ms8[0] + ms8[1]) + (ms8[2] + ms8[3])) + ((ms8[4] + ms8[5]) + (ms8[6] + ms8[7])));
    st64<SC1>(v2u{__float_as_uint(s), tag}, rws, q == 0 ? c * 8 : OOB, WS_MODG + (mkg * 3072 + mcg * 64 + w * 16) * 8 + (b < 192 ? 0u : OOB));
    }
    SB();
#pragma unroll
    for (int v = 1; v < 8; ++v) {
      const f32x2 z0 = cmul(t1[v], c0), z1 = cmul(t1[v], c1);
      TROW(v, z0); TROW(8 + v, z1);
      if ((v & 1) == 0) {
        const f32x2 z2 = cmul(t1[v], c2), z3 = cmul(t1[v], c3);
        TROW(16 + v / 2, z2); TROW(20 + v / 2, z3);
        SB();
      }
    }
  }
#pragma unroll
  for (int i = 0; i < 2; ++i) *(v4u*)(smem + LDS_WI + (i * 64 + (tid >> 2)) * 80 + (tid & 3) * 16) = xi[i];
  *(v4u*)(smem + LDS_WO + (tid >> 3) * 144 + (tid & 7) * 16) = xo;
  bar();
  SB();
  v4u gr[4], gb_[4];
  v2u gm[4], gc[4];
#define LOAD_GS() _Pragma("unroll") for (int i_ = 0; i_ < 4; ++i_) { gr[i_] = ld128<SC1>(rws, tid * 16, WS_SR + i_ * 4096); gb_[i_] = ld128<SC1>(rws, tid * 16, WS_SB + i_ * 4096); }
#define LOAD_GM() _Pragma("unroll") for (int i_ = 0; i_ < 4; ++i_) { gm[i_] = ld64<SC1>(rws, gm_off, WS_MODG + i_ * 24576); gc[i_] = ld64<SC1>(rws, gc_off, WS_MODG + i_ * 24576); }
  v4u ug[4], wt[2];
#define LOAD_UG() _Pragma("unroll") for (int i_ = 0; i_ < 4; ++i_) ug[i_] = ld128<SC1>(rws, tid * 16, WS_UG + pb * 16384 + i_ * 4096)
  {
    v4u fa[2][8], fb[8];
    const unsigned ta = a_tab + eh * (32 * 528);
#pragma unroll
    for (int ks = 0; ks < 3; ++ks) { fb[ks] = ldsr<v4u>(a_tab + 48 * 528 + ks * 64); fa[0][ks] = ldsr<v4u>(ta + ks * 64); fa[1][ks] = ldsr<v4u>(a_tab + 16 * 528 + ks * 64); }
    SB();
    LOAD_UG();
    SB();
    f32x4 kk[2][2];
    f32x2 us;
    v2u wh_[6];
#pragma unroll
    for (int i = 0; i < 4; ++i) kk[i >> 1][i & 1] = f32x4{0.f, 0.f, 0.f, 0.f};
#pragma unroll
    for (int ks = 0; ks < 8; ++ks) {
      if (ks < 5) { fb[3 + ks] = ldsr<v4u>(a_tab + 48 * 528 + (3 + ks) * 64); fa[0][3 + ks] = ldsr<v4u>(ta + (3 + ks) * 64); fa[1][3 + ks] = ldsr<v4u>(a_tab + 16 * 528 + (3 + ks) * 64); }
      if (ks == 5) { us = *(const f32x2*)(smem + LDS_GS + 1024 + cch * 8); wh_[4] = trread_a(a_wo); wh_[5] = trread_a(a_wo + 576); }
      if (ks == 6) { wh_[0] = trread_a(a_wi); wh_[1] = trread_a(a_wi + 320); wh_[2] = trread_a(a_wi + 8); wh_[3] = trread_a(a_wi + 328); }
      kk[0][ks >> 2] = mfma_h(fa[0][ks], fb[ks], kk[0][ks >> 2]); kk[1][ks >> 2] = mfma_h(fa[1][ks], fb[ks], kk[1][ks >> 2]);
      SB();
    }
    wt[0] = v4u{wh_[0][0], wh_[0][1], wh_[1][0], wh_[1][1]};
    wt[1] = v4u{wh_[2][0], wh_[2][1], wh_[3][0], wh_[3][1]};
    st128<SC1>(v4u{wh_[4][0], wh_[4][1], wh_[5][0], wh_[5][1]}, rws, l16, WS_WOT + ((xcd * 4 + w) * 22 + idx) * 1024 + wp_oob);
    LOAD_GS();
    SB();
#pragma unroll
    for (int i = 0; i < 4; ++i) PINA(kk[i >> 1][i & 1]);
    f32x4 k0 = kk[0][0] * us[0] + kk[0][1] * us[1];
    const f32x4 k1 = kk[1][0] * us[0] + kk[1][1] * us[1];
    k0[0] += ns == 0 ? bf2f(q_dsk) : 0.f;
#pragma unroll
    for (int r = 0; r < 4; ++r) { ldsw<float>(co[0][r], k0[r]); ldsw<float>(co[1][r], k1[r]); }
  }
  bar();
  {
    float v[2][9];
#pragma unroll
    for (int j = 0; j < 9; ++j) v[0][j] = ldsr<float>(pos4[j]);
    LOAD_GM();
#pragma unroll
    for (int j = 0; j < 9; ++j) v[1][j] = ldsr<float>(pos4[j] + KP_CH);
    ldsw<v4u>(a_pad, v4u{0u, 0u, 0u, 0u});
    SB();
#pragma unroll
    for (int ch = 0; ch < 2; ++ch) {
      unsigned char* rk = smem + ch * CV_CH + tid * 16;
#pragma unroll
      for (int sft = 0; sft < 2; ++sft)
        *(v4u*)(rk + sft * CV_RK) = v4u{pk2(v[ch][sft], v[ch][sft + 1]), pk2(v[ch][sft + 2], v[ch][sft + 3]), pk2(v[ch][sft + 4], v[ch][sft + 5]), pk2(v[ch][sft + 6], v[ch][sft + 7])};
    }
    SB();
    unsigned m = 0;
#pragma unroll
    for (int i = 0; i < 4; ++i) { m = tacc(m, ug[i], tag); m = tacc(m, gr[i], tag); m = tacc(m, gb_[i], tag); }
    if (__builtin_expect(any_nz(m), 1)) {
      unsigned sp = 0;
      do {
        MEMFENCE(); LOAD_UG(); LOAD_GS(); m = 0;
#pragma unroll
        for (int i = 0; i < 4; ++i) { m = tacc(m, ug[i], tag); m = tacc(m, gr[i], tag); m = tacc(m, gb_[i], tag); }
      } while (any_nz(m) & (++sp <= SPIN_CAP));
    }
    f32x2 nn[4][2];
#pragma unroll
    for (int i = 0; i < 4; ++i) {
      nn[i][0] = fma2(unpk(ug[i][0]), bc2(__uint_as_float(gr[i][0])), bc2(__uint_as_float(gb_[i][0])));
      nn[i][1] = fma2(unpk(ug[i][2]), bc2(__uint_as_float(gr[i][2])), bc2(__uint_as_float(gb_[i][2])));
      PIN(nn[i][0]); PIN(nn[i][1]);
      *(unsigned*)(smem + LDS_U + i * 1024 + tid * 4) = __builtin_amdgcn_perm(ug[i][2], ug[i][0], 0x05040100u);
      *(unsigned*)(smem + LDS_U + 4096 + i * 1024 + tid * 4) = __builtin_amdgcn_perm(ug[i][2], ug[i][0], 0x07060302u);
    }
    SB();
    m = 0;
#pragma unroll
    for (int i = 0; i < 4; ++i) m = tx(tx(m, gm[i][1], tag), gc[i][1], tag);
    if (__builtin_expect(any_nz(m), 1)) {
      unsigned sp = 0;
      do {
        MEMFENCE(); LOAD_GM(); m = 0;
#pragma unroll
        for (int i = 0; i < 4; ++i) m = tx(tx(m, gm[i][1], tag), gc[i][1], tag);
      } while (any_nz(m) & (++sp <= SPIN_CAP));
    }
    const float mv = rbf((__uint_as_float(gm[0][0]) + __uint_as_float(gm[1][0])) + (__uint_as_float(gm[2][0]) + __uint_as_float(gm[3][0])));
    *(u16*)(smem + LDS_GS + tid * 2) = (u16)pk2(mv + (cch ? 0.f : 1.0f), 0.f);
    const float cv = rbf((__uint_as_float(gc[0][0]) + __uint_as_float(gc[1][0])) + (__uint_as_float(gc[2][0]) + __uint_as_float(gc[3][0])));
    const float gav = dpp<0x104>(cv), gav2 = dpp<0x102>(cv);
    const float hbv = cv * gav, hsv = rbf(1.0f + cv) * gav2;
    const f32x2 hb = {__builtin_bit_cast(float, __builtin_amdgcn_readlane(__builtin_bit_cast(int, hbv), 0)), __builtin_bit_cast(float, __builtin_amdgcn_readlane(__builtin_bit_cast(int, hbv), 1))};
    const f32x2 hs = {__builtin_bit_cast(float, __builtin_amdgcn_readlane(__builtin_bit_cast(int, hsv), 2)), __builtin_bit_cast(float, __builtin_amdgcn_readlane(__builtin_bit_cast(int, hsv), 3))};
    SB();
#pragma unroll
    for (int i = 4; i < 8; ++i) LOAD_WSPN(i);
    st128<SC1>(mk, rws, l16, WS_X1T + dk * 4096 + eh * 2048);
    st128<SC1>(mk, rws, l16 + 1024, WS_X1T + dk * 4096 + eh * 2048);
    __builtin_amdgcn_raw_buffer_store_b32(~0u, rws, lane * 4, WS_ST2 + ((mt * 2 + kh) * 16 + nt * 2 + uh) * 256, SC1);
    bspq[0] |= bspq[1] << 16;
    PIN(bspq[0]);
#pragma unroll
    for (int i = 0; i < 4; ++i) {
      const f32x2 h0 = fma2(nn[i][0], hs, hb), h1 = fma2(nn[i][1], hs, hb);
      *(unsigned*)(smem + CV_H + 512 + i * 1024 + tid * 4) = pk2(h0[0], h1[0]);
      *(unsigned*)(smem + CV_CH + CV_H + 512 + i * 1024 + tid * 4) = pk2(h0[1], h1[1]);
    }
  }
  asm volatile("" ::"v"(codepf));
  bar();
#define CV_A(ee, k) ({ const unsigned a_ = ab[(k) >> 1] + 192 - 64 * (ee) + 512 * (1 - ((k) & 1)); v4u{ldsr<unsigned>(a_), ldsr<unsigned>(a_ + 4), ldsr<unsigned>(a_ + 8), ldsr<unsigned>(a_ + 12)}; })
#define CV_B(ee, k) ldsr<v4u>(bb + 192 - 64 * (ee) + 512 * (k))
  const unsigned slotA = LDS_W + w * 16384, slotB = slotA + 8192;
  v4u wq[8][8];
#define READ_W(dl, f) ldsr<v4u>(rdo4[(((f) >> 1) & 1) + ((f) >> 2) * 2] + (dl) + ((f) & 1) * 512)
  unsigned dso;
#define DMA_W(ck, slot, i) do { \
    if (((i) & 3) == 0) asm volatile("s_add_u32 m0, %1, %2\n\ts_add_u32 %0, %3, %4\n\tbuffer_load_dwordx4 %5, %6, %0 offen lds" \
                 : "=&s"(dso) : "s"(lds0 + (slot)), "i"((i) * 1024), "s"(wsp_s + ((CKMAP(ck) + kr) & 7) * 128), "i"((i) * 32768), "v"(dv[0]), "s"(rwsp) : "scc", "memory"); \
    else asm volatile("buffer_load_dwordx4 %0, %1, %2 offen offset:%3 lds" :: "v"(dv[(i) & 3]), "s"(rwsp), "s"(dso), "i"(((i) & 3) * 1024) : "memory"); } while (0)
  auto dma_sel = [&](auto CK, unsigned slot, int i) __attribute__((always_inline)) {
    constexpr int ck = decltype(CK)::value;
    switch (i) {
      case 0: DMA_W(ck, slot, 0); break; case 1: DMA_W(ck, slot, 1); break; case 2: DMA_W(ck, slot, 2); break; case 3: DMA_W(ck, slot, 3); break;
      case 4: DMA_W(ck, slot, 4); break; case 5: DMA_W(ck, slot, 5); break; case 6: DMA_W(ck, slot, 6); break; default: DMA_W(ck, slot, 7); break;
    }
  };
  v4u cA[2][8], cB[2][8];
  v4u ones4 = {ONES, ONES, ONES, ONES};
  PIN(ones4);
  f32x4 cb4[4];
  {
    const v4u gbf = *(const v4u*)(smem + LDS_GS + w * 64 + q * 16), sbf = *(const v4u*)(smem + LDS_GS + 256 + w * 64 + q * 16);
#pragma unroll
    for (int k = 0; k < 4; ++k) { cA[0][k] = CV_A(0, k); cB[0][k] = CV_B(0, k); }
    SB();
    const f32x4 z4 = {0.f, 0.f, 0.f, 0.f};
#pragma unroll
    for (int e = 0; e < 2; ++e) {
      v4u o;
#pragma unroll
      for (int jj = 0; jj < 4; ++jj) o[jj] = pk2(unpk(wt[e][jj]) * unpk(gbf[jj]));
      st128<SC1>(o, rws, l16 + e * 16384, WS_WP + wp_fr);
      cb4[2 * e + 1] = mfma(sbf, wt[e], z4);
      cb4[2 * e] = mfma(ones4, o, z4);
    }
  }
  SB();
  f32x4 cacc[8];
  v2u ue[4];
  float uf[16];
  unsigned flagv;
  unsigned wro[4], a_cp, a_ue, o_csb, o_flag;
  gu32* p_flag = (gu32*)(p.ws + WS_CNT + (256 + (tid & 7) * 16 + (idx & 15)) * 128);
  {
    f32x2 cp[4];
    float s0, s1, s2, s3;
    sfor<144>([&](auto G) __attribute__((always_inline)) {
      constexpr int g = decltype(G)::value, ee = g / 36, j = g % 36, t = CVO.pp[j] + CVO.qq[j];
      constexpr bool first = ee == 0 && CVO.first[j];
      cacc[t] = mfma(cA[ee & 1][CVO.qq[j]], cB[ee & 1][CVO.pp[j]], first ? f32x4{0.f, 0.f, 0.f, 0.f} : cacc[t]);
      SB();
      if constexpr (g < 8) {
        if constexpr (g & 1) cB[0][4 + (g >> 1)] = CV_B(0, 4 + (g >> 1));
        else cA[0][4 + (g >> 1)] = CV_A(0, 4 + (g >> 1));
      }
      constexpr int jn = ee == 0 ? (j >= 12 && (j - 12) % 3 != 2 ? (j - 12) / 3 * 2 + (j - 12) % 3 : -1) : (j & 1) ? j >> 1 : -1;
      if constexpr (ee < 3 && jn >= 0 && jn < 16) {
        if constexpr (jn & 1) cB[(ee + 1) & 1][jn >> 1] = CV_B(ee + 1, jn >> 1);
        else cA[(ee + 1) & 1][jn >> 1] = CV_A(ee + 1, jn >> 1);
      }
      if constexpr (g == 8) { a_cp = lds0 + LDS_S + 4096 + c * 16; PIN(a_cp); }
      if constexpr (g == 9) { cb4[0][1] = cb4[1][0]; cb4[0][2] = cb4[2][0]; PINA(cb4[0]); }
      if constexpr (g == 10) { cb4[0][3] = cb4[3][0]; PINA(cb4[0]); }
      if constexpr (g == 11) ldsw<f32x4>(a_cp + w * 256, cb4[0]);
      if constexpr (g == 17) { a_cp += (q & 1) * 8; PIN(a_cp); }
      if constexpr (g == 20) { o_csb = q * 512 + c * 8; PIN(o_csb); }
      if constexpr (g == 23) { o_csb = tid < 32 ? o_csb : OOB; PIN(o_csb); }
      if constexpr (g == 26) { PIN(p_flag); }
      if constexpr (g == FLAG_AT) {
        asm volatile("s_waitcnt vmcnt(%0) lgkmcnt(8)" ::"i"(FLAG_AT > 38 ? 4 + (FLAG_AT - 38 + 3) / 4 : 4) : "memory");
        __builtin_amdgcn_s_barrier();
        if (tid < 8) (void)__hip_atomic_fetch_add(p_flag, 1u, __ATOMIC_RELAXED, __HIP_MEMORY_SCOPE_AGENT);
      }
      if constexpr (g == FLAG_AT + 4 || g == FLAG_AT + 8) {
        cp[(g - FLAG_AT - 4) / 2] = ldsr<f32x2>(a_cp + (g - FLAG_AT - 4) * 128);
        cp[(g - FLAG_AT - 4) / 2 + 1] = ldsr<f32x2>(a_cp + (g - FLAG_AT - 4) * 128 + 256);
      }
      if constexpr (g == FLAG_AT + 16) { s0 = cp[0][0] + cp[1][0]; PIN(s0); s2 = cp[0][1] + cp[1][1]; PIN(s2); }
      if constexpr (g == FLAG_AT + 20) { s1 = cp[2][0] + cp[3][0]; PIN(s1); s3 = cp[2][1] + cp[3][1]; PIN(s3); }
      if constexpr (g == FLAG_AT + 24) { s0 += s1; PIN(s0); s2 += s3; PIN(s2); }
      if constexpr (g == FLAG_AT + 28) st64<SC1>(v2u{__float_as_uint(s0), __float_as_uint(s2)}, rws, o_csb, WS_CSB + wpr * 1024 + wkg * 128 + wp_oob);
      if constexpr (g == 14 || g == 29 || g == 32 || g == 35) DMA_W(1, slotA, g == 14 ? 0 : (g - 26) / 3);
      if constexpr (g >= 38 && g < 86 && (g & 3) == 2) DMA_W(1 + (g + 10) / 64, g < 54 ? slotA : slotB, (4 + (g - 38) / 4) & 7);
      if constexpr (g == 88) asm volatile("s_waitcnt vmcnt(8)" ::: "memory");
      if constexpr (g >= 90 && g < 106 && (g & 1) == 0) wq[1][(g - 90) >> 1] = READ_W(0, (g - 90) >> 1);
      if constexpr (g >= 68 && g < 72) { wro[g - 68] = (lds0 + slotA + (lane >> 3) * 128 + (((lane & 7) ^ ((lane >> 4) & 1)) * 16)) ^ ((g - 68) * 32); PIN(wro[g - 68]); }
      if constexpr (g == 105) { a_ue = lds0 + LDS_U + cch * 4096 + eh * 2048 + c * 32 + q * 8; PIN(a_ue); }
      if constexpr (g == 106 || g == 107) { ue[(g - 106) * 2] = ldsr<v2u>(a_ue + (g - 106) * 1024); ue[(g - 106) * 2 + 1] = ldsr<v2u>(a_ue + (g - 106) * 1024 + 512); }
      if constexpr (g >= 108 && g < 140 && (g & 3) == 0) ldsw<v4u>(wro[((g - 108) >> 2) & 3] + ((g - 108) >> 2) * 1024, wr[0][(g - 108) >> 2]);
      if constexpr (g >= 109 && g < 125 && (g & 1) == 1) {
        constexpr int x = (g - 109) >> 1;
        if constexpr (x >= 2) { uf[2 * (x - 2)] = bflo(ue[(x - 2) >> 1][x & 1]); PIN(uf[2 * (x - 2)]); uf[2 * (x - 2) + 1] = bfhi(ue[(x - 2) >> 1][x & 1]); PIN(uf[2 * (x - 2) + 1]); }
      }
      if constexpr (g == 118 || g == 122) { constexpr int x = 6 + (g == 122); uf[2 * x] = bflo(ue[3][x & 1]); PIN(uf[2 * x]); uf[2 * x + 1] = bfhi(ue[3][x & 1]); PIN(uf[2 * x + 1]); }
      if constexpr (g == 110) { o_flag = w == 0 ? c * 128 : OOB; PIN(o_flag); }
      if constexpr (g == 126) asm volatile("s_waitcnt vmcnt(0)" ::: "memory");
      if constexpr (g >= 127 && g < 143 && (g & 1) == 1) wq[2][(g - 127) >> 1] = READ_W(8192, (g - 127) >> 1);
      if constexpr (g == POLL_AT) flagv = __builtin_amdgcn_raw_buffer_load_b32(rws, o_flag, WS_CNT + (256 + xcd * 16) * 128, SC1);
      SB();
    });
  }
  unsigned late;
  {
    f32x4* red = (f32x4*)(smem + LDS_X) + w * 256 + lane;
    f32x4 keep[4];
    if (eh) {
#pragma unroll
      for (int t = 0; t < 4; ++t) red[t * 64] = cacc[t];
      SB();
#pragma unroll
      for (int t = 0; t < 4; ++t) { PINA(cacc[4 + t]); keep[t] = cacc[4 + t]; PIN(keep[t]); }
    } else {
#pragma unroll
      for (int t = 0; t < 4; ++t) red[t * 64] = cacc[4 + t];
      SB();
#pragma unroll
      for (int t = 0; t < 4; ++t) { PINA(cacc[t]); keep[t] = cacc[t]; PIN(keep[t]); }
    }
    f32x2 pre[4][2];
#pragma unroll
    for (int t = 0; t < 4; ++t) {
      pre[t][0] = f32x2{uf[4 * t], uf[4 * t + 1]} + lo2(keep[t]);
      pre[t][1] = f32x2{uf[4 * t + 2], uf[4 * t + 3]} + hi2(keep[t]);
      PIN(pre[t][0]); PIN(pre[t][1]);
    }
    if (w == 0) *(unsigned*)(smem + LDS_S) = __builtin_amdgcn_ballot_w64(flagv < 16u) != 0ull;
    bar();
    late = *(const unsigned*)(smem + LDS_S);
    const f32x4* rd = (const f32x4*)(smem + LDS_X) + (w ^ 1) * 256 + lane;
    f32x4 oth[4];
#pragma unroll
    for (int t = 0; t < 4; ++t) oth[t] = rd[t * 64];
#pragma unroll
    for (int t = 0; t < 4; ++t) {
      v2u o;
      o[0] = nomark(pk2(pre[t][0] + lo2(oth[t])));
      o[1] = pk2(pre[t][1] + hi2(oth[t]));
      st64<SC1>(o, rws, xoff + t * 512, 0);
    }
  }
  SB();
  v4u wf[3][8];
  const unsigned rot = uh * 4;
#define LOAD_WP(half, k, i) ld128<SC1>(rws, l16 + ((i) & 3) * 1024, WS_WP + (((half) * 44 + UO(k)) * 16 + kh * 8 + (((i) & 4) ^ rot)) * 1024 + ((k) == 2 ? oob3 : 0u))
#define LOAD_XS(i) ld128<SC1>(rws, wsp_v, WS_X1T + ((i) * 32 + w * 8) * 4096 + mt * 128)
  v4u xs[16];
  unsigned tb[2][4], xdst;
  SB();
  int x1o;
  unsigned z2o[3], dgo[4], rdo[2], xw, xrd, a_fin, a_fr;
  v4u mkv = mk;
  v2u cz[2][3];
  f32x2 csb[2][3];
  unsigned gq[4];
#define LOAD_CZ(hf, kc) ld64<SC1>(rws, lane * 8, WS_CSB + ((hf) * 44 + UO(kc)) * 512 + ((kc) == 2 ? oob3 : 0u))
  {
    if (__builtin_amdgcn_readfirstlane(late)) {
      if (w == 0) { hint_wait(rws, c * 128, WS_CNT + (256 + xcd * 16) * 128, 16u); __builtin_amdgcn_s_sleep(LATE_SLEEP); }
      __builtin_amdgcn_s_barrier();
    }
    MEMFENCE();
#pragma unroll
    for (int i = 0; i < 3; ++i)
#pragma unroll
      for (int k = 0; k < 3; ++k) wf[k][i] = LOAD_WP(1, k, i);
    SB();
#pragma unroll
  for (int f = 0; f < 8; ++f) wq[0][f] = READ_W(0, f);
#pragma unroll
  for (int f = 0; f < 24; ++f) PINA(wq[f >> 3][f & 7]);
    int lz = lane;
    PIN(lz);
  {
    const int row = 8 * (lz >> 4) + ((lz >> 2) & 3);
    const int sg = ((row >> 1) & 1) | (((row >> 3) & 1) << 1);
#pragma unroll
    for (int tu = 0; tu < 4; ++tu) {
      const unsigned t0 = lds0 + LDS_A + kh * 32768 + row * 128 + ((tu ^ (2 * kh) ^ sg) * 32) + (lz & 3) * 8;
      tb[0][tu] = t0 + uh * 16384;
      tb[1][tu] = t0 + (uh ^ 1) * 16384;
    }
    const int rl = w * 8 + (lz >> 3), chn = lz & 7;
    const int sg2 = ((rl >> 1) & 1) | (((rl >> 3) & 1) << 1);
    xdst = lds0 + LDS_A + rl * 128 + (((chn >> 1) ^ sg2) * 32) + (chn & 1) * 16;
  }
  {
    const int row = 8 * (lz >> 4) + ((lz >> 2) & 3);
    const int sg = ((row >> 1) & 1) | (((row >> 3) & 1) << 1);
    x1o = LDS_A + nt * 8192 + row * 128 + ((w ^ sg) * 32) + (lz & 3) * 8;
  }
#pragma unroll
  for (int k = 0; k < 3; ++k) { z2o[k] = lz * 16 + WS_Z2 + (UO(k) * 64 + mt * 2 + kh) * 1024 + (k == 2 ? oob3 : 0u); }
#pragma unroll
    for (int tu = 0; tu < 8; ++tu) PIN(tb[tu >> 2][tu & 3]);
    PIN(xdst); PIN(x1o);
    {
      const int c2 = lz & 15;
      const bool dg = (c2 >> 2) == (lz >> 4);
      const unsigned dump = lds0 + LDS_W + 65536 + lz * 16;
      dgo[0] = dg ? lds0 + LDS_S + (w * 64 + kh * 32 + c2) * 4 : dump; dgo[1] = dg ? lds0 + LDS_S + (w * 64 + (kh ^ 1) * 32 + c2) * 4 : dump;
      dgo[2] = dg ? lds0 + LDS_S + 2048 + (w * 64 + kh * 32 + c2) * 16 : dump; dgo[3] = dg ? lds0 + LDS_S + 2048 + (w * 64 + (kh ^ 1) * 32 + c2) * 16 : dump;
      rdo[0] = lds0 + LDS_S + (kh * 32 + (lz & 31)) * 4; rdo[1] = lds0 + LDS_S + 2048 + (kh * 32 + (lz & 31)) * 16 + (lz & 3) * 4;
      xw = lds0 + LDS_X + w * 6144 + lz * 16; xrd = lds0 + LDS_X + (w ^ 1) * 6144 + lz * 16;
      a_fin = lds0 + LDS_S + 1024 + w * 256 + lz * 4; a_fr = lds0 + LDS_S + 1024 + w * 256 + (lz >> 4) * 16;
      PIN(dgo[0]); PIN(dgo[1]); PIN(dgo[2]); PIN(dgo[3]); PIN(rdo[0]); PIN(rdo[1]); PIN(xw); PIN(xrd); PIN(a_fin); PIN(a_fr); PIN(mkv);
    }
#pragma unroll
    for (int k = 0; k < 3; ++k) PIN(z2o[k]);
    SB();
#pragma unroll
    for (int i = 3; i < WF_EARLY; ++i)
#pragma unroll
      for (int k = 0; k < 3; ++k) wf[k][i] = LOAD_WP(1, k, i);
    SB();
#pragma unroll
    for (int i = 0; i < 16; ++i) xs[i] = LOAD_XS(i);
    SB();
    unsigned m = 0;
#pragma unroll
    for (int i = 0; i < 16; ++i) {
      m = macc(m, xs[i]); ldsw<v4u>(xdst + i * 4096, xs[i]);
      if (WF_EARLY * 3 + i < 24) wf[(WF_EARLY * 3 + i) % 3][(WF_EARLY * 3 + i) / 3] = LOAD_WP(1, (WF_EARLY * 3 + i) % 3, (WF_EARLY * 3 + i) / 3);
      SB();
    }
    PIN(m);
    RETRY_BEGIN(m)
#pragma unroll
      for (int i = 0; i < 16; ++i) xs[i] = LOAD_XS(i);
      SB();
#pragma unroll
      for (int i = 0; i < 16; ++i) { m = macc(m, xs[i]); ldsw<v4u>(xdst + i * 4096, xs[i]); }
    RETRY_END(m)
  }
  bar();
#define AHALF(tu, i, h) trread_a(tb[(i) >> 2][tu] + ((i) & 3) * 4096 + (h) * 512)
  f32x4 acc[3][4];
  v4u wg[3][8];
  v2u ah[2][4][2];
  float* st = (float*)(smem + LDS_S);
  float* fin = st + 256 + w * 64;
  f32x4* xch = (f32x4*)(smem + LDS_X);
  f32x4 r4[2], m4[2];
  {
    f32x4 s1[4], s2[4];
    constexpr int LA = 4;
    v2u ar[LA + 1][2];
    float pa[4], pb[4];
#define UTILE(u) ((u) < 8 ? (u) >> 2 : (((u) >> 3) + 1) & 3)
#define UKS(u) ((u) < 8 ? (u) & 3 : (u) & 7)
#pragma unroll
    for (int u = 0; u < LA; ++u) { ar[u][0] = AHALF(UTILE(u), UKS(u), 0); ar[u][1] = AHALF(UTILE(u), UKS(u), 1); }
#pragma unroll
    for (int i = 0; i < 6; ++i) cz[i / 3][i % 3] = LOAD_CZ(i / 3, i % 3);
#pragma unroll
    for (int i = 0; i < 4; ++i) gq[i] = __builtin_amdgcn_raw_buffer_load_b32(rws, lane * 8, WS_MODG + (i * 3072 + 5 * 512 + nt * 64) * 8, SC1);
    DMA_W(3, slotB, 0);
    SB();
    sfor<128>([&](auto G) __attribute__((always_inline)) {
      constexpr int m = decltype(G)::value, u = Z2O.u[m], k = Z2O.kind[m], pos = Z2O.pos[m], tu = UTILE(u), i = UKS(u);
      const v4u a = {ar[u % (LA + 1)][0][0], ar[u % (LA + 1)][0][1], ar[u % (LA + 1)][1][0], ar[u % (LA + 1)][1][1]};
      const f32x4 z4 = {0.f, 0.f, 0.f, 0.f};
      if constexpr (k < 3) acc[k][tu] = mfma(a, wf[k][i], (i == 0 && tu >= 2) ? z4 : acc[k][tu]);
      if constexpr (k == 3) s1[tu] = mfma(ones4, a, i == 0 ? z4 : s1[tu]);
      if constexpr (k == 4) s2[tu] = mfma(a, a, i == 0 ? z4 : s2[tu]);
      SB();
      if constexpr (pos < 2 && u + LA < 40) ar[(u + LA) % (LA + 1)][pos] = AHALF(UTILE(u + LA), UKS(u + LA), pos);
      if constexpr (pos < 2 && u >= 36) ah[0][u - 36][pos] = AHALF(u - 36, 0, pos);
      if constexpr (u < 8 && pos == 0 && u != 4) { if constexpr (u > 0) DMA_W(3, slotB, u); }
      if constexpr (u == 4 && pos == 0) DMA_W(3, slotB, 4);
      if constexpr (u >= 12 && u < 15 && pos == 0) st128<SC1>(mkv, rws, z2o[u - 12], 0);
      if constexpr (m == 23 || m == 28 || m == 55 || m == 70) {
        constexpr int x = m == 23 ? 0 : m == 28 ? 1 : m == 55 ? 2 : 3;
        PINA(s1[x]);
        ldsw<float>(dgo[x >> 1] + (x & 1) * 64, s1[x][0]);
        if constexpr (x < 3) ldsw<f32x4>(dgo[2 + (x >> 1)] + (x & 1) * 256, s2[x]);
      }
      if constexpr (m == 71) ldsw<f32x4>(dgo[3] + 256, s2[3]);
      if constexpr (m == 35) __builtin_amdgcn_s_waitcnt(0x0F70);
      if constexpr (m == 46 || m == 51 || m == 56 || m == 61) {
        constexpr int x = (m - 46) / 5 * 2;
        wq[3][x] = READ_W(8192, x); wq[3][x + 1] = READ_W(8192, x + 1);
      }
      if constexpr (m == 50 || m == 60 || m == 65) ldsw<f32x4>(xw + (m == 50 ? 0 : m == 60 ? 1 : 2) * 2048, acc[m == 50 ? 0 : m == 60 ? 1 : 2][2]);
      if constexpr (m == 38 || m == 41 || m == 44 || m == 47) DMA_W(4, slotA, (m - 38) / 3);
      if constexpr (m == 66) DMA_W(4, slotA, 4);
      if constexpr (m == 68 || m == 72 || m == 78) DMA_W(4, slotA, m == 68 ? 5 : m == 72 ? 6 : 7);
      if constexpr (m == 73 || m == 94) bar();
      if constexpr (m >= 74 && m < 77) acc[m - 74][0] = ldsr<f32x4>(xrd + (m - 74) * 2048);
      if constexpr (m == 82 || m == 85 || m == 88) ldsw<f32x4>(xw + (m - 82) / 3 * 2048 + 1024, acc[(m - 82) / 3][3]);
      if constexpr (m >= 95 && m < 98) acc[m - 95][1] = ldsr<f32x4>(xrd + (m - 95) * 2048 + 1024);
      if constexpr (m == 77) {
#pragma unroll
        for (int f = 0; f < 8; ++f) PINA(wq[3][f]);
      }
      if constexpr (m >= 83 && m < 93 && (m - 83) % 3 != 2) {
        constexpr int x = (m - 83) / 3 * 2 + (m - 83) % 3;
        if constexpr (x < 4) pa[x] = ldsr<float>(rdo[0] + x * 256);
        else pb[x - 4] = ldsr<float>(rdo[1] + (x - 4) * 1024);
      }
      if constexpr (m == 93) pb[3] = ldsr<float>(rdo[1] + 3072);
      if constexpr (m == 98) { pa[0] += pa[1]; PIN(pa[0]); }
      if constexpr (m == 99) { pa[2] += pa[3]; PIN(pa[2]); }
      if constexpr (m == 101) { pb[0] += pb[1]; PIN(pb[0]); }
      if constexpr (m == 102) { pb[2] += pb[3]; PIN(pb[2]); }
      if constexpr (m == 104) { pa[0] += pa[2]; PIN(pa[0]); }
      if constexpr (m == 105) { pb[0] += pb[2]; PIN(pb[0]); }
      if constexpr (m == 107) { pa[0] *= (1.0f / D); PIN(pa[0]); }
      if constexpr (m == 108) { pa[1] = pa[0] * pa[0]; PIN(pa[1]); }
      if constexpr (m == 110) { pb[0] = __builtin_fmaf(pb[0], 1.0f / D, -pa[1]); PIN(pb[0]); }
      if constexpr (m == 111) { pb[0] = __builtin_amdgcn_fmed3f(pb[0], 0.f, 3.0e38f); PIN(pb[0]); }
      if constexpr (m == 113) { pb[0] += EPS; PIN(pb[0]); }
      if constexpr (m == 114) { pb[0] = __builtin_amdgcn_rsqf(pb[0]); PIN(pb[0]); }
      if constexpr (m == 116) { pa[0] = lane < 32 ? 1.0f : -pa[0]; PIN(pa[0]); }
      if constexpr (m == 117) { pb[0] *= pa[0]; PIN(pb[0]); }
      if constexpr (m == 119) ldsw<float>(a_fin, pb[0]);
      if constexpr (m == 120 || m == 122 || m == 123 || m == 125) {
        constexpr int x = m == 120 ? 0 : m == 122 ? 1 : m == 123 ? 2 : 3;
        if constexpr (x & 1) r4[x >> 1] = ldsr<f32x4>(a_fr + (x >> 1) * 64);
        else m4[x >> 1] = ldsr<f32x4>(a_fr + 128 + (x >> 1) * 64);
      }
      if constexpr (m >= 100 && m < 124 && (m - 100) % 3 == 0) DMA_W(5, slotB, (m - 100) / 3);
      SB();
    });
  }
#pragma unroll
  for (int x = 0; x < 3; ++x) {
    const float y = xrow_sum4(__uint_as_float(cz[1][x][0]), __uint_as_float(cz[1][x][1]), __uint_as_float(cz[1][x][0]), __uint_as_float(cz[1][x][1]));
    auto sw = __builtin_amdgcn_permlane16_swap(__float_as_uint(y), __float_as_uint(y), false, false);
    csb[1][x] = f32x2{__uint_as_float(sw[0]), __uint_as_float(sw[1])};
  }
#pragma unroll
  for (int k = 0; k < 6; ++k) PINA(acc[k >> 1][k & 1]);
  f32x4 z2v[3][2];
  {
    f32x4 rs[2], rq[2];
#pragma unroll
    for (int t = 0; t < 2; ++t) {
#pragma unroll
      for (int k = 0; k < 3; ++k) {
        const f32x4 a = acc[k][t];
        const f32x2 zl = gelu2(fma2(lo2(a), lo2(r4[t]), fma2(lo2(m4[t]), bc2(csb[1][k][0]), bc2(csb[1][k][1]))));
        const f32x2 zh = gelu2(fma2(hi2(a), hi2(r4[t]), fma2(hi2(m4[t]), bc2(csb[1][k][0]), bc2(csb[1][k][1]))));
        z2v[k][t] = f32x4{zl[0], zl[1], zh[0], zh[1]};
        if (k == 0) { rs[t] = z2v[k][t]; rq[t] = z2v[k][t] * z2v[k][t]; }
        else { rs[t] += z2v[k][t]; rq[t] += z2v[k][t] * z2v[k][t]; }
        if (((t * 3 + k) & 1) == 0) {
#pragma unroll
          for (int f = (t * 3 + k) * 4; f < (t * 3 + k) * 4 + 8; ++f) wg[f % 3][f / 3] = LOAD_WP(0, f % 3, f / 3);
        } else {
          PIN(z2v[k][t]);
          SB();
        }
      }
    }
    float v8[8], v4[4], v2[2];
    const bool b3 = c & 8, b2 = c & 4, b1 = c & 2, b0 = c & 1;
#pragma unroll
    for (int r_ = 0; r_ < 4; ++r_) {
      const float a0 = rs[0][r_] + dpp<0x128>(rs[0][r_]), a1 = rs[1][r_] + dpp<0x128>(rs[1][r_]);
      const float q0 = rq[0][r_] + dpp<0x128>(rq[0][r_]), q1 = rq[1][r_] + dpp<0x128>(rq[1][r_]);
      v8[2 * r_] = b3 ? a1 : a0; v8[2 * r_ + 1] = b3 ? q1 : q0;
    }
#pragma unroll
    for (int j = 0; j < 4; ++j) {
      const float x0 = v8[j] + dpp<0x141>(v8[j]), x1 = v8[4 + j] + dpp<0x141>(v8[4 + j]);
      v4[j] = b2 ? x1 : x0;
    }
#pragma unroll
    for (int j = 0; j < 2; ++j) {
      const float x0 = v4[j] + dpp<0x4E>(v4[j]), x1 = v4[2 + j] + dpp<0x4E>(v4[2 + j]);
      v2[j] = b1 ? x1 : x0;
    }
    const float x0 = v2[0] + dpp<0xB1>(v2[0]), x1 = v2[1] + dpp<0xB1>(v2[1]);
    const float outv = b0 ? x1 : x0;
    __builtin_amdgcn_raw_buffer_store_b32(nomark(__float_as_uint(outv)), rws, lane * 4, WS_ST2 + ((mt * 2 + kh) * 16 + nt * 2 + uh) * 256, SC1);
  }
  SB();
#pragma unroll
  for (int tu = 0; tu < 4; ++tu)
#pragma unroll
    for (int k = 0; k < 3; ++k) acc[k][tu] = f32x4{0.f, 0.f, 0.f, 0.f};
  unsigned k3v[6];
  {
    const unsigned k0 = l16 + w * 6144, w3 = (w + 1) >> 2;
    k3v[0] = k0; k3v[1] = k0 + 2048 - w3 * 1024; k3v[2] = k3v[1] + 1024; k3v[3] = k0 + 5120 - w3 * 2048;
    k3v[4] = k0 + 1024 + (w3 << 30); k3v[5] = k3v[4] + 3072;
  }
  v2u x1h[2][2];
  v4u wo[6][4];
#define LOAD_WO(f) wo[(f) >> 2][(f) & 3] = ld128<SC1>(rws, k3v[(f) >> 2], WS_WOT + (nt * 4 + ((f) & 3)) * 22 * 1024)
  auto dma_sel2 = [&](int i, int x) __attribute__((always_inline)) {
    if (i < 4) dma_sel(IC<6>{}, slotA, (i - 2) * 4 + x); else dma_sel(IC<7>{}, slotB, (i - 4) * 4 + x);
  };
  f32x4 pt[4];
  float* c1s = st + 768 + w * 96;
#define LOAD_PT(i) asf(ld128<SC1>(rws, l16 + (i) * 1024, WS_ST2 + (mt * 2 + kh) * 4096))
  SB();
#pragma unroll
  for (int i = 0; i < 8; ++i) {
#pragma unroll
    for (int j = 0; j < 12; ++j) {
      const int tu = j / 3, k = j % 3;
      const v4u a = {ah[i & 1][tu][0][0], ah[i & 1][tu][0][1], ah[i & 1][tu][1][0], ah[i & 1][tu][1][1]};
      acc[k][tu] = mfma(wg[k][i], a, acc[k][tu]);
      SB();
      if (j < 8 && i < 7) ah[(i + 1) & 1][j >> 1][j & 1] = AHALF(j >> 1, i + 1, j & 1);
      if (j < 4 && i == 7) x1h[j >> 1][j & 1] = trread(smem + x1o + (j >> 1) * 4096 + (j & 1) * 512);
      if (i == 0 && j == 0) asm volatile("s_waitcnt vmcnt(24)" ::: "memory");
      if (i < 2 && j >= 4) wq[4 + i][j - 4] = READ_W(i * 8192, j - 4);
      if (i == 3 && j == 0) {
#pragma unroll
        for (int f = 0; f < 16; ++f) PINA(wq[4 + (f >> 3)][f & 7]);
      }
      if (i == 2 && j == 8) USE(wg[2][7]);
      if (i >= 2 && i < 6 && j == 9) dma_sel2(i, 0);
      if (i >= 3 && i < 7 && (j == 1 || j == 4 || j == 7)) dma_sel2(i - 1, (j + 2) / 3);
      if (j >= 8 && i == 6) pt[j - 8] = LOAD_PT(j - 8);
      if (i == 7 && j >= 8) LOAD_WO(j - 8);
      if (i == 5 && j == 10) {
        asm volatile("s_waitcnt vmcnt(30)" ::: "memory");
        __builtin_amdgcn_s_barrier();
      }
      SB();
    }
  }
#pragma unroll
  for (int k = 0; k < 3; ++k)
#pragma unroll
    for (int t = 0; t < 2; ++t) xch[(w * 6 + k * 2 + t) * 64 + lane] = acc[k][2 + t];
  SB();
  {
    unsigned m = 0;
#pragma unroll
    for (int i = 0; i < 4; ++i) m = macc4(m, __builtin_bit_cast(v4u, pt[i]));
    RETRY_BEGIN(m)
#pragma unroll
      for (int i = 0; i < 4; ++i) { pt[i] = LOAD_PT(i); m = macc4(m, __builtin_bit_cast(v4u, pt[i])); }
    RETRY_END(m)
    if (tid < 4) hint_add(p.ws, 64 + (nt * 4 + (mt >> 3)) * 4 + tid);
    const f32x4 sx = (pt[0] + pt[1]) + (pt[2] + pt[3]);
    const float tv = xrow_sum4(sx[0], sx[2], sx[1], sx[3]);
    auto sw = __builtin_amdgcn_permlane32_swap(__float_as_uint(tv), __float_as_uint(tv), false, false);
    const float mm = __uint_as_float(sw[0]) * (1.0f / FH);
    const float r = __builtin_amdgcn_rsqf(fmaxf(__uint_as_float(sw[1]) * (1.0f / FH) - mm * mm, 0.f) + EPS);
    float* f2 = st + 512 + w * 64;
    f2[(q >> 1) * 32 + 2 * c + (q & 1)] = (q >> 1) ? -r * mm : r;
#pragma unroll
    for (int t = 0; t < 2; ++t) { r4[t] = *(const f32x4*)(f2 + q * 8 + t * 4); m4[t] = *(const f32x4*)(f2 + 32 + q * 8 + t * 4); }
#pragma unroll
    for (int k = 0; k < 3; ++k) {
      v4u o;
#pragma unroll
      for (int t = 0; t < 2; ++t) {
        o[2 * t] = nomark(pk2(fma2(lo2(z2v[k][t]), lo2(r4[t]), lo2(m4[t]))));
        o[2 * t + 1] = pk2(fma2(hi2(z2v[k][t]), hi2(r4[t]), hi2(m4[t])));
      }
      st128<SC1>(o, rws, z2o[k], 0);
    }
  }
  c1s[lane] = xrow_sum4(__uint_as_float(cz[0][0][0]), __uint_as_float(cz[0][0][1]), __uint_as_float(cz[0][1][0]), __uint_as_float(cz[0][1][1]));
  c1s[64 + (lane & 31)] = xrow_sum4(__uint_as_float(cz[0][2][0]), __uint_as_float(cz[0][2][1]), __uint_as_float(cz[0][2][0]), __uint_as_float(cz[0][2][1]));
  st[1408 + lane] = rbf((__uint_as_float(gq[0]) + __uint_as_float(gq[1])) + (__uint_as_float(gq[2]) + __uint_as_float(gq[3])));
  SB();
  f32x4 z1f[3][2];
  constexpr int RD = 12;
  v4u zf[RD];
#define ZUNIT(ps) ((ps) < 2 ? ut0 + (ps) : (ps) < 4 ? uo0 + (ps) - 2 : (ps) == 4 ? ut2 : uo2)
#define LOAD_Z(n) ld128<SC1>(rws, zv[((n) >> 1) & 7] + ((n) & 1) * 1024, WS_Z2 + (ZUNIT((n) >> 4) * 64 + w * 16) * 1024 + ((n) >= 64 ? oob3 : 0u))
  unsigned zv[8];
#pragma unroll
  for (int i = 0; i < 8; ++i) { zv[i] = l16 + ((CKMAP(i) + kr) & 7) * 2048; PIN(zv[i]); }
  {
    bar();
#pragma unroll
    for (int k = 0; k < 6; ++k) PINA(acc[k >> 1][k & 1]);
    f32x4 oth[3][2], cs1[3], bs1[3];
    float r1[2], m1[2];
#pragma unroll
    for (int t = 0; t < 2; ++t) { r1[t] = fin[t * 16 + c]; m1[t] = fin[32 + t * 16 + c]; }
#pragma unroll
    for (int k = 0; k < 3; ++k) {
      cs1[k] = *(const f32x4*)(c1s + k * 32 + q * 4); bs1[k] = *(const f32x4*)(c1s + k * 32 + 16 + q * 4);
#pragma unroll
      for (int t = 0; t < 2; ++t) oth[k][t] = xch[((w ^ 1) * 6 + k * 2 + t) * 64 + lane];
    }
#pragma unroll
    for (int i = 4; i < WO0; ++i) LOAD_WO(i);
    SB();
    f32x2 z1a[6][2];
#pragma unroll
    for (int i = 0; i < 6; ++i) {
      const int k = i >> 1, t = i & 1;
      const f32x4 a = acc[k][t] + oth[k][t];
      z1a[i][0] = fma2(lo2(a), bc2(r1[t]), fma2(lo2(cs1[k]), bc2(m1[t]), lo2(bs1[k])));
      z1a[i][1] = fma2(hi2(a), bc2(r1[t]), fma2(hi2(cs1[k]), bc2(m1[t]), hi2(bs1[k])));
    }
#pragma unroll
    for (int i = 0; i < GELU_PRE; ++i) {
      const f32x2 lo = gelu2(z1a[i][0]), hi = gelu2(z1a[i][1]);
      z1f[i >> 1][i & 1] = f32x4{lo[0], lo[1], hi[0], hi[1]};
      PIN(z1f[i >> 1][i & 1]);
    }
#pragma unroll
    for (int i = GELU_PRE; i < 6; ++i) { PIN(z1a[i][0]); PIN(z1a[i][1]); }
    SB();
#pragma unroll
    for (int f = 0; f < 16; ++f) wq[6 + (f >> 3)][f & 7] = READ_W((f >> 3) * 8192, f & 7);
    SB();
    hint_wait(rws, 0u, WS_CNT + (64 + (nt * 4 + w) * 4 + (xcd >> 1)) * 128, 8u);
#pragma unroll
    for (int n = 0; n < RD; ++n) zf[n] = LOAD_Z(n);
    SB();
#pragma unroll
    for (int i = GELU_PRE; i < 6; ++i) {
      const f32x2 lo = gelu2(z1a[i][0]), hi = gelu2(z1a[i][1]);
      z1f[i >> 1][i & 1] = f32x4{lo[0], lo[1], hi[0], hi[1]};
      PIN(z1f[i >> 1][i & 1]);
    }
  }
  f32x4* xb = (f32x4*)(smem + LDS_A) + lane;
  f32x4* yb = (f32x4*)(smem + LDS_A + 49152) + lane;
  f32x4 ac2[2][4], ini[4], yo[2];
  const float bs0 = bflo(bspq[0]), bs1_ = bfhi(bspq[0]);
  f32x4 bq0 = {bs0, bs0, bs0, bs0}, bq1 = {bs1_, bs1_, bs1_, bs1_};
  const f32x4 z4 = {0.f, 0.f, 0.f, 0.f};
  PINA(bq0); PINA(bq1);
  unsigned vmk = l16 + WS_V + ((mt * 4 + w) * 22 + pr0) * 1024;
  gu32* p_fl4 = (gu32*)(p.ws + WS_CNT + (192 + mt * 2 + (tid & 1)) * 128);
  PIN(vmk); PIN(mkv);
  unsigned voff[3];
  voff[0] = l16 + WS_V + ((mt * 4 + 2 * kh) * 22 + (uo0 >> 1)) * 1024;
  voff[1] = l16 + WS_V + ((mt * 4 + 2 * kh) * 22 + (uo2 >> 1)) * 1024 + (uo2 & 1) * 8 + oob3;
  voff[2] = voff[0] + 22 * 1024;
  PIN(voff[0]); PIN(voff[1]); PIN(voff[2]);
  float ev[4];
  v4u eo[2];
  f32x4 yx[2][2];
  v4u vs[6][4];
#define LOAD_V(f) vs[(f) >> 2][(f) & 3] = ld128<SC1>(rws, k3v[(f) >> 2], WS_V + (mt * 4 + (((f) & 3) ^ w)) * 22 * 1024)
  SB();
  sfor<6>([&](auto P) __attribute__((always_inline)) {
    constexpr int ps = decltype(P)::value, cur = ps & 1, prv = cur ^ 1;
    constexpr bool prevT = ps == 1 || ps == 2 || ps == 5, epi = ps == 3 || ps == 4;
    unsigned zm;
    sfor<64>([&](auto G) __attribute__((always_inline)) {
      constexpr int g = decltype(G)::value, ks = g >> 2, tu = g & 3, ck = ks >> 1, k2 = ks & 1, n = ps * 16 + ks;
      const f32x4 cin = ks > 0 ? ac2[cur][tu] : (ps == 2 || ps == 3) ? ini[tu] : ps == 5 ? z4 : tu == 0 ? bq0 : tu == 1 ? bq1 : z4;
      ac2[cur][tu] = mfma(zf[n % RD], wq[ck][k2 * 4 + tu], cin);
      SB();
      if constexpr (tu == 1) { zm = ks == 0 ? (zf[n % RD][0] > zf[n % RD][2] ? zf[n % RD][0] : zf[n % RD][2]) : macc(zm, zf[n % RD]); PIN(zm); }
      if constexpr (tu == 3) {
        if constexpr (n + RD < 96) zf[n % RD] = LOAD_Z(n + RD);
        else if constexpr (WO0 + n + RD - 96 < 16) LOAD_WO(WO0 + n + RD - 96);
      }
      if constexpr (prevT && g >= 2 && g < 18 && (g & 3) == 2) xb[((w * 3 + (ps == 5 ? 2 : ps - 1)) * 4 + (g >> 2)) * 64] = ac2[prv][g >> 2];
      if constexpr (epi && (g == 2 || g == 6)) yb[((w * 3 + ps - 3) * 2 + (g >> 2)) * 64] = ac2[prv][2 + (g >> 2)];
      if constexpr (ps >= 1 && g == (epi ? 12 : 24)) {
        if constexpr (ps == 3) asm volatile("s_waitcnt vmcnt(20)" ::: "memory");
        bar();
      }
      if constexpr (epi && (g == 13 || g == 14)) yo[g - 13] = yb[(((w ^ 1) * 3 + ps - 3) * 2 + g - 13) * 64];
      if constexpr (epi && g >= 18 && g < 50 && (g & 1) == 0) {
        constexpr int t = (g - 18) / 16, ph = ((g - 18) % 16) >> 1, h = (ps - 3) * 2;
        if constexpr (ph == 0) PINA(ac2[prv][t]);
        if constexpr (ph < 4) { ev[ph] = ac2[prv][t][ph] + yo[t][ph]; PIN(ev[ph]); }
        else if constexpr (ph < 6) { ev[2 * ph - 8] *= z1f[ps - 3][t][2 * ph - 8]; PIN(ev[2 * ph - 8]); ev[2 * ph - 7] *= z1f[ps - 3][t][2 * ph - 7]; PIN(ev[2 * ph - 7]); }
        else if constexpr (ph == 6) { eo[t][h] = nomark(pk2(ev[0], ev[1])); PIN(eo[t]); }
        else {
          eo[t][h + 1] = pk2(ev[2], ev[3]);
          if constexpr (ps == 4) st128<SC1>(eo[t], rws, voff[0 + 2 * t], 0);
          else PIN(eo[t]);
        }
      }
      if constexpr ((ps == 1 || ps == 2) && g >= 48 && g < 56 && (g & 1) == 0) ini[(g - 48) >> 1] = xb[(((w ^ 2) * 3 + ps - 1) * 4 + ((g - 48) >> 1)) * 64];
      if constexpr (ps == 5 && (g == 25 || g == 26)) { yx[0][g - 25] = xb[(((w ^ 2) * 3 + 2) * 4 + g - 25) * 64]; yx[1][g - 25] = xb[(((w ^ 3) * 3 + 2) * 4 + 2 + g - 25) * 64]; }
      if constexpr (ps == 0 && (g == 22 || g == 38 || g == 54)) st128<SC1>(mkv, rws, vmk + (g - 22) / 16 * 1024, g == 54 ? oob3 : 0u);
      if constexpr (ps * 64 + g == HINT4_G) { if (tid < 2) (void)__hip_atomic_fetch_add(p_fl4, 1u, __ATOMIC_RELAXED, __HIP_MEMORY_SCOPE_AGENT); }
      if constexpr (ps == 2 && g == 40) PIN(p_fl4);
      if constexpr (ps == 5 && g == POLL4_G) flagv = __builtin_amdgcn_raw_buffer_load_b32(rws, 0, WS_CNT + (192 + mt * 2 + (xcd & 1)) * 128, SC1);
      if constexpr (ps == 5 && g == VPRE_G) {
        if (__builtin_expect(__builtin_amdgcn_readfirstlane(flagv) < 8u, 0)) hint_wait(rws, 0u, WS_CNT + (192 + mt * 2 + (xcd & 1)) * 128, 8u);
        MEMFENCE();
      }
      if constexpr (ps == 5 && g > VPRE_G && (g & 1) == 0 && (g - VPRE_G - 1) / 2 < 12) LOAD_V((g - VPRE_G - 1) / 2);
      SB();
    });
    if (__builtin_expect(any_mark(zm), 0)) {
      int l2 = lane;
      PIN(l2);
#pragma unroll
      for (int tu = 0; tu < 4; ++tu) ac2[cur][tu] = (ps == 2 || ps == 3) ? xb[(((w ^ 2) * 3 + ps - 2) * 4 + tu) * 64] : ps == 5 ? z4 : tu == 0 ? bq0 : tu == 1 ? bq1 : z4;
      if constexpr (ps < 5) {
        sfor<4>([&](auto K) __attribute__((always_inline)) {
          constexpr int k4 = decltype(K)::value * 4;
          v4u zz[4];
          unsigned sp = 0;
          do {
            MEMFENCE();
            zm = 0;
#pragma unroll
            for (int i = 0; i < 4; ++i) { zz[i] = LOAD_Z(ps * 16 + k4 + i); zm = macc(zm, zz[i]); }
          } while (any_mark(zm) & (++sp <= SPIN_CAP));
#pragma unroll
          for (int i = 0; i < 4; ++i)
#pragma unroll
            for (int tu = 0; tu < 4; ++tu) ac2[cur][tu] = mfma(zz[i], wq[(k4 + i) >> 1][(i & 1) * 4 + tu], ac2[cur][tu]);
        });
      } else
#pragma unroll 1
      for (int ks = 0; ks < 16; ++ks) {
        const int kk = ((CKMAP(ks >> 1) + kr) & 7) * 2 + (ks & 1);
        v4u zz, ww[4];
        unsigned sp = 0;
        do { MEMFENCE(); zz = ld128<SC1>(rws, l2 * 16 + kk * 1024, WS_Z2 + (ZUNIT(ps) * 64 + w * 16) * 1024 + (ps >= 4 ? oob3 : 0u)); } while (any_mark(macc(0u, zz)) & (++sp <= SPIN_CAP));
#pragma unroll
        for (int tu = 0; tu < 4; ++tu) ww[tu] = ld128<0>(rwsp, perm(tu ^ (2 * kh), l2 & 15) * 4096 + (l2 >> 4) * 16 + kk * 64, wsp_s);
#pragma unroll
        for (int tu = 0; tu < 4; ++tu) ac2[cur][tu] = mfma(zz, ww[tu], ac2[cur][tu]);
      }
    }
  });
  {
#pragma unroll
    for (int t = 0; t < 2; ++t) yb[((w * 3 + 2) * 2 + t) * 64] = ac2[1][2 + t];
#pragma unroll
    for (int t = 0; t < 2; ++t) { yx[0][t] = (yx[0][t] + yx[1][t]) + ac2[1][t]; PIN(yx[0][t]); }
    bar();
#pragma unroll
    for (int t = 0; t < 2; ++t) yo[t] = yb[(((w ^ 1) * 3 + 2) * 2 + t) * 64];
#pragma unroll
    for (int t = 0; t < 2; ++t) {
      const f32x4 g = (yx[0][t] + yo[t]) * z1f[2][t];
      st64<SC1>(v2u{nomark(pk2(lo2(g))), pk2(hi2(g))}, rws, voff[1], t * 22 * 1024);
    }
  }
  SB();
  f32x4 gb4[4];
  f32x2 x1f[2][4];
  f32x4 a3[4][4];
  unsigned a_rw, a_rr[3], o_out;

  {
    {
      unsigned m = 0;
      sfor<64>([&](auto G) __attribute__((always_inline)) {
        constexpr int g = decltype(G)::value, ks = g >> 4, tu = (g >> 2) & 3, e = g & 3;
        a3[e][tu] = mfma(wo[ks][e], vs[ks][tu], ks == 0 ? f32x4{0.f, 0.f, 0.f, 0.f} : a3[e][tu]);
        SB();
        if constexpr (e == 0 && g < 16) LOAD_V(12 + (g >> 2));
        if constexpr (e == 2 && g < 32) LOAD_WO(16 + (g >> 2));
        if constexpr (g >= LATE_G && g < LATE_G + 16 && (g & 1) == 0) LOAD_V(16 + (((g - LATE_G) >> 1) & 1) * 4 + ((((g - LATE_G) >> 2) + 1) & 3));
        if constexpr (e == 1) { m = macc(m, vs[ks][tu]); PIN(m); }
        SB();
      });
      if (__builtin_expect(any_mark(m), 0)) {
        sfor<4>([&](auto K) __attribute__((always_inline)) {
          constexpr int ks = decltype(K)::value;
          unsigned sp = 0;
          do {
            MEMFENCE();
            m = 0;
#pragma unroll
            for (int tu = 0; tu < 4; ++tu) { LOAD_V(ks * 4 + tu); m = macc(m, vs[ks][tu]); }
          } while (any_mark(m) & (++sp <= SPIN_CAP));
#pragma unroll
          for (int tu = 0; tu < 4; ++tu)
#pragma unroll
            for (int e = 0; e < 4; ++e) a3[e][tu] = mfma(wo[ks][e], vs[ks][tu], ks == 0 ? f32x4{0.f, 0.f, 0.f, 0.f} : a3[e][tu]);
        });
      }
    }
#pragma unroll
    for (int i = 0; i < 4; ++i) gb4[i] = *(const f32x4*)(st + 1408 + (i >> 1) * 32 + q * 8 + (i & 1) * 4);
#pragma unroll
    for (int i = 0; i < 8; ++i) { x1f[i >> 2][i & 3] = unpk(x1h[i >> 2][(i >> 1) & 1][i & 1]); PIN(x1f[i >> 2][i & 3]); }
    int lz = lane;
    PIN(lz);
    a_rw = lds0 + LDS_W + w * 12288 + lz * 16;
#pragma unroll
    for (int rel = 1; rel < 4; ++rel) { a_rr[rel - 1] = lds0 + LDS_W + ((w ^ rel) * 3 + rel - 1) * 4096 + lz * 16; PIN(a_rr[rel - 1]); }
    o_out = (mt * 64 + perm(w, lz & 15)) * 1024 + (lz >> 4) * 16 + nt * 128;
    PIN(a_rw); PIN(o_out);
    SB();
    unsigned m = 0;
#pragma unroll
    for (int f = 16; f < 24; ++f) m = macc(m, vs[f >> 2][f & 3]);
    RETRY_BEGIN(m)
#pragma unroll
      for (int f = 16; f < 24; ++f) { LOAD_V(f); m = macc(m, vs[f >> 2][f & 3]); }
    RETRY_END(m)
    sfor<32>([&](auto G) __attribute__((always_inline)) {
      constexpr int g = decltype(G)::value, tu = ((g >> 3) + 1) & 3, ks = 4 + ((g >> 2) & 1), e = g & 3;
      a3[e][tu] = mfma(wo[ks][e], vs[ks][tu], a3[e][tu]);
      SB();
      if constexpr (g >= 8 && (g & 1) == 0) ldsw<f32x4>(a_rw + (((g >> 3) - 1) * 4 + ((g >> 1) & 3)) * 1024, a3[(g >> 1) & 3][g >> 3]);
      SB();
    });
  }
  {
#pragma unroll
    for (int e = 0; e < 4; ++e) PIN(a3[e][0]);
    bar();
    f32x4 o3[4][3];
#pragma unroll
    for (int e = 0; e < 4; ++e)
#pragma unroll
      for (int rel = 1; rel < 4; ++rel) o3[e][rel - 1] = ldsr<f32x4>(a_rr[rel - 1] + e * 1024);
    const rsrc_t rout = mkrs(p.out, L * D * 2);
    SB();
#pragma unroll
    for (int pp = 0; pp < 2; ++pp) {
      v4u o;
#pragma unroll
      for (int h = 0; h < 2; ++h) {
        const int e = pp * 2 + h;
        const f32x4 y = (a3[e][0] + o3[e][0]) + (o3[e][1] + o3[e][2]);
        o[2 * h] = pk2(fma2(lo2(y), lo2(gb4[e]), x1f[pp][2 * h]));
        o[2 * h + 1] = pk2(fma2(hi2(y), hi2(gb4[e]), x1f[pp][2 * h + 1]));
      }
      st128<0>(o, rout, o_out + pp * 64, 0);
    }
  }
  if (b == 0 && tid == 0) *(unsigned*)(p.ws + WS_EPOCH) = tag_s;
  asm volatile("adaln_code_end:");
}

extern "C" void kernel_launch(void* const* d_in, const int* in_sizes, int n_in, void* d_out, int out_size, void* d_ws,
                              size_t ws_size, hipStream_t stream) {
  Params p;
  p.u = (const u16*)d_in[0];
  p.lar = (const u16*)d_in[1];
  p.aim = (const u16*)d_in[2];
  p.cre = (const u16*)d_in[3];
  p.cim = (const u16*)d_in[4];
  p.ldt = (const u16*)d_in[5];
  p.dsk = (const u16*)d_in[6];
  p.win = (const u16*)d_in[8];
  p.wsp = (const u16*)d_in[9];
  p.bsp = (const u16*)d_in[10];
  p.wout = (const u16*)d_in[11];
  p.c = (const u16*)d_in[12];
  p.wmod = (const u16*)d_in[13];
  p.out = (u16*)d_out;
  p.ws = (unsigned char*)d_ws;
  (void)hipFuncSetAttribute((const void*)adaln_mega, hipFuncAttributeMaxDynamicSharedMemorySize, MEGA_LDS);
  adaln_mega<<<256, 256, MEGA_LDS, stream>>>(p);
}
```

```cpp
#include <hip/hip_runtime.h>
#include <hip/hip_bf16.h>
#include <stdint.h>
#include <utility>

#define LAS __attribute__((address_space(3)))
typedef unsigned v4u __attribute__((ext_vector_type(4)));
typedef unsigned v2u __attribute__((ext_vector_type(2)));
typedef float f32x4 __attribute__((ext_vector_type(4)));
typedef float f32x2 __attribute__((ext_vector_type(2)));
typedef short bf16x8 __attribute__((ext_vector_type(8)));
typedef _Float16 f16x8 __attribute__((ext_vector_type(8)));
typedef _Float16 f16x2 __attribute__((ext_vector_type(2)));
typedef __bf16 bf16x2_t __attribute__((ext_vector_type(2)));
typedef unsigned short u16;
typedef __amdgpu_buffer_rsrc_t rsrc_t;

constexpr int L = 2048, D = 512, NM = 128, FH = 688, FF = 1376;
constexpr float EPS = 1e-5f;
constexpr int SC1 = 16;
constexpr unsigned OOB = 0x40000000u;
constexpr unsigned SPIN_CAP = 1u << 16;

constexpr unsigned WS_EPOCH = 0;
constexpr unsigned WS_CNT = 4096;
constexpr unsigned WS_SR = WS_CNT + 512 * 128;
constexpr unsigned WS_SB = WS_SR + 2048 * 8;
constexpr unsigned WS_MODG = WS_SB + 2048 * 8;
constexpr unsigned WS_CSB = WS_MODG + 3072 * 32;
constexpr unsigned WS_ST2 = WS_CSB + 88 * 16 * 32;
constexpr unsigned WS_UG = 1u << 20;
static_assert(WS_ST2 + 32 * 2 * 16 * 32 * 8 <= WS_UG, "small buffers");
constexpr unsigned WS_X1T = WS_UG + 256 * 2048 * 8;
constexpr unsigned WS_WP = WS_X1T + 512 * 2048 * 2;
constexpr unsigned WS_WOT = WS_WP + 88 * 16 * 1024;
constexpr unsigned WS_Z2 = WS_WOT + 32 * 22 * 1024;
constexpr unsigned WS_V = WS_Z2 + 44 * 64 * 1024;
constexpr unsigned WS_END = WS_V + 128 * 22 * 1024;

struct Params {
  const u16 *u, *lar, *aim, *cre, *cim, *ldt, *dsk, *win, *wsp, *bsp, *wout, *c, *wmod;
  u16* out;
  unsigned char* ws;
};

#define DI __device__ __forceinline__
template <int V> struct IC { static constexpr int value = V; };
template <class F, int... I> DI void sfor_(F&& f, std::integer_sequence<int, I...>) { int d[] = {(f(IC<I>{}), 0)...}; (void)d; }
template <int N, class F> DI void sfor(F&& f) { sfor_(f, std::make_integer_sequence<int, N>{}); }
DI float bf2f(unsigned b) { return __builtin_bit_cast(float, b << 16); }
DI float bflo(unsigned w) { return __builtin_bit_cast(float, w << 16); }
DI float bfhi(unsigned w) { return __builtin_bit_cast(float, w & 0xFFFF0000u); }
DI f32x2 unpk(unsigned w) { return f32x2{bflo(w), bfhi(w)}; }
DI unsigned pk2(f32x2 v) { return __builtin_bit_cast(unsigned, __builtin_convertvector(v, bf16x2_t)); }
DI unsigned pk2(float lo, float hi) { return pk2(f32x2{lo, hi}); }
DI unsigned nomark(unsigned x) { return x < 0xFFFFFFFEu ? x : 0xFFFFFFFEu; }
DI float rbf(float x) { return bflo(pk2(x, 0.f)); }
DI f32x2 fma2(f32x2 a, f32x2 b, f32x2 c) { return __builtin_elementwise_fma(a, b, c); }
DI f32x2 lo2(f32x4 v) { return f32x2{v[0], v[1]}; }
DI f32x2 hi2(f32x4 v) { return f32x2{v[2], v[3]}; }
DI f32x2 bc2(float x) { return f32x2{x, x}; }
DI f32x4 mfma(v4u a, v4u b, f32x4 c) {
  return __builtin_amdgcn_mfma_f32_16x16x32_bf16(__builtin_bit_cast(bf16x8, a), __builtin_bit_cast(bf16x8, b), c, 0, 0, 0);
}
DI f32x4 mfma_h(v4u a, v4u b, f32x4 c) {
  return __builtin_amdgcn_mfma_f32_16x16x32_f16(__builtin_bit_cast(f16x8, a), __builtin_bit_cast(f16x8, b), c, 0, 0, 0);
}
DI float dot2(unsigned a, unsigned b, float acc) {
  return __builtin_amdgcn_fdot2_f32_bf16(__builtin_bit_cast(bf16x2_t, a), __builtin_bit_cast(bf16x2_t, b), acc, false);
}
constexpr unsigned ONES = 0x3F803F80u;
DI int perm(int u, int c) { return 32 * (u >> 1) + 8 * (c >> 2) + 4 * (u & 1) + (c & 3); }
template <int CTRL> DI float dpp(float x) {
  return __builtin_bit_cast(float, __builtin_amdgcn_mov_dpp(__builtin_bit_cast(int, x), CTRL, 0xf, 0xf, true));
}
DI float row16_sum(float x) {
  x += dpp<0xB1>(x); x += dpp<0x4E>(x); x += dpp<0x141>(x); x += dpp<0x128>(x);
  return x;
}
DI float row16_max(float x) {
  x = fmaxf(x, dpp<0xB1>(x)); x = fmaxf(x, dpp<0x4E>(x)); x = fmaxf(x, dpp<0x141>(x)); x = fmaxf(x, dpp<0x128>(x));
  return x;
}
DI float xrow_sum(float x) {
  auto s = __builtin_amdgcn_permlane16_swap(__float_as_uint(x), __float_as_uint(x), false, false);
  x = __uint_as_float(s[0]) + __uint_as_float(s[1]);
  auto t = __builtin_amdgcn_permlane32_swap(__float_as_uint(x), __float_as_uint(x), false, false);
  return __uint_as_float(t[0]) + __uint_as_float(t[1]);
}
DI float xrow_sum4(float a, float b, float c, float d) {
  auto s = __builtin_amdgcn_permlane16_swap(__float_as_uint(a), __float_as_uint(b), false, false);
  auto t = __builtin_amdgcn_permlane16_swap(__float_as_uint(c), __float_as_uint(d), false, false);
  const float x = __uint_as_float(s[0]) + __uint_as_float(s[1]), y = __uint_as_float(t[0]) + __uint_as_float(t[1]);
  auto z = __builtin_amdgcn_permlane32_swap(__float_as_uint(x), __float_as_uint(y), false, false);
  return __uint_as_float(z[0]) + __uint_as_float(z[1]);
}
DI float xrow_max(float x) {
  auto s = __builtin_amdgcn_permlane16_swap(__float_as_uint(x), __float_as_uint(x), false, false);
  x = fmaxf(__uint_as_float(s[0]), __uint_as_float(s[1]));
  auto t = __builtin_amdgcn_permlane32_swap(__float_as_uint(x), __float_as_uint(x), false, false);
  return fmaxf(__uint_as_float(t[0]), __uint_as_float(t[1]));
}
DI f32x2 gelu2(f32x2 x) {
  const float k0 = -2.0f * 0.7978845608028654f * 1.4426950408889634f, k1 = k0 * 0.044715f;
  const f32x2 a = x * fma2(x * x, bc2(k1), bc2(k0));
  const f32x2 e = f32x2{__builtin_amdgcn_exp2f(a[0]), __builtin_amdgcn_exp2f(a[1])} + bc2(1.0f);
  return x * f32x2{__builtin_amdgcn_rcpf(e[0]), __builtin_amdgcn_rcpf(e[1])};
}
DI rsrc_t mkrs(const void* ptr, unsigned bytes) { return __builtin_amdgcn_make_buffer_rsrc((void*)ptr, 0, (int)bytes, 0x00020000); }
template <int AUX> DI v4u ld128(rsrc_t rs, unsigned v, unsigned s) { return __builtin_amdgcn_raw_buffer_load_b128(rs, (int)v, (int)s, AUX); }
template <int AUX> DI v2u ld64(rsrc_t rs, unsigned v, unsigned s) { return __builtin_amdgcn_raw_buffer_load_b64(rs, (int)v, (int)s, AUX); }
template <int AUX> DI void st128(v4u x, rsrc_t rs, unsigned v, unsigned s) { __builtin_amdgcn_raw_buffer_store_b128(x, rs, (int)(v + s), 0, AUX); }
template <int AUX> DI void st64(v2u x, rsrc_t rs, unsigned v, unsigned s) { __builtin_amdgcn_raw_buffer_store_b64(x, rs, (int)v, (int)s, AUX); }
DI f32x4 asf(v4u x) { return __builtin_bit_cast(f32x4, x); }
DI float sum4(f32x4 x) { return (x[0] + x[1]) + (x[2] + x[3]); }
DI unsigned umax3(unsigned a, unsigned b, unsigned c) { unsigned m = a > b ? a : b; return m > c ? m : c; }
DI unsigned macc4(unsigned m, v4u x) { return umax3(umax3(m, x[0], x[1]), x[2], x[3]); }
DI unsigned macc(unsigned m, v4u x) { return umax3(m, x[0], x[2]); }
DI bool any_mark(unsigned m) { return __builtin_amdgcn_ballot_w64(m == 0xFFFFFFFFu) != 0ull; }
#define SB() __builtin_amdgcn_sched_barrier(0)
#define PIN(x) asm volatile("" : "+v"(x))
#define PINA(x) asm volatile("" : "+a"(x))
#define USE(x) asm volatile("" ::"v"(x))
#define RETRY_BEGIN(m) if (__builtin_expect(any_mark(m), 0)) { unsigned sp_ = 0; do { MEMFENCE(); m = 0;
#define RETRY_END(m) } while (any_mark(m) & (++sp_ <= SPIN_CAP)); }
#define MEMFENCE() asm volatile("" ::: "memory")
DI void bar() { asm volatile("s_waitcnt lgkmcnt(0)" ::: "memory"); __builtin_amdgcn_s_barrier(); asm volatile("" ::: "memory"); }

DI f32x2 cmul(f32x2 a, f32x2 b) {
  f32x2 t = bc2(a[0]) * b;
  asm("v_pk_fma_f32 %0, %1, %2, %0 op_sel:[1,1,0] op_sel_hi:[1,0,1] neg_lo:[0,1,0]" : "+v"(t) : "v"(a), "v"(b));
  return t;
}
DI f32x2 cexp_p(float are2, float rev_hi, float rev_lo, float p) {
  const float mag = __builtin_amdgcn_exp2f(are2 * p);
  const float hi = rev_hi * p;
  const float fr = (hi - __builtin_rintf(hi)) + rev_lo * p;
  return f32x2{mag * __builtin_amdgcn_cosf(fr), mag * __builtin_amdgcn_sinf(fr)};
}
DI float exp_acc(float x) {
  const float hi = x * 1.4426950408889634f;
  const float lo = __builtin_fmaf(x, 1.4426950408889634f, -hi) + x * 1.925963033500011e-8f;
  const float e = __builtin_amdgcn_exp2f(hi);
  return __builtin_fmaf(e, lo * 0.6931471805599453f, e);
}
DI unsigned pkh(float a, float b) {
  f16x2 v = {(_Float16)a, (_Float16)b};
  return __builtin_bit_cast(unsigned, v);
}

constexpr int CV_RK = 4160;
constexpr int CV_H = 2 * CV_RK;
constexpr int CV_CH = CV_H + 4608;
constexpr int LDS_A = 0;
constexpr int LDS_U = 2 * CV_CH;
constexpr int LDS_GS = LDS_U + 8192;
constexpr int LDS_TAB = 36864;
constexpr int KP_CH = 13840;
constexpr int LDS_KP = LDS_TAB + 2 * 64 * 528 + 16;
constexpr int LDS_LN = 0, LDS_MR = 8448;
constexpr int LDS_WI = 145472, LDS_WO = LDS_WI + 10240;
constexpr int LDS_X = 65536;
constexpr int LDS_S = LDS_X + 24576;
constexpr int LDS_W = LDS_S + 6144;
constexpr int MEGA_LDS = LDS_W + 65536 + 2048;
static_assert(LDS_WO + 4608 <= MEGA_LDS && LDS_KP + 2 * KP_CH <= LDS_WI && LDS_S >= 73728, "lds");
static_assert(LDS_GS + 1280 <= LDS_TAB && LDS_KP + 2 * KP_CH <= MEGA_LDS && MEGA_LDS <= 160 * 1024, "lds");
constexpr int FLAG_AT = 48, POLL_AT = 130, WF_EARLY = 5, LATE_SLEEP = 6, GELU_PRE = 4, WO0 = 12, HINT4_G = 280, POLL4_G = 16, VPRE_G = 37, LATE_G = 44;
static_assert(FLAG_AT >= 36 && FLAG_AT <= 56 && FLAG_AT % 4 == 0, "flag position");
static_assert(HINT4_G > 3 * 64 + 12, "behind the marker barrier");
#define CKMAP(ck) ((int)((0x75316420u >> (4 * (ck))) & 7u))

template <class T> DI T ldsr(unsigned a) { return *(const LAS T*)(size_t)a; }
template <class T> DI void ldsw(unsigned a, T v) { *(LAS T*)(size_t)a = v; }
DI v2u trread(const unsigned char* ptr) {
  typedef short v4i16_t __attribute__((ext_vector_type(4)));
  return __builtin_bit_cast(v2u, __builtin_amdgcn_ds_read_tr16_b64_v4i16((LAS v4i16_t*)ptr));
}
DI v2u trread_a(unsigned a) {
  typedef short v4i16_t __attribute__((ext_vector_type(4)));
  return __builtin_bit_cast(v2u, __builtin_amdgcn_ds_read_tr16_b64_v4i16((LAS v4i16_t*)(size_t)a));
}
struct CvOrder { int qq[36], pp[36]; bool first[36]; };
constexpr CvOrder cv_order() {
  CvOrder o{};
  int n = 0;
  for (int m = 0; m < 8; ++m) {
    for (int j = 0; j < m; ++j) if (m + j < 8) { o.qq[n] = m; o.pp[n] = j; ++n; }
    for (int j = 0; j <= m; ++j) if (m + j < 8) { o.qq[n] = j; o.pp[n] = m; ++n; }
  }
  for (int i = 0; i < 36; ++i) {
    o.first[i] = true;
    for (int j = 0; j < i; ++j) if (o.qq[j] + o.pp[j] == o.qq[i] + o.pp[i]) o.first[i] = false;
  }
  return o;
}
constexpr CvOrder CVO = cv_order();
struct Z2Order { int u[128], kind[128], pos[128]; };
constexpr Z2Order z2_order() {
  Z2Order o{};
  int n = 0;
  for (int u = 0; u < 40; ++u) {
    int pos = 0;
    const bool st = u < 24 && (u < 8 || (u & 7) < 4);
    for (int k = 0; k < 5; ++k)
      if (k < 3 ? u >= 8 : st) { o.u[n] = u; o.kind[n] = k; o.pos[n] = pos++; ++n; }
  }
  return o;
}
constexpr Z2Order Z2O = z2_order();

typedef __attribute__((address_space(1))) unsigned gu32;
DI unsigned tx(unsigned m, unsigned x, unsigned tag) { return __builtin_amdgcn_bitop3_b32(m, x, tag, 0xF6); }
DI unsigned tacc(unsigned m, v4u x, unsigned tag) { return tx(tx(m, x[1], tag), x[3], tag); }
DI bool any_nz(unsigned m) { return __builtin_amdgcn_ballot_w64(m != 0u) != 0ull; }
DI void hint_add(unsigned char* ws, int line) { (void)__hip_atomic_fetch_add((gu32*)(ws + WS_CNT + line * 128), 1u, __ATOMIC_RELAXED, __HIP_MEMORY_SCOPE_AGENT); }
DI void hint_wait(rsrc_t rws, unsigned voff, unsigned soff, unsigned want) {
  unsigned sp = 0;
  for (;;) {
    MEMFENCE();
    const unsigned v = __builtin_amdgcn_raw_buffer_load_b32(rws, (int)voff, (int)soff, SC1);
    if (!((__builtin_amdgcn_ballot_w64(v < want) != 0ull) & (++sp <= SPIN_CAP))) break;
    __builtin_amdgcn_s_sleep(2);
  }
  MEMFENCE();
}

extern "C" __global__ void __launch_bounds__(256, 1) adaln_mega(Params p) {
  extern __shared__ __attribute__((aligned(16))) unsigned char smem[];
  const int tid = threadIdx.x, lane = tid & 63, w = __builtin_amdgcn_readfirstlane(tid >> 6);
  const int b = blockIdx.x, xcd = b & 7, idx = b >> 3;
  const int pb = xcd * 32 + idx;
  const unsigned l16 = lane * 16;
  v2u codepf;
  {
    unsigned long long pc;
    int rel_m, rel_e;
    asm volatile("s_getpc_b64 %0\n\ts_mov_b32 %1, adaln_mega@rel32@lo+4\n\ts_mov_b32 %2, adaln_code_end@rel32@lo+12" : "=s"(pc), "=s"(rel_m), "=s"(rel_e));
    const rsrc_t rcode = mkrs((const void*)(pc + (long long)rel_m), rel_e > rel_m ? (unsigned)(rel_e - rel_m) & ~15u : 0u);
    codepf = ld64<0>(rcode, tid * 8, idx * 2048);
  }
  SB();
  const rsrc_t ru = mkrs(p.u, L * D * 2);
  v4u xr[2];
  xr[0] = ld128<0>(ru, l16, (pb * 8 + w * 2) * 1024);
  xr[1] = ld128<0>(ru, l16 + 1024, (pb * 8 + w * 2) * 1024);
  const int mkg = b & 3, mcg = b >> 2;
  const unsigned cq = p.c[mkg * 128 + (tid & 3) * 32 + (tid >> 3)];
  const unsigned epoch = *(const unsigned*)(p.ws + WS_EPOCH);
  const rsrc_t rws = mkrs(p.ws, WS_END);
  __builtin_amdgcn_raw_buffer_store_b32(0u, rws, tid < 2 ? tid * 32768u : OOB, WS_CNT + b * 128, SC1);
  SB();
  const int cch = w >> 1, eh = w & 1, ns = tid & 127;
  const unsigned q_cre = p.cre[pb * 256 + tid], q_cim = p.cim[pb * 256 + tid], q_lar = p.lar[pb * 256 + tid], q_aim = p.aim[pb * 256 + tid];
  const unsigned q_ldt = p.ldt[pb * 2 + cch], q_dsk = p.dsk[pb * 2 + cch];
  v4u xm[4];
#define LOAD_XM() { \
  const rsrc_t rmod = mkrs(p.wmod, D * 3072 * 2); \
_Pragma("unroll") \
  for (int i = 0; i < 4; ++i) xm[i] = ld128<0>(rmod, (tid >> 3) * 6144 + (tid & 7) * 16, (mkg * 128 + i * 32) * 6144 + mcg * 128 + (b < 192 ? 0u : OOB)); }
  SB();
  LOAD_XM();
  SB();
  const int c = lane & 15, q = lane >> 4;
  const unsigned lds0 = (unsigned)(size_t)(LAS unsigned char*)smem;
  unsigned pos4[9];
  {
    const float f4 = (float)(8188 - tid * 32);
#pragma unroll
    for (int j = 0; j < 10; j += 2) {
      const unsigned pp = pk2(bc2(f4) - f32x2{4.f * j, 4.f * j + 4.f});
      const int p0 = (int)bflo(pp), p1 = (int)bfhi(pp);
      pos4[j] = p0 + ((p0 >> 5) << 2) + lds0 + LDS_KP;
      PIN(pos4[j]);
      if (j + 1 < 9) { pos4[j + 1] = p1 + ((p1 >> 5) << 2) + lds0 + LDS_KP; PIN(pos4[j + 1]); }
    }
  }
  unsigned co[2][4];
  {
    const unsigned b0 = (eh ? 1152 + 576 * q : 144 * (q >> 1) + 4 * (q & 1)) * 4 + 36 * c + lds0 + LDS_KP + cch * KP_CH;
    const unsigned b1 = (q < 2 ? 288 + 144 * q : 568 + 4 * q) * 4 + 36 * c + lds0 + LDS_KP + cch * KP_CH;
    const unsigned s0 = eh ? 576 : 4, s1 = q < 2 ? 8 : 576;
#pragma unroll
    for (int r = 0; r < 4; ++r) { co[0][r] = b0 + r * s0; co[1][r] = b1 + r * s1; PIN(co[0][r]); PIN(co[1][r]); }
  }
  *(unsigned*)(smem + LDS_KP - 16 + cch * KP_CH + (lane & 3) * 4) = 0u;
  unsigned a_pad, a_wi, a_wo, a_tab, gm_off, gc_off;
  a_pad = lds0 + cch * CV_CH + (lane < 32 ? CV_H + l16 : ((lane >> 1) & 1) * CV_RK + 4096 + (lane & 1) * 16);
  a_wi = lds0 + LDS_WI + (w * 32 + 8 * q + ((lane >> 2) & 3)) * 80 + (lane & 3) * 16;
  a_wo = lds0 + LDS_WO + (8 * q + ((lane >> 2) & 3)) * 144 + (w >> 1) * 64 + (lane & 3) * 16 + (w & 1) * 8;
  a_tab = lds0 + LDS_TAB + cch * (64 * 528) + c * 528 + q * 16;
  gm_off = ((4 - cch) * 512 + mkg * 128 + ns) * 8;
  gc_off = (((lane & 7) > 5 ? 0 : ((lane & 7) >> 1)) * 512 + pb * 2 + (lane & 1)) * 8;
  PIN(a_pad); PIN(a_wi); PIN(a_wo); PIN(a_tab); PIN(gm_off); PIN(gc_off);
  unsigned ab[4], bb, dv[4], rdo4[4], xoff;
#pragma unroll
  for (int i = 0; i < 4; ++i) {
    ab[i] = lds0 + cch * CV_CH + (1 - (c & 1)) * CV_RK + 2 * (2030 - 2 * (c >> 1) + 8 * q) - eh * 256 - 192 - 512 - 1024 * i;
    dv[i] = (lane >> 3) * 4096 + (((lane & 7) ^ ((lane >> 4) & 1) ^ (i << 1)) * 16) + i * (32768 - 1024);
    rdo4[i] = lds0 + LDS_W + w * 16384 + ((perm((i & 1) * 2 ^ (2 * (w & 1)), c) * 128 + ((q ^ (c >> 1)) * 16)) ^ ((i >> 1) * 64));
    PIN(ab[i]); PIN(dv[i]); PIN(rdo4[i]);
  }
  bb = lds0 + cch * CV_CH + CV_H + 2 * (256 - 16 + 16 * c + 8 * q) - eh * 256 - 192;
  xoff = WS_X1T + (pb * 2 + cch) * 4096 + eh * 2048 + ((c >> 1) * 32 + ((q & 1) * 4 + 2 * (c & 1) + (q >> 1)) * 4) * 2;
  PIN(bb); PIN(xoff);
  SB();
  {
    *(v4u*)(smem + LDS_LN + (w * 2) * 1040 + l16) = xr[0];
    *(v4u*)(smem + LDS_LN + (w * 2 + 1) * 1040 + l16) = xr[1];
    float sm[2], sq[2];
#pragma unroll
    for (int i = 0; i < 2; ++i) {
      sm[i] = dot2(xr[i][0], ONES, dot2(xr[i][1], ONES, dot2(xr[i][2], ONES, dot2(xr[i][3], ONES, 0.f))));
      sq[i] = dot2(xr[i][0], xr[i][0], dot2(xr[i][1], xr[i][1], dot2(xr[i][2], xr[i][2], dot2(xr[i][3], xr[i][3], 0.f))));
    }
    const float tv = row16_sum(xrow_sum4(sm[0], sm[1], sq[0], sq[1]));
    auto sw = __builtin_amdgcn_permlane32_swap(__float_as_uint(tv), __float_as_uint(tv), false, false);
    const float m = __uint_as_float(sw[0]) * (1.0f / D);
    const float r = __builtin_amdgcn_rsqf(fmaxf(__uint_as_float(sw[1]) * (1.0f / D) - m * m, 0.f) + EPS);
    ((float*)(smem + LDS_GS + 1088))[(q >> 1) * 8 + w * 2 + (q & 1)] = (q >> 1) ? -m * r : r;
  }
  const unsigned tag = epoch * 2654435761u + 0x9E3779B9u;
  asm volatile("s_waitcnt vmcnt(8)" ::: "memory");
  bar();
  {
    unsigned ua[4][2];
#pragma unroll
    for (int i = 0; i < 4; ++i) {
      const unsigned char* src = smem + LDS_LN + (tid & 3) * 2080 + (i * 64 + (tid >> 2)) * 4;
      ua[i][0] = *(const unsigned*)src; ua[i][1] = *(const unsigned*)(src + 1040);
    }
    const unsigned lnv = ((const unsigned*)(smem + LDS_GS + 1088))[c];
    SB();
#pragma unroll
    for (int i = 0; i < 4; ++i) st128<SC1>(v4u{ua[i][0], tag, ua[i][1], tag}, rws, (tid >> 2) * 16384 + (tid & 3) * 16, WS_UG + i * 64 * 16384 + pb * 64);
    st64<SC1>(v2u{lnv, tag}, rws, tid < 16 ? (c >> 3) * 16384 + (c & 7) * 8 : OOB, WS_SR + pb * 64);
  }
  SB();
  const unsigned wp_oob = b < 176 ? 0u : OOB;
  const int wpr = b >> 2, wkg = b & 3;
  v4u xi[2], xo;
  {
    const rsrc_t rwin = mkrs(p.win, D * FF * 2), rwout = mkrs(p.wout, FH * D * 2);
    const int hf = wpr >= 22, pp = wpr - 22 * hf;
    const unsigned v = ((tid >> 2) * (FF * 2) + (tid & 3) * 16) | ((pp == 21 ? tid & 2 : 0) << 29);
#pragma unroll
    for (int i = 0; i < 2; ++i) xi[i] = ld128<0>(rwin, v, (wkg * 128 + i * 64) * (FF * 2) + (hf * FH + pp * 32) * 2 + wp_oob);
    xo = ld128<0>(rwout, (tid >> 3) * 1024 + (tid & 7) * 16, idx * 32 * 1024 + xcd * 128);
  }
  SB();
  const int d0 = pb * 2, dk = d0 + cch;
  const int mt = (xcd >> 1) * 8 + (idx & 7), nt = (xcd & 1) * 4 + (idx >> 3);
  const int big = (nt + 2) >> 3;
  const int np = 3 - big, pr0 = 3 * nt - big * (nt - 6);
  const int kh = w & 1, uh = w >> 1;
  const int uo0 = pr0 * 2 + uh * (4 - 2 * big), ut0 = pr0 * 2 + (uh ^ 1) * (4 - 2 * big), uo2 = pr0 * 2 + 2 + uh, ut2 = pr0 * 2 + 2 + (uh ^ 1);
#define UO(k) ((k) == 2 ? uo2 : uo0 + (k))
#define UT(k) ((k) == 2 ? ut2 : ut0 + (k))
  const unsigned oob3 = (unsigned)big << 30;
  const int kr = 2 * (idx >> 3) + (xcd & 1) + 4 * (idx & 1);
  const rsrc_t rwsp = mkrs(p.wsp, L * L * 2);
  const unsigned wp_fr = (wpr * 2 * 16 + wkg * 4 + w) * 1024 + wp_oob;
  v4u wr[1][8];
  const unsigned wsp_v = (lane >> 3) * 4096 + (lane & 7) * 16, wsp_s = mt * 64 * 4096 + w * 1024;
#define LOAD_WSP1(ck, i) wr[ck][i] = ld128<0>(rwsp, wsp_v + (((ck) + kr) & 7) * 128, wsp_s + (i) * 8 * 4096)
#define LOAD_WSPN(n) LOAD_WSP1((n) >> 3, (n) & 7)
  const v4u mk = {~0u, ~0u, ~0u, ~0u};
  unsigned bspq[2];
#pragma unroll
  for (int t = 0; t < 2; ++t) bspq[t] = p.bsp[mt * 64 + perm(2 * kh + t, c)];
  const unsigned tag_s = __builtin_amdgcn_readfirstlane(tag);
  {
    const float dt = rbf(exp_acc(bf2f(q_ldt)));
    const float cre = bf2f(q_cre) * dt, cim = bf2f(q_cim) * dt;
    const float are2 = -rbf(exp_acc(bf2f(q_lar))) * dt * 1.4426950408889634f;
    const float aim = bf2f(q_aim) * dt;
    const float rev_hi = aim * 0.15915494309189535f;
    const float rev_lo = __builtin_fmaf(aim, 0.15915494309189535f, -rev_hi) + aim * 6.4206383e-9f;
    int ex = __builtin_amdgcn_frexp_expf(xrow_max(row16_max(fmaxf(fabsf(cre), fabsf(cim))))) + 1;
    ex = ex < -100 ? -100 : ex;
    const float scale = __builtin_amdgcn_ldexpf(1.f, -ex);
    ((float*)(smem + LDS_GS + 1024))[w] = __builtin_amdgcn_ldexpf(2.f, ex);
    const f32x2 c0 = {cre * scale, cim * scale};
    const f32x2 e1 = cexp_p(are2, rev_hi, rev_lo, 1.f), e8 = cexp_p(are2, rev_hi, rev_lo, 8.f), e64 = cexp_p(are2, rev_hi, rev_lo, 64.f);
    unsigned char* tn = smem + LDS_TAB + cch * (64 * 528) + ns * 4;
#define TROW(r, z) *(unsigned*)(tn + (r) * 528) = pkh((z)[0], -(z)[1])
    SB();
    const f32x2 e128 = cmul(e64, e64), e2 = cmul(e1, e1), e256 = cmul(e128, e128), e4 = cmul(e2, e2), e512 = cmul(e256, e256);
    f32x2 f0 = {1.f, 0.f}, f1 = e64;
    f32x2 ca = cmul(c0, e512), cb = cmul(ca, e512);
#pragma unroll
    for (int j = 0; j < 8; ++j) {
      *(unsigned*)(tn + (48 + j) * 528) = pkh(f0[0], f0[1]);
      *(unsigned*)(tn + (56 + j) * 528) = pkh(f1[0], f1[1]);
      TROW(32 + j, cb);
      f0 = j == 0 ? e8 : cmul(f0, e8); f1 = cmul(f1, e8); cb = cmul(cb, e128);
      if (j < 4) {
        const f32x2 z = cmul(ca, e4);
        TROW(24 + j, ca); TROW(28 + j, z);
        ca = cmul(ca, e128);
      }
      if (j == 3 || j == 7) {
        PIN(f0); PIN(f1); PIN(cb);
        LOAD_WSPN(j >> 2 << 1); LOAD_WSPN((j >> 2 << 1) + 1);
        SB();
      }
    }
    TROW(40, cb);
    SB();
  {
    const float cv = bf2f(cq);
    const float sl = rbf(cv * __builtin_amdgcn_rcpf(1.0f + __builtin_amdgcn_exp2f(cv * -1.4426950408889634f)));
    const float sc[4] = {dpp<0x00>(sl), dpp<0x55>(sl), dpp<0xAA>(sl), dpp<0xFF>(sl)};
    f32x2 a[4];
#pragma unroll
    for (int i = 0; i < 4; ++i)
#pragma unroll
      for (int j = 0; j < 4; ++j) a[j] = i == 0 ? unpk(xm[i][j]) * bc2(sc[i]) : fma2(unpk(xm[i][j]), bc2(sc[i]), a[j]);
    float* red = (float*)(smem + LDS_MR) + (tid >> 3) * 68 + (tid & 7) * 8;
    *(f32x4*)red = f32x4{a[0][0], a[0][1], a[1][0], a[1][1]};
    *(f32x4*)(red + 4) = f32x4{a[2][0], a[2][1], a[3][0], a[3][1]};
  }
    bar();
    float ms8[8];
#pragma unroll
    for (int i = 0; i < 8; ++i) ms8[i] = ((const float*)(smem + LDS_MR))[(q * 8 + i) * 68 + w * 16 + c];
    SB();
    f32x2 t1[8];
    t1[1] = e1; t1[2] = e2; t1[4] = e4;
    t1[3] = cmul(e2, e1); t1[5] = cmul(e4, e1); t1[6] = cmul(e2, e4); t1[7] = cmul(t1[6], e1);
    const f32x2 c1 = cmul(c0, e128), c2 = cmul(c0, e256), c3 = cmul(c2, e128);
    TROW(0, c0); TROW(8, c1); TROW(16, c2); TROW(20, c3);
    {
    const float s = xrow_sum(((ms8[0] + ms8[1]) + (ms8[2] + ms8[3])) + ((ms8[4] + ms8[5]) + (ms8[6] + ms8[7])));
    st64<SC1>(v2u{__float_as_uint(s), tag}, rws, q == 0 ? c * 8 : OOB, WS_MODG + (mkg * 3072 + mcg * 64 + w * 16) * 8 + (b < 192 ? 0u : OOB));
    }
    SB();
#pragma unroll
    for (int v = 1; v < 8; ++v) {
      const f32x2 z0 = cmul(t1[v], c0), z1 = cmul(t1[v], c1);
      TROW(v, z0); TROW(8 + v, z1);
      if ((v & 1) == 0) {
        const f32x2 z2 = cmul(t1[v], c2), z3 = cmul(t1[v], c3);
        TROW(16 + v / 2, z2); TROW(20 + v / 2, z3);
        SB();
      }
    }
  }
#pragma unroll
  for (int i = 0; i < 2; ++i) *(v4u*)(smem + LDS_WI + (i * 64 + (tid >> 2)) * 80 + (tid & 3) * 16) = xi[i];
  *(v4u*)(smem + LDS_WO + (tid >> 3) * 144 + (tid & 7) * 16) = xo;
  bar();
  SB();
  v4u gr[4], gb_[4];
  v2u gm[4], gc[4];
#define LOAD_GS() _Pragma("unroll") for (int i_ = 0; i_ < 4; ++i_) { gr[i_] = ld128<SC1>(rws, tid * 16, WS_SR + i_ * 4096); gb_[i_] = ld128<SC1>(rws, tid * 16, WS_SB + i_ * 4096); }
#define LOAD_GM() _Pragma("unroll") for (int i_ = 0; i_ < 4; ++i_) { gm[i_] = ld64<SC1>(rws, gm_off, WS_MODG + i_ * 24576); gc[i_] = ld64<SC1>(rws, gc_off, WS_MODG + i_ * 24576); }
  v4u ug[4], wt[2];
#define LOAD_UG() _Pragma("unroll") for (int i_ = 0; i_ < 4; ++i_) ug[i_] = ld128<SC1>(rws, tid * 16, WS_UG + pb * 16384 + i_ * 4096)
  {
    v4u fa[2][8], fb[8];
    const unsigned ta = a_tab + eh * (32 * 528);
#pragma unroll
    for (int ks = 0; ks < 3; ++ks) { fb[ks] = ldsr<v4u>(a_tab + 48 * 528 + ks * 64); fa[0][ks] = ldsr<v4u>(ta + ks * 64); fa[1][ks] = ldsr<v4u>(a_tab + 16 * 528 + ks * 64); }
    SB();
    LOAD_UG();
    SB();
    f32x4 kk[2][2];
    f32x2 us;
    v2u wh_[6];
#pragma unroll
    for (int i = 0; i < 4; ++i) kk[i >> 1][i & 1] = f32x4{0.f, 0.f, 0.f, 0.f};
#pragma unroll
    for (int ks = 0; ks < 8; ++ks) {
      if (ks < 5) { fb[3 + ks] = ldsr<v4u>(a_tab + 48 * 528 + (3 + ks) * 64); fa[0][3 + ks] = ldsr<v4u>(ta + (3 + ks) * 64); fa[1][3 + ks] = ldsr<v4u>(a_tab + 16 * 528 + (3 + ks) * 64); }
      if (ks == 5) { us = *(const f32x2*)(smem + LDS_GS + 1024 + cch * 8); wh_[4] = trread_a(a_wo); wh_[5] = trread_a(a_wo + 576); }
      if (ks == 6) { wh_[0] = trread_a(a_wi); wh_[1] = trread_a(a_wi + 320); wh_[2] = trread_a(a_wi + 8); wh_[3] = trread_a(a_wi + 328); }
      kk[0][ks >> 2] = mfma_h(fa[0][ks], fb[ks], kk[0][ks >> 2]); kk[1][ks >> 2] = mfma_h(fa[1][ks], fb[ks], kk[1][ks >> 2]);
      SB();
    }
    wt[0] = v4u{wh_[0][0], wh_[0][1], wh_[1][0], wh_[1][1]};
    wt[1] = v4u{wh_[2][0], wh_[2][1], wh_[3][0], wh_[3][1]};
    st128<SC1>(v4u{wh_[4][0], wh_[4][1], wh_[5][0], wh_[5][1]}, rws, l16, WS_WOT + ((xcd * 4 + w) * 22 + idx) * 1024 + wp_oob);
    LOAD_GS();
    SB();
#pragma unroll
    for (int i = 0; i < 4; ++i) PINA(kk[i >> 1][i & 1]);
    f32x4 k0 = kk[0][0] * us[0] + kk[0][1] * us[1];
    const f32x4 k1 = kk[1][0] * us[0] + kk[1][1] * us[1];
    k0[0] += ns == 0 ? bf2f(q_dsk) : 0.f;
#pragma unroll
    for (int r = 0; r < 4; ++r) { ldsw<float>(co[0][r], k0[r]); ldsw<float>(co[1][r], k1[r]); }
  }
  bar();
  {
    float v[2][9];
#pragma unroll
    for (int j = 0; j < 9; ++j) v[0][j] = ldsr<float>(pos4[j]);
    LOAD_GM();
#pragma unroll
    for (int j = 0; j < 9; ++j) v[1][j] = ldsr<float>(pos4[j] + KP_CH);
    ldsw<v4u>(a_pad, v4u{0u, 0u, 0u, 0u});
    SB();
#pragma unroll
    for (int ch = 0; ch < 2; ++ch) {
      unsigned char* rk = smem + ch * CV_CH + tid * 16;
#pragma unroll
      for (int sft = 0; sft < 2; ++sft)
        *(v4u*)(rk + sft * CV_RK) = v4u{pk2(v[ch][sft], v[ch][sft + 1]), pk2(v[ch][sft + 2], v[ch][sft + 3]), pk2(v[ch][sft + 4], v[ch][sft + 5]), pk2(v[ch][sft + 6], v[ch][sft + 7])};
    }
    SB();
    unsigned m = 0;
#pragma unroll
    for (int i = 0; i < 4; ++i) { m = tacc(m, ug[i], tag); m = tacc(m, gr[i], tag); m = tacc(m, gb_[i], tag); }
    if (__builtin_expect(any_nz(m), 1)) {
      unsigned sp = 0;
      do {
        MEMFENCE(); LOAD_UG(); LOAD_GS(); m = 0;
#pragma unroll
        for (int i = 0; i < 4; ++i) { m = tacc(m, ug[i], tag); m = tacc(m, gr[i], tag); m = tacc(m, gb_[i], tag); }
      } while (any_nz(m) & (++sp <= SPIN_CAP));
    }
    f32x2 nn[4][2];
#pragma unroll
    for (int i = 0; i < 4; ++i) {
      nn[i][0] = fma2(unpk(ug[i][0]), bc2(__uint_as_float(gr[i][0])), bc2(__uint_as_float(gb_[i][0])));
      nn[i][1] = fma2(unpk(ug[i][2]), bc2(__uint_as_float(gr[i][2])), bc2(__uint_as_float(gb_[i][2])));
      PIN(nn[i][0]); PIN(nn[i][1]);
      *(unsigned*)(smem + LDS_U + i * 1024 + tid * 4) = __builtin_amdgcn_perm(ug[i][2], ug[i][0], 0x05040100u);
      *(unsigned*)(smem + LDS_U + 4096 + i * 1024 + tid * 4) = __builtin_amdgcn_perm(ug[i][2], ug[i][0], 0x07060302u);
    }
    SB();
    m = 0;
#pragma unroll
    for (int i = 0; i < 4; ++i) m = tx(tx(m, gm[i][1], tag), gc[i][1], tag);
    if (__builtin_expect(any_nz(m), 1)) {
      unsigned sp = 0;
      do {
        MEMFENCE(); LOAD_GM(); m = 0;
#pragma unroll
        for (int i = 0; i < 4; ++i) m = tx(tx(m, gm[i][1], tag), gc[i][1], tag);
      } while (any_nz(m) & (++sp <= SPIN_CAP));
    }
    const float mv = rbf((__uint_as_float(gm[0][0]) + __uint_as_float(gm[1][0])) + (__uint_as_float(gm[2][0]) + __uint_as_float(gm[3][0])));
    *(u16*)(smem + LDS_GS + tid * 2) = (u16)pk2(mv + (cch ? 0.f : 1.0f), 0.f);
    const float cv = rbf((__uint_as_float(gc[0][0]) + __uint_as_float(gc[1][0])) + (__uint_as_float(gc[2][0]) + __uint_as_float(gc[3][0])));
    const float gav = dpp<0x104>(cv), gav2 = dpp<0x102>(cv);
    const float hbv = cv * gav, hsv = rbf(1.0f + cv) * gav2;
    const f32x2 hb = {__builtin_bit_cast(float, __builtin_amdgcn_readlane(__builtin_bit_cast(int, hbv), 0)), __builtin_bit_cast(float, __builtin_amdgcn_readlane(__builtin_bit_cast(int, hbv), 1))};
    const f32x2 hs = {__builtin_bit_cast(float, __builtin_amdgcn_readlane(__builtin_bit_cast(int, hsv), 2)), __builtin_bit_cast(float, __builtin_amdgcn_readlane(__builtin_bit_cast(int, hsv), 3))};
    SB();
#pragma unroll
    for (int i = 4; i < 8; ++i) LOAD_WSPN(i);
    st128<SC1>(mk, rws, l16, WS_X1T + dk * 4096 + eh * 2048);
    st128<SC1>(mk, rws, l16 + 1024, WS_X1T + dk * 4096 + eh * 2048);
    __builtin_amdgcn_raw_buffer_store_b32(~0u, rws, lane * 4, WS_ST2 + ((mt * 2 + kh) * 16 + nt * 2 + uh) * 256, SC1);
    bspq[0] |= bspq[1] << 16;
    PIN(bspq[0]);
#pragma unroll
    for (int i = 0; i < 4; ++i) {
      const f32x2 h0 = fma2(nn[i][0], hs, hb), h1 = fma2(nn[i][1], hs, hb);
      *(unsigned*)(smem + CV_H + 512 + i * 1024 + tid * 4) = pk2(h0[0], h1[0]);
      *(unsigned*)(smem + CV_CH + CV_H + 512 + i * 1024 + tid * 4) = pk2(h0[1], h1[1]);
    }
  }
  asm volatile("" ::"v"(codepf));
  bar();
#define CV_A(ee, k) ({ const unsigned a_ = ab[(k) >> 1] + 192 - 64 * (ee) + 512 * (1 - ((k) & 1)); v4u{ldsr<unsigned>(a_), ldsr<unsigned>(a_ + 4), ldsr<unsigned>(a_ + 8), ldsr<unsigned>(a_ + 12)}; })
#define CV_B(ee, k) ldsr<v4u>(bb + 192 - 64 * (ee) + 512 * (k))
  const unsigned slotA = LDS_W + w * 16384, slotB = slotA + 8192;
  v4u wq[8][8];
#define READ_W(dl, f) ldsr<v4u>(rdo4[(((f) >> 1) & 1) + ((f) >> 2) * 2] + (dl) + ((f) & 1) * 512)
  unsigned dso;
#define DMA_W(ck, slot, i) do { \
    if (((i) & 3) == 0) asm volatile("s_add_u32 m0, %1, %2\n\ts_add_u32 %0, %3, %4\n\tbuffer_load_dwordx4 %5, %6, %0 offen lds" \
                 : "=&s"(dso) : "s"(lds0 + (slot)), "i"((i) * 1024), "s"(wsp_s + ((CKMAP(ck) + kr) & 7) * 128), "i"((i) * 32768), "v"(dv[0]), "s"(rwsp) : "scc", "memory"); \
    else asm volatile("buffer_load_dwordx4 %0, %1, %2 offen offset:%3 lds" :: "v"(dv[(i) & 3]), "s"(rwsp), "s"(dso), "i"(((i) & 3) * 1024) : "memory"); } while (0)
  auto dma_sel = [&](auto CK, unsigned slot, int i) __attribute__((always_inline)) {
    constexpr int ck = decltype(CK)::value;
    switch (i) {
      case 0: DMA_W(ck, slot, 0); break; case 1: DMA_W(ck, slot, 1); break; case 2: DMA_W(ck, slot, 2); break; case 3: DMA_W(ck, slot, 3); break;
      case 4: DMA_W(ck, slot, 4); break; case 5: DMA_W(ck, slot, 5); break; case 6: DMA_W(ck, slot, 6); break; default: DMA_W(ck, slot, 7); break;
    }
  };
  v4u cA[2][8], cB[2][8];
  v4u ones4 = {ONES, ONES, ONES, ONES};
  PIN(ones4);
  f32x4 cb4[4];
  {
    const v4u gbf = *(const v4u*)(smem + LDS_GS + w * 64 + q * 16), sbf = *(const v4u*)(smem + LDS_GS + 256 + w * 64 + q * 16);
#pragma unroll
    for (int k = 0; k < 4; ++k) { cA[0][k] = CV_A(0, k); cB[0][k] = CV_B(0, k); }
    SB();
    const f32x4 z4 = {0.f, 0.f, 0.f, 0.f};
#pragma unroll
    for (int e = 0; e < 2; ++e) {
      v4u o;
#pragma unroll
      for (int jj = 0; jj < 4; ++jj) o[jj] = pk2(unpk(wt[e][jj]) * unpk(gbf[jj]));
      st128<SC1>(o, rws, l16 + e * 16384, WS_WP + wp_fr);
      cb4[2 * e + 1] = mfma(sbf, wt[e], z4);
      cb4[2 * e] = mfma(ones4, o, z4);
    }
  }
  SB();
  f32x4 cacc[8];
  v2u ue[4];
  float uf[16];
  unsigned flagv;
  unsigned wro[4], a_cp, a_ue, o_csb, o_flag;
  gu32* p_flag = (gu32*)(p.ws + WS_CNT + (256 + (tid & 7) * 16 + (idx & 15)) * 128);
  {
    f32x2 cp[4];
    float s0, s1, s2, s3;
    sfor<144>([&](auto G) __attribute__((always_inline)) {
      constexpr int g = decltype(G)::value, ee = g / 36, j = g % 36, t = CVO.pp[j] + CVO.qq[j];
      constexpr bool first = ee == 0 && CVO.first[j];
      cacc[t] = mfma(cA[ee & 1][CVO.qq[j]], cB[ee & 1][CVO.pp[j]], first ? f32x4{0.f, 0.f, 0.f, 0.f} : cacc[t]);
      SB();
      if constexpr (g < 8) {
        if constexpr (g & 1) cB[0][4 + (g >> 1)] = CV_B(0, 4 + (g >> 1));
        else cA[0][4 + (g >> 1)] = CV_A(0, 4 + (g >> 1));
      }
      constexpr int jn = ee == 0 ? (j >= 12 && (j - 12) % 3 != 2 ? (j - 12) / 3 * 2 + (j - 12) % 3 : -1) : (j & 1) ? j >> 1 : -1;
      if constexpr (ee < 3 && jn >= 0 && jn < 16) {
        if constexpr (jn & 1) cB[(ee + 1) & 1][jn >> 1] = CV_B(ee + 1, jn >> 1);
        else cA[(ee + 1) & 1][jn >> 1] = CV_A(ee + 1, jn >> 1);
      }
      if constexpr (g == 8) { a_cp = lds0 + LDS_S + 4096 + c * 16; PIN(a_cp); }
      if constexpr (g == 9) { cb4[0][1] = cb4[1][0]; cb4[0][2] = cb4[2][0]; PINA(cb4[0]); }
      if constexpr (g == 10) { cb4[0][3] = cb4[3][0]; PINA(cb4[0]); }
      if constexpr (g == 11) ldsw<f32x4>(a_cp + w * 256, cb4[0]);
      if constexpr (g == 17) { a_cp += (q & 1) * 8; PIN(a_cp); }
      if constexpr (g == 20) { o_csb = q * 512 + c * 8; PIN(o_csb); }
      if constexpr (g == 23) { o_csb = tid < 32 ? o_csb : OOB; PIN(o_csb); }
      if constexpr (g == 26) { PIN(p_flag); }
      if constexpr (g == FLAG_AT) {
        asm volatile("s_waitcnt vmcnt(%0) lgkmcnt(8)" ::"i"(FLAG_AT > 38 ? 4 + (FLAG_AT - 38 + 3) / 4 : 4) : "memory");
        __builtin_amdgcn_s_barrier();
        if (tid < 8) (void)__hip_atomic_fetch_add(p_flag, 1u, __ATOMIC_RELAXED, __HIP_MEMORY_SCOPE_AGENT);
      }
      if constexpr (g == FLAG_AT + 4 || g == FLAG_AT + 8) {
        cp[(g - FLAG_AT - 4) / 2] = ldsr<f32x2>(a_cp + (g - FLAG_AT - 4) * 128);
        cp[(g - FLAG_AT - 4) / 2 + 1] = ldsr<f32x2>(a_cp + (g - FLAG_AT - 4) * 128 + 256);
      }
      if constexpr (g == FLAG_AT + 16) { s0 = cp[0][0] + cp[1][0]; PIN(s0); s2 = cp[0][1] + cp[1][1]; PIN(s2); }
      if constexpr (g == FLAG_AT + 20) { s1 = cp[2][0] + cp[3][0]; PIN(s1); s3 = cp[2][1] + cp[3][1]; PIN(s3); }
      if constexpr (g == FLAG_AT + 24) { s0 += s1; PIN(s0); s2 += s3; PIN(s2); }
      if constexpr (g == FLAG_AT + 28) st64<SC1>(v2u{__float_as_uint(s0), __float_as_uint(s2)}, rws, o_csb, WS_CSB + wpr * 1024 + wkg * 128 + wp_oob);
      if constexpr (g == 14 || g == 29 || g == 32 || g == 35) DMA_W(1, slotA, g == 14 ? 0 : (g - 26) / 3);
      if constexpr (g >= 38 && g < 86 && (g & 3) == 2) DMA_W(1 + (g + 10) / 64, g < 54 ? slotA : slotB, (4 + (g - 38) / 4) & 7);
      if constexpr (g == 88) asm volatile("s_waitcnt vmcnt(8)" ::: "memory");
      if constexpr (g >= 90 && g < 106 && (g & 1) == 0) wq[1][(g - 90) >> 1] = READ_W(0, (g - 90) >> 1);
      if constexpr (g >= 68 && g < 72) { wro[g - 68] = (lds0 + slotA + (lane >> 3) * 128 + (((lane & 7) ^ ((lane >> 4) & 1)) * 16)) ^ ((g - 68) * 32); PIN(wro[g - 68]); }
      if constexpr (g == 105) { a_ue = lds0 + LDS_U + cch * 4096 + eh * 2048 + c * 32 + q * 8; PIN(a_ue); }
      if constexpr (g == 106 || g == 107) { ue[(g - 106) * 2] = ldsr<v2u>(a_ue + (g - 106) * 1024); ue[(g - 106) * 2 + 1] = ldsr<v2u>(a_ue + (g - 106) * 1024 + 512); }
      if constexpr (g >= 108 && g < 140 && (g & 3) == 0) ldsw<v4u>(wro[((g - 108) >> 2) & 3] + ((g - 108) >> 2) * 1024, wr[0][(g - 108) >> 2]);
      if constexpr (g >= 109 && g < 125 && (g & 1) == 1) {
        constexpr int x = (g - 109) >> 1;
        if constexpr (x >= 2) { uf[2 * (x - 2)] = bflo(ue[(x - 2) >> 1][x & 1]); PIN(uf[2 * (x - 2)]); uf[2 * (x - 2) + 1] = bfhi(ue[(x - 2) >> 1][x & 1]); PIN(uf[2 * (x - 2) + 1]); }
      }
      if constexpr (g == 118 || g == 122) { constexpr int x = 6 + (g == 122); uf[2 * x] = bflo(ue[3][x & 1]); PIN(uf[2 * x]); uf[2 * x + 1] = bfhi(ue[3][x & 1]); PIN(uf[2 * x + 1]); }
      if constexpr (g == 110) { o_flag = w == 0 ? c * 128 : OOB; PIN(o_flag); }
      if constexpr (g == 126) asm volatile("s_waitcnt vmcnt(0)" ::: "memory");
      if constexpr (g >= 127 && g < 143 && (g & 1) == 1) wq[2][(g - 127) >> 1] = READ_W(8192, (g - 127) >> 1);
      if constexpr (g == POLL_AT) flagv = __builtin_amdgcn_raw_buffer_load_b32(rws, o_flag, WS_CNT + (256 + xcd * 16) * 128, SC1);
      SB();
    });
  }
  unsigned late;
  {
    f32x4* red = (f32x4*)(smem + LDS_X) + w * 256 + lane;
    f32x4 keep[4];
    if (eh) {
#pragma unroll
      for (int t = 0; t < 4; ++t) red[t * 64] = cacc[t];
      SB();
#pragma unroll
      for (int t = 0; t < 4; ++t) { PINA(cacc[4 + t]); keep[t] = cacc[4 + t]; PIN(keep[t]); }
    } else {
#pragma unroll
      for (int t = 0; t < 4; ++t) red[t * 64] = cacc[4 + t];
      SB();
#pragma unroll
      for (int t = 0; t < 4; ++t) { PINA(cacc[t]); keep[t] = cacc[t]; PIN(keep[t]); }
    }
    f32x2 pre[4][2];
#pragma unroll
    for (int t = 0; t < 4; ++t) {
      pre[t][0] = f32x2{uf[4 * t], uf[4 * t + 1]} + lo2(keep[t]);
      pre[t][1] = f32x2{uf[4 * t + 2], uf[4 * t + 3]} + hi2(keep[t]);
      PIN(pre[t][0]); PIN(pre[t][1]);
    }
    if (w == 0) *(unsigned*)(smem + LDS_S) = __builtin_amdgcn_ballot_w64(flagv < 16u) != 0ull;
    bar();
    late = *(const unsigned*)(smem + LDS_S);
    const f32x4* rd = (const f32x4*)(smem + LDS_X) + (w ^ 1) * 256 + lane;
    f32x4 oth[4];
#pragma unroll
    for (int t = 0; t < 4; ++t) oth[t] = rd[t * 64];
#pragma unroll
    for (int t = 0; t < 4; ++t) {
      v2u o;
      o[0] = nomark(pk2(pre[t][0] + lo2(oth[t])));
      o[1] = pk2(pre[t][1] + hi2(oth[t]));
      st64<SC1>(o, rws, xoff + t * 512, 0);
    }
  }
  SB();
  v4u wf[3][8];
  const unsigned rot = uh * 4;
#define LOAD_WP(half, k, i) ld128<SC1>(rws, l16 + ((i) & 3) * 1024, WS_WP + (((half) * 44 + UO(k)) * 16 + kh * 8 + (((i) & 4) ^ rot)) * 1024 + ((k) == 2 ? oob3 : 0u))
#define LOAD_XS(i) ld128<SC1>(rws, wsp_v, WS_X1T + ((i) * 32 + w * 8) * 4096 + mt * 128)
  v4u xs[16];
  unsigned tb[2][4], xdst;
  SB();
  int x1o;
  unsigned z2o[3], dgo[4], rdo[2], xw, xrd, a_fin, a_fr;
  v4u mkv = mk;
  v2u cz[2][3];
  f32x2 csb[2][3];
  unsigned gq[4];
#define LOAD_CZ(hf, kc) ld64<SC1>(rws, lane * 8, WS_CSB + ((hf) * 44 + UO(kc)) * 512 + ((kc) == 2 ? oob3 : 0u))
  {
    if (__builtin_amdgcn_readfirstlane(late)) {
      if (w == 0) { hint_wait(rws, c * 128, WS_CNT + (256 + xcd * 16) * 128, 16u); __builtin_amdgcn_s_sleep(LATE_SLEEP); }
      __builtin_amdgcn_s_barrier();
    }
    MEMFENCE();
#pragma unroll
    for (int i = 0; i < 3; ++i)
#pragma unroll
      for (int k = 0; k < 3; ++k) wf[k][i] = LOAD_WP(1, k, i);
    SB();
#pragma unroll
  for (int f = 0; f < 8; ++f) wq[0][f] = READ_W(0, f);
#pragma unroll
  for (int f = 0; f < 24; ++f) PINA(wq[f >> 3][f & 7]);
    int lz = lane;
    PIN(lz);
  {
    const int row = 8 * (lz >> 4) + ((lz >> 2) & 3);
    const int sg = ((row >> 1) & 1) | (((row >> 3) & 1) << 1);
#pragma unroll
    for (int tu = 0; tu < 4; ++tu) {
      const unsigned t0 = lds0 + LDS_A + kh * 32768 + row * 128 + ((tu ^ (2 * kh) ^ sg) * 32) + (lz & 3) * 8;
      tb[0][tu] = t0 + uh * 16384;
      tb[1][tu] = t0 + (uh ^ 1) * 16384;
    }
    const int rl = w * 8 + (lz >> 3), chn = lz & 7;
    const int sg2 = ((rl >> 1) & 1) | (((rl >> 3) & 1) << 1);
    xdst = lds0 + LDS_A + rl * 128 + (((chn >> 1) ^ sg2) * 32) + (chn & 1) * 16;
  }
  {
    const int row = 8 * (lz >> 4) + ((lz >> 2) & 3);
    const int sg = ((row >> 1) & 1) | (((row >> 3) & 1) << 1);
    x1o = LDS_A + nt * 8192 + row * 128 + ((w ^ sg) * 32) + (lz & 3) * 8;
  }
#pragma unroll
  for (int k = 0; k < 3; ++k) { z2o[k] = lz * 16 + WS_Z2 + (UO(k) * 64 + mt * 2 + kh) * 1024 + (k == 2 ? oob3 : 0u); }
#pragma unroll
    for (int tu = 0; tu < 8; ++tu) PIN(tb[tu >> 2][tu & 3]);
    PIN(xdst); PIN(x1o);
    {
      const int c2 = lz & 15;
      const bool dg = (c2 >> 2) == (lz >> 4);
      const unsigned dump = lds0 + LDS_W + 65536 + lz * 16;
      dgo[0] = dg ? lds0 + LDS_S + (w * 64 + kh * 32 + c2) * 4 : dump; dgo[1] = dg ? lds0 + LDS_S + (w * 64 + (kh ^ 1) * 32 + c2) * 4 : dump;
      dgo[2] = dg ? lds0 + LDS_S + 2048 + (w * 64 + kh * 32 + c2) * 16 : dump; dgo[3] = dg ? lds0 + LDS_S + 2048 + (w * 64 + (kh ^ 1) * 32 + c2) * 16 : dump;
      rdo[0] = lds0 + LDS_S + (kh * 32 + (lz & 31)) * 4; rdo[1] = lds0 + LDS_S + 2048 + (kh * 32 + (lz & 31)) * 16 + (lz & 3) * 4;
      xw = lds0 + LDS_X + w * 6144 + lz * 16; xrd = lds0 + LDS_X + (w ^ 1) * 6144 + lz * 16;
      a_fin = lds0 + LDS_S + 1024 + w * 256 + lz * 4; a_fr = lds0 + LDS_S + 1024 + w * 256 + (lz >> 4) * 16;
      PIN(dgo[0]); PIN(dgo[1]); PIN(dgo[2]); PIN(dgo[3]); PIN(rdo[0]); PIN(rdo[1]); PIN(xw); PIN(xrd); PIN(a_fin); PIN(a_fr); PIN(mkv);
    }
#pragma unroll
    for (int k = 0; k < 3; ++k) PIN(z2o[k]);
    SB();
#pragma unroll
    for (int i = 3; i < WF_EARLY; ++i)
#pragma unroll
      for (int k = 0; k < 3; ++k) wf[k][i] = LOAD_WP(1, k, i);
    SB();
#pragma unroll
    for (int i = 0; i < 16; ++i) xs[i] = LOAD_XS(i);
    SB();
    unsigned m = 0;
#pragma unroll
    for (int i = 0; i < 16; ++i) {
      m = macc(m, xs[i]); ldsw<v4u>(xdst + i * 4096, xs[i]);
      if (WF_EARLY * 3 + i < 24) wf[(WF_EARLY * 3 + i) % 3][(WF_EARLY * 3 + i) / 3] = LOAD_WP(1, (WF_EARLY * 3 + i) % 3, (WF_EARLY * 3 + i) / 3);
      SB();
    }
    PIN(m);
    RETRY_BEGIN(m)
#pragma unroll
      for (int i = 0; i < 16; ++i) xs[i] = LOAD_XS(i);
      SB();
#pragma unroll
      for (int i = 0; i < 16; ++i) { m = macc(m, xs[i]); ldsw<v4u>(xdst + i * 4096, xs[i]); }
    RETRY_END(m)
  }
  bar();
#define AHALF(tu, i, h) trread_a(tb[(i) >> 2][tu] + ((i) & 3) * 4096 + (h) * 512)
  f32x4 acc[3][4];
  v4u wg[3][8];
  v2u ah[2][4][2];
  float* st = (float*)(smem + LDS_S);
  float* fin = st + 256 + w * 64;
  f32x4* xch = (f32x4*)(smem + LDS_X);
  f32x4 r4[2], m4[2];
  {
    f32x4 s1[4], s2[4];
    constexpr int LA = 4;
    v2u ar[LA + 1][2];
    float pa[4], pb[4];
#define UTILE(u) ((u) < 8 ? (u) >> 2 : (((u) >> 3) + 1) & 3)
#define UKS(u) ((u) < 8 ? (u) & 3 : (u) & 7)
#pragma unroll
    for (int u = 0; u < LA; ++u) { ar[u][0] = AHALF(UTILE(u), UKS(u), 0); ar[u][1] = AHALF(UTILE(u), UKS(u), 1); }
#pragma unroll
    for (int i = 0; i < 6; ++i) cz[i / 3][i % 3] = LOAD_CZ(i / 3, i % 3);
#pragma unroll
    for (int i = 0; i < 4; ++i) gq[i] = __builtin_amdgcn_raw_buffer_load_b32(rws, lane * 8, WS_MODG + (i * 3072 + 5 * 512 + nt * 64) * 8, SC1);
    DMA_W(3, slotB, 0);
    SB();
    sfor<128>([&](auto G) __attribute__((always_inline)) {
      constexpr int m = decltype(G)::value, u = Z2O.u[m], k = Z2O.kind[m], pos = Z2O.pos[m], tu = UTILE(u), i = UKS(u);
      const v4u a = {ar[u % (LA + 1)][0][0], ar[u % (LA + 1)][0][1], ar[u % (LA + 1)][1][0], ar[u % (LA + 1)][1][1]};
      const f32x4 z4 = {0.f, 0.f, 0.f, 0.f};
      if constexpr (k < 3) acc[k][tu] = mfma(a, wf[k][i], (i == 0 && tu >= 2) ? z4 : acc[k][tu]);
      if constexpr (k == 3) s1[tu] = mfma(ones4, a, i == 0 ? z4 : s1[tu]);
      if constexpr (k == 4) s2[tu] = mfma(a, a, i == 0 ? z4 : s2[tu]);
      SB();
      if constexpr (pos < 2 && u + LA < 40) ar[(u + LA) % (LA + 1)][pos] = AHALF(UTILE(u + LA), UKS(u + LA), pos);
      if constexpr (pos < 2 && u >= 36) ah[0][u - 36][pos] = AHALF(u - 36, 0, pos);
      if constexpr (u < 8 && pos == 0 && u != 4) { if constexpr (u > 0) DMA_W(3, slotB, u); }
      if constexpr (u == 4 && pos == 0) DMA_W(3, slotB, 4);
      if constexpr (u >= 12 && u < 15 && pos == 0) st128<SC1>(mkv, rws, z2o[u - 12], 0);
      if constexpr (m == 23 || m == 28 || m == 55 || m == 70) {
        constexpr int x = m == 23 ? 0 : m == 28 ? 1 : m == 55 ? 2 : 3;
        PINA(s1[x]);
        ldsw<float>(dgo[x >> 1] + (x & 1) * 64, s1[x][0]);
        if constexpr (x < 3) ldsw<f32x4>(dgo[2 + (x >> 1)] + (x & 1) * 256, s2[x]);
      }
      if constexpr (m == 71) ldsw<f32x4>(dgo[3] + 256, s2[3]);
      if constexpr (m == 35) __builtin_amdgcn_s_waitcnt(0x0F70);
      if constexpr (m == 46 || m == 51 || m == 56 || m == 61) {
        constexpr int x = (m - 46) / 5 * 2;
        wq[3][x] = READ_W(8192, x); wq[3][x + 1] = READ_W(8192, x + 1);
      }
      if constexpr (m == 50 || m == 60 || m == 65) ldsw<f32x4>(xw + (m == 50 ? 0 : m == 60 ? 1 : 2) * 2048, acc[m == 50 ? 0 : m == 60 ? 1 : 2][2]);
      if constexpr (m == 38 || m == 41 || m == 44 || m == 47) DMA_W(4, slotA, (m - 38) / 3);
      if constexpr (m == 66) DMA_W(4, slotA, 4);
      if constexpr (m == 68 || m == 72 || m == 78) DMA_W(4, slotA, m == 68 ? 5 : m == 72 ? 6 : 7);
      if constexpr (m == 73 || m == 94) bar();
      if constexpr (m >= 74 && m < 77) acc[m - 74][0] = ldsr<f32x4>(xrd + (m - 74) * 2048);
      if constexpr (m == 82 || m == 85 || m == 88) ldsw<f32x4>(xw + (m - 82) / 3 * 2048 + 1024, acc[(m - 82) / 3][3]);
      if constexpr (m >= 95 && m < 98) acc[m - 95][1] = ldsr<f32x4>(xrd + (m - 95) * 2048 + 1024);
      if constexpr (m == 77) {
#pragma unroll
        for (int f = 0; f < 8; ++f) PINA(wq[3][f]);
      }
      if constexpr (m >= 83 && m < 93 && (m - 83) % 3 != 2) {
        constexpr int x = (m - 83) / 3 * 2 + (m - 83) % 3;
        if constexpr (x < 4) pa[x] = ldsr<float>(rdo[0] + x * 256);
        else pb[x - 4] = ldsr<float>(rdo[1] + (x - 4) * 1024);
      }
      if constexpr (m == 93) pb[3] = ldsr<float>(rdo[1] + 3072);
      if constexpr (m == 98) { pa[0] += pa[1]; PIN(pa[0]); }
      if constexpr (m == 99) { pa[2] += pa[3]; PIN(pa[2]); }
      if constexpr (m == 101) { pb[0] += pb[1]; PIN(pb[0]); }
      if constexpr (m == 102) { pb[2] += pb[3]; PIN(pb[2]); }
      if constexpr (m == 104) { pa[0] += pa[2]; PIN(pa[0]); }
      if constexpr (m == 105) { pb[0] += pb[2]; PIN(pb[0]); }
      if constexpr (m == 107) { pa[0] *= (1.0f / D); PIN(pa[0]); }
      if constexpr (m == 108) { pa[1] = pa[0] * pa[0]; PIN(pa[1]); }
      if constexpr (m == 110) { pb[0] = __builtin_fmaf(pb[0], 1.0f / D, -pa[1]); PIN(pb[0]); }
      if constexpr (m == 111) { pb[0] = __builtin_amdgcn_fmed3f(pb[0], 0.f, 3.0e38f); PIN(pb[0]); }
      if constexpr (m == 113) { pb[0] += EPS; PIN(pb[0]); }
      if constexpr (m == 114) { pb[0] = __builtin_amdgcn_rsqf(pb[0]); PIN(pb[0]); }
      if constexpr (m == 116) { pa[0] = lane < 32 ? 1.0f : -pa[0]; PIN(pa[0]); }
      if constexpr (m == 117) { pb[0] *= pa[0]; PIN(pb[0]); }
      if constexpr (m == 119) ldsw<float>(a_fin, pb[0]);
      if constexpr (m == 120 || m == 122 || m == 123 || m == 125) {
        constexpr int x = m == 120 ? 0 : m == 122 ? 1 : m == 123 ? 2 : 3;
        if constexpr (x & 1) r4[x >> 1] = ldsr<f32x4>(a_fr + (x >> 1) * 64);
        else m4[x >> 1] = ldsr<f32x4>(a_fr + 128 + (x >> 1) * 64);
      }
      if constexpr (m >= 100 && m < 124 && (m - 100) % 3 == 0) DMA_W(5, slotB, (m - 100) / 3);
      SB();
    });
  }
#pragma unroll
  for (int x = 0; x < 3; ++x) {
    const float y = xrow_sum4(__uint_as_float(cz[1][x][0]), __uint_as_float(cz[1][x][1]), __uint_as_float(cz[1][x][0]), __uint_as_float(cz[1][x][1]));
    auto sw = __builtin_amdgcn_permlane16_swap(__float_as_uint(y), __float_as_uint(y), false, false);
    csb[1][x] = f32x2{__uint_as_float(sw[0]), __uint_as_float(sw[1])};
  }
#pragma unroll
  for (int k = 0; k < 6; ++k) PINA(acc[k >> 1][k & 1]);
  f32x4 z2v[3][2];
  {
    f32x4 rs[2], rq[2];
#pragma unroll
    for (int t = 0; t < 2; ++t) {
#pragma unroll
      for (int k = 0; k < 3; ++k) {
        const f32x4 a = acc[k][t];
        const f32x2 zl = gelu2(fma2(lo2(a), lo2(r4[t]), fma2(lo2(m4[t]), bc2(csb[1][k][0]), bc2(csb[1][k][1]))));
        const f32x2 zh = gelu2(fma2(hi2(a), hi2(r4[t]), fma2(hi2(m4[t]), bc2(csb[1][k][0]), bc2(csb[1][k][1]))));
        z2v[k][t] = f32x4{zl[0], zl[1], zh[0], zh[1]};
        if (k == 0) { rs[t] = z2v[k][t]; rq[t] = z2v[k][t] * z2v[k][t]; }
        else { rs[t] += z2v[k][t]; rq[t] += z2v[k][t] * z2v[k][t]; }
        if (((t * 3 + k) & 1) == 0) {
#pragma unroll
          for (int f = (t * 3 + k) * 4; f < (t * 3 + k) * 4 + 8; ++f) wg[f % 3][f / 3] = LOAD_WP(0, f % 3, f / 3);
        } else {
          PIN(z2v[k][t]);
          SB();
        }
      }
    }
    float v8[8], v4[4], v2[2];
    const bool b3 = c & 8, b2 = c & 4, b1 = c & 2, b0 = c & 1;
#pragma unroll
    for (int r_ = 0; r_ < 4; ++r_) {
      const float a0 = rs[0][r_] + dpp<0x128>(rs[0][r_]), a1 = rs[1][r_] + dpp<0x128>(rs[1][r_]);
      const float q0 = rq[0][r_] + dpp<0x128>(rq[0][r_]), q1 = rq[1][r_] + dpp<0x128>(rq[1][r_]);
      v8[2 * r_] = b3 ? a1 : a0; v8[2 * r_ + 1] = b3 ? q1 : q0;
    }
#pragma unroll
    for (int j = 0; j < 4; ++j) {
      const float x0 = v8[j] + dpp<0x141>(v8[j]), x1 = v8[4 + j] + dpp<0x141>(v8[4 + j]);
      v4[j] = b2 ? x1 : x0;
    }
#pragma unroll
    for (int j = 0; j < 2; ++j) {
      const float x0 = v4[j] + dpp<0x4E>(v4[j]), x1 = v4[2 + j] + dpp<0x4E>(v4[2 + j]);
      v2[j] = b1 ? x1 : x0;
    }
    const float x0 = v2[0] + dpp<0xB1>(v2[0]), x1 = v2[1] + dpp<0xB1>(v2[1]);
    const float outv = b0 ? x1 : x0;
    __builtin_amdgcn_raw_buffer_store_b32(nomark(__float_as_uint(outv)), rws, lane * 4, WS_ST2 + ((mt * 2 + kh) * 16 + nt * 2 + uh) * 256, SC1);
  }
  SB();
#pragma unroll
  for (int tu = 0; tu < 4; ++tu)
#pragma unroll
    for (int k = 0; k < 3; ++k) acc[k][tu] = f32x4{0.f, 0.f, 0.f, 0.f};
  unsigned k3v[6];
  {
    const unsigned k0 = l16 + w * 6144, w3 = (w + 1) >> 2;
    k3v[0] = k0; k3v[1] = k0 + 2048 - w3 * 1024; k3v[2] = k3v[1] + 1024; k3v[3] = k0 + 5120 - w3 * 2048;
    k3v[4] = k0 + 1024 + (w3 << 30); k3v[5] = k3v[4] + 3072;
  }
  v2u x1h[2][2];
  v4u wo[6][4];
#define LOAD_WO(f) wo[(f) >> 2][(f) & 3] = ld128<SC1>(rws, k3v[(f) >> 2], WS_WOT + (nt * 4 + ((f) & 3)) * 22 * 1024)
  auto dma_sel2 = [&](int i, int x) __attribute__((always_inline)) {
    if (i < 4) dma_sel(IC<6>{}, slotA, (i - 2) * 4 + x); else dma_sel(IC<7>{}, slotB, (i - 4) * 4 + x);
  };
  f32x4 pt[4];
  float* c1s = st + 768 + w * 96;
#define LOAD_PT(i) asf(ld128<SC1>(rws, l16 + (i) * 1024, WS_ST2 + (mt * 2 + kh) * 4096))
  SB();
#pragma unroll
  for (int i = 0; i < 8; ++i) {
#pragma unroll
    for (int j = 0; j < 12; ++j) {
      const int tu = j / 3, k = j % 3;
      const v4u a = {ah[i & 1][tu][0][0], ah[i & 1][tu][0][1], ah[i & 1][tu][1][0], ah[i & 1][tu][1][1]};
      acc[k][tu] = mfma(wg[k][i], a, acc[k][tu]);
      SB();
      if (j < 8 && i < 7) ah[(i + 1) & 1][j >> 1][j & 1] = AHALF(j >> 1, i + 1, j & 1);
      if (j < 4 && i == 7) x1h[j >> 1][j & 1] = trread(smem + x1o + (j >> 1) * 4096 + (j & 1) * 512);
      if (i == 0 && j == 0) asm volatile("s_waitcnt vmcnt(24)" ::: "memory");
      if (i < 2 && j >= 4) wq[4 + i][j - 4] = READ_W(i * 8192, j - 4);
      if (i == 3 && j == 0) {
#pragma unroll
        for (int f = 0; f < 16; ++f) PINA(wq[4 + (f >> 3)][f & 7]);
      }
      if (i == 2 && j == 8) USE(wg[2][7]);
      if (i >= 2 && i < 6 && j == 9) dma_sel2(i, 0);
      if (i >= 3 && i < 7 && (j == 1 || j == 4 || j == 7)) dma_sel2(i - 1, (j + 2) / 3);
      if (j >= 6 && j < 10 && i == 7) pt[j - 6] = LOAD_PT(j - 6);
      if (i == 5 && j == 10) {
        asm volatile("s_waitcnt vmcnt(30)" ::: "memory");
        __builtin_amdgcn_s_barrier();
      }
      SB();
    }
  }
#pragma unroll
  for (int k = 0; k < 3; ++k)
#pragma unroll
    for (int t = 0; t < 2; ++t) xch[(w * 6 + k * 2 + t) * 64 + lane] = acc[k][2 + t];
  SB();
  {
    unsigned m = 0;
#pragma unroll
    for (int i = 0; i < 4; ++i) m = macc4(m, __builtin_bit_cast(v4u, pt[i]));
    RETRY_BEGIN(m)
#pragma unroll
      for (int i = 0; i < 4; ++i) { pt[i] = LOAD_PT(i); m = macc4(m, __builtin_bit_cast(v4u, pt[i])); }
    RETRY_END(m)
    if (tid < 4) hint_add(p.ws, 64 + (nt * 4 + (mt >> 3)) * 4 + tid);
    const f32x4 sx = (pt[0] + pt[1]) + (pt[2] + pt[3]);
    const float tv = xrow_sum4(sx[0], sx[2], sx[1], sx[3]);
    auto sw = __builtin_amdgcn_permlane32_swap(__float_as_uint(tv), __float_as_uint(tv), false, false);
    const float mm = __uint_as_float(sw[0]) * (1.0f / FH);
    const float r = __builtin_amdgcn_rsqf(fmaxf(__uint_as_float(sw[1]) * (1.0f / FH) - mm * mm, 0.f) + EPS);
    float* f2 = st + 512 + w * 64;
    f2[(q >> 1) * 32 + 2 * c + (q & 1)] = (q >> 1) ? -r * mm : r;
#pragma unroll
    for (int t = 0; t < 2; ++t) { r4[t] = *(const f32x4*)(f2 + q * 8 + t * 4); m4[t] = *(const f32x4*)(f2 + 32 + q * 8 + t * 4); }
#pragma unroll
    for (int k = 0; k < 3; ++k) {
      v4u o;
#pragma unroll
      for (int t = 0; t < 2; ++t) {
        o[2 * t] = nomark(pk2(fma2(lo2(z2v[k][t]), lo2(r4[t]), lo2(m4[t]))));
        o[2 * t + 1] = pk2(fma2(hi2(z2v[k][t]), hi2(r4[t]), hi2(m4[t])));
      }
      st128<SC1>(o, rws, z2o[k], 0);
    }
  }
  c1s[lane] = xrow_sum4(__uint_as_float(cz[0][0][0]), __uint_as_float(cz[0][0][1]), __uint_as_float(cz[0][1][0]), __uint_as_float(cz[0][1][1]));
  c1s[64 + (lane & 31)] = xrow_sum4(__uint_as_float(cz[0][2][0]), __uint_as_float(cz[0][2][1]), __uint_as_float(cz[0][2][0]), __uint_as_float(cz[0][2][1]));
  st[1408 + lane] = rbf((__uint_as_float(gq[0]) + __uint_as_float(gq[1])) + (__uint_as_float(gq[2]) + __uint_as_float(gq[3])));
  SB();
  f32x4 z1f[3][2];
  constexpr int RD = 12;
  v4u zf[RD];
#define ZUNIT(ps) ((ps) < 2 ? ut0 + (ps) : (ps) < 4 ? uo0 + (ps) - 2 : (ps) == 4 ? ut2 : uo2)
#define LOAD_Z(n) ld128<SC1>(rws, zv[((n) >> 1) & 7] + ((n) & 1) * 1024, WS_Z2 + (ZUNIT((n) >> 4) * 64 + w * 16) * 1024 + ((n) >= 64 ? oob3 : 0u))
  unsigned zv[8];
#pragma unroll
  for (int i = 0; i < 8; ++i) { zv[i] = l16 + ((CKMAP(i) + kr) & 7) * 2048; PIN(zv[i]); }
  {
    bar();
#pragma unroll
    for (int k = 0; k < 6; ++k) PINA(acc[k >> 1][k & 1]);
    f32x4 oth[3][2], cs1[3], bs1[3];
    float r1[2], m1[2];
#pragma unroll
    for (int t = 0; t < 2; ++t) { r1[t] = fin[t * 16 + c]; m1[t] = fin[32 + t * 16 + c]; }
#pragma unroll
    for (int k = 0; k < 3; ++k) {
      cs1[k] = *(const f32x4*)(c1s + k * 32 + q * 4); bs1[k] = *(const f32x4*)(c1s + k * 32 + 16 + q * 4);
#pragma unroll
      for (int t = 0; t < 2; ++t) oth[k][t] = xch[((w ^ 1) * 6 + k * 2 + t) * 64 + lane];
    }
#pragma unroll
    for (int i = 0; i < WO0; ++i) LOAD_WO(i);
    SB();
    f32x2 z1a[6][2];
#pragma unroll
    for (int i = 0; i < 6; ++i) {
      const int k = i >> 1, t = i & 1;
      const f32x4 a = acc[k][t] + oth[k][t];
      z1a[i][0] = fma2(lo2(a), bc2(r1[t]), fma2(lo2(cs1[k]), bc2(m1[t]), lo2(bs1[k])));
      z1a[i][1] = fma2(hi2(a), bc2(r1[t]), fma2(hi2(cs1[k]), bc2(m1[t]), hi2(bs1[k])));
    }
#pragma unroll
    for (int i = 0; i < GELU_PRE; ++i) {
      const f32x2 lo = gelu2(z1a[i][0]), hi = gelu2(z1a[i][1]);
      z1f[i >> 1][i & 1] = f32x4{lo[0], lo[1], hi[0], hi[1]};
      PIN(z1f[i >> 1][i & 1]);
    }
#pragma unroll
    for (int i = GELU_PRE; i < 6; ++i) { PIN(z1a[i][0]); PIN(z1a[i][1]); }
    SB();
#pragma unroll
    for (int f = 0; f < 16; ++f) wq[6 + (f >> 3)][f & 7] = READ_W((f >> 3) * 8192, f & 7);
    SB();
    hint_wait(rws, 0u, WS_CNT + (64 + (nt * 4 + w) * 4 + (xcd >> 1)) * 128, 8u);
#pragma unroll
    for (int n = 0; n < RD; ++n) zf[n] = LOAD_Z(n);
    SB();
#pragma unroll
    for (int i = GELU_PRE; i < 6; ++i) {
      const f32x2 lo = gelu2(z1a[i][0]), hi = gelu2(z1a[i][1]);
      z1f[i >> 1][i & 1] = f32x4{lo[0], lo[1], hi[0], hi[1]};
      PIN(z1f[i >> 1][i & 1]);
    }
  }
  f32x4* xb = (f32x4*)(smem + LDS_A) + lane;
  f32x4* yb = (f32x4*)(smem + LDS_A + 49152) + lane;
  f32x4 ac2[2][4], ini[4], yo[2];
  const float bs0 = bflo(bspq[0]), bs1_ = bfhi(bspq[0]);
  f32x4 bq0 = {bs0, bs0, bs0, bs0}, bq1 = {bs1_, bs1_, bs1_, bs1_};
  const f32x4 z4 = {0.f, 0.f, 0.f, 0.f};
  PINA(bq0); PINA(bq1);
  unsigned vmk = l16 + WS_V + ((mt * 4 + w) * 22 + pr0) * 1024;
  gu32* p_fl4 = (gu32*)(p.ws + WS_CNT + (192 + mt * 2 + (tid & 1)) * 128);
  PIN(vmk); PIN(mkv);
  unsigned voff[3];
  voff[0] = l16 + WS_V + ((mt * 4 + 2 * kh) * 22 + (uo0 >> 1)) * 1024;
  voff[1] = l16 + WS_V + ((mt * 4 + 2 * kh) * 22 + (uo2 >> 1)) * 1024 + (uo2 & 1) * 8 + oob3;
  voff[2] = voff[0] + 22 * 1024;
  PIN(voff[0]); PIN(voff[1]); PIN(voff[2]);
  float ev[4];
  v4u eo[2];
  f32x4 yx[2][2];
  v4u vs[6][4];
#define LOAD_V(f) vs[(f) >> 2][(f) & 3] = ld128<SC1>(rws, k3v[(f) >> 2], WS_V + (mt * 4 + (((f) & 3) ^ w)) * 22 * 1024)
  SB();
  sfor<6>([&](auto P) __attribute__((always_inline)) {
    constexpr int ps = decltype(P)::value, cur = ps & 1, prv = cur ^ 1;
    constexpr bool prevT = ps == 1 || ps == 2 || ps == 5, epi = ps == 3 || ps == 4;
    unsigned zm;
    sfor<64>([&](auto G) __attribute__((always_inline)) {
      constexpr int g = decltype(G)::value, ks = g >> 2, tu = g & 3, ck = ks >> 1, k2 = ks & 1, n = ps * 16 + ks;
      const f32x4 cin = ks > 0 ? ac2[cur][tu] : (ps == 2 || ps == 3) ? ini[tu] : ps == 5 ? z4 : tu == 0 ? bq0 : tu == 1 ? bq1 : z4;
      ac2[cur][tu] = mfma(zf[n % RD], wq[ck][k2 * 4 + tu], cin);
      SB();
      if constexpr (tu == 1) { zm = ks == 0 ? (zf[n % RD][0] > zf[n % RD][2] ? zf[n % RD][0] : zf[n % RD][2]) : macc(zm, zf[n % RD]); PIN(zm); }
      if constexpr (tu == 3) {
        if constexpr (n + RD < 96) zf[n % RD] = LOAD_Z(n + RD);
        else if constexpr (WO0 + n + RD - 96 < 16) LOAD_WO(WO0 + n + RD - 96);
      }
      if constexpr (prevT && g >= 2 && g < 18 && (g & 3) == 2) xb[((w * 3 + (ps == 5 ? 2 : ps - 1)) * 4 + (g >> 2)) * 64] = ac2[prv][g >> 2];
      if constexpr (epi && (g == 2 || g == 6)) yb[((w * 3 + ps - 3) * 2 + (g >> 2)) * 64] = ac2[prv][2 + (g >> 2)];
      if constexpr (ps >= 1 && g == (epi ? 12 : 24)) {
        if constexpr (ps == 3) asm volatile("s_waitcnt vmcnt(20)" ::: "memory");
        bar();
      }
      if constexpr (epi && (g == 13 || g == 14)) yo[g - 13] = yb[(((w ^ 1) * 3 + ps - 3) * 2 + g - 13) * 64];
      if constexpr (epi && g >= 18 && g < 50 && (g & 1) == 0) {
        constexpr int t = (g - 18) / 16, ph = ((g - 18) % 16) >> 1, h = (ps - 3) * 2;
        if constexpr (ph == 0) PINA(ac2[prv][t]);
        if constexpr (ph < 4) { ev[ph] = ac2[prv][t][ph] + yo[t][ph]; PIN(ev[ph]); }
        else if constexpr (ph < 6) { ev[2 * ph - 8] *= z1f[ps - 3][t][2 * ph - 8]; PIN(ev[2 * ph - 8]); ev[2 * ph - 7] *= z1f[ps - 3][t][2 * ph - 7]; PIN(ev[2 * ph - 7]); }
        else if constexpr (ph == 6) { eo[t][h] = nomark(pk2(ev[0], ev[1])); PIN(eo[t]); }
        else {
          eo[t][h + 1] = pk2(ev[2], ev[3]);
          if constexpr (ps == 4) st128<SC1>(eo[t], rws, voff[0 + 2 * t], 0);
          else PIN(eo[t]);
        }
      }
      if constexpr ((ps == 1 || ps == 2) && g >= 48 && g < 56 && (g & 1) == 0) ini[(g - 48) >> 1] = xb[(((w ^ 2) * 3 + ps - 1) * 4 + ((g - 48) >> 1)) * 64];
      if constexpr (ps == 5 && (g == 25 || g == 26)) { yx[0][g - 25] = xb[(((w ^ 2) * 3 + 2) * 4 + g - 25) * 64]; yx[1][g - 25] = xb[(((w ^ 3) * 3 + 2) * 4 + 2 + g - 25) * 64]; }
      if constexpr (ps == 0 && (g == 22 || g == 38 || g == 54)) st128<SC1>(mkv, rws, vmk + (g - 22) / 16 * 1024, g == 54 ? oob3 : 0u);
      if constexpr (ps * 64 + g == HINT4_G) { if (tid < 2) (void)__hip_atomic_fetch_add(p_fl4, 1u, __ATOMIC_RELAXED, __HIP_MEMORY_SCOPE_AGENT); }
      if constexpr (ps == 2 && g == 40) PIN(p_fl4);
      if constexpr (ps == 5 && g == POLL4_G) flagv = __builtin_amdgcn_raw_buffer_load_b32(rws, 0, WS_CNT + (192 + mt * 2 + (xcd & 1)) * 128, SC1);
      if constexpr (ps == 5 && g == VPRE_G) {
        if (__builtin_expect(__builtin_amdgcn_readfirstlane(flagv) < 8u, 0)) hint_wait(rws, 0u, WS_CNT + (192 + mt * 2 + (xcd & 1)) * 128, 8u);
        MEMFENCE();
      }
      if constexpr (ps == 5 && g > VPRE_G && (g & 1) == 0 && (g - VPRE_G - 1) / 2 < 12) LOAD_V((g - VPRE_G - 1) / 2);
      SB();
    });
    if (__builtin_expect(any_mark(zm), 0)) {
      int l2 = lane;
      PIN(l2);
#pragma unroll
      for (int tu = 0; tu < 4; ++tu) ac2[cur][tu] = (ps == 2 || ps == 3) ? xb[(((w ^ 2) * 3 + ps - 2) * 4 + tu) * 64] : ps == 5 ? z4 : tu == 0 ? bq0 : tu == 1 ? bq1 : z4;
      if constexpr (ps < 5) {
        sfor<4>([&](auto K) __attribute__((always_inline)) {
          constexpr int k4 = decltype(K)::value * 4;
          v4u zz[4];
          unsigned sp = 0;
          do {
            MEMFENCE();
            zm = 0;
#pragma unroll
            for (int i = 0; i < 4; ++i) { zz[i] = LOAD_Z(ps * 16 + k4 + i); zm = macc(zm, zz[i]); }
          } while (any_mark(zm) & (++sp <= SPIN_CAP));
#pragma unroll
          for (int i = 0; i < 4; ++i)
#pragma unroll
            for (int tu = 0; tu < 4; ++tu) ac2[cur][tu] = mfma(zz[i], wq[(k4 + i) >> 1][(i & 1) * 4 + tu], ac2[cur][tu]);
        });
      } else
#pragma unroll 1
      for (int ks = 0; ks < 16; ++ks) {
        const int kk = ((CKMAP(ks >> 1) + kr) & 7) * 2 + (ks & 1);
        v4u zz, ww[4];
        unsigned sp = 0;
        do { MEMFENCE(); zz = ld128<SC1>(rws, l2 * 16 + kk * 1024, WS_Z2 + (ZUNIT(ps) * 64 + w * 16) * 1024 + (ps >= 4 ? oob3 : 0u)); } while (any_mark(macc(0u, zz)) & (++sp <= SPIN_CAP));
#pragma unroll
        for (int tu = 0; tu < 4; ++tu) ww[tu] = ld128<0>(rwsp, perm(tu ^ (2 * kh), l2 & 15) * 4096 + (l2 >> 4) * 16 + kk * 64, wsp_s);
#pragma unroll
        for (int tu = 0; tu < 4; ++tu) ac2[cur][tu] = mfma(zz, ww[tu], ac2[cur][tu]);
      }
    }
  });
  {
#pragma unroll
    for (int t = 0; t < 2; ++t) yb[((w * 3 + 2) * 2 + t) * 64] = ac2[1][2 + t];
#pragma unroll
    for (int t = 0; t < 2; ++t) { yx[0][t] = (yx[0][t] + yx[1][t]) + ac2[1][t]; PIN(yx[0][t]); }
    bar();
#pragma unroll
    for (int t = 0; t < 2; ++t) yo[t] = yb[(((w ^ 1) * 3 + 2) * 2 + t) * 64];
#pragma unroll
    for (int t = 0; t < 2; ++t) {
      const f32x4 g = (yx[0][t] + yo[t]) * z1f[2][t];
      st64<SC1>(v2u{nomark(pk2(lo2(g))), pk2(hi2(g))}, rws, voff[1], t * 22 * 1024);
    }
  }
  SB();
  f32x4 gb4[4];
  f32x2 x1f[2][4];
  f32x4 a3[4][4];
  unsigned a_rw, a_rr[3], o_out;

  {
    {
      unsigned m = 0;
      sfor<64>([&](auto G) __attribute__((always_inline)) {
        constexpr int g = decltype(G)::value, ks = g >> 4, tu = (g >> 2) & 3, e = g & 3;
        a3[e][tu] = mfma(wo[ks][e], vs[ks][tu], ks == 0 ? f32x4{0.f, 0.f, 0.f, 0.f} : a3[e][tu]);
        SB();
        if constexpr (e == 0 && g < 16) LOAD_V(12 + (g >> 2));
        if constexpr (e == 2 && g < 32) LOAD_WO(16 + (g >> 2));
        if constexpr (g >= LATE_G && g < LATE_G + 16 && (g & 1) == 0) LOAD_V(16 + (((g - LATE_G) >> 1) & 1) * 4 + ((((g - LATE_G) >> 2) + 1) & 3));
        if constexpr (e == 1) { m = macc(m, vs[ks][tu]); PIN(m); }
        SB();
      });
      if (__builtin_expect(any_mark(m), 0)) {
        sfor<4>([&](auto K) __attribute__((always_inline)) {
          constexpr int ks = decltype(K)::value;
          unsigned sp = 0;
          do {
            MEMFENCE();
            m = 0;
#pragma unroll
            for (int tu = 0; tu < 4; ++tu) { LOAD_V(ks * 4 + tu); m = macc(m, vs[ks][tu]); }
          } while (any_mark(m) & (++sp <= SPIN_CAP));
#pragma unroll
          for (int tu = 0; tu < 4; ++tu)
#pragma unroll
            for (int e = 0; e < 4; ++e) a3[e][tu] = mfma(wo[ks][e], vs[ks][tu], ks == 0 ? f32x4{0.f, 0.f, 0.f, 0.f} : a3[e][tu]);
        });
      }
    }
#pragma unroll
    for (int i = 0; i < 4; ++i) gb4[i] = *(const f32x4*)(st + 1408 + (i >> 1) * 32 + q * 8 + (i & 1) * 4);
#pragma unroll
    for (int i = 0; i < 8; ++i) { x1f[i >> 2][i & 3] = unpk(x1h[i >> 2][(i >> 1) & 1][i & 1]); PIN(x1f[i >> 2][i & 3]); }
    int lz = lane;
    PIN(lz);
    a_rw = lds0 + LDS_W + w * 12288 + lz * 16;
#pragma unroll
    for (int rel = 1; rel < 4; ++rel) { a_rr[rel - 1] = lds0 + LDS_W + ((w ^ rel) * 3 + rel - 1) * 4096 + lz * 16; PIN(a_rr[rel - 1]); }
    o_out = (mt * 64 + perm(w, lz & 15)) * 1024 + (lz >> 4) * 16 + nt * 128;
    PIN(a_rw); PIN(o_out);
    SB();
    unsigned m = 0;
#pragma unroll
    for (int f = 16; f < 24; ++f) m = macc(m, vs[f >> 2][f & 3]);
    RETRY_BEGIN(m)
#pragma unroll
      for (int f = 16; f < 24; ++f) { LOAD_V(f); m = macc(m, vs[f >> 2][f & 3]); }
    RETRY_END(m)
    sfor<32>([&](auto G) __attribute__((always_inline)) {
      constexpr int g = decltype(G)::value, tu = ((g >> 3) + 1) & 3, ks = 4 + ((g >> 2) & 1), e = g & 3;
      a3[e][tu] = mfma(wo[ks][e], vs[ks][tu], a3[e][tu]);
      SB();
      if constexpr (g >= 8 && (g & 1) == 0) ldsw<f32x4>(a_rw + (((g >> 3) - 1) * 4 + ((g >> 1) & 3)) * 1024, a3[(g >> 1) & 3][g >> 3]);
      SB();
    });
  }
  {
#pragma unroll
    for (int e = 0; e < 4; ++e) PIN(a3[e][0]);
    bar();
    f32x4 o3[4][3];
#pragma unroll
    for (int e = 0; e < 4; ++e)
#pragma unroll
      for (int rel = 1; rel < 4; ++rel) o3[e][rel - 1] = ldsr<f32x4>(a_rr[rel - 1] + e * 1024);
    const rsrc_t rout = mkrs(p.out, L * D * 2);
    SB();
#pragma unroll
    for (int pp = 0; pp < 2; ++pp) {
      v4u o;
#pragma unroll
      for (int h = 0; h < 2; ++h) {
        const int e = pp * 2 + h;
        const f32x4 y = (a3[e][0] + o3[e][0]) + (o3[e][1] + o3[e][2]);
        o[2 * h] = pk2(fma2(lo2(y), lo2(gb4[e]), x1f[pp][2 * h]));
        o[2 * h + 1] = pk2(fma2(hi2(y), hi2(gb4[e]), x1f[pp][2 * h + 1]));
      }
      st128<0>(o, rout, o_out + pp * 64, 0);
    }
  }
  if (b == 0 && tid == 0) *(unsigned*)(p.ws + WS_EPOCH) = tag_s;
  asm volatile("adaln_code_end:");
}

extern "C" void kernel_launch(void* const* d_in, const int* in_sizes, int n_in, void* d_out, int out_size, void* d_ws,
                              size_t ws_size, hipStream_t stream) {
  Params p;
  p.u = (const u16*)d_in[0];
  p.lar = (const u16*)d_in[1];
  p.aim = (const u16*)d_in[2];
  p.cre = (const u16*)d_in[3];
  p.cim = (const u16*)d_in[4];
  p.ldt = (const u16*)d_in[5];
  p.dsk = (const u16*)d_in[6];
  p.win = (const u16*)d_in[8];
  p.wsp = (const u16*)d_in[9];
  p.bsp = (const u16*)d_in[10];
  p.wout = (const u16*)d_in[11];
  p.c = (const u16*)d_in[12];
  p.wmod = (const u16*)d_in[13];
  p.out = (u16*)d_out;
  p.ws = (unsigned char*)d_ws;
  (void)hipFuncSetAttribute((const void*)adaln_mega, hipFuncAttributeMaxDynamicSharedMemorySize, MEGA_LDS);
  adaln_mega<<<256, 256, MEGA_LDS, stream>>>(p);
}
```
